```python
import torch

def _softmax(x, axis=-1):
    return torch.softmax(x, dim=axis)
def _gelu(x):
    return torch.nn.functional.gelu(x, approximate="tanh")
def _silu(x):
    return torch.nn.functional.silu(x)
def _sigmoid(x):
    return torch.sigmoid(x)
def _layernorm(x, axis=-1, eps=1e-5):
    m = x.mean(dim=axis, keepdim=True)
    v = x.var(dim=axis, keepdim=True, unbiased=False)
    return (x - m) / torch.sqrt(v + eps)
def _scatter_add(tgt, idx, src, axis=0):
    return tgt.clone().index_add_(axis, idx, src)
def _segment_sum(src, seg, n):
    out = torch.zeros((n,) + src.shape[1:], dtype=src.dtype)
    return out.index_add_(0, seg, src)
def _pad(x, pw):
    flat = tuple(v for pair in reversed(pw) for v in pair)
    return torch.nn.functional.pad(x, flat)

kernel_name = 'concat_heads__ssm_conv_s4d__compressed_kv_cla__prompt'

def setup_inputs():

    _w = (lambda a: (lambda t: t.to(torch.bfloat16) if t.is_floating_point() else t)(torch.as_tensor(a)))
    def _A_setup():
        Bsz, L, D, N = 1, 2048, 512, 128
        return {
            "u": _w((lambda *s: torch.randn(*s, dtype=torch.bfloat16) * 0.05)(Bsz, L, D)),
            "log_A_real": _w((lambda *s: torch.randn(*s, dtype=torch.bfloat16) * 6.4)(D, N) * 0.5),
            "A_imag": _w((lambda *s: torch.randn(*s, dtype=torch.bfloat16) * 0.05)(D, N) * 3.14),
            "C_real": _w((lambda *s: torch.randn(*s, dtype=torch.bfloat16) * 0.05)(D, N) / N ** 0.5),
            "C_imag": _w((lambda *s: torch.randn(*s, dtype=torch.bfloat16) * 0.05)(D, N) / N ** 0.5),
            "log_dt": _w((lambda *s: torch.randn(*s, dtype=torch.bfloat16) * 0.05)(D) * 0.5 - 4.0),
            "D_skip": _w((lambda *s: torch.randn(*s, dtype=torch.bfloat16) * 0.05)(D)),
        }
    _A_in = _A_setup()

    def _B_setup():
        B, H, Sq, Sk, D, M = 1, 32, 2048, 2048, 128, 512
        Dc = 4 * D
        return {
            "x": _w((lambda *s: torch.randn(*s, dtype=torch.bfloat16) * 0.8)(B, Sq, M)),
            "c_kv": _w((lambda *s: torch.randn(*s, dtype=torch.bfloat16) * 0.2)(B, Sk, Dc)),
            "w_q": _w((lambda *s: torch.randn(*s, dtype=torch.bfloat16) * 3.2)(M, H, D) * (M ** -0.5)),
            "w_uk": _w((lambda *s: torch.randn(*s, dtype=torch.bfloat16) * 3.2)(Dc, H, D) * (Dc ** -0.5)),
            "w_uv": _w((lambda *s: torch.randn(*s, dtype=torch.bfloat16) * 0.2)(Dc, H, D) * (Dc ** -0.5)),
            "w_o": _w((lambda *s: torch.randn(*s, dtype=torch.bfloat16) * 0.2)(H, D, M) * ((H * D) ** -0.5)),
        }
    _B_in = _B_setup()

    return {
        "A_u": _A_in["u"],
        "A_log_A_real": _A_in["log_A_real"],
        "A_A_imag": _A_in["A_imag"],
        "A_C_real": _A_in["C_real"],
        "A_C_imag": _A_in["C_imag"],
        "A_log_dt": _A_in["log_dt"],
        "A_D_skip": _A_in["D_skip"],
        "B_x": _B_in["x"],
        "B_c_kv": _B_in["c_kv"],
        "B_w_q": _B_in["w_q"],
        "B_w_uk": _B_in["w_uk"],
        "B_w_uv": _B_in["w_uv"],
        "B_w_o": _B_in["w_o"],
    }

def reference(A_u, A_log_A_real, A_A_imag, A_C_real, A_C_imag, A_log_dt, A_D_skip, B_x, B_c_kv, B_w_q, B_w_uk, B_w_uv, B_w_o):
    def _A_ref(u, log_A_real, A_imag, C_real, C_imag, log_dt, D_skip):
        L = u.shape[1]
        dt = torch.exp(log_dt)
        A = -torch.exp(log_A_real) + 1j * A_imag
        C = (C_real + 1j * C_imag) * dt[:, None]
        pos = (lambda x, dt: x.to(dt))(torch.arange(L), log_dt.dtype)
        P = torch.exp((A * dt[:, None])[:, :, None] * pos[None, None, :])
        K = 2.0 * torch.einsum("dn,dnl->dl", C, P).real
        L2 = 2 * L
        u_t = (lambda x, *d: x.permute(*d))(u, 0, 2, 1)
        Kf = (lambda x, *a, **k: torch.fft.rfft(x.float(), *a, **k))(K, L2)
        Uf = (lambda x, *a, **k: torch.fft.rfft(x.float(), *a, **k))(u_t, L2)
        y = (lambda x, *a, **k: torch.fft.irfft(x, *a, **k).to(torch.bfloat16))(Uf * Kf[None, :, :], L2)[:, :, :L]
        y = (lambda x, *d: x.permute(*d))(y, 0, 2, 1)
        return y + u * D_skip[None, None, :]

    def _B_ref(x, c_kv, w_q, w_uk, w_uv, w_o):
        B, Sq, M = x.shape[0], x.shape[1], x.shape[2]
        Sk = c_kv.shape[1]
        H, D = w_q.shape[1], w_q.shape[2]
        q = torch.einsum("bqm,mhd->bhqd", x, w_q)
        k = torch.einsum("bkc,chd->bhkd", c_kv, w_uk)
        v = torch.einsum("bkc,chd->bhkd", c_kv, w_uv)
        scores = torch.einsum("bhqd,bhkd->bhqk", q, k) * (D ** -0.5)
        off = Sk - Sq
        qi = torch.arange(Sq)[:, None]
        kj = torch.arange(Sk)[None, :]
        cmask = kj <= (qi + off)
        scores = torch.where(cmask[None, None], scores, -1e9)
        p = _softmax(scores, axis=-1)
        ctx = torch.einsum("bhqk,bhkd->bhqd", p, v)
        return torch.einsum("bhqd,hdm->bqm", ctx, w_o)

    a = _A_ref(A_u, A_log_A_real, A_A_imag, A_C_real, A_C_imag, A_log_dt, A_D_skip)
    b = _B_ref(B_x, B_c_kv, B_w_q, B_w_uk, B_w_uv, B_w_o)
    return torch.concatenate([a, b], axis=-1)
```

```cpp
#include <hip/hip_runtime.h>
#include <hip/hip_bf16.h>
#include <stdint.h>

typedef unsigned short u16;
typedef short bf16x8 __attribute__((ext_vector_type(8)));
typedef short s16x4 __attribute__((ext_vector_type(4)));
typedef _Float16 f16x8 __attribute__((ext_vector_type(8)));
typedef _Float16 f16x2 __attribute__((ext_vector_type(2)));
typedef float f32x16 __attribute__((ext_vector_type(16)));
typedef float f32x4 __attribute__((ext_vector_type(4)));
typedef float f32x2 __attribute__((ext_vector_type(2)));
typedef __bf16 bf16x2_t __attribute__((ext_vector_type(2)));
typedef unsigned u32x4 __attribute__((ext_vector_type(4)));
typedef unsigned u32x2 __attribute__((ext_vector_type(2)));
#define DI __device__ __forceinline__
#define LAS __attribute__((address_space(3)))
#define MFMA32(a, b, c) __builtin_amdgcn_mfma_f32_32x32x16_bf16((a), (b), (c), 0, 0, 0)

constexpr int SEQ = 2048, NH = 32, HD = 128, DM = 512, NSSM = 128;
constexpr float QSCALE = 0.12751743082f;

DI unsigned cvtpk(float lo, float hi) { f32x2 v = {lo, hi}; bf16x2_t b = __builtin_convertvector(v, bf16x2_t); return __builtin_bit_cast(unsigned, b); }
DI float bf2f(u16 v) { return __uint_as_float((unsigned)v << 16); }
DI u16 f2bf(float f) { return (u16)(cvtpk(f, 0.f) & 0xffffu); }
DI float bfr(float f) { return bf2f(f2bf(f)); }
DI bf16x8 pack8(const f32x16& x, int s, float sc) {
  u32x4 w = {cvtpk(x[8 * s] * sc, x[8 * s + 1] * sc), cvtpk(x[8 * s + 2] * sc, x[8 * s + 3] * sc), cvtpk(x[8 * s + 4] * sc, x[8 * s + 5] * sc), cvtpk(x[8 * s + 6] * sc, x[8 * s + 7] * sc)};
  return __builtin_bit_cast(bf16x8, w);
}
typedef short v4i16_t __attribute__((ext_vector_type(4)));
DI s16x4 trread(const LAS char* p) { return __builtin_bit_cast(s16x4, __builtin_amdgcn_ds_read_tr16_b64_v4i16((LAS v4i16_t*)p)); }

__global__ __launch_bounds__(512, 2) void k_proj(const u16* __restrict__ x, const u16* __restrict__ ckv, const u16* __restrict__ wq, const u16* __restrict__ wk,
                                                 const u16* __restrict__ wv, u16* __restrict__ Qf, u16* __restrict__ KVf) {
  __shared__ __attribute__((aligned(16))) char lds[2 * 16384];
  const int tid = threadIdx.x, lane = tid & 63, w = __builtin_amdgcn_readfirstlane(tid >> 6), l32 = lane & 31, hh = lane >> 5;
  const int bid = blockIdx.x, xcd = bid & 7, r = bid >> 3, h = xcd * 4 + (r & 3), tb = r >> 2;
  const int tb32 = tb * 8 + w;
  const int lbase = hh * 2048 + ((lane & 15) >> 2) * 64 + ((lane >> 4) & 1) * 32 + (lane & 3) * 8;
  const int srow = w * 8 + ((lane >> 2) & 7), scol = (lane >> 5) * 32 + (lane & 3) * 8;
  bf16x8 act[32];
#pragma unroll
  for (int pass = 0; pass < 3; ++pass) {
    const u16* W = pass == 0 ? wk : (pass == 1 ? wv : wq);
    if (pass != 1) {
      const u16* A = (pass == 0 ? ckv : x) + (size_t)(tb32 * 32 + l32) * DM + hh * 8;
#pragma unroll
      for (int ks = 0; ks < 32; ++ks) act[ks] = *(const bf16x8*)(A + ks * 16);
    }
    f32x16 acc[4] = {};
    auto stage = [&](int kt, int buf) {
#pragma unroll
      for (int i = 0; i < 2; ++i)
        __builtin_amdgcn_global_load_lds((const unsigned*)(W + (size_t)(kt * 64 + srow) * 4096 + h * 128 + i * 64 + scol), (LAS unsigned*)(lds + buf * 16384 + (w * 4 + i * 2) * 512), 16, 0, 0);
    };
    __syncthreads();
    stage(0, 0);
#pragma unroll
    for (int kt = 0; kt < 8; ++kt) {
      asm volatile("s_waitcnt vmcnt(0)" ::: "memory");
      __syncthreads();
      if (kt + 1 < 8) stage(kt + 1, (kt + 1) & 1);
      const LAS char* base = (const LAS char*)lds + (kt & 1) * 16384 + lbase;
#pragma unroll
      for (int ks = 0; ks < 4; ++ks)
#pragma unroll
        for (int nb = 0; nb < 4; ++nb) {
          s16x4 lo = trread(base + ks * 4096 + nb * 512), hi = trread(base + ks * 4096 + nb * 512 + 256);
          bf16x8 wf = __builtin_shufflevector(lo, hi, 0, 1, 2, 3, 4, 5, 6, 7);
          if (pass == 1) acc[nb] = MFMA32(act[kt * 4 + ks], wf, acc[nb]);
          else acc[nb] = MFMA32(wf, act[kt * 4 + ks], acc[nb]);
        }
    }
    if (pass == 0) {
      u16* o = KVf + ((size_t)(h * 64 + tb32) * 16) * 512 + lane * 8;
#pragma unroll
      for (int nb = 0; nb < 4; ++nb)
#pragma unroll
        for (int s = 0; s < 2; ++s) *(bf16x8*)(o + (2 * nb + s) * 512) = pack8(acc[nb], s, 1.f);
    } else if (pass == 1) {
      u16* o = KVf + ((size_t)(h * 64 + tb32) * 16 + 8) * 512 + lane * 8;
#pragma unroll
      for (int nb = 0; nb < 4; ++nb)
#pragma unroll
        for (int s = 0; s < 2; ++s) *(bf16x8*)(o + (s * 4 + nb) * 512) = pack8(acc[nb], s, 1.f);
    } else {
      u16* o = Qf + ((size_t)(h * 64 + tb32) * 8) * 512 + lane * 8;
#pragma unroll
      for (int nb = 0; nb < 4; ++nb)
#pragma unroll
        for (int s = 0; s < 2; ++s) *(bf16x8*)(o + (2 * nb + s) * 512) = pack8(acc[nb], s, QSCALE);
    }
  }
}

DI void attn_unit(const LAS char* slot, const bf16x8 (&q)[8], f32x16 (&o)[4], float& m, float& l, int lane, int rel  ) {
  const int l32 = lane & 31, hh = lane >> 5;
  f32x16 s = {};
#pragma unroll
  for (int ds = 0; ds < 8; ++ds) { bf16x8 kf = *(const LAS bf16x8*)(slot + ds * 1024 + lane * 16); s = MFMA32(kf, q[ds], s); }
  if (rel == 0) {
#pragma unroll
    for (int r = 0; r < 16; ++r) { const int key = (r & 3) + 8 * (r >> 2) + 4 * hh; if (key > l32) s[r] = -INFINITY; }
  }
  float mx = s[0];
#pragma unroll
  for (int r = 1; r < 16; ++r) mx = fmaxf(mx, s[r]);
  mx = fmaxf(mx, __shfl_xor(mx, 32));
  const float mn = fmaxf(m, mx), f = __builtin_amdgcn_exp2f(m - mn);
  m = mn;
  float ps = 0.f;
#pragma unroll
  for (int r = 0; r < 16; ++r) { s[r] = __builtin_amdgcn_exp2f(s[r] - mn); ps += s[r]; }
  l = l * f + ps;
#pragma unroll
  for (int d = 0; d < 4; ++d)
#pragma unroll
    for (int r = 0; r < 16; ++r) o[d][r] *= f;
#pragma unroll
  for (int st = 0; st < 2; ++st) {
    bf16x8 p = pack8(s, st, 1.f);
#pragma unroll
    for (int d = 0; d < 4; ++d) { bf16x8 vf = *(const LAS bf16x8*)(slot + 8192 + (st * 4 + d) * 1024 + lane * 16); o[d] = MFMA32(vf, p, o[d]); }
  }
}
__global__ __launch_bounds__(256, 1) void k_attn(const u16* __restrict__ Qf, const u16* __restrict__ KVf, u16* __restrict__ Cf) {
  __shared__ __attribute__((aligned(16))) char lds[2 * 16384];
  const int tid = threadIdx.x, lane = tid & 63, w = __builtin_amdgcn_readfirstlane(tid >> 6);
  const int bid = blockIdx.x, xcd = bid & 7, r = bid >> 3, h = xcd * 4 + (r & 3), xa = r >> 2, xb = 15 - xa;
  const int ta = xa * 4 + w, tbq = xb * 4 + w;
  bf16x8 qa[8], qb[8];
#pragma unroll
  for (int ds = 0; ds < 8; ++ds) {
    qa[ds] = *(const bf16x8*)(Qf + ((size_t)(h * 64 + ta) * 8 + ds) * 512 + lane * 8);
    qb[ds] = *(const bf16x8*)(Qf + ((size_t)(h * 64 + tbq) * 8 + ds) * 512 + lane * 8);
  }
  f32x16 oa[4] = {}, ob[4] = {};
  float ma = -1e30f, la = 0.f, mb = -1e30f, lb = 0.f;
  const int nkb = xb * 4 + 4;
  const u16* kv = KVf + (size_t)h * 64 * 8192 + w * 2048 + lane * 8;
  auto stage = [&](int kb, int buf) {
#pragma unroll
    for (int i = 0; i < 4; ++i)
      __builtin_amdgcn_global_load_lds((const unsigned*)(kv + (size_t)kb * 8192 + i * 512), (LAS unsigned*)(lds + buf * 16384 + w * 4096 + i * 1024), 16, 0, 0);
  };
  stage(0, 0);
  for (int kb = 0; kb < nkb; ++kb) {
    asm volatile("s_waitcnt vmcnt(0)" ::: "memory");
    __syncthreads();
    if (kb + 1 < nkb) stage(kb + 1, (kb + 1) & 1);
    const LAS char* slot = (const LAS char*)lds + (kb & 1) * 16384;
    if (kb <= ta) attn_unit(slot, qa, oa, ma, la, lane, kb - ta);
    if (kb <= tbq) attn_unit(slot, qb, ob, mb, lb, lane, kb - tbq);
  }
  la += __shfl_xor(la, 32); lb += __shfl_xor(lb, 32);
  const float ia = 1.f / la, ib = 1.f / lb;
#pragma unroll
  for (int d = 0; d < 4; ++d)
#pragma unroll
    for (int s = 0; s < 2; ++s) {
      *(bf16x8*)(Cf + ((size_t)ta * 256 + h * 8 + 2 * d + s) * 512 + lane * 8) = pack8(oa[d], s, ia);
      *(bf16x8*)(Cf + ((size_t)tbq * 256 + h * 8 + 2 * d + s) * 512 + lane * 8) = pack8(ob[d], s, ib);
    }
}

__global__ __launch_bounds__(256) void k_prep_wo(const u16* __restrict__ wo, u16* __restrict__ WOf) {
  const int idx = blockIdx.x * 256 + threadIdx.x;
  const int lane = idx & 63, kstep = (idx >> 6) & 255, mb = idx >> 14;
  bf16x8 v;
#pragma unroll
  for (int j = 0; j < 8; ++j) v[j] = (short)wo[(size_t)(16 * kstep + 8 * (j >> 2) + 4 * (lane >> 5) + (j & 3)) * DM + mb * 32 + (lane & 31)];
  *(bf16x8*)(WOf + (size_t)idx * 8) = v;
}

__global__ __launch_bounds__(256, 2) void k_oproj(const u16* __restrict__ Cf, const u16* __restrict__ WOf, u16* __restrict__ out) {
  __shared__ __attribute__((aligned(16))) float red[4][4][16][64];
  const int tid = threadIdx.x, lane = tid & 63, w = __builtin_amdgcn_readfirstlane(tid >> 6);
  const int bid = blockIdx.x, xcd = bid & 7, r = bid >> 3, qt = xcd * 4 + (r & 3), mt = r >> 2;
  const u16* c0 = Cf + ((size_t)(qt * 2) * 256 + w * 64) * 512 + lane * 8;
  const u16* c1 = c0 + 256 * 512;
  const u16* w0 = WOf + ((size_t)(mt * 2) * 256 + w * 64) * 512 + lane * 8;
  const u16* w1 = w0 + 256 * 512;
  f32x16 acc[4] = {};
#pragma unroll 8
  for (int ks = 0; ks < 64; ++ks) {
    bf16x8 a0 = *(const bf16x8*)(w0 + ks * 512), a1 = *(const bf16x8*)(w1 + ks * 512), b0 = *(const bf16x8*)(c0 + ks * 512), b1 = *(const bf16x8*)(c1 + ks * 512);
    acc[0] = MFMA32(a0, b0, acc[0]); acc[1] = MFMA32(a0, b1, acc[1]); acc[2] = MFMA32(a1, b0, acc[2]); acc[3] = MFMA32(a1, b1, acc[3]);
  }
#pragma unroll
  for (int t = 0; t < 4; ++t)
#pragma unroll
    for (int rr = 0; rr < 16; ++rr) red[w][t][rr][lane] = acc[t][rr];
  __syncthreads();
  const int t = w, l32 = lane & 31, hh = lane >> 5;
  float v[16];
#pragma unroll
  for (int rr = 0; rr < 16; ++rr) v[rr] = red[0][t][rr][lane] + red[1][t][rr][lane] + red[2][t][rr][lane] + red[3][t][rr][lane];
  u16* o = out + (size_t)(qt * 64 + (t & 1) * 32 + l32) * 1024 + 512 + mt * 64 + (t >> 1) * 32 + 4 * hh;
#pragma unroll
  for (int g = 0; g < 4; ++g) { u32x2 pk = {cvtpk(v[4 * g], v[4 * g + 1]), cvtpk(v[4 * g + 2], v[4 * g + 3])}; *(u32x2*)(o + 8 * g) = pk; }
}

struct cf { float r, i; };
DI cf cmul(cf a, cf b) { return {a.r * b.r - a.i * b.i, a.r * b.i + a.i * b.r}; }
DI cf cexpk(float are, float aim, float k) {
  const float mag = __builtin_amdgcn_exp2f(are * k * 1.4426950408889634f);
  const float th = aim * k, hi = th * 0.15915494309189535f;
  const float lo = __builtin_fmaf(th, 0.15915494309189535f, -hi) + th * 4.4934210e-9f * 0.f;
  (void)lo;
  const float c_hi = 0.15915494f, c_lo = (float)(0.15915494309189535 - (double)0.15915494f);
  const float rh = th * c_hi, rl = __builtin_fmaf(th, c_hi, -rh) + th * c_lo;
  const float fr = __builtin_amdgcn_fractf(rh) + rl;
  return {mag * __builtin_amdgcn_cosf(fr), mag * __builtin_amdgcn_sinf(fr)};
}
DI unsigned pkh(float a, float b) { f32x2 v = {a, b}; f16x2 h = __builtin_convertvector(v, f16x2); return __builtin_bit_cast(unsigned, h); }
constexpr int XROW = 272;
constexpr int SSM_XA = 0, SSM_YB = 48 * XROW, SSM_KD = SSM_YB + 16 * XROW, SSM_KF = SSM_KD + 1344, SSM_U0 = SSM_KF + 512 + 4096, SSM_U1 = SSM_U0 + 4160 + 64, SSM_UR = SSM_U1 + 4160, SSM_BYTES = SSM_UR + 4096;
DI void ssm_channel(int d, LAS char* L, int lane, const u16* __restrict__ u, const u16* __restrict__ lar, const u16* __restrict__ aim, const u16* __restrict__ cre,
                    const u16* __restrict__ cim, const u16* __restrict__ ldt, const u16* __restrict__ dsk, u16* __restrict__ out) {
  u16 ur[32];
#pragma unroll
  for (int i = 0; i < 32; ++i) ur[i] = u[(size_t)(i * 64 + lane) * DM + d];
  const float dt = bfr(expf(bf2f(ldt[d]))), dskip = bf2f(dsk[d]);
  float are[2], aimv[2], cr[2], ci[2];
  float bsum = 0.f;
#pragma unroll
  for (int hf = 0; hf < 2; ++hf) {
    const int n = d * NSSM + hf * 64 + lane;
    are[hf] = -bfr(expf(bf2f(lar[n]))) * dt; aimv[hf] = bf2f(aim[n]) * dt;
    cr[hf] = 2.f * bf2f(cre[n]) * dt; ci[hf] = 2.f * bf2f(cim[n]) * dt;
    bsum += fabsf(cr[hf]) + fabsf(ci[hf]);
  }
#pragma unroll
  for (int o = 32; o > 0; o >>= 1) bsum += __shfl_xor(bsum, o);
  int eb = (int)((__float_as_uint(bsum) >> 23) & 255u); eb = eb < 20 ? 20 : (eb > 230 ? 230 : eb);
  const float sK = __uint_as_float((unsigned)(127 + 13 + 127 - eb) << 23);
  f32x4 kacc[3] = {};
#pragma unroll
  for (int hf = 0; hf < 2; ++hf) {
    const cf z1 = cexpk(are[hf], aimv[hf], 1.f), z8 = cexpk(are[hf], aimv[hf], 8.f), z128 = cexpk(are[hf], aimv[hf], 128.f);
    cf y = {1.f, 0.f};
#pragma unroll
    for (int b = 0; b < 16; ++b) { *(LAS unsigned*)(L + SSM_YB + b * XROW + lane * 4) = pkh(y.r, -y.i); y = cmul(y, z8); }
    cf zc[8]; zc[0] = {1.f, 0.f}; zc[1] = z1;
#pragma unroll
    for (int c = 2; c < 8; ++c) zc[c] = cmul(zc[c - 1], z1);
    cf e = {cr[hf] * sK, ci[hf] * sK};
#pragma unroll
    for (int a = 0; a <= 16; ++a) {
      const int nc = a < 2 ? 8 : (a < 4 ? 4 : (a < 8 ? 2 : 1)), step = 8 / nc;
      const int row0 = a < 2 ? 8 * a : (a < 4 ? 16 + 4 * (a - 2) : (a < 8 ? 24 + 2 * (a - 4) : 32 + (a - 8)));
#pragma unroll
      for (int c = 0; c < nc; ++c) { const cf xx = c == 0 ? e : cmul(e, zc[c * step]); *(LAS unsigned*)(L + SSM_XA + (row0 + c) * XROW + lane * 4) = pkh(xx.r, xx.i); }
      e = cmul(e, z128);
    }
    asm volatile("s_waitcnt lgkmcnt(0)" ::: "memory");
#pragma unroll
    for (int s = 0; s < 4; ++s) {
      const int off = (lane & 15) * XROW + (16 * s + 4 * (lane >> 4)) * 4;
      const f16x8 yb = *(const LAS f16x8*)(L + SSM_YB + off);
#pragma unroll
      for (int mt = 0; mt < 3; ++mt) { const f16x8 xa = *(const LAS f16x8*)(L + SSM_XA + mt * 16 * XROW + off); kacc[mt] = __builtin_amdgcn_mfma_f32_16x16x32_f16(xa, yb, kacc[mt], 0, 0, 0); }
    }
    asm volatile("s_waitcnt lgkmcnt(0)" ::: "memory");
  }
  {
    const int be = lane & 15, g = lane >> 4;
#pragma unroll
    for (int r = 0; r < 4; ++r) {
      const int i0 = 4 * g + r;
      const int s0 = 128 * (i0 >> 3) + (i0 & 7) + 8 * be;
      const int s1 = i0 < 8 ? 256 + 64 * (i0 >> 2) + (i0 & 3) + 4 * be : 384 + 32 * ((i0 - 8) >> 1) + (i0 & 1) + 2 * be;
      const int s2 = 512 + 16 * i0 + be;
      *(LAS _Float16*)(L + SSM_KD + s0 * 2) = (_Float16)kacc[0][r];
      *(LAS _Float16*)(L + SSM_KD + s1 * 2) = (_Float16)kacc[1][r];
      if (s2 <= 640) *(LAS _Float16*)(L + SSM_KD + s2 * 2) = (_Float16)kacc[2][r];
    }
  }
  unsigned um = 0;
#pragma unroll
  for (int i = 0; i < 32; ++i) um = max(um, (unsigned)(ur[i] & 0x7fffu));
#pragma unroll
  for (int o = 32; o > 0; o >>= 1) um = max(um, (unsigned)__shfl_xor((int)um, o));
  int eu = (int)(um >> 7); eu = eu < 20 ? 20 : (eu > 230 ? 230 : eu);
  const float sU = __uint_as_float((unsigned)(127 + 14 + 127 - eu) << 23);
#pragma unroll
  for (int i = 0; i < 32; ++i) {
    const int l = i * 64 + lane; const _Float16 hv = (_Float16)(bf2f(ur[i]) * sU);
    *(LAS _Float16*)(L + SSM_U0 + (l + 16) * 2) = hv; *(LAS _Float16*)(L + SSM_U1 + (l + 15) * 2) = hv; *(LAS u16*)(L + SSM_UR + l * 2) = ur[i];
  }
  if (lane < 16) { *(LAS u16*)(L + SSM_U0 + lane * 2) = 0; if (lane < 15) *(LAS u16*)(L + SSM_U1 + lane * 2) = 0; }
  *(LAS u32x2*)(L + SSM_KF + lane * 8) = (u32x2){0u, 0u};
  asm volatile("s_waitcnt lgkmcnt(0)" ::: "memory");
#pragma unroll
  for (int i = 0; i < 4; ++i) {
    const int G = i * 64 + lane; u16 v[8];
#pragma unroll
    for (int e = 0; e < 8; ++e) {
      const int j = G * 8 + 7 - e;
      const int sl = j < 128 ? j : (int)f2bf((float)j) - 0x4300 + 128;
      v[e] = *(const LAS u16*)(L + SSM_KD + sl * 2);
    }
    u32x4 pk = {v[0] | ((unsigned)v[1] << 16), v[2] | ((unsigned)v[3] << 16), v[4] | ((unsigned)v[5] << 16), v[6] | ((unsigned)v[7] << 16)};
    *(LAS u32x4*)(L + SSM_KF + 512 + G * 16) = pk;
  }
  asm volatile("s_waitcnt lgkmcnt(0)" ::: "memory");
  f32x4 acc[8] = {};
  const int b = lane & 15, g = lane >> 4;
  const int st = 16 * (g >> 1) - 8 * (g & 1) - 7 + b + 16;
  const LAS char* ua = (st & 1) ? L + SSM_U1 + (st - 1) * 2 : L + SSM_U0 + st * 2;
  const LAS char* kb = L + SSM_KF + 512 + (2 * b - 2 * (g >> 1) + (g & 1)) * 16;
  for (int r = 0; r < 8; ++r) {
    f16x8 af[8], bfr_[8];
#pragma unroll
    for (int q = 0; q < 8; ++q) {
      const LAS unsigned* p = (const LAS unsigned*)(ua + (8 * q + r) * 64);
      u32x4 t = {p[0], p[1], p[2], p[3]}; af[q] = __builtin_bit_cast(f16x8, t);
      bfr_[q] = *(const LAS f16x8*)(kb + (8 * q - r) * 64);
    }
#pragma unroll
    for (int t = 0; t < 8; ++t)
#pragma unroll
      for (int q = 0; q <= t; ++q) acc[t] = __builtin_amdgcn_mfma_f32_16x16x32_f16(af[q], bfr_[t - q], acc[t], 0, 0, 0);
  }
  const float inv = 1.f / (sK * sU);
#pragma unroll
  for (int t = 0; t < 8; ++t)
#pragma unroll
    for (int r = 0; r < 4; ++r) {
      const int l = 16 * (16 * t + b) + 4 * g + r;
      const float uv = bf2f(*(const LAS u16*)(L + SSM_UR + l * 2));
      out[(size_t)l * 1024 + d] = f2bf(bfr(acc[t][r] * inv) + bfr(uv * dskip));
    }
}
__global__ __launch_bounds__(128) void k_ssm(const u16* __restrict__ u, const u16* __restrict__ lar, const u16* __restrict__ aim, const u16* __restrict__ cre, const u16* __restrict__ cim,
                                            const u16* __restrict__ ldt, const u16* __restrict__ dsk, u16* __restrict__ out) {
  __shared__ __attribute__((aligned(16))) char lds[2 * SSM_BYTES];
  const int tid = threadIdx.x, lane = tid & 63, w = __builtin_amdgcn_readfirstlane(tid >> 6);
  const int bid = blockIdx.x, xcd = bid & 7, r = bid >> 3;
  const int d = xcd * 64 + r * 2 + w;
  ssm_channel(d, (LAS char*)lds + w * SSM_BYTES, lane, u, lar, aim, cre, cim, ldt, dsk, out);
}

extern "C" void kernel_launch(void* const* d_in, const int* in_sizes, int n_in, void* d_out, int out_size, void* d_ws, size_t ws_size, hipStream_t stream) {
  const u16 *u = (const u16*)d_in[0], *lar = (const u16*)d_in[1], *aim = (const u16*)d_in[2], *cre = (const u16*)d_in[3], *cim = (const u16*)d_in[4], *ldt = (const u16*)d_in[5], *dsk = (const u16*)d_in[6];
  const u16 *x = (const u16*)d_in[7], *ckv = (const u16*)d_in[8], *wq = (const u16*)d_in[9], *wk = (const u16*)d_in[10], *wv = (const u16*)d_in[11], *wo = (const u16*)d_in[12];
  u16* out = (u16*)d_out;
  char* ws = (char*)d_ws;
  u16 *Qf = (u16*)ws, *KVf = (u16*)(ws + (16u << 20)), *Cf = (u16*)(ws + (48u << 20)), *WOf = (u16*)(ws + (64u << 20));
  k_prep_wo<<<1024, 256, 0, stream>>>(wo, WOf);
  k_ssm<<<256, 128, 0, stream>>>(u, lar, aim, cre, cim, ldt, dsk, out);
  k_proj<<<256, 512, 0, stream>>>(x, ckv, wq, wk, wv, Qf, KVf);
  k_attn<<<256, 256, 0, stream>>>(Qf, KVf, Cf);
  k_oproj<<<256, 256, 0, stream>>>(Cf, WOf, out);
}
```

```cpp
#include <hip/hip_runtime.h>
#include <stdint.h>

typedef unsigned short u16;
#define KAL 256
#define LAS __attribute__((address_space(3)))

constexpr int DM = 512, NSSM = 128;
constexpr unsigned UTO = 4u << 20, KVO = 16u << 20;

#define PIN(x) asm volatile("" : "+v"(x))
constexpr int S_T = 98304;
typedef unsigned u32x4 __attribute__((ext_vector_type(4)));
typedef unsigned u32x8 __attribute__((ext_vector_type(8)));
typedef unsigned u32x16 __attribute__((ext_vector_type(16)));
typedef unsigned u32x32 __attribute__((ext_vector_type(32)));
typedef __amdgpu_buffer_rsrc_t rsrc_t;
#define RSRC(p, n) __builtin_amdgcn_make_buffer_rsrc((void*)(p), (short)0, (int)(n), 0x00020000)
#define TOUCH(p, o) asm volatile("global_load_dword %0, %1, %2" : "+v"(dm) : "v"(o), "s"(p) : "memory")
#define FIX "+{v[224:255]}"(rv), "+{s[64:79]}"(rs), "+{s[80:87]}"(rt)

#define SETUP0_ASM \
"v_mbcnt_lo_u32_b32 v240, -1, 0\n" \
"v_mbcnt_hi_u32_b32 v240, -1, v240\n" \
"s_lshl_b32 s64, %[w4k], 1\n" \
"s_movk_i32 s84, 7168\n" \
"s_movk_i32 s85, 14336\n" \
"s_movk_i32 s86, 21504\n" \
"v_lshrrev_b32_e32 v250, 4, v240\n" \
"v_and_b32_e32 v251, 7, v240\n" \
"v_xor_b32_e32 v250, v250, v251\n" \
"v_lshlrev_b32_e32 v250, 4, v250\n" \
"v_lshrrev_b32_e32 v251, 3, v240\n" \
"v_lshl_add_u32 v224, v251, 10, v250\n" \
"v_xor_b32_e32 v225, 64, v224\n" \
"v_bfe_u32 v250, v240, 2, 3\n" \
"v_lshlrev_b32_e32 v251, 13, v250\n" \
"v_lshrrev_b32_e32 v250, 5, v240\n" \
"v_lshl_add_u32 v251, v250, 6, v251\n" \
"v_and_b32_e32 v250, 3, v240\n" \
"v_lshl_add_u32 v251, v250, 4, v251\n" \
"s_lshl_b32 s60, %[w4k], 5\n" \
"v_add_u32_e32 v251, s60, v251\n" \
"v_add_u32_e32 v226, 4096, v251\n" \
"v_add_u32_e32 v227, 3200, v251\n" \
"v_xor_b32_e32 v254, 0x10820, v251\n" \
"v_xor_b32_e32 v236, 0x104a0, v251\n"
#define SETUP1_ASM \
"v_lshlrev_b32_e32 v241, 4, v240\n" \
"s_lshl_b32 s80, %[pair], 10\n" \
"v_lshlrev_b32_e32 v242, 2, v240\n" \
"v_bfe_u32 v251, v240, 2, 2\n" \
"v_lshlrev_b32_e32 v251, 6, v251\n" \
"v_lshl_add_u32 v251, v250, 3, v251\n" \
"v_lshrrev_b32_e32 v250, 4, v240\n" \
"v_lshl_add_u32 v251, v250, 11, v251\n" \
"v_and_b32_e32 v250, 1, v250\n" \
"v_lshl_add_u32 v228, v250, 5, v251\n" \
"v_xor_b32_e32 v231, 32, v228\n" \
"v_add_u32_e32 v229, 65536, v228\n" \
"v_add_u32_e32 v230, 131072, v228\n" \
"v_add_u32_e32 v232, 65536, v231\n" \
"v_add_u32_e32 v233, 131072, v231\n" \
"v_bfe_u32 v250, v240, 1, 3\n" \
"v_lshrrev_b32_e32 v251, 4, v240\n" \
"v_xor_b32_e32 v250, v250, v251\n" \
"v_and_b32_e32 v251, 15, v240\n" \
"v_lshlrev_b32_e32 v251, 7, v251\n" \
"v_lshl_add_u32 v250, v250, 4, v251\n" \
"s_add_u32 s61, s64, 32768\n" \
"v_add_u32_e32 v234, s61, v250\n" \
"v_xor_b32_e32 v235, 64, v234\n" \
"v_add_u32_e32 v238, 0x10000, v234\n" \
"v_add_u32_e32 v239, 0x10000, v235\n" \
"v_lshrrev_b32_e32 v250, 3, v240\n" \
"v_and_b32_e32 v251, 7, v240\n" \
"v_lshlrev_b32_e32 v251, 4, v251\n" \
"v_lshl_add_u32 v248, v250, 10, v251\n" \
"v_add_u32_e32 v249, 4096, v241\n" \
"v_and_b32_e32 v250, 56, v240\n" \
"v_lshlrev_b32_e32 v250, 25, v250\n" \
"v_lshl_or_b32 v253, v240, 10, v250\n" \
"s_lshl_b32 s61, %[h], 13\n" \
"s_lshl2_add_u32 s83, %[da], s61\n" \
"v_mul_u32_u24_e32 v237, 7168, v240\n" \
"v_add_u32_e32 v237, v237, v253\n" \
"v_mov_b32_e32 v252, 0xff800000\n" \
"s_lshr_b32 s81, %[w4k], 12\n" \
"s_lshl_b32 s81, 0x1111, s81\n" \
"s_lshl_b32 s82, %[pair], 12\n" \
"s_lshr_b32 s61, %[w4k], 2\n" \
"s_add_u32 s82, s82, s61\n" \
"s_add_u32 s82, s82, 10485760\n" \
"s_lshl_b32 s78, %[h], 20\n" \
"s_add_u32 s78, s78, 16777216\n" \
"s_lshl_b32 s67, %[da], 15\n" \
"s_add_u32 s67, s67, s78\n" \
"s_mov_b32 s75, 0x5f800000\n" \
"s_mov_b32 s76, 0xccbebc20\n" \
"s_mov_b32 s77, 0x3e0293ee\n" \
"s_sub_i32 s74, 0, %[da]\n" \
"s_lshr_b32 s73, %[da], 2\n" \
"s_lshl_b32 s87, s73, 10\n" \
"s_lshl2_add_u32 s87, %[h], s87\n" \
"s_lshl4_add_u32 s87, %[w4k], s87\n" \
"s_and_b32 s65, %[w4k], 8192\n" \
"s_add_u32 s66, %[w4k], s65\n" \
"s_lshl_b32 s61, s65, 2\n" \
"s_sub_u32 s62, %[w4k], s61\n" \
"s_sub_u32 s68, 114688, s61\n" \
"s_add_u32 s69, s62, 131072\n" \
"s_and_b32 s69, s69, 0x1ffff\n" \
"s_add_u32 s70, s62, 147456\n" \
"s_and_b32 s70, s70, 0x1ffff\n" \
"s_add_u32 s71, s62, 163840\n" \
"s_and_b32 s71, s71, 0x1ffff\n" \
"s_add_u32 s72, s62, 245760\n" \
"s_and_b32 s72, s72, 0x1ffff\n"
#define DMA0_ASM \
"s_add_u32 m0, %[w4k], 0\n" \
"s_mov_b32 s79, 0\n" \
"s_nop 0\n" \
"buffer_load_dwordx4 v226, %[wq], s79 offen offset:0 lds\n"
#define DMA1_ASM \
"buffer_load_dwordx4 v227, %[wq], s79 offen offset:1024 lds\n"
#define DMA2_ASM \
"buffer_load_dwordx4 v254, %[wq], s79 offen offset:2048 lds\n"
#define DMA3_ASM \
"buffer_load_dwordx4 v236, %[wq], s79 offen offset:3072 lds\n"
#define DMA4_ASM \
"s_add_u32 m0, s64, 32768\n" \
"s_nop 0\n" \
"buffer_load_dwordx4 v224, %[xa], 0 offen offset:0 lds\n"
#define DMA5_ASM \
"buffer_load_dwordx4 v225, %[xa], s84 offen offset:1024 lds\n"
#define DMA6_ASM \
"buffer_load_dwordx4 v224, %[xa], s85 offen offset:2048 lds\n"
#define DMA7_ASM \
"buffer_load_dwordx4 v225, %[xa], s86 offen offset:3072 lds\n"
#define DMA8_ASM \
"s_add_u32 m0, s64, 36864\n" \
"s_nop 0\n" \
"buffer_load_dwordx4 v224, %[xb], 0 offen offset:0 lds\n"
#define DMA9_ASM \
"buffer_load_dwordx4 v225, %[xb], s84 offen offset:1024 lds\n"
#define DMA10_ASM \
"buffer_load_dwordx4 v224, %[xb], s85 offen offset:2048 lds\n"
#define DMA11_ASM \
"buffer_load_dwordx4 v225, %[xb], s86 offen offset:3072 lds\n"
#define DMA12_ASM \
"s_add_u32 m0, s64, 65408\n" \
"s_nop 0\n" \
"buffer_load_dwordx4 v224, %[xa], 0 offen offset:128 lds\n"
#define DMA13_ASM \
"buffer_load_dwordx4 v225, %[xa], s84 offen offset:1152 lds\n"
#define DMA14_ASM \
"buffer_load_dwordx4 v224, %[xa], s85 offen offset:2176 lds\n"
#define DMA15_ASM \
"buffer_load_dwordx4 v225, %[xa], s86 offen offset:3200 lds\n"
#define DMA16_ASM \
"s_add_u32 m0, s64, 69504\n" \
"s_nop 0\n" \
"buffer_load_dwordx4 v224, %[xb], 0 offen offset:128 lds\n"
#define DMA17_ASM \
"buffer_load_dwordx4 v225, %[xb], s84 offen offset:1152 lds\n"
#define DMA18_ASM \
"buffer_load_dwordx4 v224, %[xb], s85 offen offset:2176 lds\n"
#define DMA19_ASM \
"buffer_load_dwordx4 v225, %[xb], s86 offen offset:3200 lds\n"
#define DMA20_ASM \
"s_add_u32 m0, %[w4k], 16384\n" \
"s_mov_b32 s79, 524288\n" \
"s_nop 0\n" \
"buffer_load_dwordx4 v226, %[wq], s79 offen offset:0 lds\n"
#define DMA21_ASM \
"buffer_load_dwordx4 v227, %[wq], s79 offen offset:1024 lds\n"
#define DMA22_ASM \
"buffer_load_dwordx4 v254, %[wq], s79 offen offset:2048 lds\n"
#define DMA23_ASM \
"buffer_load_dwordx4 v236, %[wq], s79 offen offset:3072 lds\n"
#define TAB_DATA \
1536,1536,1536,1536,1526,1526,1526,1526,1526,1526,1526,1510,1510,1510,1510,1510,1510,1510,1510,1510,1494,1494,1494,1494,1494,1494,1494,1478,1478,1478,1478,1478, \
1478,1478,1478,1478,1462,1462,1462,1462,1462,1462,1462,1446,1446,1446,1446,1446,1446,1446,1446,1446,1430,1430,1430,1430,1430,1430,1430,1414,1414,1414,1414,1414, \
1414,1414,1414,1414,1398,1398,1398,1398,1398,1398,1398,1382,1382,1382,1382,1382,1382,1382,1382,1382,1366,1366,1366,1366,1366,1366,1366,1350,1350,1350,1350,1350, \
1350,1350,1350,1350,1334,1334,1334,1334,1334,1334,1334,1318,1318,1318,1318,1318,1318,1318,1318,1318,1302,1302,1302,1302,1302,1302,1302,1286,1286,1286,1286,1286, \
1286,1286,1286,1286,1522,1522,1522,1522,1522,1522,1522,1506,1506,1506,1506,1506,1506,1506,1506,1506,1490,1490,1490,1490,1490,1490,1490,1474,1474,1474,1474,1474, \
1474,1474,1474,1474,1458,1458,1458,1458,1458,1458,1458,1442,1442,1442,1442,1442,1442,1442,1442,1442,1426,1426,1426,1426,1426,1426,1426,1410,1410,1410,1410,1410, \
1410,1410,1410,1410,1394,1394,1394,1394,1394,1394,1394,1378,1378,1378,1378,1378,1378,1378,1378,1378,1362,1362,1362,1362,1362,1362,1362,1346,1346,1346,1346,1346, \
1346,1346,1346,1346,1330,1330,1330,1330,1330,1330,1330,1314,1314,1314,1314,1314,1314,1314,1314,1314,1298,1298,1298,1298,1298,1298,1298,1282,1282,1282,1282,1282, \
1282,1282,1282,1282,1270,1270,1270,1270,1270,1270,1270,1254,1254,1254,1254,1254,1254,1254,1254,1254,1238,1238,1238,1238,1238,1238,1238,1222,1222,1222,1222,1222, \
1222,1222,1222,1222,1206,1206,1206,1206,1206,1206,1206,1190,1190,1190,1190,1190,1190,1190,1190,1190,1174,1174,1174,1174,1174,1174,1174,1158,1158,1158,1158,1158, \
1158,1158,1158,1158,1142,1142,1142,1142,1142,1142,1142,1126,1126,1126,1126,1126,1126,1126,1126,1126,1110,1110,1110,1110,1110,1110,1110,1094,1094,1094,1094,1094, \
1094,1094,1094,1094,1078,1078,1078,1078,1078,1078,1078,1062,1062,1062,1062,1062,1062,1062,1062,1062,1046,1046,1046,1046,1046,1046,1046,1030,1030,1030,1030,1030, \
1030,1030,1030,1030,1266,1266,1266,1266,1266,1266,1266,1250,1250,1250,1250,1250,1250,1250,1250,1250,1234,1234,1234,1234,1234,1234,1234,1218,1218,1218,1218,1218, \
1218,1218,1218,1218,1202,1202,1202,1202,1202,1202,1202,1186,1186,1186,1186,1186,1186,1186,1186,1186,1170,1170,1170,1170,1170,1170,1170,1154,1154,1154,1154,1154, \
1154,1154,1154,1154,1138,1138,1138,1138,1138,1138,1138,1122,1122,1122,1122,1122,1122,1122,1122,1122,1106,1106,1106,1106,1106,1106,1106,1090,1090,1090,1090,1090, \
1090,1090,1090,1090,1074,1074,1074,1074,1074,1074,1074,1058,1058,1058,1058,1058,1058,1058,1058,1058,1042,1042,1042,1042,1042,1042,1042,1026,1026,1026,1026,1026, \
1026,1026,1026,1026,506,506,506,506,506,506,506,490,490,490,490,490,490,490,490,490,474,474,474,474,474,474,474,458,458,458,458,458, \
458,458,458,458,442,442,442,442,442,442,442,426,426,426,426,426,426,426,426,426,410,410,410,410,410,410,410,394,394,394,394,394, \
394,394,394,394,378,378,378,378,378,378,378,362,362,362,362,362,362,362,362,362,346,346,346,346,346,346,346,330,330,330,330,330, \
330,330,330,330,314,314,314,314,314,314,314,298,298,298,298,298,298,298,298,298,282,282,282,282,282,282,282,266,266,266,266,266, \
266,266,266,266,250,250,250,250,250,250,250,234,234,234,234,234,234,234,234,234,218,218,218,218,218,218,218,202,202,202,202,202, \
202,202,202,202,186,186,186,186,186,186,186,170,170,170,170,170,170,170,170,170,154,154,154,154,154,154,154,138,138,138,138,138, \
138,138,138,138,122,122,122,122,122,122,122,106,106,106,106,106,106,106,106,106,90,90,90,90,90,90,90,74,74,74,74,74, \
74,74,74,74,58,58,58,58,58,58,58,42,42,42,42,42,42,42,42,42,26,26,26,26,26,26,26,10,10,10,10,10, \
10,10,10,10,754,754,754,754,754,754,754,738,738,738,738,738,738,738,738,738,722,722,722,722,722,722,722,706,706,706,706,706, \
706,706,706,706,690,690,690,690,690,690,690,674,674,674,674,674,674,674,674,674,658,658,658,658,658,658,658,642,642,642,642,642, \
642,642,642,642,626,626,626,626,626,626,626,610,610,610,610,610,610,610,610,610,594,594,594,594,594,594,594,578,578,578,578,578, \
578,578,578,578,562,562,562,562,562,562,562,546,546,546,546,546,546,546,546,546,530,530,530,530,530,530,530,514,514,514,514,514, \
514,514,514,514,242,242,242,242,242,242,242,226,226,226,226,226,226,226,226,226,210,210,210,210,210,210,210,194,194,194,194,194, \
194,194,194,194,178,178,178,178,178,178,178,162,162,162,162,162,162,162,162,162,146,146,146,146,146,146,146,130,130,130,130,130, \
130,130,130,130,114,114,114,114,114,114,114,98,98,98,98,98,98,98,98,98,82,82,82,82,82,82,82,66,66,66,66,66, \
66,66,66,66,50,50,50,50,50,50,50,34,34,34,34,34,34,34,34,34,18,18,18,18,18,18,18,2,2,2,2,2, \
2,2,1524,1524,1524,1022,1022,1022,1022,1022,1508,1508,1508,1006,1006,1006,1006,1006,1492,1492,1492,990,990,990,990,990,1476,1476,1476,974,974,974, \
974,974,1460,1460,1460,958,958,958,958,958,1444,1444,1444,942,942,942,942,942,1428,1428,1428,926,926,926,926,926,1412,1412,1412,910,910,910, \
910,910,1396,1396,1396,894,894,894,894,894,1380,1380,1380,878,878,878,878,878,1364,1364,1364,862,862,862,862,862,1348,1348,1348,846,846,846, \
846,846,1332,1332,1332,830,830,830,830,830,1316,1316,1316,814,814,814,814,814,1300,1300,1300,798,798,798,798,798,1284,1284,1284,782,782,782, \
782,782,1520,1520,1520,1020,1020,1020,1020,1020,1504,1504,1504,1004,1004,1004,1004,1004,1488,1488,1488,988,988,988,988,988,1472,1472,1472,972,972,972, \
972,972,1456,1456,1456,956,956,956,956,956,1440,1440,1440,940,940,940,940,940,1424,1424,1424,924,924,924,924,924,1408,1408,1408,908,908,908, \
908,908,1392,1392,1392,892,892,892,892,892,1376,1376,1376,876,876,876,876,876,1360,1360,1360,860,860,860,860,860,1344,1344,1344,844,844,844, \
844,844,1328,1328,1328,828,828,828,828,828,1312,1312,1312,812,812,812,812,812,1296,1296,1296,796,796,796,796,796,1280,1280,1280,780,780,780, \
780,780,1268,1268,1268,1018,1018,1018,1018,1018,1252,1252,1252,1002,1002,1002,1002,1002,1236,1236,1236,986,986,986,986,986,1220,1220,1220,970,970,970, \
970,970,1204,1204,1204,954,954,954,954,954,1188,1188,1188,938,938,938,938,938,1172,1172,1172,922,922,922,922,922,1156,1156,1156,906,906,906, \
906,906,1140,1140,1140,890,890,890,890,890,1124,1124,1124,874,874,874,874,874,1108,1108,1108,858,858,858,858,858,1092,1092,1092,842,842,842, \
842,842,1076,1076,1076,826,826,826,826,826,1060,1060,1060,810,810,810,810,810,1044,1044,1044,794,794,794,794,794,1028,1028,1028,778,778,778, \
778,778,1264,1264,1264,1016,1016,1016,1016,1016,1248,1248,1248,1000,1000,1000,1000,1000,1232,1232,1232,984,984,984,984,984,1216,1216,1216,968,968,968, \
968,968,1200,1200,1200,952,952,952,952,952,1184,1184,1184,936,936,936,936,936,1168,1168,1168,920,920,920,920,920,1152,1152,1152,904,904,904, \
904,904,1136,1136,1136,888,888,888,888,888,1120,1120,1120,872,872,872,872,872,1104,1104,1104,856,856,856,856,856,1088,1088,1088,840,840,840, \
840,840,1072,1072,1072,824,824,824,824,824,1056,1056,1056,808,808,808,808,808,1040,1040,1040,792,792,792,792,792,1024,1024,1024,776,776,776, \
776,510,504,504,504,508,766,766,766,494,488,488,488,492,750,750,750,478,472,472,472,476,734,734,734,462,456,456,456,460,718,718, \
718,446,440,440,440,444,702,702,702,430,424,424,424,428,686,686,686,414,408,408,408,412,670,670,670,398,392,392,392,396,654,654, \
654,382,376,376,376,380,638,638,638,366,360,360,360,364,622,622,622,350,344,344,344,348,606,606,606,334,328,328,328,332,590,590, \
590,318,312,312,312,316,574,574,574,302,296,296,296,300,558,558,558,286,280,280,280,284,542,542,542,270,264,264,264,268,526,526, \
526,254,248,248,248,252,764,764,764,238,232,232,232,236,748,748,748,222,216,216,216,220,732,732,732,206,200,200,200,204,716,716, \
716,190,184,184,184,188,700,700,700,174,168,168,168,172,684,684,684,158,152,152,152,156,668,668,668,142,136,136,136,140,652,652, \
652,126,120,120,120,124,636,636,636,110,104,104,104,108,620,620,620,94,88,88,88,92,604,604,604,78,72,72,72,76,588,588, \
588,62,56,56,56,60,572,572,572,46,40,40,40,44,556,556,556,30,24,24,24,28,540,540,540,14,8,8,8,12,524,524, \
1014,758,1010,752,1012,756,1008,762,998,742,994,736,996,740,992,746,982,726,978,720,980,724,976,730,966,710,962,704,964,708,960,714, \
950,694,946,688,948,692,944,698,934,678,930,672,932,676,928,682,918,662,914,656,916,660,912,666,902,646,898,640,900,644,896,650, \
886,630,882,624,884,628,880,634,870,614,866,608,868,612,864,618,854,598,850,592,852,596,848,602,838,582,834,576,836,580,832,586, \
822,566,818,560,820,564,816,570,806,550,802,544,804,548,800,554,790,534,786,528,788,532,784,538,774,518,770,512,772,516,768,522, \
502,246,498,240,500,244,496,760,486,230,482,224,484,228,480,744,470,214,466,208,468,212,464,728,454,198,450,192,452,196,448,712, \
438,182,434,176,436,180,432,696,422,166,418,160,420,164,416,680,406,150,402,144,404,148,400,664,390,134,386,128,388,132,384,648, \
374,118,370,112,372,116,368,632,358,102,354,96,356,100,352,616,342,86,338,80,340,84,336,600,326,70,322,64,324,68,320,584, \
310,54,306,48,308,52,304,568,294,38,290,32,292,36,288,552,278,22,274,16,276,20,272,536,262,6,258,0,260,4,256,520, \
1536,1536,1536,1526,1526,1526,1526,1526,1526,1526,1510,1510,1510,1510,1510,1510,1510,1510,1510,1494,1494,1494,1494,1494,1494,1494,1478,1478,1478,1478,1478,1478, \
1478,1478,1478,1462,1462,1462,1462,1462,1462,1462,1446,1446,1446,1446,1446,1446,1446,1446,1446,1430,1430,1430,1430,1430,1430,1430,1414,1414,1414,1414,1414,1414, \
1414,1414,1414,1398,1398,1398,1398,1398,1398,1398,1382,1382,1382,1382,1382,1382,1382,1382,1382,1366,1366,1366,1366,1366,1366,1366,1350,1350,1350,1350,1350,1350, \
1350,1350,1350,1334,1334,1334,1334,1334,1334,1334,1318,1318,1318,1318,1318,1318,1318,1318,1318,1302,1302,1302,1302,1302,1302,1302,1286,1286,1286,1286,1286,1286, \
1286,1286,1286,1522,1522,1522,1522,1522,1522,1522,1506,1506,1506,1506,1506,1506,1506,1506,1506,1490,1490,1490,1490,1490,1490,1490,1474,1474,1474,1474,1474,1474, \
1474,1474,1474,1458,1458,1458,1458,1458,1458,1458,1442,1442,1442,1442,1442,1442,1442,1442,1442,1426,1426,1426,1426,1426,1426,1426,1410,1410,1410,1410,1410,1410, \
1410,1410,1410,1394,1394,1394,1394,1394,1394,1394,1378,1378,1378,1378,1378,1378,1378,1378,1378,1362,1362,1362,1362,1362,1362,1362,1346,1346,1346,1346,1346,1346, \
1346,1346,1346,1330,1330,1330,1330,1330,1330,1330,1314,1314,1314,1314,1314,1314,1314,1314,1314,1298,1298,1298,1298,1298,1298,1298,1282,1282,1282,1282,1282,1282, \
1282,1282,1282,1270,1270,1270,1270,1270,1270,1270,1254,1254,1254,1254,1254,1254,1254,1254,1254,1238,1238,1238,1238,1238,1238,1238,1222,1222,1222,1222,1222,1222, \
1222,1222,1222,1206,1206,1206,1206,1206,1206,1206,1190,1190,1190,1190,1190,1190,1190,1190,1190,1174,1174,1174,1174,1174,1174,1174,1158,1158,1158,1158,1158,1158, \
1158,1158,1158,1142,1142,1142,1142,1142,1142,1142,1126,1126,1126,1126,1126,1126,1126,1126,1126,1110,1110,1110,1110,1110,1110,1110,1094,1094,1094,1094,1094,1094, \
1094,1094,1094,1078,1078,1078,1078,1078,1078,1078,1062,1062,1062,1062,1062,1062,1062,1062,1062,1046,1046,1046,1046,1046,1046,1046,1030,1030,1030,1030,1030,1030, \
1030,1030,1030,1266,1266,1266,1266,1266,1266,1266,1250,1250,1250,1250,1250,1250,1250,1250,1250,1234,1234,1234,1234,1234,1234,1234,1218,1218,1218,1218,1218,1218, \
1218,1218,1218,1202,1202,1202,1202,1202,1202,1202,1186,1186,1186,1186,1186,1186,1186,1186,1186,1170,1170,1170,1170,1170,1170,1170,1154,1154,1154,1154,1154,1154, \
1154,1154,1154,1138,1138,1138,1138,1138,1138,1138,1122,1122,1122,1122,1122,1122,1122,1122,1122,1106,1106,1106,1106,1106,1106,1106,1090,1090,1090,1090,1090,1090, \
1090,1090,1090,1074,1074,1074,1074,1074,1074,1074,1058,1058,1058,1058,1058,1058,1058,1058,1058,1042,1042,1042,1042,1042,1042,1042,1026,1026,1026,1026,1026,1026, \
1026,1026,1026,506,506,506,506,506,506,506,490,490,490,490,490,490,490,490,490,474,474,474,474,474,474,474,458,458,458,458,458,458, \
458,458,458,442,442,442,442,442,442,442,426,426,426,426,426,426,426,426,426,410,410,410,410,410,410,410,394,394,394,394,394,394, \
394,394,394,378,378,378,378,378,378,378,362,362,362,362,362,362,362,362,362,346,346,346,346,346,346,346,330,330,330,330,330,330, \
330,330,330,314,314,314,314,314,314,314,298,298,298,298,298,298,298,298,298,282,282,282,282,282,282,282,266,266,266,266,266,266, \
266,266,266,250,250,250,250,250,250,250,234,234,234,234,234,234,234,234,234,218,218,218,218,218,218,218,202,202,202,202,202,202, \
202,202,202,186,186,186,186,186,186,186,170,170,170,170,170,170,170,170,170,154,154,154,154,154,154,154,138,138,138,138,138,138, \
138,138,138,122,122,122,122,122,122,122,106,106,106,106,106,106,106,106,106,90,90,90,90,90,90,90,74,74,74,74,74,74, \
74,74,74,58,58,58,58,58,58,58,42,42,42,42,42,42,42,42,42,26,26,26,26,26,26,26,10,10,10,10,10,10, \
10,10,10,754,754,754,754,754,754,754,738,738,738,738,738,738,738,738,738,722,722,722,722,722,722,722,706,706,706,706,706,706, \
706,706,706,690,690,690,690,690,690,690,674,674,674,674,674,674,674,674,674,658,658,658,658,658,658,658,642,642,642,642,642,642, \
642,642,642,626,626,626,626,626,626,626,610,610,610,610,610,610,610,610,610,594,594,594,594,594,594,594,578,578,578,578,578,578, \
578,578,578,562,562,562,562,562,562,562,546,546,546,546,546,546,546,546,546,530,530,530,530,530,530,530,514,514,514,514,514,514, \
514,514,514,242,242,242,242,242,242,242,226,226,226,226,226,226,226,226,226,210,210,210,210,210,210,210,194,194,194,194,194,194, \
194,194,194,178,178,178,178,178,178,178,162,162,162,162,162,162,162,162,162,146,146,146,146,146,146,146,130,130,130,130,130,130, \
130,130,130,114,114,114,114,114,114,114,98,98,98,98,98,98,98,98,98,82,82,82,82,82,82,82,66,66,66,66,66,66, \
66,66,66,50,50,50,50,50,50,50,34,34,34,34,34,34,34,34,34,18,18,18,18,18,18,18,2,2,2,2,2,2, \
2,1524,1524,1524,1022,1022,1022,1022,1022,1508,1508,1508,1006,1006,1006,1006,1006,1492,1492,1492,990,990,990,990,990,1476,1476,1476,974,974,974,974, \
974,1460,1460,1460,958,958,958,958,958,1444,1444,1444,942,942,942,942,942,1428,1428,1428,926,926,926,926,926,1412,1412,1412,910,910,910,910, \
910,1396,1396,1396,894,894,894,894,894,1380,1380,1380,878,878,878,878,878,1364,1364,1364,862,862,862,862,862,1348,1348,1348,846,846,846,846, \
846,1332,1332,1332,830,830,830,830,830,1316,1316,1316,814,814,814,814,814,1300,1300,1300,798,798,798,798,798,1284,1284,1284,782,782,782,782, \
782,1520,1520,1520,1020,1020,1020,1020,1020,1504,1504,1504,1004,1004,1004,1004,1004,1488,1488,1488,988,988,988,988,988,1472,1472,1472,972,972,972,972, \
972,1456,1456,1456,956,956,956,956,956,1440,1440,1440,940,940,940,940,940,1424,1424,1424,924,924,924,924,924,1408,1408,1408,908,908,908,908, \
908,1392,1392,1392,892,892,892,892,892,1376,1376,1376,876,876,876,876,876,1360,1360,1360,860,860,860,860,860,1344,1344,1344,844,844,844,844, \
844,1328,1328,1328,828,828,828,828,828,1312,1312,1312,812,812,812,812,812,1296,1296,1296,796,796,796,796,796,1280,1280,1280,780,780,780,780, \
780,1268,1268,1268,1018,1018,1018,1018,1018,1252,1252,1252,1002,1002,1002,1002,1002,1236,1236,1236,986,986,986,986,986,1220,1220,1220,970,970,970,970, \
970,1204,1204,1204,954,954,954,954,954,1188,1188,1188,938,938,938,938,938,1172,1172,1172,922,922,922,922,922,1156,1156,1156,906,906,906,906, \
906,1140,1140,1140,890,890,890,890,890,1124,1124,1124,874,874,874,874,874,1108,1108,1108,858,858,858,858,858,1092,1092,1092,842,842,842,842, \
842,1076,1076,1076,826,826,826,826,826,1060,1060,1060,810,810,810,810,810,1044,1044,1044,794,794,794,794,794,1028,1028,1028,778,778,778,778, \
778,1264,1264,1264,1016,1016,1016,1016,1016,1248,1248,1248,1000,1000,1000,1000,1000,1232,1232,1232,984,984,984,984,984,1216,1216,1216,968,968,968,968, \
968,1200,1200,1200,952,952,952,952,952,1184,1184,1184,936,936,936,936,936,1168,1168,1168,920,920,920,920,920,1152,1152,1152,904,904,904,904, \
904,1136,1136,1136,888,888,888,888,888,1120,1120,1120,872,872,872,872,872,1104,1104,1104,856,856,856,856,856,1088,1088,1088,840,840,840,840, \
840,1072,1072,1072,824,824,824,824,824,1056,1056,1056,808,808,808,808,808,1040,1040,1040,792,792,792,792,792,1024,1024,1024,776,776,776,776, \
510,504,504,504,508,766,766,766,494,488,488,488,492,750,750,750,478,472,472,472,476,734,734,734,462,456,456,456,460,718,718,718, \
446,440,440,440,444,702,702,702,430,424,424,424,428,686,686,686,414,408,408,408,412,670,670,670,398,392,392,392,396,654,654,654, \
382,376,376,376,380,638,638,638,366,360,360,360,364,622,622,622,350,344,344,344,348,606,606,606,334,328,328,328,332,590,590,590, \
318,312,312,312,316,574,574,574,302,296,296,296,300,558,558,558,286,280,280,280,284,542,542,542,270,264,264,264,268,526,526,526, \
254,248,248,248,252,764,764,764,238,232,232,232,236,748,748,748,222,216,216,216,220,732,732,732,206,200,200,200,204,716,716,716, \
190,184,184,184,188,700,700,700,174,168,168,168,172,684,684,684,158,152,152,152,156,668,668,668,142,136,136,136,140,652,652,652, \
126,120,120,120,124,636,636,636,110,104,104,104,108,620,620,620,94,88,88,88,92,604,604,604,78,72,72,72,76,588,588,588, \
62,56,56,56,60,572,572,572,46,40,40,40,44,556,556,556,30,24,24,24,28,540,540,540,14,8,8,8,12,524,524,1014, \
758,1010,752,1012,756,1008,762,998,742,994,736,996,740,992,746,982,726,978,720,980,724,976,730,966,710,962,704,964,708,960,714,950, \
694,946,688,948,692,944,698,934,678,930,672,932,676,928,682,918,662,914,656,916,660,912,666,902,646,898,640,900,644,896,650,886, \
630,882,624,884,628,880,634,870,614,866,608,868,612,864,618,854,598,850,592,852,596,848,602,838,582,834,576,836,580,832,586,822, \
566,818,560,820,564,816,570,806,550,802,544,804,548,800,554,790,534,786,528,788,532,784,538,774,518,770,512,772,516,768,522,502, \
246,498,240,500,244,496,760,486,230,482,224,484,228,480,744,470,214,466,208,468,212,464,728,454,198,450,192,452,196,448,712,438, \
182,434,176,436,180,432,696,422,166,418,160,420,164,416,680,406,150,402,144,404,148,400,664,390,134,386,128,388,132,384,648,374, \
118,370,112,372,116,368,632,358,102,354,96,356,100,352,616,342,86,338,80,340,84,336,600,326,70,322,64,324,68,320,584,310, \
54,306,48,308,52,304,568,294,38,290,32,292,36,288,552,278,22,274,16,276,20,272,536,262,6,258,0,260,4,256,520,1538
#define PSH 1
#define HS_SEQ G_p G_n G_u G_x G_q
#define HS_SEQ2 ""
#define MAIN_ASM \
".LATLstart:\n" \
"s_waitcnt lgkmcnt(0)\n" \
"s_nop 3\n" \
"s_mov_b32 s60, 0x3fb8aa3b\n" \
"s_lshr_b32 s63, %[w4k], 9\n" \
"s_and_b32 s63, s63, 16\n" \
"s_lshr_b32 s63, %[dt], s63\n" \
"s_lshl_b32 s63, s63, 16\n" \
"v_mov_b32_e32 v48, s63\n" \
"s_bfe_u32 s89, %[w4k], 0x1000c\n" \
"s_mul_i32 s89, s89, 0x02020000\n" \
"s_add_u32 s89, s89, 0x01000c0c\n" \
"v_max_f32_e32 v48, 0xc3480000, v48\n" \
"v_min_f32_e32 v48, 0x43480000, v48\n" \
"v_mul_f32_e32 v50, s60, v48\n" \
"v_rndne_f32_e32 v51, v50\n" \
"v_fma_f32 v50, v48, s60, -v51\n" \
"v_fmac_f32_e32 v50, 0x32a5705f, v48\n" \
"s_waitcnt vmcnt(6)\n" \
"v_lshlrev_b32_e32 v49, 16, v2\n" \
"v_lshlrev_b32_e32 v52, 16, v3\n" \
"v_add_f32_e64 v49, |v49|, |v52|\n" \
"v_and_b32_e32 v52, 0x7fff0000, v2\n" \
"v_and_b32_e32 v53, 0x7fff0000, v3\n" \
"v_add_f32_e32 v52, v52, v53\n" \
"v_add_f32_e32 v49, v49, v52\n" \
"v_perm_b32 v0, v0, v0, s89\n" \
"v_perm_b32 v1, v1, v1, s89\n" \
"v_perm_b32 v2, v2, v2, s89\n" \
"v_add_f32_dpp v49, v49, v49 row_ror:8 row_mask:0xf bank_mask:0xf\n" \
"v_perm_b32 v3, v3, v3, s89\n" \
"v_exp_f32_e32 v50, v50\n" \
"v_cvt_i32_f32_e32 v51, v51\n" \
"v_add_f32_dpp v49, v49, v49 row_ror:4 row_mask:0xf bank_mask:0xf\n" \
"v_ldexp_f32 v50, v50, v51\n" \
"v_cvt_pk_bf16_f32 v48, 0, v50\n" \
"v_max_f32_e32 v0, 0xc3480000, v0\n" \
"v_add_f32_dpp v49, v49, v49 row_ror:2 row_mask:0xf bank_mask:0xf\n" \
"v_min_f32_e32 v0, 0x43480000, v0\n" \
"v_mul_f32_e32 v50, s60, v0\n" \
"v_rndne_f32_e32 v51, v50\n" \
"v_add_f32_dpp v49, v49, v49 row_ror:1 row_mask:0xf bank_mask:0xf\n" \
"v_fma_f32 v50, v0, s60, -v51\n" \
"v_fmac_f32_e32 v50, 0x32a5705f, v0\n" \
"v_exp_f32_e32 v50, v50\n" \
"v_add_f32_dpp v49, v49, v49 row_bcast:15 row_mask:0xa bank_mask:0xf\n" \
"v_cvt_i32_f32_e32 v51, v51\n" \
"v_ldexp_f32 v50, v50, v51\n" \
"v_cvt_pk_bf16_f32 v0, 0, v50\n" \
"v_add_f32_dpp v49, v49, v49 row_bcast:31 row_mask:0xc bank_mask:0xf\n" \
"v_mul_f32_e32 v1, v1, v48\n" \
"v_mul_f32_e64 v0, -v0, v48\n" \
"v_mul_f32_e32 v50, 0.15915494, v1\n" \
"v_mul_f32_e32 v0, s60, v0\n" \
"v_fma_f32 v51, v1, 0.15915494, -v50\n" \
"v_fmac_f32_e32 v51, 0x31dc9c88, v1\n" \
"v_add_f32_e32 v48, v48, v48\n" \
"v_readlane_b32 s63, v49, 63\n" \
"v_mul_f32_e32 v31, 0xc1000000, v50\n" \
"v_mul_f32_e32 v49, 0x3f8020c5, v48\n" \
"v_mul_f32_e32 v52, 0x41000000, v0\n" \
"v_mul_f32_e32 v2, v2, v48\n" \
"v_fract_f32_e32 v31, v31\n" \
"v_mul_f32_e32 v3, v3, v48\n" \
"v_exp_f32_e32 v52, v52\n" \
"v_mul_f32_e32 v49, s63, v49\n" \
"v_fmac_f32_e32 v31, 0xc1000000, v51\n" \
"v_bfe_u32 v49, v49, 23, 8\n" \
"v_cos_f32_e32 v30, v31\n" \
"v_max_u32_e32 v49, 20, v49\n" \
"v_sin_f32_e32 v31, v31\n" \
"v_min_u32_e32 v49, 230, v49\n" \
"v_mul_f32_e32 v30, v30, v52\n" \
"v_lshlrev_b32_e32 v49, 23, v49\n" \
"v_mul_f32_e32 v31, v31, v52\n" \
"v_add_u32_e32 v35, 0xf9800000, v49\n" \
"v_sub_u32_e32 v49, 0x85800000, v49\n" \
"v_mul_f32_e32 v36, v2, v49\n" \
"v_mul_f32_e32 v37, v3, v49\n" \
"v_fract_f32_e32 v29, v50\n" \
"v_mul_f32_e32 v33, 0x43000000, v50\n" \
"v_exp_f32_e32 v52, v0\n" \
"v_mul_f32_e32 v53, 0x43000000, v0\n" \
"v_add_f32_e32 v29, v29, v51\n" \
"v_fract_f32_e32 v33, v33\n" \
"v_cos_f32_e32 v28, v29\n" \
"v_exp_f32_e32 v53, v53\n" \
"v_sin_f32_e32 v29, v29\n" \
"v_fmac_f32_e32 v33, 0x43000000, v51\n" \
"v_mul_f32_e32 v28, v28, v52\n" \
"v_cos_f32_e32 v32, v33\n" \
"v_mul_f32_e32 v29, v29, v52\n" \
"v_sin_f32_e32 v33, v33\n" \
"v_mul_f32_e32 v32, v32, v53\n" \
"v_mul_f32_e32 v33, v33, v53\n" \
"v_mul_f32_e32 v63, 0x44800000, v50\n" \
"v_mul_f32_e32 v48, v31, v31\n" \
"v_mul_f32_e32 v52, 0x44800000, v0\n" \
"v_mul_f32_e32 v49, v31, v30\n" \
"v_fract_f32_e32 v63, v63\n" \
"v_fma_f32 v27, v30, v31, v49\n" \
"v_exp_f32_e32 v52, v52\n" \
"v_fma_f32 v26, v30, v30, -v48\n" \
"v_fmac_f32_e32 v63, 0x44800000, v51\n" \
"v_cos_f32_e32 v62, v63\n" \
"v_sin_f32_e32 v63, v63\n" \
"v_mul_f32_e32 v62, v62, v52\n" \
"v_mul_f32_e32 v63, v63, v52\n" \
"v_cvt_pk_f16_f32 v64, 1.0, v30\n" \
"v_cvt_pk_f16_f32 v72, 0, v31\n" \
"v_mul_f32_e32 v48, v27, v31\n" \
"v_mul_f32_e32 v49, v27, v30\n" \
"v_fma_f32 v57, v26, v31, v49\n" \
"v_fma_f32 v56, v26, v30, -v48\n" \
"v_cvt_pk_f16_f32 v65, v26, v56\n" \
"v_cvt_pk_f16_f32 v73, v27, v57\n" \
"v_mul_f32_e32 v48, v27, v27\n" \
"v_mul_f32_e32 v49, v27, v26\n" \
"v_fma_f32 v31, v26, v27, v49\n" \
"v_fma_f32 v30, v26, v26, -v48\n" \
"v_cvt_pk_f16_f32 v220, v30, v31\n" \
"v_mul_f32_e32 v48, v31, v31\n" \
"v_mul_f32_e32 v49, v31, v30\n" \
"v_fma_f32 v27, v30, v31, v49\n" \
"v_fma_f32 v26, v30, v30, -v48\n" \
"v_cvt_pk_f16_f32 v221, v26, v27\n" \
"v_mul_f32_e32 v48, v27, v31\n" \
"v_mul_f32_e32 v49, v27, v30\n" \
"v_fma_f32 v57, v26, v31, v49\n" \
"v_fma_f32 v56, v26, v30, -v48\n" \
"v_cvt_pk_f16_f32 v222, v56, v57\n" \
"v_pk_mul_f16 v66, v220, v64 op_sel:[0,0] op_sel_hi:[0,1]\n" \
"v_pk_mul_f16 v74, v220, v72 op_sel:[0,0] op_sel_hi:[0,1]\n" \
"v_pk_fma_f16 v66, v220, v72, v66 op_sel:[1,0,0] op_sel_hi:[1,1,1] neg_lo:[1,0,0] neg_hi:[1,0,0]\n" \
"v_pk_fma_f16 v74, v220, v64, v74 op_sel:[1,0,0] op_sel_hi:[1,1,1]\n" \
"v_pk_mul_f16 v67, v220, v65 op_sel:[0,0] op_sel_hi:[0,1]\n" \
"v_pk_mul_f16 v75, v220, v73 op_sel:[0,0] op_sel_hi:[0,1]\n" \
"v_pk_fma_f16 v67, v220, v73, v67 op_sel:[1,0,0] op_sel_hi:[1,1,1] neg_lo:[1,0,0] neg_hi:[1,0,0]\n" \
"v_pk_fma_f16 v75, v220, v65, v75 op_sel:[1,0,0] op_sel_hi:[1,1,1]\n" \
"v_pk_mul_f16 v68, v221, v64 op_sel:[0,0] op_sel_hi:[0,1]\n" \
"v_pk_mul_f16 v76, v221, v72 op_sel:[0,0] op_sel_hi:[0,1]\n" \
"v_pk_fma_f16 v68, v221, v72, v68 op_sel:[1,0,0] op_sel_hi:[1,1,1] neg_lo:[1,0,0] neg_hi:[1,0,0]\n" \
"v_pk_fma_f16 v76, v221, v64, v76 op_sel:[1,0,0] op_sel_hi:[1,1,1]\n" \
"v_pk_mul_f16 v69, v221, v65 op_sel:[0,0] op_sel_hi:[0,1]\n" \
"v_pk_mul_f16 v77, v221, v73 op_sel:[0,0] op_sel_hi:[0,1]\n" \
"v_pk_fma_f16 v69, v221, v73, v69 op_sel:[1,0,0] op_sel_hi:[1,1,1] neg_lo:[1,0,0] neg_hi:[1,0,0]\n" \
"v_pk_fma_f16 v77, v221, v65, v77 op_sel:[1,0,0] op_sel_hi:[1,1,1]\n" \
"v_pk_mul_f16 v70, v222, v64 op_sel:[0,0] op_sel_hi:[0,1]\n" \
"v_pk_mul_f16 v78, v222, v72 op_sel:[0,0] op_sel_hi:[0,1]\n" \
"v_pk_fma_f16 v70, v222, v72, v70 op_sel:[1,0,0] op_sel_hi:[1,1,1] neg_lo:[1,0,0] neg_hi:[1,0,0]\n" \
"v_pk_fma_f16 v78, v222, v64, v78 op_sel:[1,0,0] op_sel_hi:[1,1,1]\n" \
"v_pk_mul_f16 v71, v222, v65 op_sel:[0,0] op_sel_hi:[0,1]\n" \
"v_pk_mul_f16 v79, v222, v73 op_sel:[0,0] op_sel_hi:[0,1]\n" \
"v_pk_fma_f16 v71, v222, v73, v71 op_sel:[1,0,0] op_sel_hi:[1,1,1] neg_lo:[1,0,0] neg_hi:[1,0,0]\n" \
"v_pk_fma_f16 v79, v222, v65, v79 op_sel:[1,0,0] op_sel_hi:[1,1,1]\n" \
"v_mul_f32_e32 v48, v29, v29\n" \
"v_mul_f32_e32 v49, v29, v28\n" \
"v_mfma_f32_32x32x16_f16 v[112:127], v[64:67], v[16:19], 0\n" \
"v_fma_f32 v27, v28, v29, v49\n" \
"v_fma_f32 v26, v28, v28, -v48\n" \
"v_mul_f32_e32 v48, v27, v29\n" \
"v_mul_f32_e32 v49, v27, v28\n" \
"v_fma_f32 v57, v26, v29, v49\n" \
"v_fma_f32 v56, v26, v28, -v48\n" \
"v_mfma_f32_32x32x16_f16 v[128:143], v[72:75], v[16:19], 0\n" \
"v_mul_f32_e32 v48, v27, v27\n" \
"v_mul_f32_e32 v49, v27, v26\n" \
"v_fma_f32 v31, v26, v27, v49\n" \
"v_fma_f32 v30, v26, v26, -v48\n" \
"v_cvt_pk_f16_f32 v104, v30, v62\n" \
"v_cvt_pk_f16_f32 v108, v31, v63\n" \
"v_mfma_f32_32x32x16_f16 v[112:127], v[68:71], v[20:23], v[112:127]\n" \
"v_mul_f32_e32 v48, v27, v31\n" \
"v_mul_f32_e32 v49, v27, v30\n" \
"v_fma_f32 v53, v26, v31, v49\n" \
"v_fma_f32 v52, v26, v30, -v48\n" \
"v_cvt_pk_f16_f32 v105, v26, v52\n" \
"v_cvt_pk_f16_f32 v109, v27, v53\n" \
"v_mfma_f32_32x32x16_f16 v[128:143], v[76:79], v[20:23], v[128:143]\n" \
"v_mul_f32_e32 v48, v29, v31\n" \
"v_mul_f32_e32 v49, v29, v30\n" \
"v_fma_f32 v53, v28, v31, v49\n" \
"v_fma_f32 v52, v28, v30, -v48\n" \
"v_cvt_pk_f16_f32 v106, v28, v52\n" \
"v_cvt_pk_f16_f32 v110, v29, v53\n" \
"v_mul_f32_e32 v48, v57, v31\n" \
"v_mul_f32_e32 v49, v57, v30\n" \
"v_fma_f32 v53, v56, v31, v49\n" \
"v_fma_f32 v52, v56, v30, -v48\n" \
"v_cvt_pk_f16_f32 v107, v56, v52\n" \
"v_cvt_pk_f16_f32 v111, v57, v53\n" \
"v_mul_f32_e32 v48, v37, v33\n" \
"v_mul_f32_e32 v49, v37, v32\n" \
"v_fma_f32 v39, v36, v33, v49\n" \
"v_fma_f32 v38, v36, v32, -v48\n" \
"v_cvt_pk_f16_f32 v220, v36, v37\n" \
"v_cvt_pk_f16_f32 v221, v38, v39\n" \
"v_cvt_pk_f16_f32 v68, v36, v38\n" \
"v_cvt_pk_f16_f32 v76, v37, v39\n" \
"v_pk_mul_f16 v80, v220, v104 op_sel:[0,0] op_sel_hi:[0,1]\n" \
"v_pk_mul_f16 v88, v220, v108 op_sel:[0,0] op_sel_hi:[0,1]\n" \
"v_pk_fma_f16 v80, v220, v108, v80 op_sel:[1,0,0] op_sel_hi:[1,1,1] neg_lo:[1,0,0] neg_hi:[1,0,0]\n" \
"v_pk_fma_f16 v88, v220, v104, v88 op_sel:[1,0,0] op_sel_hi:[1,1,1]\n" \
"v_pk_mul_f16 v81, v220, v105 op_sel:[0,0] op_sel_hi:[0,1]\n" \
"v_pk_mul_f16 v89, v220, v109 op_sel:[0,0] op_sel_hi:[0,1]\n" \
"v_pk_fma_f16 v81, v220, v109, v81 op_sel:[1,0,0] op_sel_hi:[1,1,1] neg_lo:[1,0,0] neg_hi:[1,0,0]\n" \
"v_pk_fma_f16 v89, v220, v105, v89 op_sel:[1,0,0] op_sel_hi:[1,1,1]\n" \
"v_pk_mul_f16 v82, v220, v106 op_sel:[0,0] op_sel_hi:[0,1]\n" \
"v_pk_mul_f16 v90, v220, v110 op_sel:[0,0] op_sel_hi:[0,1]\n" \
"v_pk_fma_f16 v82, v220, v110, v82 op_sel:[1,0,0] op_sel_hi:[1,1,1] neg_lo:[1,0,0] neg_hi:[1,0,0]\n" \
"v_pk_fma_f16 v90, v220, v106, v90 op_sel:[1,0,0] op_sel_hi:[1,1,1]\n" \
"v_pk_mul_f16 v83, v220, v107 op_sel:[0,0] op_sel_hi:[0,1]\n" \
"v_pk_mul_f16 v91, v220, v111 op_sel:[0,0] op_sel_hi:[0,1]\n" \
"v_pk_fma_f16 v83, v220, v111, v83 op_sel:[1,0,0] op_sel_hi:[1,1,1] neg_lo:[1,0,0] neg_hi:[1,0,0]\n" \
"v_pk_fma_f16 v91, v220, v107, v91 op_sel:[1,0,0] op_sel_hi:[1,1,1]\n" \
"v_pk_mul_f16 v84, v221, v104 op_sel:[0,0] op_sel_hi:[0,1]\n" \
"v_pk_mul_f16 v92, v221, v108 op_sel:[0,0] op_sel_hi:[0,1]\n" \
"v_pk_fma_f16 v84, v221, v108, v84 op_sel:[1,0,0] op_sel_hi:[1,1,1] neg_lo:[1,0,0] neg_hi:[1,0,0]\n" \
"v_pk_fma_f16 v92, v221, v104, v92 op_sel:[1,0,0] op_sel_hi:[1,1,1]\n" \
"v_pk_mul_f16 v85, v221, v105 op_sel:[0,0] op_sel_hi:[0,1]\n" \
"v_pk_mul_f16 v93, v221, v109 op_sel:[0,0] op_sel_hi:[0,1]\n" \
"v_pk_fma_f16 v85, v221, v109, v85 op_sel:[1,0,0] op_sel_hi:[1,1,1] neg_lo:[1,0,0] neg_hi:[1,0,0]\n" \
"v_pk_fma_f16 v93, v221, v105, v93 op_sel:[1,0,0] op_sel_hi:[1,1,1]\n" \
"s_add_u32 m0, %[w4k], 0\n" \
"s_mov_b32 s79, 0\n" \
"v_pk_mul_f16 v86, v221, v106 op_sel:[0,0] op_sel_hi:[0,1]\n" \
"buffer_load_dwordx4 v226, %[wq], s79 offen offset:0 lds\n" \
"v_pk_mul_f16 v94, v221, v110 op_sel:[0,0] op_sel_hi:[0,1]\n" \
"v_pk_fma_f16 v86, v221, v110, v86 op_sel:[1,0,0] op_sel_hi:[1,1,1] neg_lo:[1,0,0] neg_hi:[1,0,0]\n" \
"v_pk_fma_f16 v94, v221, v106, v94 op_sel:[1,0,0] op_sel_hi:[1,1,1]\n" \
"v_pk_mul_f16 v87, v221, v107 op_sel:[0,0] op_sel_hi:[0,1]\n" \
"v_pk_mul_f16 v95, v221, v111 op_sel:[0,0] op_sel_hi:[0,1]\n" \
"v_pk_fma_f16 v87, v221, v111, v87 op_sel:[1,0,0] op_sel_hi:[1,1,1] neg_lo:[1,0,0] neg_hi:[1,0,0]\n" \
"buffer_load_dwordx4 v227, %[wq], s79 offen offset:1024 lds\n" \
"v_pk_fma_f16 v95, v221, v107, v95 op_sel:[1,0,0] op_sel_hi:[1,1,1]\n" \
"v_mul_f32_e32 v48, v63, v63\n" \
"v_mul_f32_e32 v49, v63, v62\n" \
"v_mfma_f32_32x32x16_f16 v[144:159], v[80:83], v[16:19], 0\n" \
"v_fma_f32 v63, v62, v63, v49\n" \
"v_fma_f32 v62, v62, v62, -v48\n" \
"buffer_load_dwordx4 v254, %[wq], s79 offen offset:2048 lds\n" \
"v_cvt_pk_f16_f32 v107, v62, 0\n" \
"v_cvt_pk_f16_f32 v111, v63, 0\n" \
"v_pk_mul_f16 v54, v220, v107 op_sel:[0,0] op_sel_hi:[0,1]\n" \
"v_pk_mul_f16 v62, v220, v111 op_sel:[0,0] op_sel_hi:[0,1]\n" \
"v_mfma_f32_32x32x16_f16 v[160:175], v[88:91], v[16:19], 0\n" \
"v_pk_fma_f16 v54, v220, v111, v54 op_sel:[1,0,0] op_sel_hi:[1,1,1] neg_lo:[1,0,0] neg_hi:[1,0,0]\n" \
"buffer_load_dwordx4 v236, %[wq], s79 offen offset:3072 lds\n" \
"v_pk_fma_f16 v62, v220, v107, v62 op_sel:[1,0,0] op_sel_hi:[1,1,1]\n" \
"v_cvt_pk_f16_f32 v112, v112, v113\n" \
"v_cvt_pk_f16_f32 v113, v114, v115\n" \
"v_cvt_pk_f16_f32 v114, v116, v117\n" \
"v_cvt_pk_f16_f32 v115, v118, v119\n" \
"s_add_u32 m0, s64, 32768\n" \
"v_mfma_f32_32x32x16_f16 v[144:159], v[84:87], v[20:23], v[144:159]\n" \
"buffer_load_dwordx4 v224, %[xa], 0 offen offset:0 lds\n" \
"v_cvt_pk_f16_f32 v116, v120, v121\n" \
"v_cvt_pk_f16_f32 v117, v122, v123\n" \
"v_cvt_pk_f16_f32 v118, v124, v125\n" \
"v_cvt_pk_f16_f32 v119, v126, v127\n" \
"v_cvt_pk_f16_f32 v128, v128, v129\n" \
"v_cvt_pk_f16_f32 v129, v130, v131\n" \
"buffer_load_dwordx4 v225, %[xa], s84 offen offset:1024 lds\n" \
"v_mfma_f32_32x32x16_f16 v[160:175], v[92:95], v[20:23], v[160:175]\n" \
"v_cvt_pk_f16_f32 v130, v132, v133\n" \
"v_cvt_pk_f16_f32 v131, v134, v135\n" \
"v_cvt_pk_f16_f32 v132, v136, v137\n" \
"v_cvt_pk_f16_f32 v133, v138, v139\n" \
"v_cvt_pk_f16_f32 v134, v140, v141\n" \
"buffer_load_dwordx4 v224, %[xa], s85 offen offset:2048 lds\n" \
"v_cvt_pk_f16_f32 v135, v142, v143\n" \
"v_mul_f32_e32 v48, v39, v33\n" \
"v_mul_f32_e32 v49, v39, v32\n" \
"v_fma_f32 v37, v38, v33, v49\n" \
"v_fma_f32 v36, v38, v32, -v48\n" \
"v_mul_f32_e32 v48, v37, v33\n" \
"buffer_load_dwordx4 v225, %[xa], s86 offen offset:3072 lds\n" \
"v_mul_f32_e32 v49, v37, v32\n" \
"v_fma_f32 v39, v36, v33, v49\n" \
"v_fma_f32 v38, v36, v32, -v48\n" \
"v_cvt_pk_f16_f32 v222, v36, v37\n" \
"v_cvt_pk_f16_f32 v223, v38, v39\n" \
"s_add_u32 m0, s64, 36864\n" \
"v_cvt_pk_f16_f32 v69, v36, v38\n" \
"buffer_load_dwordx4 v224, %[xb], 0 offen offset:0 lds\n" \
"v_cvt_pk_f16_f32 v77, v37, v39\n" \
"v_pk_mul_f16 v64, v222, v104 op_sel:[0,0] op_sel_hi:[0,1]\n" \
"v_pk_mul_f16 v72, v222, v108 op_sel:[0,0] op_sel_hi:[0,1]\n" \
"v_pk_fma_f16 v64, v222, v108, v64 op_sel:[1,0,0] op_sel_hi:[1,1,1] neg_lo:[1,0,0] neg_hi:[1,0,0]\n" \
"v_pk_fma_f16 v72, v222, v104, v72 op_sel:[1,0,0] op_sel_hi:[1,1,1]\n" \
"v_pk_mul_f16 v65, v222, v105 op_sel:[0,0] op_sel_hi:[0,1]\n" \
"buffer_load_dwordx4 v225, %[xb], s84 offen offset:1024 lds\n" \
"v_pk_mul_f16 v73, v222, v109 op_sel:[0,0] op_sel_hi:[0,1]\n" \
"v_pk_fma_f16 v65, v222, v109, v65 op_sel:[1,0,0] op_sel_hi:[1,1,1] neg_lo:[1,0,0] neg_hi:[1,0,0]\n" \
"v_pk_fma_f16 v73, v222, v105, v73 op_sel:[1,0,0] op_sel_hi:[1,1,1]\n" \
"v_pk_mul_f16 v66, v223, v104 op_sel:[0,0] op_sel_hi:[0,1]\n" \
"v_pk_mul_f16 v74, v223, v108 op_sel:[0,0] op_sel_hi:[0,1]\n" \
"v_pk_fma_f16 v66, v223, v108, v66 op_sel:[1,0,0] op_sel_hi:[1,1,1] neg_lo:[1,0,0] neg_hi:[1,0,0]\n" \
"buffer_load_dwordx4 v224, %[xb], s85 offen offset:2048 lds\n" \
"v_pk_fma_f16 v74, v223, v104, v74 op_sel:[1,0,0] op_sel_hi:[1,1,1]\n" \
"v_pk_mul_f16 v67, v223, v105 op_sel:[0,0] op_sel_hi:[0,1]\n" \
"v_pk_mul_f16 v75, v223, v109 op_sel:[0,0] op_sel_hi:[0,1]\n" \
"v_pk_fma_f16 v67, v223, v109, v67 op_sel:[1,0,0] op_sel_hi:[1,1,1] neg_lo:[1,0,0] neg_hi:[1,0,0]\n" \
"v_pk_fma_f16 v75, v223, v105, v75 op_sel:[1,0,0] op_sel_hi:[1,1,1]\n" \
"v_mul_f32_e32 v48, v39, v33\n" \
"buffer_load_dwordx4 v225, %[xb], s86 offen offset:3072 lds\n" \
"v_mul_f32_e32 v49, v39, v32\n" \
"v_fma_f32 v37, v38, v33, v49\n" \
"v_fma_f32 v36, v38, v32, -v48\n" \
"v_mul_f32_e32 v48, v37, v33\n" \
"v_mul_f32_e32 v49, v37, v32\n" \
"v_fma_f32 v39, v36, v33, v49\n" \
"v_fma_f32 v38, v36, v32, -v48\n" \
"v_cvt_pk_f16_f32 v220, v36, v37\n" \
"v_cvt_pk_f16_f32 v221, v38, v39\n" \
"v_cvt_pk_f16_f32 v70, v36, v38\n" \
"v_cvt_pk_f16_f32 v78, v37, v39\n" \
"v_mul_f32_e32 v48, v39, v33\n" \
"v_mul_f32_e32 v49, v39, v32\n" \
"s_add_u32 m0, s64, 65408\n" \
"v_fma_f32 v37, v38, v33, v49\n" \
"buffer_load_dwordx4 v224, %[xa], 0 offen offset:128 lds\n" \
"v_fma_f32 v36, v38, v32, -v48\n" \
"v_mul_f32_e32 v48, v37, v33\n" \
"v_mul_f32_e32 v49, v37, v32\n" \
"v_fma_f32 v39, v36, v33, v49\n" \
"v_fma_f32 v38, v36, v32, -v48\n" \
"v_cvt_pk_f16_f32 v222, v36, v37\n" \
"v_cvt_pk_f16_f32 v223, v38, v39\n" \
"v_cvt_pk_f16_f32 v71, v36, v38\n" \
"buffer_load_dwordx4 v225, %[xa], s84 offen offset:1152 lds\n" \
"v_cvt_pk_f16_f32 v79, v37, v39\n" \
"v_cvt_pk_f16_f32 v144, v144, v145\n" \
"v_cvt_pk_f16_f32 v145, v146, v147\n" \
"v_mfma_f32_32x32x16_f16 v[176:191], v[64:67], v[16:19], 0\n" \
"v_cvt_pk_f16_f32 v146, v148, v149\n" \
"v_cvt_pk_f16_f32 v147, v150, v151\n" \
"v_cvt_pk_f16_f32 v148, v152, v153\n" \
"v_cvt_pk_f16_f32 v149, v154, v155\n" \
"buffer_load_dwordx4 v224, %[xa], s85 offen offset:2176 lds\n" \
"v_cvt_pk_f16_f32 v150, v156, v157\n" \
"v_cvt_pk_f16_f32 v151, v158, v159\n" \
"v_mfma_f32_32x32x16_f16 v[192:207], v[72:75], v[16:19], 0\n" \
"v_cvt_pk_f16_f32 v160, v160, v161\n" \
"v_cvt_pk_f16_f32 v161, v162, v163\n" \
"v_cvt_pk_f16_f32 v162, v164, v165\n" \
"v_cvt_pk_f16_f32 v163, v166, v167\n" \
"v_cvt_pk_f16_f32 v164, v168, v169\n" \
"buffer_load_dwordx4 v225, %[xa], s86 offen offset:3200 lds\n" \
"v_cvt_pk_f16_f32 v165, v170, v171\n" \
"v_mfma_f32_32x32x16_f16 v[176:191], v[68:71], v[20:23], v[176:191]\n" \
"v_cvt_pk_f16_f32 v166, v172, v173\n" \
"v_cvt_pk_f16_f32 v167, v174, v175\n" \
"s_waitcnt vmcnt(19)\n" \
"ds_write_b128 v12, v[4:7] offset:0\n" \
"ds_write_b128 v12, v[8:11] offset:1152\n" \
"s_add_u32 m0, s64, 69504\n" \
"ds_read2_b32 v[40:41], v13 offset0:0 offset1:36\n" \
"buffer_load_dwordx4 v224, %[xb], 0 offen offset:128 lds\n" \
"v_mfma_f32_32x32x16_f16 v[192:207], v[76:79], v[20:23], v[192:207]\n" \
"ds_read2_b32 v[42:43], v13 offset0:72 offset1:108\n" \
"ds_read2_b32 v[44:45], v13 offset0:16 offset1:52\n" \
"ds_read2_b32 v[46:47], v13 offset0:88 offset1:124\n" \
"s_mov_b32 s89, 0x20000\n" \
"v_pk_mul_f16 v80, v220, v104 op_sel:[0,0] op_sel_hi:[0,1]\n" \
"v_pk_mul_f16 v88, v220, v108 op_sel:[0,0] op_sel_hi:[0,1]\n" \
"v_mfma_f32_16x16x32_f16 v[208:211], v[144:147], v[112:115], 0\n" \
"buffer_load_dwordx4 v225, %[xb], s84 offen offset:1152 lds\n" \
"v_pk_fma_f16 v80, v220, v108, v80 op_sel:[1,0,0] op_sel_hi:[1,1,1] neg_lo:[1,0,0] neg_hi:[1,0,0]\n" \
"v_pk_fma_f16 v88, v220, v104, v88 op_sel:[1,0,0] op_sel_hi:[1,1,1]\n" \
"v_pk_mul_f16 v81, v221, v104 op_sel:[0,0] op_sel_hi:[0,1]\n" \
"v_mfma_f32_16x16x32_f16 v[208:211], v[148:151], v[116:119], v[208:211]\n" \
"v_pk_mul_f16 v89, v221, v108 op_sel:[0,0] op_sel_hi:[0,1]\n" \
"v_pk_fma_f16 v81, v221, v108, v81 op_sel:[1,0,0] op_sel_hi:[1,1,1] neg_lo:[1,0,0] neg_hi:[1,0,0]\n" \
"v_pk_fma_f16 v89, v221, v104, v89 op_sel:[1,0,0] op_sel_hi:[1,1,1]\n" \
"v_mfma_f32_16x16x32_f16 v[208:211], v[160:163], v[128:131], v[208:211]\n" \
"buffer_load_dwordx4 v224, %[xb], s85 offen offset:2176 lds\n" \
"v_pk_mul_f16 v82, v222, v104 op_sel:[0,0] op_sel_hi:[0,1]\n" \
"v_pk_mul_f16 v90, v222, v108 op_sel:[0,0] op_sel_hi:[0,1]\n" \
"v_pk_fma_f16 v82, v222, v108, v82 op_sel:[1,0,0] op_sel_hi:[1,1,1] neg_lo:[1,0,0] neg_hi:[1,0,0]\n" \
"v_mfma_f32_16x16x32_f16 v[208:211], v[164:167], v[132:135], v[208:211]\n" \
"v_pk_fma_f16 v90, v222, v104, v90 op_sel:[1,0,0] op_sel_hi:[1,1,1]\n" \
"v_pk_mul_f16 v83, v223, v104 op_sel:[0,0] op_sel_hi:[0,1]\n" \
"v_pk_mul_f16 v91, v223, v108 op_sel:[0,0] op_sel_hi:[0,1]\n" \
"v_pk_fma_f16 v83, v223, v108, v83 op_sel:[1,0,0] op_sel_hi:[1,1,1] neg_lo:[1,0,0] neg_hi:[1,0,0]\n" \
"buffer_load_dwordx4 v225, %[xb], s86 offen offset:3200 lds\n" \
"v_pk_fma_f16 v91, v223, v104, v91 op_sel:[1,0,0] op_sel_hi:[1,1,1]\n" \
"v_mov_b32_e32 v84, v54\n" \
"v_mov_b32_e32 v92, v62\n" \
"v_cvt_pk_f16_f32 v176, v176, v177\n" \
"v_cvt_pk_f16_f32 v177, v178, v179\n" \
"v_mfma_f32_32x32x16_f16 v[144:159], v[80:83], v[16:19], 0\n" \
"v_cvt_pk_f16_f32 v178, v180, v181\n" \
"s_add_u32 m0, %[w4k], 16384\n" \
"s_mov_b32 s79, 524288\n" \
"v_cvt_pk_f16_f32 v179, v182, v183\n" \
"buffer_load_dwordx4 v226, %[wq], s79 offen offset:0 lds\n" \
"v_cvt_pk_f16_f32 v180, v184, v185\n" \
"v_cvt_pk_f16_f32 v181, v186, v187\n" \
"v_cvt_pk_f16_f32 v182, v188, v189\n" \
"v_mfma_f32_32x32x16_f16 v[160:175], v[88:91], v[16:19], 0\n" \
"v_cvt_pk_f16_f32 v183, v190, v191\n" \
"v_cvt_pk_f16_f32 v192, v192, v193\n" \
"v_cvt_pk_f16_f32 v193, v194, v195\n" \
"v_cvt_pk_f16_f32 v194, v196, v197\n" \
"buffer_load_dwordx4 v227, %[wq], s79 offen offset:1024 lds\n" \
"v_cvt_pk_f16_f32 v195, v198, v199\n" \
"v_mfma_f32_32x32x16_f16 v[144:159], v[84:87], v[20:23], v[144:159]\n" \
"v_cvt_pk_f16_f32 v196, v200, v201\n" \
"v_cvt_pk_f16_f32 v197, v202, v203\n" \
"v_cvt_pk_f16_f32 v198, v204, v205\n" \
"v_cvt_pk_f16_f32 v199, v206, v207\n" \
"s_add_u32 s63, %[w4k], 147456\n" \
"v_mfma_f32_32x32x16_f16 v[160:175], v[92:95], v[20:23], v[160:175]\n" \
"buffer_load_dwordx4 v254, %[wq], s79 offen offset:2048 lds\n" \
"v_add_u32_e32 v24, s63, v241\n" \
"s_lshr_b32 s60, %[w4k], 2\n" \
"s_and_b32 s60, s60, 1024\n" \
"v_add_u32_e32 v25, s60, v24\n" \
"v_xor_b32_e32 v34, 4096, v25\n" \
"v_cmp_ne_u32_e64 s[90:91], v24, v25\n" \
"v_mfma_f32_16x16x32_f16 v[212:215], v[176:179], v[112:115], 0\n" \
"s_waitcnt lgkmcnt(0)\n" \
"buffer_load_dwordx4 v236, %[wq], s79 offen offset:3072 lds\n" \
"v_mfma_f32_16x16x32_f16 v[212:215], v[180:183], v[116:119], v[212:215]\n" \
"buffer_store_dwordx4 v[40:43], v14, %[wsr], 0 offen sc1\n" \
"v_mfma_f32_16x16x32_f16 v[212:215], v[192:195], v[128:131], v[212:215]\n" \
"buffer_store_dwordx4 v[44:47], v14, %[wsr], s89 offen sc1\n" \
"v_mfma_f32_16x16x32_f16 v[212:215], v[196:199], v[132:135], v[212:215]\n" \
"s_nop 1\n" \
"v_cvt_pk_f16_f32 v144, v144, v145\n" \
"v_cvt_pk_f16_f32 v145, v146, v147\n" \
"v_cvt_pk_f16_f32 v146, v148, v149\n" \
"v_cvt_pk_f16_f32 v147, v150, v151\n" \
"v_cvt_pk_f16_f32 v148, v152, v153\n" \
"v_cvt_pk_f16_f32 v149, v154, v155\n" \
"buffer_load_dword v243, v242, %[wsr], s80 offen offset:0 sc1\n" \
"v_cvt_pk_f16_f32 v150, v156, v157\n" \
"v_cvt_pk_f16_f32 v151, v158, v159\n" \
"v_cvt_pk_f16_f32 v160, v160, v161\n" \
"v_cvt_pk_f16_f32 v161, v162, v163\n" \
"buffer_load_dword v245, v242, %[wsr], s80 offen offset:512 sc1\n" \
"v_cvt_pk_f16_f32 v162, v164, v165\n" \
"v_cvt_pk_f16_f32 v163, v166, v167\n" \
"v_cvt_pk_f16_f32 v164, v168, v169\n" \
"v_cvt_pk_f16_f32 v165, v170, v171\n" \
"buffer_load_dword v246, v242, %[wsr], s80 offen offset:768 sc1\n" \
"v_cvt_pk_f16_f32 v166, v172, v173\n" \
"v_cvt_pk_f16_f32 v167, v174, v175\n" \
"s_nop 0\n" \
"v_mfma_f32_16x16x32_f16 v[216:219], v[144:147], v[112:115], 0\n" \
"v_mfma_f32_16x16x32_f16 v[216:219], v[148:151], v[116:119], v[216:219]\n" \
"v_mfma_f32_16x16x32_f16 v[216:219], v[160:163], v[128:131], v[216:219]\n" \
"v_mfma_f32_16x16x32_f16 v[216:219], v[164:167], v[132:135], v[216:219]\n" \
"v_cvt_pk_f16_f32 v208, v208, v209\n" \
"v_cvt_pk_f16_f32 v209, v210, v211\n" \
"v_cvt_pk_f16_f32 v210, v212, v213\n" \
"v_cvt_pk_f16_f32 v211, v214, v215\n" \
"v_lshrrev_b32_e32 v250, 2, v240\n" \
"v_lshl_add_u32 v250, %[h], 4, v250\n" \
"v_lshlrev_b32_e32 v250, 7, v250\n" \
"ds_write_b128 v24, v[208:211]\n" \
"buffer_load_dword v61, v250, %[ca], 0 offen\n" \
"s_nop 2\n" \
"v_cvt_pk_f16_f32 v212, v216, v217\n" \
"v_cvt_pk_f16_f32 v213, v218, v219\n" \
"ds_write_b64 v24, v[212:213] offset:1024\n" \
"s_waitcnt vmcnt(18) lgkmcnt(0)\n" \
"s_barrier\n" \
"ds_read_b64_tr_b16 v[192:193], v228 offset:0\n" \
"ds_read_b64_tr_b16 v[194:195], v228 offset:256\n" \
"ds_read_b128 a[0:3], v234 offset:0\n" \
"ds_read_b128 a[4:7], v234 offset:2048\n" \
"ds_read_b128 a[8:11], v234 offset:4096\n" \
"ds_read_b128 a[12:15], v234 offset:6144\n" \
"ds_read_b64_tr_b16 v[196:197], v231 offset:0\n" \
"ds_read_b64_tr_b16 v[198:199], v231 offset:256\n" \
"ds_read_b64_tr_b16 v[200:201], v228 offset:512\n" \
"ds_read_b64_tr_b16 v[202:203], v228 offset:768\n" \
"ds_read_b64_tr_b16 v[204:205], v231 offset:512\n" \
"ds_read_b64_tr_b16 v[206:207], v231 offset:768\n" \
"ds_read_b64_tr_b16 v[208:209], v228 offset:1024\n" \
"ds_read_b64_tr_b16 v[210:211], v228 offset:1280\n" \
"ds_read_b128 a[16:19], v235 offset:0\n" \
"ds_read_b128 a[20:23], v235 offset:2048\n" \
"ds_read_b128 a[24:27], v235 offset:4096\n" \
"ds_read_b128 a[28:31], v235 offset:6144\n" \
"s_waitcnt lgkmcnt(10)\n" \
"v_mfma_f32_16x16x32_bf16 v[128:131], v[192:195], a[0:3], 0\n" \
"ds_read_b64_tr_b16 v[212:213], v231 offset:1024\n" \
"ds_read_b128 v[56:59], v25\n" \
"v_mfma_f32_16x16x32_bf16 v[132:135], v[192:195], a[4:7], 0\n" \
"s_add_u32 m0, s64, 98048\n" \
"ds_read_b64_tr_b16 v[214:215], v231 offset:1280\n" \
"ds_read_b128 v[20:23], v34\n" \
"v_mfma_f32_16x16x32_bf16 v[136:139], v[192:195], a[8:11], 0\n" \
"buffer_load_dwordx4 v224, %[xa], 0 offen offset:256 lds\n" \
"v_mfma_f32_16x16x32_bf16 v[140:143], v[192:195], a[12:15], 0\n" \
"v_mfma_f32_16x16x32_bf16 v[64:67], v[196:199], a[0:3], 0\n" \
"ds_read_b64_tr_b16 v[192:193], v228 offset:1536\n" \
"v_mfma_f32_16x16x32_bf16 v[68:71], v[196:199], a[4:7], 0\n" \
"ds_read_b64_tr_b16 v[194:195], v228 offset:1792\n" \
"v_mfma_f32_16x16x32_bf16 v[72:75], v[196:199], a[8:11], 0\n" \
"buffer_load_dwordx4 v225, %[xa], s84 offen offset:1280 lds\n" \
"v_mfma_f32_16x16x32_bf16 v[76:79], v[196:199], a[12:15], 0\n" \
"s_waitcnt lgkmcnt(12)\n" \
"s_waitcnt lgkmcnt(2)\n" \
"v_pk_add_f16 v56, v56, v20\n" \
"v_mfma_f32_16x16x32_bf16 v[144:147], v[200:203], a[0:3], 0\n" \
"ds_read_b64_tr_b16 v[196:197], v231 offset:1536\n" \
"v_pk_add_f16 v57, v57, v21\n" \
"v_mfma_f32_16x16x32_bf16 v[148:151], v[200:203], a[4:7], 0\n" \
"ds_read_b64_tr_b16 v[198:199], v231 offset:1792\n" \
"v_pk_add_f16 v58, v58, v22\n" \
"v_mfma_f32_16x16x32_bf16 v[152:155], v[200:203], a[8:11], 0\n" \
"buffer_load_dwordx4 v224, %[xa], s85 offen offset:2304 lds\n" \
"v_mfma_f32_16x16x32_bf16 v[156:159], v[200:203], a[12:15], 0\n" \
"v_pk_add_f16 v59, v59, v23\n" \
"v_cndmask_b32_e64 v59, v59, v35, s[90:91]\n" \
"v_mfma_f32_16x16x32_bf16 v[96:99], v[204:207], a[0:3], 0\n" \
"ds_read_b64_tr_b16 v[200:201], v228 offset:8192\n" \
"v_lshrrev_b32_e32 v216, 2, v240\n" \
"v_mfma_f32_16x16x32_bf16 v[100:103], v[204:207], a[4:7], 0\n" \
"ds_read_b64_tr_b16 v[202:203], v228 offset:8448\n" \
"v_bfe_u32 v217, v240, 1, 1\n" \
"v_mfma_f32_16x16x32_bf16 v[104:107], v[204:207], a[8:11], 0\n" \
"buffer_load_dwordx4 v225, %[xa], s86 offen offset:3328 lds\n" \
"v_mfma_f32_16x16x32_bf16 v[108:111], v[204:207], a[12:15], 0\n" \
"s_waitcnt vmcnt(14)\n" \
"v_lshlrev_b32_e32 v216, 13, v216\n" \
"v_mfma_f32_16x16x32_bf16 v[160:163], v[208:211], a[0:3], 0\n" \
"ds_read_b64_tr_b16 v[204:205], v231 offset:8192\n" \
"ds_read_b128 a[32:35], v234 offset:32768\n" \
"v_mfma_f32_16x16x32_bf16 v[164:167], v[208:211], a[4:7], 0\n" \
"s_add_u32 m0, s64, 102144\n" \
"ds_read_b64_tr_b16 v[206:207], v231 offset:8448\n" \
"ds_read_b128 a[36:39], v234 offset:34816\n" \
"v_mfma_f32_16x16x32_bf16 v[168:171], v[208:211], a[8:11], 0\n" \
"buffer_load_dwordx4 v224, %[xb], 0 offen offset:256 lds\n" \
"v_mfma_f32_16x16x32_bf16 v[172:175], v[208:211], a[12:15], 0\n" \
"s_lshl_b32 s62, %[da], 17\n" \
"v_lshl_add_u32 v216, v217, 7, v216\n" \
"s_add_u32 s62, s62, 4096\n" \
"v_mfma_f32_16x16x32_bf16 v[80:83], v[212:215], a[0:3], 0\n" \
"s_add_u32 s61, s62, 0x200000\n" \
"ds_read_b64_tr_b16 v[208:209], v228 offset:8704\n" \
"ds_read_b128 a[40:43], v234 offset:36864\n" \
"v_mfma_f32_16x16x32_bf16 v[84:87], v[212:215], a[4:7], 0\n" \
"ds_read_b64_tr_b16 v[210:211], v228 offset:8960\n" \
"ds_read_b128 a[44:47], v234 offset:38912\n" \
"v_mfma_f32_16x16x32_bf16 v[88:91], v[212:215], a[8:11], 0\n" \
"buffer_load_dwordx4 v225, %[xb], s84 offen offset:1280 lds\n" \
"v_mfma_f32_16x16x32_bf16 v[92:95], v[212:215], a[12:15], 0\n" \
"s_waitcnt lgkmcnt(10)\n" \
"v_mfma_f32_16x16x32_bf16 v[176:179], v[192:195], a[0:3], 0\n" \
"ds_read_b64_tr_b16 v[212:213], v231 offset:8704\n" \
"ds_read_b128 a[48:51], v235 offset:32768\n" \
"v_mfma_f32_16x16x32_bf16 v[180:183], v[192:195], a[4:7], 0\n" \
"ds_read_b64_tr_b16 v[214:215], v231 offset:8960\n" \
"ds_read_b128 a[52:55], v235 offset:34816\n" \
"v_mfma_f32_16x16x32_bf16 v[184:187], v[192:195], a[8:11], 0\n" \
"buffer_load_dwordx4 v224, %[xb], s85 offen offset:2304 lds\n" \
"v_mfma_f32_16x16x32_bf16 v[188:191], v[192:195], a[12:15], 0\n" \
"v_mfma_f32_16x16x32_bf16 v[112:115], v[196:199], a[0:3], 0\n" \
"ds_read_b64_tr_b16 v[192:193], v228 offset:9216\n" \
"ds_read_b128 a[56:59], v235 offset:36864\n" \
"v_mfma_f32_16x16x32_bf16 v[116:119], v[196:199], a[4:7], 0\n" \
"ds_read_b64_tr_b16 v[194:195], v228 offset:9472\n" \
"ds_read_b128 a[60:63], v235 offset:38912\n" \
"v_mfma_f32_16x16x32_bf16 v[120:123], v[196:199], a[8:11], 0\n" \
"buffer_load_dwordx4 v225, %[xb], s86 offen offset:3328 lds\n" \
"v_mfma_f32_16x16x32_bf16 v[124:127], v[196:199], a[12:15], 0\n" \
"s_waitcnt lgkmcnt(13)\n" \
"v_mfma_f32_16x16x32_bf16 v[128:131], v[200:203], a[16:19], v[128:131]\n" \
"s_add_u32 m0, %[w4k], 131072\n" \
"ds_read_b64_tr_b16 v[196:197], v231 offset:9216\n" \
"v_mfma_f32_16x16x32_bf16 v[132:135], v[200:203], a[20:23], v[132:135]\n" \
"s_mov_b32 s79, 1048576\n" \
"ds_read_b64_tr_b16 v[198:199], v231 offset:9472\n" \
"v_mfma_f32_16x16x32_bf16 v[136:139], v[200:203], a[24:27], v[136:139]\n" \
"buffer_load_dwordx4 v226, %[wq], s79 offen offset:0 lds\n" \
"v_mfma_f32_16x16x32_bf16 v[140:143], v[200:203], a[28:31], v[140:143]\n" \
"v_mfma_f32_16x16x32_bf16 v[64:67], v[204:207], a[16:19], v[64:67]\n" \
"ds_read_b64_tr_b16 v[200:201], v228 offset:9728\n" \
"v_mfma_f32_16x16x32_bf16 v[68:71], v[204:207], a[20:23], v[68:71]\n" \
"ds_read_b64_tr_b16 v[202:203], v228 offset:9984\n" \
"v_mfma_f32_16x16x32_bf16 v[72:75], v[204:207], a[24:27], v[72:75]\n" \
"buffer_load_dwordx4 v227, %[wq], s79 offen offset:1024 lds\n" \
"v_mfma_f32_16x16x32_bf16 v[76:79], v[204:207], a[28:31], v[76:79]\n" \
"s_waitcnt lgkmcnt(4)\n" \
"v_mfma_f32_16x16x32_bf16 v[144:147], v[208:211], a[16:19], v[144:147]\n" \
"ds_read_b64_tr_b16 v[204:205], v231 offset:9728\n" \
"v_lshrrev_b32_e32 v218, 1, v240\n" \
"v_mfma_f32_16x16x32_bf16 v[148:151], v[208:211], a[20:23], v[148:151]\n" \
"ds_read_b64_tr_b16 v[206:207], v231 offset:9984\n" \
"v_lshlrev_b32_e32 v218, 7, v218\n" \
"v_mfma_f32_16x16x32_bf16 v[152:155], v[208:211], a[24:27], v[152:155]\n" \
"buffer_load_dwordx4 v254, %[wq], s79 offen offset:2048 lds\n" \
"v_mfma_f32_16x16x32_bf16 v[156:159], v[208:211], a[28:31], v[156:159]\n" \
"s_waitcnt vmcnt(17)\n" \
"s_barrier\n" \
"v_mfma_f32_16x16x32_bf16 v[96:99], v[212:215], a[16:19], v[96:99]\n" \
"ds_read_b64_tr_b16 v[208:209], v228 offset:16384\n" \
"v_add_u32_e32 v218, %[wj], v218\n" \
"v_mfma_f32_16x16x32_bf16 v[100:103], v[212:215], a[20:23], v[100:103]\n" \
"ds_read_b64_tr_b16 v[210:211], v228 offset:16640\n" \
"v_mfma_f32_16x16x32_bf16 v[104:107], v[212:215], a[24:27], v[104:107]\n" \
"buffer_load_dwordx4 v236, %[wq], s79 offen offset:3072 lds\n" \
"v_mfma_f32_16x16x32_bf16 v[108:111], v[212:215], a[28:31], v[108:111]\n" \
"s_waitcnt lgkmcnt(6)\n" \
"v_mfma_f32_16x16x32_bf16 v[160:163], v[192:195], a[16:19], v[160:163]\n" \
"ds_read_b64_tr_b16 v[212:213], v231 offset:16384\n" \
"v_mfma_f32_16x16x32_bf16 v[164:167], v[192:195], a[20:23], v[164:167]\n" \
"s_add_u32 m0, s64, 32384\n" \
"ds_read_b64_tr_b16 v[214:215], v231 offset:16640\n" \
"v_mfma_f32_16x16x32_bf16 v[168:171], v[192:195], a[24:27], v[168:171]\n" \
"buffer_load_dwordx4 v224, %[xa], 0 offen offset:384 lds\n" \
"v_mfma_f32_16x16x32_bf16 v[172:175], v[192:195], a[28:31], v[172:175]\n" \
"v_mfma_f32_16x16x32_bf16 v[80:83], v[196:199], a[16:19], v[80:83]\n" \
"ds_read_b64_tr_b16 v[192:193], v228 offset:16896\n" \
"v_mfma_f32_16x16x32_bf16 v[84:87], v[196:199], a[20:23], v[84:87]\n" \
"ds_read_b64_tr_b16 v[194:195], v228 offset:17152\n" \
"v_mfma_f32_16x16x32_bf16 v[88:91], v[196:199], a[24:27], v[88:91]\n" \
"buffer_load_dwordx4 v225, %[xa], s84 offen offset:1408 lds\n" \
"v_mfma_f32_16x16x32_bf16 v[92:95], v[196:199], a[28:31], v[92:95]\n" \
"s_waitcnt lgkmcnt(6)\n" \
"v_mfma_f32_16x16x32_bf16 v[176:179], v[200:203], a[16:19], v[176:179]\n" \
"ds_read_b64_tr_b16 v[196:197], v231 offset:16896\n" \
"v_mfma_f32_16x16x32_bf16 v[180:183], v[200:203], a[20:23], v[180:183]\n" \
"ds_read_b64_tr_b16 v[198:199], v231 offset:17152\n" \
"v_mfma_f32_16x16x32_bf16 v[184:187], v[200:203], a[24:27], v[184:187]\n" \
"buffer_load_dwordx4 v224, %[xa], s85 offen offset:2432 lds\n" \
"v_mfma_f32_16x16x32_bf16 v[188:191], v[200:203], a[28:31], v[188:191]\n" \
"v_mfma_f32_16x16x32_bf16 v[112:115], v[204:207], a[16:19], v[112:115]\n" \
"ds_read_b64_tr_b16 v[200:201], v228 offset:17408\n" \
"v_mfma_f32_16x16x32_bf16 v[116:119], v[204:207], a[20:23], v[116:119]\n" \
"ds_read_b64_tr_b16 v[202:203], v228 offset:17664\n" \
"v_mfma_f32_16x16x32_bf16 v[120:123], v[204:207], a[24:27], v[120:123]\n" \
"buffer_load_dwordx4 v225, %[xa], s86 offen offset:3456 lds\n" \
"v_mfma_f32_16x16x32_bf16 v[124:127], v[204:207], a[28:31], v[124:127]\n" \
"s_waitcnt lgkmcnt(6)\n" \
"v_mfma_f32_16x16x32_bf16 v[128:131], v[208:211], a[32:35], v[128:131]\n" \
"ds_read_b64_tr_b16 v[204:205], v231 offset:17408\n" \
"v_mfma_f32_16x16x32_bf16 v[132:135], v[208:211], a[36:39], v[132:135]\n" \
"s_add_u32 m0, s64, 36480\n" \
"ds_read_b64_tr_b16 v[206:207], v231 offset:17664\n" \
"v_mfma_f32_16x16x32_bf16 v[136:139], v[208:211], a[40:43], v[136:139]\n" \
"buffer_load_dwordx4 v224, %[xb], 0 offen offset:384 lds\n" \
"v_mfma_f32_16x16x32_bf16 v[140:143], v[208:211], a[44:47], v[140:143]\n" \
"v_mfma_f32_16x16x32_bf16 v[76:79], v[212:215], a[44:47], v[76:79]\n" \
"ds_read_b64_tr_b16 v[208:209], v228 offset:17920\n" \
"v_mfma_f32_16x16x32_bf16 v[72:75], v[212:215], a[40:43], v[72:75]\n" \
"ds_read_b64_tr_b16 v[210:211], v228 offset:18176\n" \
"v_mfma_f32_16x16x32_bf16 v[68:71], v[212:215], a[36:39], v[68:71]\n" \
"buffer_load_dwordx4 v225, %[xb], s84 offen offset:1408 lds\n" \
"v_mfma_f32_16x16x32_bf16 v[64:67], v[212:215], a[32:35], v[64:67]\n" \
"s_waitcnt lgkmcnt(6)\n" \
"v_mfma_f32_16x16x32_bf16 v[144:147], v[192:195], a[32:35], v[144:147]\n" \
"ds_read_b64_tr_b16 v[212:213], v231 offset:17920\n" \
"v_mfma_f32_16x16x32_bf16 v[148:151], v[192:195], a[36:39], v[148:151]\n" \
"ds_read_b64_tr_b16 v[214:215], v231 offset:18176\n" \
"v_mfma_f32_16x16x32_bf16 v[152:155], v[192:195], a[40:43], v[152:155]\n" \
"buffer_load_dwordx4 v224, %[xb], s85 offen offset:2432 lds\n" \
"v_mfma_f32_16x16x32_bf16 v[156:159], v[192:195], a[44:47], v[156:159]\n" \
"v_mfma_f32_16x16x32_bf16 v[108:111], v[196:199], a[44:47], v[108:111]\n" \
"ds_read_b64_tr_b16 v[192:193], v228 offset:24576\n" \
"v_mfma_f32_16x16x32_bf16 v[104:107], v[196:199], a[40:43], v[104:107]\n" \
"ds_read_b64_tr_b16 v[194:195], v228 offset:24832\n" \
"v_mfma_f32_16x16x32_bf16 v[100:103], v[196:199], a[36:39], v[100:103]\n" \
"buffer_load_dwordx4 v225, %[xb], s86 offen offset:3456 lds\n" \
"v_mfma_f32_16x16x32_bf16 v[96:99], v[196:199], a[32:35], v[96:99]\n" \
"s_waitcnt lgkmcnt(6)\n" \
"v_mfma_f32_16x16x32_bf16 v[160:163], v[200:203], a[32:35], v[160:163]\n" \
"s_add_u32 m0, %[w4k], 147456\n" \
"ds_read_b64_tr_b16 v[196:197], v231 offset:24576\n" \
"v_mfma_f32_16x16x32_bf16 v[164:167], v[200:203], a[36:39], v[164:167]\n" \
"s_mov_b32 s79, 1572864\n" \
"ds_read_b64_tr_b16 v[198:199], v231 offset:24832\n" \
"v_mfma_f32_16x16x32_bf16 v[168:171], v[200:203], a[40:43], v[168:171]\n" \
"buffer_load_dwordx4 v226, %[wq], s79 offen offset:0 lds\n" \
"v_mfma_f32_16x16x32_bf16 v[172:175], v[200:203], a[44:47], v[172:175]\n" \
"v_mfma_f32_16x16x32_bf16 v[92:95], v[204:207], a[44:47], v[92:95]\n" \
"ds_read_b64_tr_b16 v[200:201], v228 offset:25088\n" \
"v_mfma_f32_16x16x32_bf16 v[88:91], v[204:207], a[40:43], v[88:91]\n" \
"ds_read_b64_tr_b16 v[202:203], v228 offset:25344\n" \
"v_mfma_f32_16x16x32_bf16 v[84:87], v[204:207], a[36:39], v[84:87]\n" \
"buffer_load_dwordx4 v227, %[wq], s79 offen offset:1024 lds\n" \
"v_mfma_f32_16x16x32_bf16 v[80:83], v[204:207], a[32:35], v[80:83]\n" \
"s_waitcnt lgkmcnt(6)\n" \
"s_waitcnt vmcnt(14)\n" \
"v_mfma_f32_16x16x32_bf16 v[176:179], v[208:211], a[32:35], v[176:179]\n" \
"ds_read_b64_tr_b16 v[204:205], v231 offset:25088\n" \
"ds_read_b128 a[64:67], v238 offset:0\n" \
"v_mfma_f32_16x16x32_bf16 v[180:183], v[208:211], a[36:39], v[180:183]\n" \
"ds_read_b64_tr_b16 v[206:207], v231 offset:25344\n" \
"ds_read_b128 a[68:71], v238 offset:2048\n" \
"v_mfma_f32_16x16x32_bf16 v[184:187], v[208:211], a[40:43], v[184:187]\n" \
"buffer_load_dwordx4 v254, %[wq], s79 offen offset:2048 lds\n" \
"v_mfma_f32_16x16x32_bf16 v[188:191], v[208:211], a[44:47], v[188:191]\n" \
"v_mfma_f32_16x16x32_bf16 v[124:127], v[212:215], a[44:47], v[124:127]\n" \
"ds_read_b64_tr_b16 v[208:209], v228 offset:25600\n" \
"ds_read_b128 a[72:75], v238 offset:4096\n" \
"v_mfma_f32_16x16x32_bf16 v[120:123], v[212:215], a[40:43], v[120:123]\n" \
"ds_read_b64_tr_b16 v[210:211], v228 offset:25856\n" \
"ds_read_b128 a[76:79], v238 offset:6144\n" \
"v_mfma_f32_16x16x32_bf16 v[116:119], v[212:215], a[36:39], v[116:119]\n" \
"buffer_load_dwordx4 v236, %[wq], s79 offen offset:3072 lds\n" \
"v_mfma_f32_16x16x32_bf16 v[112:115], v[212:215], a[32:35], v[112:115]\n" \
"s_waitcnt lgkmcnt(10)\n" \
"v_mfma_f32_16x16x32_bf16 v[128:131], v[192:195], a[48:51], v[128:131]\n" \
"ds_read_b64_tr_b16 v[212:213], v231 offset:25600\n" \
"ds_read_b128 a[80:83], v239 offset:0\n" \
"v_mfma_f32_16x16x32_bf16 v[132:135], v[192:195], a[52:55], v[132:135]\n" \
"s_add_u32 m0, s64, 65024\n" \
"ds_read_b64_tr_b16 v[214:215], v231 offset:25856\n" \
"ds_read_b128 a[84:87], v239 offset:2048\n" \
"v_mfma_f32_16x16x32_bf16 v[136:139], v[192:195], a[56:59], v[136:139]\n" \
"buffer_load_dwordx4 v224, %[xa], 0 offen offset:512 lds\n" \
"v_mfma_f32_16x16x32_bf16 v[140:143], v[192:195], a[60:63], v[140:143]\n" \
"v_mfma_f32_16x16x32_bf16 v[76:79], v[196:199], a[60:63], v[76:79]\n" \
"ds_read_b64_tr_b16 v[192:193], v228 offset:26112\n" \
"ds_read_b128 a[88:91], v239 offset:4096\n" \
"v_mfma_f32_16x16x32_bf16 v[72:75], v[196:199], a[56:59], v[72:75]\n" \
"ds_read_b64_tr_b16 v[194:195], v228 offset:26368\n" \
"ds_read_b128 a[92:95], v239 offset:6144\n" \
"v_mfma_f32_16x16x32_bf16 v[68:71], v[196:199], a[52:55], v[68:71]\n" \
"buffer_load_dwordx4 v225, %[xa], s84 offen offset:1536 lds\n" \
"v_mfma_f32_16x16x32_bf16 v[64:67], v[196:199], a[48:51], v[64:67]\n" \
"s_waitcnt lgkmcnt(13)\n" \
"v_mfma_f32_16x16x32_bf16 v[144:147], v[200:203], a[48:51], v[144:147]\n" \
"ds_read_b64_tr_b16 v[196:197], v231 offset:26112\n" \
"v_mfma_f32_16x16x32_bf16 v[148:151], v[200:203], a[52:55], v[148:151]\n" \
"ds_read_b64_tr_b16 v[198:199], v231 offset:26368\n" \
"v_mfma_f32_16x16x32_bf16 v[152:155], v[200:203], a[56:59], v[152:155]\n" \
"buffer_load_dwordx4 v224, %[xa], s85 offen offset:2560 lds\n" \
"v_mfma_f32_16x16x32_bf16 v[156:159], v[200:203], a[60:63], v[156:159]\n" \
"s_waitcnt vmcnt(15)\n" \
"s_barrier\n" \
"v_mfma_f32_16x16x32_bf16 v[108:111], v[204:207], a[60:63], v[108:111]\n" \
"ds_read_b64_tr_b16 v[200:201], v230 offset:0\n" \
"v_mfma_f32_16x16x32_bf16 v[104:107], v[204:207], a[56:59], v[104:107]\n" \
"ds_read_b64_tr_b16 v[202:203], v230 offset:256\n" \
"v_mfma_f32_16x16x32_bf16 v[100:103], v[204:207], a[52:55], v[100:103]\n" \
"buffer_load_dwordx4 v225, %[xa], s86 offen offset:3584 lds\n" \
"v_mfma_f32_16x16x32_bf16 v[96:99], v[204:207], a[48:51], v[96:99]\n" \
"s_waitcnt lgkmcnt(4)\n" \
"v_mfma_f32_16x16x32_bf16 v[160:163], v[208:211], a[48:51], v[160:163]\n" \
"ds_read_b64_tr_b16 v[204:205], v233 offset:0\n" \
"v_mfma_f32_16x16x32_bf16 v[164:167], v[208:211], a[52:55], v[164:167]\n" \
"s_add_u32 m0, s64, 69120\n" \
"ds_read_b64_tr_b16 v[206:207], v233 offset:256\n" \
"v_mfma_f32_16x16x32_bf16 v[168:171], v[208:211], a[56:59], v[168:171]\n" \
"buffer_load_dwordx4 v224, %[xb], 0 offen offset:512 lds\n" \
"v_mfma_f32_16x16x32_bf16 v[172:175], v[208:211], a[60:63], v[172:175]\n" \
"v_mfma_f32_16x16x32_bf16 v[92:95], v[212:215], a[60:63], v[92:95]\n" \
"ds_read_b64_tr_b16 v[208:209], v230 offset:512\n" \
"v_mfma_f32_16x16x32_bf16 v[88:91], v[212:215], a[56:59], v[88:91]\n" \
"ds_read_b64_tr_b16 v[210:211], v230 offset:768\n" \
"v_mfma_f32_16x16x32_bf16 v[84:87], v[212:215], a[52:55], v[84:87]\n" \
"buffer_load_dwordx4 v225, %[xb], s84 offen offset:1536 lds\n" \
"v_mfma_f32_16x16x32_bf16 v[80:83], v[212:215], a[48:51], v[80:83]\n" \
"s_waitcnt lgkmcnt(6)\n" \
"v_mfma_f32_16x16x32_bf16 v[176:179], v[192:195], a[48:51], v[176:179]\n" \
"ds_read_b64_tr_b16 v[212:213], v233 offset:512\n" \
"v_mfma_f32_16x16x32_bf16 v[180:183], v[192:195], a[52:55], v[180:183]\n" \
"ds_read_b64_tr_b16 v[214:215], v233 offset:768\n" \
"v_mfma_f32_16x16x32_bf16 v[184:187], v[192:195], a[56:59], v[184:187]\n" \
"buffer_load_dwordx4 v224, %[xb], s85 offen offset:2560 lds\n" \
"v_mfma_f32_16x16x32_bf16 v[188:191], v[192:195], a[60:63], v[188:191]\n" \
"v_mfma_f32_16x16x32_bf16 v[124:127], v[196:199], a[60:63], v[124:127]\n" \
"ds_read_b64_tr_b16 v[192:193], v230 offset:1024\n" \
"v_mfma_f32_16x16x32_bf16 v[120:123], v[196:199], a[56:59], v[120:123]\n" \
"ds_read_b64_tr_b16 v[194:195], v230 offset:1280\n" \
"v_mfma_f32_16x16x32_bf16 v[116:119], v[196:199], a[52:55], v[116:119]\n" \
"buffer_load_dwordx4 v225, %[xb], s86 offen offset:3584 lds\n" \
"v_mfma_f32_16x16x32_bf16 v[112:115], v[196:199], a[48:51], v[112:115]\n" \
"s_waitcnt lgkmcnt(6)\n" \
"v_mfma_f32_16x16x32_bf16 v[128:131], v[200:203], a[64:67], v[128:131]\n" \
"s_add_u32 m0, %[w4k], 0\n" \
"ds_read_b64_tr_b16 v[196:197], v233 offset:1024\n" \
"v_mfma_f32_16x16x32_bf16 v[132:135], v[200:203], a[68:71], v[132:135]\n" \
"s_mov_b32 s79, 2097152\n" \
"ds_read_b64_tr_b16 v[198:199], v233 offset:1280\n" \
"v_mfma_f32_16x16x32_bf16 v[136:139], v[200:203], a[72:75], v[136:139]\n" \
"buffer_load_dwordx4 v226, %[wq], s79 offen offset:0 lds\n" \
"v_mfma_f32_16x16x32_bf16 v[140:143], v[200:203], a[76:79], v[140:143]\n" \
"v_mfma_f32_16x16x32_bf16 v[76:79], v[204:207], a[76:79], v[76:79]\n" \
"ds_read_b64_tr_b16 v[200:201], v230 offset:1536\n" \
"v_mfma_f32_16x16x32_bf16 v[72:75], v[204:207], a[72:75], v[72:75]\n" \
"ds_read_b64_tr_b16 v[202:203], v230 offset:1792\n" \
"v_mfma_f32_16x16x32_bf16 v[68:71], v[204:207], a[68:71], v[68:71]\n" \
"buffer_load_dwordx4 v227, %[wq], s79 offen offset:1024 lds\n" \
"v_mfma_f32_16x16x32_bf16 v[64:67], v[204:207], a[64:67], v[64:67]\n" \
"s_waitcnt lgkmcnt(6)\n" \
"v_mfma_f32_16x16x32_bf16 v[144:147], v[208:211], a[64:67], v[144:147]\n" \
"ds_read_b64_tr_b16 v[204:205], v233 offset:1536\n" \
"v_mfma_f32_16x16x32_bf16 v[148:151], v[208:211], a[68:71], v[148:151]\n" \
"ds_read_b64_tr_b16 v[206:207], v233 offset:1792\n" \
"v_mfma_f32_16x16x32_bf16 v[152:155], v[208:211], a[72:75], v[152:155]\n" \
"buffer_load_dwordx4 v254, %[wq], s79 offen offset:2048 lds\n" \
"v_mfma_f32_16x16x32_bf16 v[156:159], v[208:211], a[76:79], v[156:159]\n" \
"v_mfma_f32_16x16x32_bf16 v[108:111], v[212:215], a[76:79], v[108:111]\n" \
"ds_read_b64_tr_b16 v[208:209], v230 offset:8192\n" \
"v_mfma_f32_16x16x32_bf16 v[104:107], v[212:215], a[72:75], v[104:107]\n" \
"ds_read_b64_tr_b16 v[210:211], v230 offset:8448\n" \
"v_mfma_f32_16x16x32_bf16 v[100:103], v[212:215], a[68:71], v[100:103]\n" \
"buffer_load_dwordx4 v236, %[wq], s79 offen offset:3072 lds\n" \
"v_mfma_f32_16x16x32_bf16 v[96:99], v[212:215], a[64:67], v[96:99]\n" \
"s_waitcnt lgkmcnt(6)\n" \
"s_waitcnt vmcnt(16)\n" \
"v_mfma_f32_16x16x32_bf16 v[160:163], v[192:195], a[64:67], v[160:163]\n" \
"ds_read_b64_tr_b16 v[212:213], v233 offset:8192\n" \
"ds_read_b128 a[96:99], v234 offset:0\n" \
"v_mfma_f32_16x16x32_bf16 v[164:167], v[192:195], a[68:71], v[164:167]\n" \
"s_add_u32 m0, s64, 97664\n" \
"ds_read_b64_tr_b16 v[214:215], v233 offset:8448\n" \
"ds_read_b128 a[100:103], v234 offset:2048\n" \
"v_mfma_f32_16x16x32_bf16 v[168:171], v[192:195], a[72:75], v[168:171]\n" \
"buffer_load_dwordx4 v224, %[xa], 0 offen offset:640 lds\n" \
"v_mfma_f32_16x16x32_bf16 v[172:175], v[192:195], a[76:79], v[172:175]\n" \
"v_mfma_f32_16x16x32_bf16 v[92:95], v[196:199], a[76:79], v[92:95]\n" \
"ds_read_b64_tr_b16 v[192:193], v230 offset:8704\n" \
"ds_read_b128 a[104:107], v234 offset:4096\n" \
"v_mfma_f32_16x16x32_bf16 v[88:91], v[196:199], a[72:75], v[88:91]\n" \
"ds_read_b64_tr_b16 v[194:195], v230 offset:8960\n" \
"ds_read_b128 a[108:111], v234 offset:6144\n" \
"v_mfma_f32_16x16x32_bf16 v[84:87], v[196:199], a[68:71], v[84:87]\n" \
"buffer_load_dwordx4 v225, %[xa], s84 offen offset:1664 lds\n" \
"v_mfma_f32_16x16x32_bf16 v[80:83], v[196:199], a[64:67], v[80:83]\n" \
"s_waitcnt lgkmcnt(10)\n" \
"v_mfma_f32_16x16x32_bf16 v[176:179], v[200:203], a[64:67], v[176:179]\n" \
"ds_read_b64_tr_b16 v[196:197], v233 offset:8704\n" \
"ds_read_b128 a[112:115], v235 offset:0\n" \
"v_mfma_f32_16x16x32_bf16 v[180:183], v[200:203], a[68:71], v[180:183]\n" \
"ds_read_b64_tr_b16 v[198:199], v233 offset:8960\n" \
"ds_read_b128 a[116:119], v235 offset:2048\n" \
"v_mfma_f32_16x16x32_bf16 v[184:187], v[200:203], a[72:75], v[184:187]\n" \
"buffer_load_dwordx4 v224, %[xa], s85 offen offset:2688 lds\n" \
"v_mfma_f32_16x16x32_bf16 v[188:191], v[200:203], a[76:79], v[188:191]\n" \
"v_mfma_f32_16x16x32_bf16 v[124:127], v[204:207], a[76:79], v[124:127]\n" \
"ds_read_b64_tr_b16 v[200:201], v230 offset:9216\n" \
"ds_read_b128 a[120:123], v235 offset:4096\n" \
"v_mfma_f32_16x16x32_bf16 v[120:123], v[204:207], a[72:75], v[120:123]\n" \
"ds_read_b64_tr_b16 v[202:203], v230 offset:9472\n" \
"ds_read_b128 a[124:127], v235 offset:6144\n" \
"v_mfma_f32_16x16x32_bf16 v[116:119], v[204:207], a[68:71], v[116:119]\n" \
"buffer_load_dwordx4 v225, %[xa], s86 offen offset:3712 lds\n" \
"v_mfma_f32_16x16x32_bf16 v[112:115], v[204:207], a[64:67], v[112:115]\n" \
"s_waitcnt lgkmcnt(13)\n" \
"v_mfma_f32_16x16x32_bf16 v[128:131], v[208:211], a[80:83], v[128:131]\n" \
"ds_read_b64_tr_b16 v[204:205], v233 offset:9216\n" \
"v_mfma_f32_16x16x32_bf16 v[132:135], v[208:211], a[84:87], v[132:135]\n" \
"s_add_u32 m0, s64, 101760\n" \
"ds_read_b64_tr_b16 v[206:207], v233 offset:9472\n" \
"v_mfma_f32_16x16x32_bf16 v[136:139], v[208:211], a[88:91], v[136:139]\n" \
"buffer_load_dwordx4 v224, %[xb], 0 offen offset:640 lds\n" \
"v_mfma_f32_16x16x32_bf16 v[140:143], v[208:211], a[92:95], v[140:143]\n" \
"v_mfma_f32_16x16x32_bf16 v[76:79], v[212:215], a[92:95], v[76:79]\n" \
"ds_read_b64_tr_b16 v[208:209], v230 offset:9728\n" \
"v_mfma_f32_16x16x32_bf16 v[72:75], v[212:215], a[88:91], v[72:75]\n" \
"ds_read_b64_tr_b16 v[210:211], v230 offset:9984\n" \
"v_mfma_f32_16x16x32_bf16 v[68:71], v[212:215], a[84:87], v[68:71]\n" \
"buffer_load_dwordx4 v225, %[xb], s84 offen offset:1664 lds\n" \
"v_mfma_f32_16x16x32_bf16 v[64:67], v[212:215], a[80:83], v[64:67]\n" \
"s_waitcnt lgkmcnt(4)\n" \
"v_mfma_f32_16x16x32_bf16 v[144:147], v[192:195], a[80:83], v[144:147]\n" \
"ds_read_b64_tr_b16 v[212:213], v233 offset:9728\n" \
"v_mfma_f32_16x16x32_bf16 v[148:151], v[192:195], a[84:87], v[148:151]\n" \
"ds_read_b64_tr_b16 v[214:215], v233 offset:9984\n" \
"v_mfma_f32_16x16x32_bf16 v[152:155], v[192:195], a[88:91], v[152:155]\n" \
"buffer_load_dwordx4 v224, %[xb], s85 offen offset:2688 lds\n" \
"v_mfma_f32_16x16x32_bf16 v[156:159], v[192:195], a[92:95], v[156:159]\n" \
"s_waitcnt vmcnt(19)\n" \
"s_barrier\n" \
"v_mfma_f32_16x16x32_bf16 v[108:111], v[196:199], a[92:95], v[108:111]\n" \
"ds_read_b64_tr_b16 v[192:193], v230 offset:16384\n" \
"v_mfma_f32_16x16x32_bf16 v[104:107], v[196:199], a[88:91], v[104:107]\n" \
"ds_read_b64_tr_b16 v[194:195], v230 offset:16640\n" \
"v_mfma_f32_16x16x32_bf16 v[100:103], v[196:199], a[84:87], v[100:103]\n" \
"buffer_load_dwordx4 v225, %[xb], s86 offen offset:3712 lds\n" \
"v_mfma_f32_16x16x32_bf16 v[96:99], v[196:199], a[80:83], v[96:99]\n" \
"s_waitcnt lgkmcnt(6)\n" \
"v_mfma_f32_16x16x32_bf16 v[160:163], v[200:203], a[80:83], v[160:163]\n" \
"ds_read_b64_tr_b16 v[196:197], v233 offset:16384\n" \
"v_mfma_f32_16x16x32_bf16 v[164:167], v[200:203], a[84:87], v[164:167]\n" \
"ds_read_b64_tr_b16 v[198:199], v233 offset:16640\n" \
"v_mfma_f32_16x16x32_bf16 v[168:171], v[200:203], a[88:91], v[168:171]\n" \
"buffer_store_dwordx4 v[56:59], v241, %[wsr], s82 offen sc1\n" \
"v_mfma_f32_16x16x32_bf16 v[172:175], v[200:203], a[92:95], v[172:175]\n" \
"v_mfma_f32_16x16x32_bf16 v[92:95], v[204:207], a[92:95], v[92:95]\n" \
"ds_read_b64_tr_b16 v[200:201], v230 offset:16896\n" \
"v_mfma_f32_16x16x32_bf16 v[88:91], v[204:207], a[88:91], v[88:91]\n" \
"ds_read_b64_tr_b16 v[202:203], v230 offset:17152\n" \
"v_mfma_f32_16x16x32_bf16 v[84:87], v[204:207], a[84:87], v[84:87]\n" \
"buffer_atomic_add v252, v60, %[wsr], 0 offen offset:0 sc1\n" \
"v_mfma_f32_16x16x32_bf16 v[80:83], v[204:207], a[80:83], v[80:83]\n" \
"s_waitcnt lgkmcnt(6)\n" \
"v_mfma_f32_16x16x32_bf16 v[176:179], v[208:211], a[80:83], v[176:179]\n" \
"ds_read_b64_tr_b16 v[204:205], v233 offset:16896\n" \
"v_mfma_f32_16x16x32_bf16 v[180:183], v[208:211], a[84:87], v[180:183]\n" \
"ds_read_b64_tr_b16 v[206:207], v233 offset:17152\n" \
"v_mfma_f32_16x16x32_bf16 v[184:187], v[208:211], a[88:91], v[184:187]\n" \
"buffer_load_dword v217, v216, %[wk], s62 offen nt\n" \
"v_mfma_f32_16x16x32_bf16 v[188:191], v[208:211], a[92:95], v[188:191]\n" \
"v_mfma_f32_16x16x32_bf16 v[124:127], v[212:215], a[92:95], v[124:127]\n" \
"ds_read_b64_tr_b16 v[208:209], v230 offset:17408\n" \
"v_mfma_f32_16x16x32_bf16 v[120:123], v[212:215], a[88:91], v[120:123]\n" \
"ds_read_b64_tr_b16 v[210:211], v230 offset:17664\n" \
"v_mfma_f32_16x16x32_bf16 v[116:119], v[212:215], a[84:87], v[116:119]\n" \
"buffer_load_dword v217, v216, %[wk], s62 offen\n" \
"v_mfma_f32_16x16x32_bf16 v[112:115], v[212:215], a[80:83], v[112:115]\n" \
"s_waitcnt lgkmcnt(6)\n" \
"s_waitcnt vmcnt(16)\n" \
"v_mfma_f32_16x16x32_bf16 v[128:131], v[192:195], a[96:99], v[128:131]\n" \
"ds_read_b64_tr_b16 v[212:213], v233 offset:17408\n" \
"ds_read_b128 a[128:131], v234 offset:32768\n" \
"v_mfma_f32_16x16x32_bf16 v[132:135], v[192:195], a[100:103], v[132:135]\n" \
"ds_read_b64_tr_b16 v[214:215], v233 offset:17664\n" \
"ds_read_b128 a[132:135], v234 offset:34816\n" \
"v_mfma_f32_16x16x32_bf16 v[136:139], v[192:195], a[104:107], v[136:139]\n" \
"v_mfma_f32_16x16x32_bf16 v[140:143], v[192:195], a[108:111], v[140:143]\n" \
"v_mfma_f32_16x16x32_bf16 v[76:79], v[196:199], a[108:111], v[76:79]\n" \
"ds_read_b64_tr_b16 v[192:193], v230 offset:17920\n" \
"ds_read_b128 a[136:139], v234 offset:36864\n" \
"v_mfma_f32_16x16x32_bf16 v[72:75], v[196:199], a[104:107], v[72:75]\n" \
"ds_read_b64_tr_b16 v[194:195], v230 offset:18176\n" \
"ds_read_b128 a[140:143], v234 offset:38912\n" \
"v_mfma_f32_16x16x32_bf16 v[68:71], v[196:199], a[100:103], v[68:71]\n" \
"v_mfma_f32_16x16x32_bf16 v[64:67], v[196:199], a[96:99], v[64:67]\n" \
"s_waitcnt lgkmcnt(10)\n" \
"v_mfma_f32_16x16x32_bf16 v[144:147], v[200:203], a[96:99], v[144:147]\n" \
"ds_read_b64_tr_b16 v[196:197], v233 offset:17920\n" \
"ds_read_b128 a[144:147], v235 offset:32768\n" \
"v_mfma_f32_16x16x32_bf16 v[148:151], v[200:203], a[100:103], v[148:151]\n" \
"ds_read_b64_tr_b16 v[198:199], v233 offset:18176\n" \
"ds_read_b128 a[148:151], v235 offset:34816\n" \
"v_mfma_f32_16x16x32_bf16 v[152:155], v[200:203], a[104:107], v[152:155]\n" \
"v_mfma_f32_16x16x32_bf16 v[156:159], v[200:203], a[108:111], v[156:159]\n" \
"v_mfma_f32_16x16x32_bf16 v[108:111], v[204:207], a[108:111], v[108:111]\n" \
"ds_read_b64_tr_b16 v[200:201], v230 offset:24576\n" \
"ds_read_b128 a[152:155], v235 offset:36864\n" \
"v_mfma_f32_16x16x32_bf16 v[104:107], v[204:207], a[104:107], v[104:107]\n" \
"ds_read_b64_tr_b16 v[202:203], v230 offset:24832\n" \
"ds_read_b128 a[156:159], v235 offset:38912\n" \
"v_mfma_f32_16x16x32_bf16 v[100:103], v[204:207], a[100:103], v[100:103]\n" \
"v_mfma_f32_16x16x32_bf16 v[96:99], v[204:207], a[96:99], v[96:99]\n" \
"s_waitcnt lgkmcnt(13)\n" \
"v_mfma_f32_16x16x32_bf16 v[160:163], v[208:211], a[96:99], v[160:163]\n" \
"ds_read_b64_tr_b16 v[204:205], v233 offset:24576\n" \
"v_mfma_f32_16x16x32_bf16 v[164:167], v[208:211], a[100:103], v[164:167]\n" \
"ds_read_b64_tr_b16 v[206:207], v233 offset:24832\n" \
"v_mfma_f32_16x16x32_bf16 v[168:171], v[208:211], a[104:107], v[168:171]\n" \
"v_mfma_f32_16x16x32_bf16 v[172:175], v[208:211], a[108:111], v[172:175]\n" \
"v_mfma_f32_16x16x32_bf16 v[92:95], v[212:215], a[108:111], v[92:95]\n" \
"ds_read_b64_tr_b16 v[208:209], v230 offset:25088\n" \
"v_mfma_f32_16x16x32_bf16 v[88:91], v[212:215], a[104:107], v[88:91]\n" \
"ds_read_b64_tr_b16 v[210:211], v230 offset:25344\n" \
"v_mfma_f32_16x16x32_bf16 v[84:87], v[212:215], a[100:103], v[84:87]\n" \
"v_mfma_f32_16x16x32_bf16 v[80:83], v[212:215], a[96:99], v[80:83]\n" \
"s_waitcnt lgkmcnt(4)\n" \
"v_mfma_f32_16x16x32_bf16 v[176:179], v[192:195], a[96:99], v[176:179]\n" \
"ds_read_b64_tr_b16 v[212:213], v233 offset:25088\n" \
"v_mfma_f32_16x16x32_bf16 v[180:183], v[192:195], a[100:103], v[180:183]\n" \
"ds_read_b64_tr_b16 v[214:215], v233 offset:25344\n" \
"v_mfma_f32_16x16x32_bf16 v[184:187], v[192:195], a[104:107], v[184:187]\n" \
"v_mfma_f32_16x16x32_bf16 v[188:191], v[192:195], a[108:111], v[188:191]\n" \
"v_mfma_f32_16x16x32_bf16 v[124:127], v[196:199], a[108:111], v[124:127]\n" \
"ds_read_b64_tr_b16 v[192:193], v230 offset:25600\n" \
"v_mfma_f32_16x16x32_bf16 v[120:123], v[196:199], a[104:107], v[120:123]\n" \
"ds_read_b64_tr_b16 v[194:195], v230 offset:25856\n" \
"v_mfma_f32_16x16x32_bf16 v[116:119], v[196:199], a[100:103], v[116:119]\n" \
"v_mfma_f32_16x16x32_bf16 v[112:115], v[196:199], a[96:99], v[112:115]\n" \
"s_waitcnt lgkmcnt(6)\n" \
"v_mfma_f32_16x16x32_bf16 v[128:131], v[200:203], a[112:115], v[128:131]\n" \
"ds_read_b64_tr_b16 v[196:197], v233 offset:25600\n" \
"v_mfma_f32_16x16x32_bf16 v[132:135], v[200:203], a[116:119], v[132:135]\n" \
"ds_read_b64_tr_b16 v[198:199], v233 offset:25856\n" \
"v_mfma_f32_16x16x32_bf16 v[136:139], v[200:203], a[120:123], v[136:139]\n" \
"v_mfma_f32_16x16x32_bf16 v[140:143], v[200:203], a[124:127], v[140:143]\n" \
"v_mfma_f32_16x16x32_bf16 v[76:79], v[204:207], a[124:127], v[76:79]\n" \
"s_add_u32 m0, %[w4k], 16384\n" \
"ds_read_b64_tr_b16 v[200:201], v230 offset:26112\n" \
"v_mfma_f32_16x16x32_bf16 v[72:75], v[204:207], a[120:123], v[72:75]\n" \
"s_mov_b32 s79, 2621440\n" \
"ds_read_b64_tr_b16 v[202:203], v230 offset:26368\n" \
"v_mfma_f32_16x16x32_bf16 v[68:71], v[204:207], a[116:119], v[68:71]\n" \
"buffer_load_dwordx4 v226, %[wq], s79 offen offset:0 lds\n" \
"v_mfma_f32_16x16x32_bf16 v[64:67], v[204:207], a[112:115], v[64:67]\n" \
"s_waitcnt lgkmcnt(6)\n" \
"v_mfma_f32_16x16x32_bf16 v[144:147], v[208:211], a[112:115], v[144:147]\n" \
"ds_read_b64_tr_b16 v[204:205], v233 offset:26112\n" \
"v_mfma_f32_16x16x32_bf16 v[148:151], v[208:211], a[116:119], v[148:151]\n" \
"ds_read_b64_tr_b16 v[206:207], v233 offset:26368\n" \
"v_mfma_f32_16x16x32_bf16 v[152:155], v[208:211], a[120:123], v[152:155]\n" \
"buffer_load_dwordx4 v227, %[wq], s79 offen offset:1024 lds\n" \
"v_mfma_f32_16x16x32_bf16 v[156:159], v[208:211], a[124:127], v[156:159]\n" \
"s_waitcnt vmcnt(14)\n" \
"s_barrier\n" \
"v_mfma_f32_16x16x32_bf16 v[108:111], v[212:215], a[124:127], v[108:111]\n" \
"ds_read_b64_tr_b16 v[208:209], v228 offset:0\n" \
"v_mfma_f32_16x16x32_bf16 v[104:107], v[212:215], a[120:123], v[104:107]\n" \
"ds_read_b64_tr_b16 v[210:211], v228 offset:256\n" \
"v_mfma_f32_16x16x32_bf16 v[100:103], v[212:215], a[116:119], v[100:103]\n" \
"buffer_load_dwordx4 v254, %[wq], s79 offen offset:2048 lds\n" \
"v_mfma_f32_16x16x32_bf16 v[96:99], v[212:215], a[112:115], v[96:99]\n" \
"s_waitcnt lgkmcnt(6)\n" \
"s_waitcnt vmcnt(7)\n" \
"v_mfma_f32_16x16x32_bf16 v[160:163], v[192:195], a[112:115], v[160:163]\n" \
"ds_read_b64_tr_b16 v[212:213], v231 offset:0\n" \
"ds_read_b128 a[160:163], v238 offset:0\n" \
"v_mfma_f32_16x16x32_bf16 v[164:167], v[192:195], a[116:119], v[164:167]\n" \
"ds_read_b64_tr_b16 v[214:215], v231 offset:256\n" \
"ds_read_b128 a[164:167], v238 offset:2048\n" \
"v_mfma_f32_16x16x32_bf16 v[168:171], v[192:195], a[120:123], v[168:171]\n" \
"buffer_load_dwordx4 v236, %[wq], s79 offen offset:3072 lds\n" \
"v_mfma_f32_16x16x32_bf16 v[172:175], v[192:195], a[124:127], v[172:175]\n" \
"v_mfma_f32_16x16x32_bf16 v[92:95], v[196:199], a[124:127], v[92:95]\n" \
"ds_read_b64_tr_b16 v[192:193], v228 offset:512\n" \
"ds_read_b128 a[168:171], v238 offset:4096\n" \
"v_mfma_f32_16x16x32_bf16 v[88:91], v[196:199], a[120:123], v[88:91]\n" \
"s_add_u32 m0, s64, 32000\n" \
"ds_read_b64_tr_b16 v[194:195], v228 offset:768\n" \
"ds_read_b128 a[172:175], v238 offset:6144\n" \
"v_mfma_f32_16x16x32_bf16 v[84:87], v[196:199], a[116:119], v[84:87]\n" \
"buffer_load_dwordx4 v224, %[xa], 0 offen offset:768 lds\n" \
"v_mfma_f32_16x16x32_bf16 v[80:83], v[196:199], a[112:115], v[80:83]\n" \
"s_waitcnt lgkmcnt(10)\n" \
"v_mfma_f32_16x16x32_bf16 v[176:179], v[200:203], a[112:115], v[176:179]\n" \
"ds_read_b64_tr_b16 v[196:197], v231 offset:512\n" \
"ds_read_b128 a[176:179], v239 offset:0\n" \
"v_mfma_f32_16x16x32_bf16 v[180:183], v[200:203], a[116:119], v[180:183]\n" \
"ds_read_b64_tr_b16 v[198:199], v231 offset:768\n" \
"ds_read_b128 a[180:183], v239 offset:2048\n" \
"v_mfma_f32_16x16x32_bf16 v[184:187], v[200:203], a[120:123], v[184:187]\n" \
"buffer_load_dwordx4 v225, %[xa], s84 offen offset:1792 lds\n" \
"v_mfma_f32_16x16x32_bf16 v[188:191], v[200:203], a[124:127], v[188:191]\n" \
"s_barrier\n" \
"v_mfma_f32_16x16x32_bf16 v[124:127], v[204:207], a[124:127], v[124:127]\n" \
"ds_read_b64_tr_b16 v[200:201], v228 offset:1024\n" \
"ds_read_b128 a[184:187], v239 offset:4096\n" \
"v_mfma_f32_16x16x32_bf16 v[120:123], v[204:207], a[120:123], v[120:123]\n" \
"ds_read_b64_tr_b16 v[202:203], v228 offset:1280\n" \
"ds_read_b128 a[188:191], v239 offset:6144\n" \
"v_mfma_f32_16x16x32_bf16 v[116:119], v[204:207], a[116:119], v[116:119]\n" \
"buffer_load_dwordx4 v224, %[xa], s85 offen offset:2816 lds\n" \
"v_mfma_f32_16x16x32_bf16 v[112:115], v[204:207], a[112:115], v[112:115]\n" \
"s_waitcnt lgkmcnt(13)\n" \
"v_mfma_f32_16x16x32_bf16 v[128:131], v[208:211], a[128:131], v[128:131]\n" \
"ds_read_b64_tr_b16 v[204:205], v231 offset:1024\n" \
"v_mfma_f32_16x16x32_bf16 v[132:135], v[208:211], a[132:135], v[132:135]\n" \
"ds_read_b64_tr_b16 v[206:207], v231 offset:1280\n" \
"v_mfma_f32_16x16x32_bf16 v[136:139], v[208:211], a[136:139], v[136:139]\n" \
"buffer_load_dwordx4 v225, %[xa], s86 offen offset:3840 lds\n" \
"v_mfma_f32_16x16x32_bf16 v[140:143], v[208:211], a[140:143], v[140:143]\n" \
"v_mfma_f32_16x16x32_bf16 v[76:79], v[212:215], a[140:143], v[76:79]\n" \
"ds_read_b64_tr_b16 v[208:209], v228 offset:1536\n" \
"v_mfma_f32_16x16x32_bf16 v[72:75], v[212:215], a[136:139], v[72:75]\n" \
"s_add_u32 m0, s64, 36096\n" \
"ds_read_b64_tr_b16 v[210:211], v228 offset:1792\n" \
"v_mfma_f32_16x16x32_bf16 v[68:71], v[212:215], a[132:135], v[68:71]\n" \
"buffer_load_dwordx4 v224, %[xb], 0 offen offset:768 lds\n" \
"v_mfma_f32_16x16x32_bf16 v[64:67], v[212:215], a[128:131], v[64:67]\n" \
"s_waitcnt lgkmcnt(4)\n" \
"v_mfma_f32_16x16x32_bf16 v[144:147], v[192:195], a[128:131], v[144:147]\n" \
"ds_read_b64_tr_b16 v[212:213], v231 offset:1536\n" \
"v_mfma_f32_16x16x32_bf16 v[148:151], v[192:195], a[132:135], v[148:151]\n" \
"ds_read_b64_tr_b16 v[214:215], v231 offset:1792\n" \
"v_mfma_f32_16x16x32_bf16 v[152:155], v[192:195], a[136:139], v[152:155]\n" \
"buffer_load_dwordx4 v225, %[xb], s84 offen offset:1792 lds\n" \
"v_mfma_f32_16x16x32_bf16 v[156:159], v[192:195], a[140:143], v[156:159]\n" \
"v_mfma_f32_16x16x32_bf16 v[108:111], v[196:199], a[140:143], v[108:111]\n" \
"ds_read_b64_tr_b16 v[192:193], v228 offset:8192\n" \
"v_mfma_f32_16x16x32_bf16 v[104:107], v[196:199], a[136:139], v[104:107]\n" \
"ds_read_b64_tr_b16 v[194:195], v228 offset:8448\n" \
"v_mfma_f32_16x16x32_bf16 v[100:103], v[196:199], a[132:135], v[100:103]\n" \
"buffer_load_dwordx4 v224, %[xb], s85 offen offset:2816 lds\n" \
"v_mfma_f32_16x16x32_bf16 v[96:99], v[196:199], a[128:131], v[96:99]\n" \
"s_waitcnt lgkmcnt(6)\n" \
"v_mfma_f32_16x16x32_bf16 v[160:163], v[200:203], a[128:131], v[160:163]\n" \
"ds_read_b64_tr_b16 v[196:197], v231 offset:8192\n" \
"v_mfma_f32_16x16x32_bf16 v[164:167], v[200:203], a[132:135], v[164:167]\n" \
"ds_read_b64_tr_b16 v[198:199], v231 offset:8448\n" \
"v_mfma_f32_16x16x32_bf16 v[168:171], v[200:203], a[136:139], v[168:171]\n" \
"buffer_load_dwordx4 v225, %[xb], s86 offen offset:3840 lds\n" \
"v_mfma_f32_16x16x32_bf16 v[172:175], v[200:203], a[140:143], v[172:175]\n" \
"v_mfma_f32_16x16x32_bf16 v[92:95], v[204:207], a[140:143], v[92:95]\n" \
"s_add_u32 m0, %[w4k], 131072\n" \
"ds_read_b64_tr_b16 v[200:201], v228 offset:8704\n" \
"v_mfma_f32_16x16x32_bf16 v[88:91], v[204:207], a[136:139], v[88:91]\n" \
"s_mov_b32 s79, 3145728\n" \
"ds_read_b64_tr_b16 v[202:203], v228 offset:8960\n" \
"v_mfma_f32_16x16x32_bf16 v[84:87], v[204:207], a[132:135], v[84:87]\n" \
"buffer_load_dwordx4 v226, %[wq], s79 offen offset:0 lds\n" \
"v_mfma_f32_16x16x32_bf16 v[80:83], v[204:207], a[128:131], v[80:83]\n" \
"s_waitcnt lgkmcnt(6)\n" \
"v_mfma_f32_16x16x32_bf16 v[176:179], v[208:211], a[128:131], v[176:179]\n" \
"ds_read_b64_tr_b16 v[204:205], v231 offset:8704\n" \
"v_mfma_f32_16x16x32_bf16 v[180:183], v[208:211], a[132:135], v[180:183]\n" \
"ds_read_b64_tr_b16 v[206:207], v231 offset:8960\n" \
"v_mfma_f32_16x16x32_bf16 v[184:187], v[208:211], a[136:139], v[184:187]\n" \
"buffer_load_dwordx4 v227, %[wq], s79 offen offset:1024 lds\n" \
"v_mfma_f32_16x16x32_bf16 v[188:191], v[208:211], a[140:143], v[188:191]\n" \
"v_mfma_f32_16x16x32_bf16 v[124:127], v[212:215], a[140:143], v[124:127]\n" \
"ds_read_b64_tr_b16 v[208:209], v228 offset:9216\n" \
"v_mfma_f32_16x16x32_bf16 v[120:123], v[212:215], a[136:139], v[120:123]\n" \
"ds_read_b64_tr_b16 v[210:211], v228 offset:9472\n" \
"v_mfma_f32_16x16x32_bf16 v[116:119], v[212:215], a[132:135], v[116:119]\n" \
"buffer_load_dwordx4 v254, %[wq], s79 offen offset:2048 lds\n" \
"v_mfma_f32_16x16x32_bf16 v[112:115], v[212:215], a[128:131], v[112:115]\n" \
"s_waitcnt lgkmcnt(6)\n" \
"v_mfma_f32_16x16x32_bf16 v[128:131], v[192:195], a[144:147], v[128:131]\n" \
"ds_read_b64_tr_b16 v[212:213], v231 offset:9216\n" \
"v_mfma_f32_16x16x32_bf16 v[132:135], v[192:195], a[148:151], v[132:135]\n" \
"ds_read_b64_tr_b16 v[214:215], v231 offset:9472\n" \
"v_mfma_f32_16x16x32_bf16 v[136:139], v[192:195], a[152:155], v[136:139]\n" \
"buffer_load_dwordx4 v236, %[wq], s79 offen offset:3072 lds\n" \
"v_mfma_f32_16x16x32_bf16 v[140:143], v[192:195], a[156:159], v[140:143]\n" \
"v_mfma_f32_16x16x32_bf16 v[76:79], v[196:199], a[156:159], v[76:79]\n" \
"ds_read_b64_tr_b16 v[192:193], v228 offset:9728\n" \
"v_mfma_f32_16x16x32_bf16 v[72:75], v[196:199], a[152:155], v[72:75]\n" \
"s_add_u32 m0, s64, 64640\n" \
"ds_read_b64_tr_b16 v[194:195], v228 offset:9984\n" \
"v_mfma_f32_16x16x32_bf16 v[68:71], v[196:199], a[148:151], v[68:71]\n" \
"buffer_load_dwordx4 v224, %[xa], 0 offen offset:896 lds\n" \
"v_mfma_f32_16x16x32_bf16 v[64:67], v[196:199], a[144:147], v[64:67]\n" \
"s_waitcnt lgkmcnt(6)\n" \
"v_mfma_f32_16x16x32_bf16 v[144:147], v[200:203], a[144:147], v[144:147]\n" \
"ds_read_b64_tr_b16 v[196:197], v231 offset:9728\n" \
"v_mfma_f32_16x16x32_bf16 v[148:151], v[200:203], a[148:151], v[148:151]\n" \
"ds_read_b64_tr_b16 v[198:199], v231 offset:9984\n" \
"v_mfma_f32_16x16x32_bf16 v[152:155], v[200:203], a[152:155], v[152:155]\n" \
"buffer_load_dwordx4 v225, %[xa], s84 offen offset:1920 lds\n" \
"v_mfma_f32_16x16x32_bf16 v[156:159], v[200:203], a[156:159], v[156:159]\n" \
"s_waitcnt vmcnt(14)\n" \
"s_barrier\n" \
"v_mfma_f32_16x16x32_bf16 v[108:111], v[204:207], a[156:159], v[108:111]\n" \
"ds_read_b64_tr_b16 v[200:201], v228 offset:16384\n" \
"v_mfma_f32_16x16x32_bf16 v[104:107], v[204:207], a[152:155], v[104:107]\n" \
"ds_read_b64_tr_b16 v[202:203], v228 offset:16640\n" \
"v_mfma_f32_16x16x32_bf16 v[100:103], v[204:207], a[148:151], v[100:103]\n" \
"buffer_load_dwordx4 v224, %[xa], s85 offen offset:2944 lds\n" \
"v_mfma_f32_16x16x32_bf16 v[96:99], v[204:207], a[144:147], v[96:99]\n" \
"s_waitcnt lgkmcnt(6)\n" \
"v_mfma_f32_16x16x32_bf16 v[160:163], v[208:211], a[144:147], v[160:163]\n" \
"ds_read_b64_tr_b16 v[204:205], v231 offset:16384\n" \
"v_mfma_f32_16x16x32_bf16 v[164:167], v[208:211], a[148:151], v[164:167]\n" \
"ds_read_b64_tr_b16 v[206:207], v231 offset:16640\n" \
"v_mfma_f32_16x16x32_bf16 v[168:171], v[208:211], a[152:155], v[168:171]\n" \
"buffer_load_dwordx4 v225, %[xa], s86 offen offset:3968 lds\n" \
"v_mfma_f32_16x16x32_bf16 v[172:175], v[208:211], a[156:159], v[172:175]\n" \
"v_mfma_f32_16x16x32_bf16 v[92:95], v[212:215], a[156:159], v[92:95]\n" \
"ds_read_b64_tr_b16 v[208:209], v228 offset:16896\n" \
"v_mfma_f32_16x16x32_bf16 v[88:91], v[212:215], a[152:155], v[88:91]\n" \
"s_add_u32 m0, s64, 68736\n" \
"ds_read_b64_tr_b16 v[210:211], v228 offset:17152\n" \
"v_mfma_f32_16x16x32_bf16 v[84:87], v[212:215], a[148:151], v[84:87]\n" \
"buffer_load_dwordx4 v224, %[xb], 0 offen offset:896 lds\n" \
"v_mfma_f32_16x16x32_bf16 v[80:83], v[212:215], a[144:147], v[80:83]\n" \
"s_waitcnt lgkmcnt(6)\n" \
"v_mfma_f32_16x16x32_bf16 v[176:179], v[192:195], a[144:147], v[176:179]\n" \
"ds_read_b64_tr_b16 v[212:213], v231 offset:16896\n" \
"v_mfma_f32_16x16x32_bf16 v[180:183], v[192:195], a[148:151], v[180:183]\n" \
"ds_read_b64_tr_b16 v[214:215], v231 offset:17152\n" \
"v_mfma_f32_16x16x32_bf16 v[184:187], v[192:195], a[152:155], v[184:187]\n" \
"buffer_load_dwordx4 v225, %[xb], s84 offen offset:1920 lds\n" \
"v_mfma_f32_16x16x32_bf16 v[188:191], v[192:195], a[156:159], v[188:191]\n" \
"s_barrier\n" \
"v_mfma_f32_16x16x32_bf16 v[124:127], v[196:199], a[156:159], v[124:127]\n" \
"ds_read_b64_tr_b16 v[192:193], v228 offset:17408\n" \
"v_mfma_f32_16x16x32_bf16 v[120:123], v[196:199], a[152:155], v[120:123]\n" \
"ds_read_b64_tr_b16 v[194:195], v228 offset:17664\n" \
"v_mfma_f32_16x16x32_bf16 v[116:119], v[196:199], a[148:151], v[116:119]\n" \
"buffer_load_dwordx4 v224, %[xb], s85 offen offset:2944 lds\n" \
"v_mfma_f32_16x16x32_bf16 v[112:115], v[196:199], a[144:147], v[112:115]\n" \
"s_waitcnt lgkmcnt(6)\n" \
"v_mfma_f32_16x16x32_bf16 v[128:131], v[200:203], a[160:163], v[128:131]\n" \
"ds_read_b64_tr_b16 v[196:197], v231 offset:17408\n" \
"v_mfma_f32_16x16x32_bf16 v[132:135], v[200:203], a[164:167], v[132:135]\n" \
"ds_read_b64_tr_b16 v[198:199], v231 offset:17664\n" \
"v_mfma_f32_16x16x32_bf16 v[136:139], v[200:203], a[168:171], v[136:139]\n" \
"buffer_load_dwordx4 v225, %[xb], s86 offen offset:3968 lds\n" \
"v_mfma_f32_16x16x32_bf16 v[140:143], v[200:203], a[172:175], v[140:143]\n" \
"v_mfma_f32_16x16x32_bf16 v[76:79], v[204:207], a[172:175], v[76:79]\n" \
"s_add_u32 m0, %[w4k], 147456\n" \
"ds_read_b64_tr_b16 v[200:201], v228 offset:17920\n" \
"v_mfma_f32_16x16x32_bf16 v[72:75], v[204:207], a[168:171], v[72:75]\n" \
"s_mov_b32 s79, 3670016\n" \
"ds_read_b64_tr_b16 v[202:203], v228 offset:18176\n" \
"v_mfma_f32_16x16x32_bf16 v[68:71], v[204:207], a[164:167], v[68:71]\n" \
"buffer_load_dwordx4 v226, %[wq], s79 offen offset:0 lds\n" \
"v_mfma_f32_16x16x32_bf16 v[64:67], v[204:207], a[160:163], v[64:67]\n" \
"s_waitcnt lgkmcnt(6)\n" \
"v_mfma_f32_16x16x32_bf16 v[144:147], v[208:211], a[160:163], v[144:147]\n" \
"ds_read_b64_tr_b16 v[204:205], v231 offset:17920\n" \
"v_mfma_f32_16x16x32_bf16 v[148:151], v[208:211], a[164:167], v[148:151]\n" \
"ds_read_b64_tr_b16 v[206:207], v231 offset:18176\n" \
"v_mfma_f32_16x16x32_bf16 v[152:155], v[208:211], a[168:171], v[152:155]\n" \
"buffer_load_dwordx4 v227, %[wq], s79 offen offset:1024 lds\n" \
"v_mfma_f32_16x16x32_bf16 v[156:159], v[208:211], a[172:175], v[156:159]\n" \
"v_mfma_f32_16x16x32_bf16 v[108:111], v[212:215], a[172:175], v[108:111]\n" \
"ds_read_b64_tr_b16 v[208:209], v228 offset:24576\n" \
"v_mfma_f32_16x16x32_bf16 v[104:107], v[212:215], a[168:171], v[104:107]\n" \
"ds_read_b64_tr_b16 v[210:211], v228 offset:24832\n" \
"v_mfma_f32_16x16x32_bf16 v[100:103], v[212:215], a[164:167], v[100:103]\n" \
"buffer_load_dwordx4 v254, %[wq], s79 offen offset:2048 lds\n" \
"v_mfma_f32_16x16x32_bf16 v[96:99], v[212:215], a[160:163], v[96:99]\n" \
"s_waitcnt lgkmcnt(6)\n" \
"v_mfma_f32_16x16x32_bf16 v[160:163], v[192:195], a[160:163], v[160:163]\n" \
"ds_read_b64_tr_b16 v[212:213], v231 offset:24576\n" \
"v_mfma_f32_16x16x32_bf16 v[164:167], v[192:195], a[164:167], v[164:167]\n" \
"ds_read_b64_tr_b16 v[214:215], v231 offset:24832\n" \
"v_mfma_f32_16x16x32_bf16 v[168:171], v[192:195], a[168:171], v[168:171]\n" \
"buffer_load_dwordx4 v236, %[wq], s79 offen offset:3072 lds\n" \
"v_mfma_f32_16x16x32_bf16 v[172:175], v[192:195], a[172:175], v[172:175]\n" \
"s_waitcnt vmcnt(16)\n" \
"v_mfma_f32_16x16x32_bf16 v[92:95], v[196:199], a[172:175], v[92:95]\n" \
"ds_read_b64_tr_b16 v[192:193], v228 offset:25088\n" \
"ds_read_b128 a[192:195], v234 offset:0\n" \
"v_mfma_f32_16x16x32_bf16 v[88:91], v[196:199], a[168:171], v[88:91]\n" \
"s_add_u32 m0, s64, 98304\n" \
"ds_read_b64_tr_b16 v[194:195], v228 offset:25344\n" \
"ds_read_b128 a[196:199], v234 offset:2048\n" \
"v_mfma_f32_16x16x32_bf16 v[84:87], v[196:199], a[164:167], v[84:87]\n" \
"buffer_load_dwordx4 v224, %[ca], 0 offen offset:0 lds\n" \
"v_mfma_f32_16x16x32_bf16 v[80:83], v[196:199], a[160:163], v[80:83]\n" \
"s_waitcnt lgkmcnt(8)\n" \
"v_mfma_f32_16x16x32_bf16 v[176:179], v[200:203], a[160:163], v[176:179]\n" \
"ds_read_b64_tr_b16 v[196:197], v231 offset:25088\n" \
"ds_read_b128 a[200:203], v234 offset:4096\n" \
"v_mfma_f32_16x16x32_bf16 v[180:183], v[200:203], a[164:167], v[180:183]\n" \
"ds_read_b64_tr_b16 v[198:199], v231 offset:25344\n" \
"ds_read_b128 a[204:207], v234 offset:6144\n" \
"v_mfma_f32_16x16x32_bf16 v[184:187], v[200:203], a[168:171], v[184:187]\n" \
"buffer_load_dwordx4 v225, %[ca], s84 offen offset:1024 lds\n" \
"v_mfma_f32_16x16x32_bf16 v[188:191], v[200:203], a[172:175], v[188:191]\n" \
"v_mfma_f32_16x16x32_bf16 v[124:127], v[204:207], a[172:175], v[124:127]\n" \
"ds_read_b64_tr_b16 v[200:201], v228 offset:25600\n" \
"ds_read_b128 a[208:211], v235 offset:0\n" \
"v_mfma_f32_16x16x32_bf16 v[120:123], v[204:207], a[168:171], v[120:123]\n" \
"ds_read_b64_tr_b16 v[202:203], v228 offset:25856\n" \
"ds_read_b128 a[212:215], v235 offset:2048\n" \
"v_mfma_f32_16x16x32_bf16 v[116:119], v[204:207], a[164:167], v[116:119]\n" \
"buffer_load_dwordx4 v224, %[ca], s85 offen offset:2048 lds\n" \
"v_mfma_f32_16x16x32_bf16 v[112:115], v[204:207], a[160:163], v[112:115]\n" \
"s_waitcnt lgkmcnt(12)\n" \
"v_mfma_f32_16x16x32_bf16 v[128:131], v[208:211], a[176:179], v[128:131]\n" \
"ds_read_b64_tr_b16 v[204:205], v231 offset:25600\n" \
"ds_read_b128 a[216:219], v235 offset:4096\n" \
"v_mfma_f32_16x16x32_bf16 v[132:135], v[208:211], a[180:183], v[132:135]\n" \
"ds_read_b64_tr_b16 v[206:207], v231 offset:25856\n" \
"ds_read_b128 a[220:223], v235 offset:6144\n" \
"v_mfma_f32_16x16x32_bf16 v[136:139], v[208:211], a[184:187], v[136:139]\n" \
"buffer_load_dwordx4 v225, %[ca], s86 offen offset:3072 lds\n" \
"v_mfma_f32_16x16x32_bf16 v[140:143], v[208:211], a[188:191], v[140:143]\n" \
"v_mfma_f32_16x16x32_bf16 v[76:79], v[212:215], a[188:191], v[76:79]\n" \
"ds_read_b64_tr_b16 v[208:209], v228 offset:26112\n" \
"v_mfma_f32_16x16x32_bf16 v[72:75], v[212:215], a[184:187], v[72:75]\n" \
"s_add_u32 m0, s64, 102400\n" \
"ds_read_b64_tr_b16 v[210:211], v228 offset:26368\n" \
"v_mfma_f32_16x16x32_bf16 v[68:71], v[212:215], a[180:183], v[68:71]\n" \
"buffer_load_dwordx4 v224, %[cb], 0 offen offset:0 lds\n" \
"v_mfma_f32_16x16x32_bf16 v[64:67], v[212:215], a[176:179], v[64:67]\n" \
"s_waitcnt lgkmcnt(11)\n" \
"v_mfma_f32_16x16x32_bf16 v[144:147], v[192:195], a[176:179], v[144:147]\n" \
"ds_read_b64_tr_b16 v[212:213], v231 offset:26112\n" \
"v_mfma_f32_16x16x32_bf16 v[148:151], v[192:195], a[180:183], v[148:151]\n" \
"ds_read_b64_tr_b16 v[214:215], v231 offset:26368\n" \
"v_mfma_f32_16x16x32_bf16 v[152:155], v[192:195], a[184:187], v[152:155]\n" \
"buffer_load_dwordx4 v225, %[cb], s84 offen offset:1024 lds\n" \
"v_mfma_f32_16x16x32_bf16 v[156:159], v[192:195], a[188:191], v[156:159]\n" \
"s_waitcnt vmcnt(18)\n" \
"s_barrier\n" \
"v_mfma_f32_16x16x32_bf16 v[108:111], v[196:199], a[188:191], v[108:111]\n" \
"ds_read_b64_tr_b16 v[192:193], v230 offset:0\n" \
"v_mfma_f32_16x16x32_bf16 v[104:107], v[196:199], a[184:187], v[104:107]\n" \
"ds_read_b64_tr_b16 v[194:195], v230 offset:256\n" \
"v_mfma_f32_16x16x32_bf16 v[100:103], v[196:199], a[180:183], v[100:103]\n" \
"buffer_load_dwordx4 v224, %[cb], s85 offen offset:2048 lds\n" \
"v_mfma_f32_16x16x32_bf16 v[96:99], v[196:199], a[176:179], v[96:99]\n" \
"s_waitcnt lgkmcnt(6)\n" \
"v_mfma_f32_16x16x32_bf16 v[160:163], v[200:203], a[176:179], v[160:163]\n" \
"ds_read_b64_tr_b16 v[196:197], v233 offset:0\n" \
"v_mfma_f32_16x16x32_bf16 v[164:167], v[200:203], a[180:183], v[164:167]\n" \
"ds_read_b64_tr_b16 v[198:199], v233 offset:256\n" \
"v_mfma_f32_16x16x32_bf16 v[168:171], v[200:203], a[184:187], v[168:171]\n" \
"buffer_load_dwordx4 v225, %[cb], s86 offen offset:3072 lds\n" \
"v_mfma_f32_16x16x32_bf16 v[172:175], v[200:203], a[188:191], v[172:175]\n" \
"v_mfma_f32_16x16x32_bf16 v[92:95], v[204:207], a[188:191], v[92:95]\n" \
"s_add_u32 m0, %[w4k], 0\n" \
"ds_read_b64_tr_b16 v[200:201], v230 offset:512\n" \
"v_mfma_f32_16x16x32_bf16 v[88:91], v[204:207], a[184:187], v[88:91]\n" \
"s_mov_b32 s79, 0\n" \
"ds_read_b64_tr_b16 v[202:203], v230 offset:768\n" \
"v_mfma_f32_16x16x32_bf16 v[84:87], v[204:207], a[180:183], v[84:87]\n" \
"buffer_load_dwordx4 v226, %[wk], s79 offen offset:0 lds\n" \
"v_mfma_f32_16x16x32_bf16 v[80:83], v[204:207], a[176:179], v[80:83]\n" \
"s_waitcnt lgkmcnt(6)\n" \
"v_mfma_f32_16x16x32_bf16 v[176:179], v[208:211], a[176:179], v[176:179]\n" \
"ds_read_b64_tr_b16 v[204:205], v233 offset:512\n" \
"v_mfma_f32_16x16x32_bf16 v[180:183], v[208:211], a[180:183], v[180:183]\n" \
"ds_read_b64_tr_b16 v[206:207], v233 offset:768\n" \
"v_mfma_f32_16x16x32_bf16 v[184:187], v[208:211], a[184:187], v[184:187]\n" \
"buffer_load_dwordx4 v227, %[wk], s79 offen offset:1024 lds\n" \
"v_mfma_f32_16x16x32_bf16 v[188:191], v[208:211], a[188:191], v[188:191]\n" \
"s_barrier\n" \
"v_mfma_f32_16x16x32_bf16 v[124:127], v[212:215], a[188:191], v[124:127]\n" \
"ds_read_b64_tr_b16 v[208:209], v230 offset:1024\n" \
"v_mfma_f32_16x16x32_bf16 v[120:123], v[212:215], a[184:187], v[120:123]\n" \
"ds_read_b64_tr_b16 v[210:211], v230 offset:1280\n" \
"v_mfma_f32_16x16x32_bf16 v[116:119], v[212:215], a[180:183], v[116:119]\n" \
"buffer_load_dwordx4 v254, %[wk], s79 offen offset:2048 lds\n" \
"v_mfma_f32_16x16x32_bf16 v[112:115], v[212:215], a[176:179], v[112:115]\n" \
"s_waitcnt lgkmcnt(6)\n" \
"v_mfma_f32_16x16x32_bf16 v[128:131], v[192:195], a[192:195], v[128:131]\n" \
"ds_read_b64_tr_b16 v[212:213], v233 offset:1024\n" \
"v_mfma_f32_16x16x32_bf16 v[132:135], v[192:195], a[196:199], v[132:135]\n" \
"ds_read_b64_tr_b16 v[214:215], v233 offset:1280\n" \
"v_mfma_f32_16x16x32_bf16 v[136:139], v[192:195], a[200:203], v[136:139]\n" \
"buffer_load_dwordx4 v236, %[wk], s79 offen offset:3072 lds\n" \
"v_mfma_f32_16x16x32_bf16 v[140:143], v[192:195], a[204:207], v[140:143]\n" \
"s_waitcnt vmcnt(16)\n" \
"v_mfma_f32_16x16x32_bf16 v[76:79], v[196:199], a[204:207], v[76:79]\n" \
"s_add_u32 m0, %[w4k], 16384\n" \
"ds_read_b64_tr_b16 v[192:193], v230 offset:1536\n" \
"ds_read_b128 a[224:227], v234 offset:32768\n" \
"v_mfma_f32_16x16x32_bf16 v[72:75], v[196:199], a[200:203], v[72:75]\n" \
"s_mov_b32 s79, 524288\n" \
"ds_read_b64_tr_b16 v[194:195], v230 offset:1792\n" \
"ds_read_b128 a[228:231], v234 offset:34816\n" \
"v_mfma_f32_16x16x32_bf16 v[68:71], v[196:199], a[196:199], v[68:71]\n" \
"buffer_load_dwordx4 v226, %[wk], s79 offen offset:0 lds\n" \
"v_mfma_f32_16x16x32_bf16 v[64:67], v[196:199], a[192:195], v[64:67]\n" \
"s_waitcnt lgkmcnt(8)\n" \
"v_mfma_f32_16x16x32_bf16 v[144:147], v[200:203], a[192:195], v[144:147]\n" \
"ds_read_b64_tr_b16 v[196:197], v233 offset:1536\n" \
"ds_read_b128 a[232:235], v234 offset:36864\n" \
"v_mfma_f32_16x16x32_bf16 v[148:151], v[200:203], a[196:199], v[148:151]\n" \
"ds_read_b64_tr_b16 v[198:199], v233 offset:1792\n" \
"ds_read_b128 a[236:239], v234 offset:38912\n" \
"v_mfma_f32_16x16x32_bf16 v[152:155], v[200:203], a[200:203], v[152:155]\n" \
"buffer_load_dwordx4 v227, %[wk], s79 offen offset:1024 lds\n" \
"v_mfma_f32_16x16x32_bf16 v[156:159], v[200:203], a[204:207], v[156:159]\n" \
"v_mfma_f32_16x16x32_bf16 v[108:111], v[204:207], a[204:207], v[108:111]\n" \
"ds_read_b64_tr_b16 v[200:201], v230 offset:8192\n" \
"ds_read_b128 a[240:243], v235 offset:32768\n" \
"v_mfma_f32_16x16x32_bf16 v[104:107], v[204:207], a[200:203], v[104:107]\n" \
"ds_read_b64_tr_b16 v[202:203], v230 offset:8448\n" \
"ds_read_b128 a[244:247], v235 offset:34816\n" \
"v_mfma_f32_16x16x32_bf16 v[100:103], v[204:207], a[196:199], v[100:103]\n" \
"buffer_load_dwordx4 v254, %[wk], s79 offen offset:2048 lds\n" \
"v_mfma_f32_16x16x32_bf16 v[96:99], v[204:207], a[192:195], v[96:99]\n" \
"s_waitcnt lgkmcnt(12)\n" \
"v_mfma_f32_16x16x32_bf16 v[160:163], v[208:211], a[192:195], v[160:163]\n" \
"ds_read_b64_tr_b16 v[204:205], v233 offset:8192\n" \
"ds_read_b128 a[248:251], v235 offset:36864\n" \
"v_mfma_f32_16x16x32_bf16 v[164:167], v[208:211], a[196:199], v[164:167]\n" \
"ds_read_b64_tr_b16 v[206:207], v233 offset:8448\n" \
"ds_read_b128 a[252:255], v235 offset:38912\n" \
"v_mfma_f32_16x16x32_bf16 v[168:171], v[208:211], a[200:203], v[168:171]\n" \
"buffer_load_dwordx4 v236, %[wk], s79 offen offset:3072 lds\n" \
"v_mfma_f32_16x16x32_bf16 v[172:175], v[208:211], a[204:207], v[172:175]\n" \
"v_mfma_f32_16x16x32_bf16 v[92:95], v[212:215], a[204:207], v[92:95]\n" \
"ds_read_b64_tr_b16 v[208:209], v230 offset:8704\n" \
"v_mfma_f32_16x16x32_bf16 v[88:91], v[212:215], a[200:203], v[88:91]\n" \
"s_add_u32 m0, s64, 32640\n" \
"ds_read_b64_tr_b16 v[210:211], v230 offset:8960\n" \
"v_mfma_f32_16x16x32_bf16 v[84:87], v[212:215], a[196:199], v[84:87]\n" \
"buffer_load_dwordx4 v224, %[ca], 0 offen offset:128 lds\n" \
"v_mfma_f32_16x16x32_bf16 v[80:83], v[212:215], a[192:195], v[80:83]\n" \
"s_waitcnt lgkmcnt(11)\n" \
"v_mfma_f32_16x16x32_bf16 v[176:179], v[192:195], a[192:195], v[176:179]\n" \
"ds_read_b64_tr_b16 v[212:213], v233 offset:8704\n" \
"v_mfma_f32_16x16x32_bf16 v[180:183], v[192:195], a[196:199], v[180:183]\n" \
"ds_read_b64_tr_b16 v[214:215], v233 offset:8960\n" \
"v_mfma_f32_16x16x32_bf16 v[184:187], v[192:195], a[200:203], v[184:187]\n" \
"buffer_load_dwordx4 v225, %[ca], s84 offen offset:1152 lds\n" \
"v_mfma_f32_16x16x32_bf16 v[188:191], v[192:195], a[204:207], v[188:191]\n" \
"v_mfma_f32_16x16x32_bf16 v[124:127], v[196:199], a[204:207], v[124:127]\n" \
"ds_read_b64_tr_b16 v[192:193], v230 offset:9216\n" \
"v_mfma_f32_16x16x32_bf16 v[120:123], v[196:199], a[200:203], v[120:123]\n" \
"ds_read_b64_tr_b16 v[194:195], v230 offset:9472\n" \
"v_mfma_f32_16x16x32_bf16 v[116:119], v[196:199], a[196:199], v[116:119]\n" \
"buffer_load_dwordx4 v224, %[ca], s85 offen offset:2176 lds\n" \
"v_mfma_f32_16x16x32_bf16 v[112:115], v[196:199], a[192:195], v[112:115]\n" \
"s_waitcnt lgkmcnt(6)\n" \
"v_mfma_f32_16x16x32_bf16 v[128:131], v[200:203], a[208:211], v[128:131]\n" \
"ds_read_b64_tr_b16 v[196:197], v233 offset:9216\n" \
"v_mfma_f32_16x16x32_bf16 v[132:135], v[200:203], a[212:215], v[132:135]\n" \
"ds_read_b64_tr_b16 v[198:199], v233 offset:9472\n" \
"v_mfma_f32_16x16x32_bf16 v[136:139], v[200:203], a[216:219], v[136:139]\n" \
"buffer_load_dwordx4 v225, %[ca], s86 offen offset:3200 lds\n" \
"v_mfma_f32_16x16x32_bf16 v[140:143], v[200:203], a[220:223], v[140:143]\n" \
"v_mfma_f32_16x16x32_bf16 v[76:79], v[204:207], a[220:223], v[76:79]\n" \
"ds_read_b64_tr_b16 v[200:201], v230 offset:9728\n" \
"v_mfma_f32_16x16x32_bf16 v[72:75], v[204:207], a[216:219], v[72:75]\n" \
"s_add_u32 m0, s64, 36736\n" \
"ds_read_b64_tr_b16 v[202:203], v230 offset:9984\n" \
"v_mfma_f32_16x16x32_bf16 v[68:71], v[204:207], a[212:215], v[68:71]\n" \
"buffer_load_dwordx4 v224, %[cb], 0 offen offset:128 lds\n" \
"v_mfma_f32_16x16x32_bf16 v[64:67], v[204:207], a[208:211], v[64:67]\n" \
"s_waitcnt lgkmcnt(6)\n" \
"v_mfma_f32_16x16x32_bf16 v[144:147], v[208:211], a[208:211], v[144:147]\n" \
"ds_read_b64_tr_b16 v[204:205], v233 offset:9728\n" \
"v_mfma_f32_16x16x32_bf16 v[148:151], v[208:211], a[212:215], v[148:151]\n" \
"ds_read_b64_tr_b16 v[206:207], v233 offset:9984\n" \
"v_mfma_f32_16x16x32_bf16 v[152:155], v[208:211], a[216:219], v[152:155]\n" \
"buffer_load_dwordx4 v225, %[cb], s84 offen offset:1152 lds\n" \
"v_mfma_f32_16x16x32_bf16 v[156:159], v[208:211], a[220:223], v[156:159]\n" \
"s_waitcnt vmcnt(22)\n" \
"s_barrier\n" \
"v_mfma_f32_16x16x32_bf16 v[108:111], v[212:215], a[220:223], v[108:111]\n" \
"ds_read_b64_tr_b16 v[208:209], v230 offset:16384\n" \
"v_mfma_f32_16x16x32_bf16 v[104:107], v[212:215], a[216:219], v[104:107]\n" \
"ds_read_b64_tr_b16 v[210:211], v230 offset:16640\n" \
"v_mfma_f32_16x16x32_bf16 v[100:103], v[212:215], a[212:215], v[100:103]\n" \
"buffer_load_dwordx4 v224, %[cb], s85 offen offset:2176 lds\n" \
"v_mfma_f32_16x16x32_bf16 v[96:99], v[212:215], a[208:211], v[96:99]\n" \
"s_waitcnt lgkmcnt(6)\n" \
"v_mfma_f32_16x16x32_bf16 v[160:163], v[192:195], a[208:211], v[160:163]\n" \
"ds_read_b64_tr_b16 v[212:213], v230 offset:24576\n" \
"v_mfma_f32_16x16x32_bf16 v[164:167], v[192:195], a[212:215], v[164:167]\n" \
"ds_read_b64_tr_b16 v[214:215], v230 offset:24832\n" \
"v_mfma_f32_16x16x32_bf16 v[168:171], v[192:195], a[216:219], v[168:171]\n" \
"buffer_load_dwordx4 v225, %[cb], s86 offen offset:3200 lds\n" \
"v_mfma_f32_16x16x32_bf16 v[172:175], v[192:195], a[220:223], v[172:175]\n" \
"s_waitcnt vmcnt(16)\n" \
"v_mfma_f32_16x16x32_bf16 v[92:95], v[196:199], a[220:223], v[92:95]\n" \
"ds_read_b64_tr_b16 v[192:193], v233 offset:16384\n" \
"ds_read_b128 a[0:3], v238 offset:0\n" \
"v_mfma_f32_16x16x32_bf16 v[88:91], v[196:199], a[216:219], v[88:91]\n" \
"s_add_u32 m0, s64, 65280\n" \
"ds_read_b64_tr_b16 v[194:195], v233 offset:16640\n" \
"ds_read_b128 a[4:7], v238 offset:2048\n" \
"v_mfma_f32_16x16x32_bf16 v[84:87], v[196:199], a[212:215], v[84:87]\n" \
"buffer_load_dwordx4 v224, %[ca], 0 offen offset:256 lds\n" \
"v_mfma_f32_16x16x32_bf16 v[80:83], v[196:199], a[208:211], v[80:83]\n" \
"s_waitcnt lgkmcnt(8)\n" \
"v_mfma_f32_16x16x32_bf16 v[176:179], v[200:203], a[208:211], v[176:179]\n" \
"ds_read_b64_tr_b16 v[196:197], v233 offset:24576\n" \
"ds_read_b128 a[8:11], v238 offset:4096\n" \
"v_mfma_f32_16x16x32_bf16 v[180:183], v[200:203], a[212:215], v[180:183]\n" \
"ds_read_b64_tr_b16 v[198:199], v233 offset:24832\n" \
"ds_read_b128 a[12:15], v238 offset:6144\n" \
"v_mfma_f32_16x16x32_bf16 v[184:187], v[200:203], a[216:219], v[184:187]\n" \
"buffer_load_dwordx4 v225, %[ca], s84 offen offset:1280 lds\n" \
"v_mfma_f32_16x16x32_bf16 v[188:191], v[200:203], a[220:223], v[188:191]\n" \
"s_barrier\n" \
"v_mfma_f32_16x16x32_bf16 v[124:127], v[204:207], a[220:223], v[124:127]\n" \
"ds_read_b64_tr_b16 v[200:201], v230 offset:16896\n" \
"ds_read_b128 a[16:19], v239 offset:0\n" \
"v_mfma_f32_16x16x32_bf16 v[120:123], v[204:207], a[216:219], v[120:123]\n" \
"ds_read_b64_tr_b16 v[202:203], v230 offset:17152\n" \
"ds_read_b128 a[20:23], v239 offset:2048\n" \
"v_mfma_f32_16x16x32_bf16 v[116:119], v[204:207], a[212:215], v[116:119]\n" \
"buffer_load_dwordx4 v224, %[ca], s85 offen offset:2304 lds\n" \
"v_mfma_f32_16x16x32_bf16 v[112:115], v[204:207], a[208:211], v[112:115]\n" \
"s_waitcnt lgkmcnt(12)\n" \
"v_mfma_f32_16x16x32_bf16 v[128:131], v[208:211], a[224:227], v[128:131]\n" \
"ds_read_b64_tr_b16 v[204:205], v230 offset:25088\n" \
"ds_read_b128 a[24:27], v239 offset:4096\n" \
"v_mfma_f32_16x16x32_bf16 v[132:135], v[208:211], a[228:231], v[132:135]\n" \
"ds_read_b64_tr_b16 v[206:207], v230 offset:25344\n" \
"ds_read_b128 a[28:31], v239 offset:6144\n" \
"v_mfma_f32_16x16x32_bf16 v[136:139], v[208:211], a[232:235], v[136:139]\n" \
"buffer_load_dwordx4 v225, %[ca], s86 offen offset:3328 lds\n" \
"v_mfma_f32_16x16x32_bf16 v[140:143], v[208:211], a[236:239], v[140:143]\n" \
"v_mfma_f32_16x16x32_bf16 v[128:131], v[212:215], a[240:243], v[128:131]\n" \
"ds_read_b64_tr_b16 v[208:209], v233 offset:16896\n" \
"v_mfma_f32_16x16x32_bf16 v[132:135], v[212:215], a[244:247], v[132:135]\n" \
"s_add_u32 m0, s64, 69376\n" \
"ds_read_b64_tr_b16 v[210:211], v233 offset:17152\n" \
"v_mfma_f32_16x16x32_bf16 v[136:139], v[212:215], a[248:251], v[136:139]\n" \
"buffer_load_dwordx4 v224, %[cb], 0 offen offset:256 lds\n" \
"v_mfma_f32_16x16x32_bf16 v[140:143], v[212:215], a[252:255], v[140:143]\n" \
"s_waitcnt lgkmcnt(11)\n" \
"v_mul_f32_e32 v128, s77, v128\n" \
"v_mul_f32_e32 v129, s77, v129\n" \
"v_mul_f32_e32 v130, s77, v130\n" \
"v_mul_f32_e32 v131, s77, v131\n" \
"v_cvt_pk_bf16_f32 v128, v128, v129\n" \
"v_mfma_f32_16x16x32_bf16 v[64:67], v[192:195], a[224:227], v[64:67]\n" \
"ds_read_b64_tr_b16 v[212:213], v233 offset:25088\n" \
"v_cvt_pk_bf16_f32 v129, v130, v131\n" \
"v_mul_f32_e32 v132, s77, v132\n" \
"v_mul_f32_e32 v133, s77, v133\n" \
"v_mul_f32_e32 v134, s77, v134\n" \
"v_mul_f32_e32 v135, s77, v135\n" \
"v_mfma_f32_16x16x32_bf16 v[68:71], v[192:195], a[228:231], v[68:71]\n" \
"ds_read_b64_tr_b16 v[214:215], v233 offset:25344\n" \
"v_cvt_pk_bf16_f32 v132, v132, v133\n" \
"v_cvt_pk_bf16_f32 v133, v134, v135\n" \
"v_mul_f32_e32 v136, s77, v136\n" \
"v_mul_f32_e32 v137, s77, v137\n" \
"v_mul_f32_e32 v138, s77, v138\n" \
"v_mfma_f32_16x16x32_bf16 v[72:75], v[192:195], a[232:235], v[72:75]\n" \
"buffer_load_dwordx4 v225, %[cb], s84 offen offset:1280 lds\n" \
"v_mul_f32_e32 v139, s77, v139\n" \
"v_cvt_pk_bf16_f32 v136, v136, v137\n" \
"v_cvt_pk_bf16_f32 v137, v138, v139\n" \
"v_mul_f32_e32 v140, s77, v140\n" \
"v_mul_f32_e32 v141, s77, v141\n" \
"v_mfma_f32_16x16x32_bf16 v[76:79], v[192:195], a[236:239], v[76:79]\n" \
"v_mul_f32_e32 v142, s77, v142\n" \
"v_mul_f32_e32 v143, s77, v143\n" \
"v_mfma_f32_16x16x32_bf16 v[64:67], v[196:199], a[240:243], v[64:67]\n" \
"ds_read_b64_tr_b16 v[192:193], v230 offset:17408\n" \
"v_cvt_pk_bf16_f32 v140, v140, v141\n" \
"v_mfma_f32_16x16x32_bf16 v[68:71], v[196:199], a[244:247], v[68:71]\n" \
"ds_read_b64_tr_b16 v[194:195], v230 offset:17664\n" \
"v_cvt_pk_bf16_f32 v141, v142, v143\n" \
"v_mfma_f32_16x16x32_bf16 v[72:75], v[196:199], a[248:251], v[72:75]\n" \
"buffer_load_dwordx4 v224, %[cb], s85 offen offset:2304 lds\n" \
"v_mfma_f32_16x16x32_bf16 v[76:79], v[196:199], a[252:255], v[76:79]\n" \
"s_waitcnt lgkmcnt(6)\n" \
"v_mul_f32_e32 v64, s77, v64\n" \
"v_mul_f32_e32 v65, s77, v65\n" \
"v_mul_f32_e32 v66, s77, v66\n" \
"v_mul_f32_e32 v67, s77, v67\n" \
"v_cvt_pk_bf16_f32 v130, v64, v65\n" \
"v_mfma_f32_16x16x32_bf16 v[144:147], v[200:203], a[224:227], v[144:147]\n" \
"ds_read_b64_tr_b16 v[196:197], v230 offset:25600\n" \
"v_cvt_pk_bf16_f32 v131, v66, v67\n" \
"v_mul_f32_e32 v68, s77, v68\n" \
"v_mul_f32_e32 v69, s77, v69\n" \
"v_mul_f32_e32 v70, s77, v70\n" \
"v_mul_f32_e32 v71, s77, v71\n" \
"v_mfma_f32_16x16x32_bf16 v[148:151], v[200:203], a[228:231], v[148:151]\n" \
"ds_read_b64_tr_b16 v[198:199], v230 offset:25856\n" \
"v_cvt_pk_bf16_f32 v134, v68, v69\n" \
"v_cvt_pk_bf16_f32 v135, v70, v71\n" \
"v_mul_f32_e32 v72, s77, v72\n" \
"v_mul_f32_e32 v73, s77, v73\n" \
"v_mul_f32_e32 v74, s77, v74\n" \
"v_mfma_f32_16x16x32_bf16 v[152:155], v[200:203], a[232:235], v[152:155]\n" \
"buffer_load_dwordx4 v225, %[cb], s86 offen offset:3328 lds\n" \
"v_mul_f32_e32 v75, s77, v75\n" \
"v_cvt_pk_bf16_f32 v138, v72, v73\n" \
"v_cvt_pk_bf16_f32 v139, v74, v75\n" \
"v_mul_f32_e32 v76, s77, v76\n" \
"v_mul_f32_e32 v77, s77, v77\n" \
"v_mfma_f32_16x16x32_bf16 v[156:159], v[200:203], a[236:239], v[156:159]\n" \
"v_mul_f32_e32 v78, s77, v78\n" \
"v_mul_f32_e32 v79, s77, v79\n" \
"v_mfma_f32_16x16x32_bf16 v[144:147], v[204:207], a[240:243], v[144:147]\n" \
"s_add_u32 m0, %[w4k], 131072\n" \
"ds_read_b64_tr_b16 v[200:201], v233 offset:17408\n" \
"v_cvt_pk_bf16_f32 v142, v76, v77\n" \
"v_mfma_f32_16x16x32_bf16 v[148:151], v[204:207], a[244:247], v[148:151]\n" \
"s_mov_b32 s79, 1048576\n" \
"ds_read_b64_tr_b16 v[202:203], v233 offset:17664\n" \
"v_cvt_pk_bf16_f32 v143, v78, v79\n" \
"v_mfma_f32_16x16x32_bf16 v[152:155], v[204:207], a[248:251], v[152:155]\n" \
"buffer_load_dwordx4 v226, %[wk], s79 offen offset:0 lds\n" \
"v_mfma_f32_16x16x32_bf16 v[156:159], v[204:207], a[252:255], v[156:159]\n" \
"s_waitcnt lgkmcnt(6)\n" \
"v_mul_f32_e32 v144, s77, v144\n" \
"v_mul_f32_e32 v145, s77, v145\n" \
"v_mul_f32_e32 v146, s77, v146\n" \
"v_mul_f32_e32 v147, s77, v147\n" \
"v_cvt_pk_bf16_f32 v144, v144, v145\n" \
"v_mfma_f32_16x16x32_bf16 v[96:99], v[208:211], a[224:227], v[96:99]\n" \
"ds_read_b64_tr_b16 v[204:205], v233 offset:25600\n" \
"v_cvt_pk_bf16_f32 v145, v146, v147\n" \
"v_mul_f32_e32 v148, s77, v148\n" \
"v_mul_f32_e32 v149, s77, v149\n" \
"v_mul_f32_e32 v150, s77, v150\n" \
"v_mul_f32_e32 v151, s77, v151\n" \
"v_mfma_f32_16x16x32_bf16 v[100:103], v[208:211], a[228:231], v[100:103]\n" \
"ds_read_b64_tr_b16 v[206:207], v233 offset:25856\n" \
"v_cvt_pk_bf16_f32 v148, v148, v149\n" \
"v_cvt_pk_bf16_f32 v149, v150, v151\n" \
"v_mul_f32_e32 v152, s77, v152\n" \
"v_mul_f32_e32 v153, s77, v153\n" \
"v_mul_f32_e32 v154, s77, v154\n" \
"v_mfma_f32_16x16x32_bf16 v[104:107], v[208:211], a[232:235], v[104:107]\n" \
"buffer_load_dwordx4 v227, %[wk], s79 offen offset:1024 lds\n" \
"v_mul_f32_e32 v155, s77, v155\n" \
"v_cvt_pk_bf16_f32 v152, v152, v153\n" \
"v_cvt_pk_bf16_f32 v153, v154, v155\n" \
"v_mul_f32_e32 v156, s77, v156\n" \
"v_mul_f32_e32 v157, s77, v157\n" \
"v_mfma_f32_16x16x32_bf16 v[108:111], v[208:211], a[236:239], v[108:111]\n" \
"v_mul_f32_e32 v158, s77, v158\n" \
"v_mul_f32_e32 v159, s77, v159\n" \
"v_mfma_f32_16x16x32_bf16 v[96:99], v[212:215], a[240:243], v[96:99]\n" \
"ds_read_b64_tr_b16 v[208:209], v230 offset:17920\n" \
"v_cvt_pk_bf16_f32 v156, v156, v157\n" \
"v_mfma_f32_16x16x32_bf16 v[100:103], v[212:215], a[244:247], v[100:103]\n" \
"ds_read_b64_tr_b16 v[210:211], v230 offset:18176\n" \
"v_cvt_pk_bf16_f32 v157, v158, v159\n" \
"v_mfma_f32_16x16x32_bf16 v[104:107], v[212:215], a[248:251], v[104:107]\n" \
"buffer_load_dwordx4 v254, %[wk], s79 offen offset:2048 lds\n" \
"v_mfma_f32_16x16x32_bf16 v[108:111], v[212:215], a[252:255], v[108:111]\n" \
"s_waitcnt lgkmcnt(6)\n" \
"v_mul_f32_e32 v96, s77, v96\n" \
"v_mul_f32_e32 v97, s77, v97\n" \
"v_mul_f32_e32 v98, s77, v98\n" \
"v_mul_f32_e32 v99, s77, v99\n" \
"v_mfma_f32_16x16x32_bf16 v[160:163], v[192:195], a[224:227], v[160:163]\n" \
"ds_read_b64_tr_b16 v[212:213], v230 offset:26112\n" \
"v_cvt_pk_bf16_f32 v146, v96, v97\n" \
"v_mfma_f32_16x16x32_bf16 v[164:167], v[192:195], a[228:231], v[164:167]\n" \
"ds_read_b64_tr_b16 v[214:215], v230 offset:26368\n" \
"v_cvt_pk_bf16_f32 v147, v98, v99\n" \
"v_mfma_f32_16x16x32_bf16 v[168:171], v[192:195], a[232:235], v[168:171]\n" \
"buffer_load_dwordx4 v236, %[wk], s79 offen offset:3072 lds\n" \
"v_mfma_f32_16x16x32_bf16 v[172:175], v[192:195], a[236:239], v[172:175]\n" \
"v_mul_f32_e32 v100, s77, v100\n" \
"v_mul_f32_e32 v101, s77, v101\n" \
"v_mfma_f32_16x16x32_bf16 v[160:163], v[196:199], a[240:243], v[160:163]\n" \
"ds_read_b64_tr_b16 v[192:193], v233 offset:17920\n" \
"v_mul_f32_e32 v102, s77, v102\n" \
"v_mfma_f32_16x16x32_bf16 v[164:167], v[196:199], a[244:247], v[164:167]\n" \
"s_add_u32 m0, s64, 97920\n" \
"ds_read_b64_tr_b16 v[194:195], v233 offset:18176\n" \
"v_mul_f32_e32 v103, s77, v103\n" \
"v_mfma_f32_16x16x32_bf16 v[168:171], v[196:199], a[248:251], v[168:171]\n" \
"buffer_load_dwordx4 v224, %[ca], 0 offen offset:384 lds\n" \
"v_mfma_f32_16x16x32_bf16 v[172:175], v[196:199], a[252:255], v[172:175]\n" \
"s_waitcnt lgkmcnt(6)\n" \
"v_cvt_pk_bf16_f32 v150, v100, v101\n" \
"v_cvt_pk_bf16_f32 v151, v102, v103\n" \
"v_mfma_f32_16x16x32_bf16 v[80:83], v[200:203], a[224:227], v[80:83]\n" \
"ds_read_b64_tr_b16 v[196:197], v233 offset:26112\n" \
"v_mul_f32_e32 v104, s77, v104\n" \
"v_mfma_f32_16x16x32_bf16 v[84:87], v[200:203], a[228:231], v[84:87]\n" \
"ds_read_b64_tr_b16 v[198:199], v233 offset:26368\n" \
"v_mul_f32_e32 v105, s77, v105\n" \
"v_mfma_f32_16x16x32_bf16 v[88:91], v[200:203], a[232:235], v[88:91]\n" \
"buffer_load_dwordx4 v225, %[ca], s84 offen offset:1408 lds\n" \
"v_mfma_f32_16x16x32_bf16 v[92:95], v[200:203], a[236:239], v[92:95]\n" \
"s_waitcnt vmcnt(22)\n" \
"s_barrier\n" \
"v_mfma_f32_16x16x32_bf16 v[80:83], v[204:207], a[240:243], v[80:83]\n" \
"ds_read_b64_tr_b16 v[200:201], v228 offset:0\n" \
"v_mul_f32_e32 v106, s77, v106\n" \
"v_mfma_f32_16x16x32_bf16 v[84:87], v[204:207], a[244:247], v[84:87]\n" \
"ds_read_b64_tr_b16 v[202:203], v228 offset:256\n" \
"v_mul_f32_e32 v107, s77, v107\n" \
"v_mfma_f32_16x16x32_bf16 v[88:91], v[204:207], a[248:251], v[88:91]\n" \
"buffer_load_dwordx4 v224, %[ca], s85 offen offset:2432 lds\n" \
"v_mfma_f32_16x16x32_bf16 v[92:95], v[204:207], a[252:255], v[92:95]\n" \
"s_waitcnt lgkmcnt(6)\n" \
"v_cvt_pk_bf16_f32 v154, v104, v105\n" \
"v_cvt_pk_bf16_f32 v155, v106, v107\n" \
"v_mfma_f32_16x16x32_bf16 v[176:179], v[208:211], a[224:227], v[176:179]\n" \
"ds_read_b64_tr_b16 v[204:205], v231 offset:0\n" \
"v_mul_f32_e32 v108, s77, v108\n" \
"v_mfma_f32_16x16x32_bf16 v[180:183], v[208:211], a[228:231], v[180:183]\n" \
"ds_read_b64_tr_b16 v[206:207], v231 offset:256\n" \
"v_mul_f32_e32 v109, s77, v109\n" \
"v_mfma_f32_16x16x32_bf16 v[184:187], v[208:211], a[232:235], v[184:187]\n" \
"buffer_load_dwordx4 v225, %[ca], s86 offen offset:3456 lds\n" \
"v_mfma_f32_16x16x32_bf16 v[188:191], v[208:211], a[236:239], v[188:191]\n" \
"s_waitcnt vmcnt(16)\n" \
"v_mul_f32_e32 v110, s77, v110\n" \
"v_mul_f32_e32 v111, s77, v111\n" \
"v_mfma_f32_16x16x32_bf16 v[176:179], v[212:215], a[240:243], v[176:179]\n" \
"ds_read_b64_tr_b16 v[208:209], v228 offset:512\n" \
"ds_read_b128 a[32:35], v234 offset:0\n" \
"v_mfma_f32_16x16x32_bf16 v[180:183], v[212:215], a[244:247], v[180:183]\n" \
"s_add_u32 m0, s64, 102016\n" \
"ds_read_b64_tr_b16 v[210:211], v228 offset:768\n" \
"ds_read_b128 a[36:39], v234 offset:2048\n" \
"v_mfma_f32_16x16x32_bf16 v[184:187], v[212:215], a[248:251], v[184:187]\n" \
"buffer_load_dwordx4 v224, %[cb], 0 offen offset:384 lds\n" \
"v_mfma_f32_16x16x32_bf16 v[188:191], v[212:215], a[252:255], v[188:191]\n" \
"s_waitcnt lgkmcnt(8)\n" \
"v_cvt_pk_bf16_f32 v158, v108, v109\n" \
"v_cvt_pk_bf16_f32 v159, v110, v111\n" \
"v_mfma_f32_16x16x32_bf16 v[112:115], v[192:195], a[224:227], v[112:115]\n" \
"ds_read_b64_tr_b16 v[212:213], v231 offset:512\n" \
"ds_read_b128 a[40:43], v234 offset:4096\n" \
"v_mfma_f32_16x16x32_bf16 v[116:119], v[192:195], a[228:231], v[116:119]\n" \
"ds_read_b64_tr_b16 v[214:215], v231 offset:768\n" \
"ds_read_b128 a[44:47], v234 offset:6144\n" \
"v_mfma_f32_16x16x32_bf16 v[120:123], v[192:195], a[232:235], v[120:123]\n" \
"buffer_load_dwordx4 v225, %[cb], s84 offen offset:1408 lds\n" \
"v_mfma_f32_16x16x32_bf16 v[124:127], v[192:195], a[236:239], v[124:127]\n" \
"s_barrier\n" \
"v_mfma_f32_16x16x32_bf16 v[112:115], v[196:199], a[240:243], v[112:115]\n" \
"ds_read_b64_tr_b16 v[192:193], v228 offset:8192\n" \
"ds_read_b128 a[48:51], v235 offset:0\n" \
"v_mfma_f32_16x16x32_bf16 v[116:119], v[196:199], a[244:247], v[116:119]\n" \
"ds_read_b64_tr_b16 v[194:195], v228 offset:8448\n" \
"ds_read_b128 a[52:55], v235 offset:2048\n" \
"v_mfma_f32_16x16x32_bf16 v[120:123], v[196:199], a[248:251], v[120:123]\n" \
"buffer_load_dwordx4 v224, %[cb], s85 offen offset:2432 lds\n" \
"v_mfma_f32_16x16x32_bf16 v[124:127], v[196:199], a[252:255], v[124:127]\n" \
"s_waitcnt lgkmcnt(12)\n" \
"v_mul_f32_e32 v160, s77, v160\n" \
"v_mul_f32_e32 v161, s77, v161\n" \
"v_mfma_f32_16x16x32_bf16 v[0:3], v[200:203], a[0:3], 0\n" \
"ds_read_b64_tr_b16 v[196:197], v231 offset:8192\n" \
"ds_read_b128 a[56:59], v235 offset:4096\n" \
"v_mfma_f32_16x16x32_bf16 v[4:7], v[200:203], a[4:7], 0\n" \
"ds_read_b64_tr_b16 v[198:199], v231 offset:8448\n" \
"ds_read_b128 a[60:63], v235 offset:6144\n" \
"v_mfma_f32_16x16x32_bf16 v[8:11], v[200:203], a[8:11], 0\n" \
"buffer_load_dwordx4 v225, %[cb], s86 offen offset:3456 lds\n" \
"v_mfma_f32_16x16x32_bf16 v[12:15], v[200:203], a[12:15], 0\n" \
"v_mul_f32_e32 v162, s77, v162\n" \
"v_mul_f32_e32 v163, s77, v163\n" \
"v_mfma_f32_16x16x32_bf16 v[32:35], v[204:207], a[0:3], 0\n" \
"s_add_u32 m0, %[w4k], 147456\n" \
"ds_read_b64_tr_b16 v[200:201], v228 offset:8704\n" \
"v_cvt_pk_bf16_f32 v160, v160, v161\n" \
"v_mfma_f32_16x16x32_bf16 v[36:39], v[204:207], a[4:7], 0\n" \
"s_mov_b32 s79, 1572864\n" \
"ds_read_b64_tr_b16 v[202:203], v228 offset:8960\n" \
"v_cvt_pk_bf16_f32 v161, v162, v163\n" \
"v_mfma_f32_16x16x32_bf16 v[40:43], v[204:207], a[8:11], 0\n" \
"buffer_load_dwordx4 v226, %[wk], s79 offen offset:0 lds\n" \
"v_mfma_f32_16x16x32_bf16 v[44:47], v[204:207], a[12:15], 0\n" \
"s_waitcnt lgkmcnt(11)\n" \
"v_mul_f32_e32 v164, s77, v164\n" \
"v_mul_f32_e32 v165, s77, v165\n" \
"v_mfma_f32_16x16x32_bf16 v[16:19], v[208:211], a[0:3], 0\n" \
"ds_read_b64_tr_b16 v[204:205], v231 offset:8704\n" \
"v_mul_f32_e32 v166, s77, v166\n" \
"v_mfma_f32_16x16x32_bf16 v[20:23], v[208:211], a[4:7], 0\n" \
"ds_read_b64_tr_b16 v[206:207], v231 offset:8960\n" \
"v_mul_f32_e32 v167, s77, v167\n" \
"v_mfma_f32_16x16x32_bf16 v[24:27], v[208:211], a[8:11], 0\n" \
"buffer_load_dwordx4 v227, %[wk], s79 offen offset:1024 lds\n" \
"v_mfma_f32_16x16x32_bf16 v[28:31], v[208:211], a[12:15], 0\n" \
"v_cvt_pk_bf16_f32 v164, v164, v165\n" \
"v_cvt_pk_bf16_f32 v165, v166, v167\n" \
"v_mfma_f32_16x16x32_bf16 v[48:51], v[212:215], a[0:3], 0\n" \
"ds_read_b64_tr_b16 v[208:209], v228 offset:16384\n" \
"v_mul_f32_e32 v168, s77, v168\n" \
"v_mfma_f32_16x16x32_bf16 v[52:55], v[212:215], a[4:7], 0\n" \
"ds_read_b64_tr_b16 v[210:211], v228 offset:16640\n" \
"v_mul_f32_e32 v169, s77, v169\n" \
"v_mfma_f32_16x16x32_bf16 v[56:59], v[212:215], a[8:11], 0\n" \
"buffer_load_dwordx4 v254, %[wk], s79 offen offset:2048 lds\n" \
"v_mfma_f32_16x16x32_bf16 v[60:63], v[212:215], a[12:15], 0\n" \
"s_waitcnt lgkmcnt(6)\n" \
"v_mul_f32_e32 v170, s77, v170\n" \
"v_mul_f32_e32 v171, s77, v171\n" \
"v_mfma_f32_16x16x32_bf16 v[0:3], v[192:195], a[16:19], v[0:3]\n" \
"ds_read_b64_tr_b16 v[212:213], v231 offset:16384\n" \
"v_cvt_pk_bf16_f32 v168, v168, v169\n" \
"v_mfma_f32_16x16x32_bf16 v[4:7], v[192:195], a[20:23], v[4:7]\n" \
"ds_read_b64_tr_b16 v[214:215], v231 offset:16640\n" \
"v_cvt_pk_bf16_f32 v169, v170, v171\n" \
"v_mfma_f32_16x16x32_bf16 v[8:11], v[192:195], a[24:27], v[8:11]\n" \
"buffer_load_dwordx4 v236, %[wk], s79 offen offset:3072 lds\n" \
"v_mfma_f32_16x16x32_bf16 v[12:15], v[192:195], a[28:31], v[12:15]\n" \
"s_waitcnt vmcnt(16)\n" \
"v_mul_f32_e32 v172, s77, v172\n" \
"v_mul_f32_e32 v173, s77, v173\n" \
"v_mfma_f32_16x16x32_bf16 v[32:35], v[196:199], a[16:19], v[32:35]\n" \
"ds_read_b64_tr_b16 v[192:193], v228 offset:16896\n" \
"ds_read_b128 a[64:67], v234 offset:32768\n" \
"v_mfma_f32_16x16x32_bf16 v[36:39], v[196:199], a[20:23], v[36:39]\n" \
"s_add_u32 m0, s64, 32256\n" \
"ds_read_b64_tr_b16 v[194:195], v228 offset:17152\n" \
"ds_read_b128 a[68:71], v234 offset:34816\n" \
"v_mfma_f32_16x16x32_bf16 v[40:43], v[196:199], a[24:27], v[40:43]\n" \
"buffer_load_dwordx4 v224, %[ca], 0 offen offset:512 lds\n" \
"v_mfma_f32_16x16x32_bf16 v[44:47], v[196:199], a[28:31], v[44:47]\n" \
"s_waitcnt lgkmcnt(8)\n" \
"v_mul_f32_e32 v174, s77, v174\n" \
"v_mul_f32_e32 v175, s77, v175\n" \
"v_mfma_f32_16x16x32_bf16 v[16:19], v[200:203], a[16:19], v[16:19]\n" \
"ds_read_b64_tr_b16 v[196:197], v231 offset:16896\n" \
"ds_read_b128 a[72:75], v234 offset:36864\n" \
"v_mfma_f32_16x16x32_bf16 v[20:23], v[200:203], a[20:23], v[20:23]\n" \
"ds_read_b64_tr_b16 v[198:199], v231 offset:17152\n" \
"ds_read_b128 a[76:79], v234 offset:38912\n" \
"v_mfma_f32_16x16x32_bf16 v[24:27], v[200:203], a[24:27], v[24:27]\n" \
"buffer_load_dwordx4 v225, %[ca], s84 offen offset:1536 lds\n" \
"v_mfma_f32_16x16x32_bf16 v[28:31], v[200:203], a[28:31], v[28:31]\n" \
"v_cvt_pk_bf16_f32 v172, v172, v173\n" \
"v_cvt_pk_bf16_f32 v173, v174, v175\n" \
"v_mfma_f32_16x16x32_bf16 v[48:51], v[204:207], a[16:19], v[48:51]\n" \
"ds_read_b64_tr_b16 v[200:201], v228 offset:24576\n" \
"ds_read_b128 a[80:83], v235 offset:32768\n" \
"v_mfma_f32_16x16x32_bf16 v[52:55], v[204:207], a[20:23], v[52:55]\n" \
"ds_read_b64_tr_b16 v[202:203], v228 offset:24832\n" \
"ds_read_b128 a[84:87], v235 offset:34816\n" \
"v_mfma_f32_16x16x32_bf16 v[56:59], v[204:207], a[24:27], v[56:59]\n" \
"buffer_load_dwordx4 v224, %[ca], s85 offen offset:2560 lds\n" \
"v_mfma_f32_16x16x32_bf16 v[60:63], v[204:207], a[28:31], v[60:63]\n" \
"s_waitcnt lgkmcnt(12)\n" \
"v_mul_f32_e32 v80, s77, v80\n" \
"v_mul_f32_e32 v81, s77, v81\n" \
"v_mfma_f32_16x16x32_bf16 v[0:3], v[208:211], a[32:35], v[0:3]\n" \
"ds_read_b64_tr_b16 v[204:205], v231 offset:24576\n" \
"ds_read_b128 a[88:91], v235 offset:36864\n" \
"v_mfma_f32_16x16x32_bf16 v[4:7], v[208:211], a[36:39], v[4:7]\n" \
"ds_read_b64_tr_b16 v[206:207], v231 offset:24832\n" \
"ds_read_b128 a[92:95], v235 offset:38912\n" \
"v_mfma_f32_16x16x32_bf16 v[8:11], v[208:211], a[40:43], v[8:11]\n" \
"buffer_load_dwordx4 v225, %[ca], s86 offen offset:3584 lds\n" \
"v_mfma_f32_16x16x32_bf16 v[12:15], v[208:211], a[44:47], v[12:15]\n" \
"v_mul_f32_e32 v82, s77, v82\n" \
"v_mul_f32_e32 v83, s77, v83\n" \
"v_mfma_f32_16x16x32_bf16 v[32:35], v[212:215], a[32:35], v[32:35]\n" \
"ds_read_b64_tr_b16 v[208:209], v228 offset:25088\n" \
"v_cvt_pk_bf16_f32 v162, v80, v81\n" \
"v_mfma_f32_16x16x32_bf16 v[36:39], v[212:215], a[36:39], v[36:39]\n" \
"s_add_u32 m0, s64, 36352\n" \
"ds_read_b64_tr_b16 v[210:211], v228 offset:25344\n" \
"v_cvt_pk_bf16_f32 v163, v82, v83\n" \
"v_mfma_f32_16x16x32_bf16 v[40:43], v[212:215], a[40:43], v[40:43]\n" \
"buffer_load_dwordx4 v224, %[cb], 0 offen offset:512 lds\n" \
"v_mfma_f32_16x16x32_bf16 v[44:47], v[212:215], a[44:47], v[44:47]\n" \
"s_waitcnt lgkmcnt(11)\n" \
"v_mul_f32_e32 v84, s77, v84\n" \
"v_mul_f32_e32 v85, s77, v85\n" \
"v_mfma_f32_16x16x32_bf16 v[16:19], v[192:195], a[32:35], v[16:19]\n" \
"ds_read_b64_tr_b16 v[212:213], v231 offset:25088\n" \
"v_mul_f32_e32 v86, s77, v86\n" \
"v_mfma_f32_16x16x32_bf16 v[20:23], v[192:195], a[36:39], v[20:23]\n" \
"ds_read_b64_tr_b16 v[214:215], v231 offset:25344\n" \
"v_mul_f32_e32 v87, s77, v87\n" \
"v_mfma_f32_16x16x32_bf16 v[24:27], v[192:195], a[40:43], v[24:27]\n" \
"buffer_load_dwordx4 v225, %[cb], s84 offen offset:1536 lds\n" \
"v_mfma_f32_16x16x32_bf16 v[28:31], v[192:195], a[44:47], v[28:31]\n" \
"s_waitcnt vmcnt(18)\n" \
"s_barrier\n" \
"v_mfma_f32_16x16x32_bf16 v[48:51], v[196:199], a[32:35], v[48:51]\n" \
"ds_read_b64_tr_b16 v[192:193], v230 offset:0\n" \
"v_cvt_pk_bf16_f32 v166, v84, v85\n" \
"v_mfma_f32_16x16x32_bf16 v[52:55], v[196:199], a[36:39], v[52:55]\n" \
"ds_read_b64_tr_b16 v[194:195], v230 offset:256\n" \
"v_cvt_pk_bf16_f32 v167, v86, v87\n" \
"v_mfma_f32_16x16x32_bf16 v[56:59], v[196:199], a[40:43], v[56:59]\n" \
"buffer_load_dwordx4 v224, %[cb], s85 offen offset:2560 lds\n" \
"v_mfma_f32_16x16x32_bf16 v[60:63], v[196:199], a[44:47], v[60:63]\n" \
"s_waitcnt lgkmcnt(6)\n" \
"v_mul_f32_e32 v88, s77, v88\n" \
"v_mul_f32_e32 v89, s77, v89\n" \
"v_mfma_f32_16x16x32_bf16 v[0:3], v[200:203], a[48:51], v[0:3]\n" \
"ds_read_b64_tr_b16 v[196:197], v233 offset:0\n" \
"v_mul_f32_e32 v90, s77, v90\n" \
"v_mfma_f32_16x16x32_bf16 v[4:7], v[200:203], a[52:55], v[4:7]\n" \
"ds_read_b64_tr_b16 v[198:199], v233 offset:256\n" \
"v_mul_f32_e32 v91, s77, v91\n" \
"v_mfma_f32_16x16x32_bf16 v[8:11], v[200:203], a[56:59], v[8:11]\n" \
"buffer_load_dwordx4 v225, %[cb], s86 offen offset:3584 lds\n" \
"v_mfma_f32_16x16x32_bf16 v[12:15], v[200:203], a[60:63], v[12:15]\n" \
"v_cvt_pk_bf16_f32 v170, v88, v89\n" \
"v_cvt_pk_bf16_f32 v171, v90, v91\n" \
"v_mfma_f32_16x16x32_bf16 v[32:35], v[204:207], a[48:51], v[32:35]\n" \
"ds_read_b64_tr_b16 v[200:201], v230 offset:512\n" \
"v_mul_f32_e32 v92, s77, v92\n" \
"v_mfma_f32_16x16x32_bf16 v[36:39], v[204:207], a[52:55], v[36:39]\n" \
"s_add_u32 m0, s64, 64896\n" \
"ds_read_b64_tr_b16 v[202:203], v230 offset:768\n" \
"v_mul_f32_e32 v93, s77, v93\n" \
"v_mfma_f32_16x16x32_bf16 v[40:43], v[204:207], a[56:59], v[40:43]\n" \
"buffer_load_dwordx4 v224, %[ca], 0 offen offset:640 lds\n" \
"v_mfma_f32_16x16x32_bf16 v[44:47], v[204:207], a[60:63], v[44:47]\n" \
"s_waitcnt lgkmcnt(6)\n" \
"v_mul_f32_e32 v94, s77, v94\n" \
"v_mul_f32_e32 v95, s77, v95\n" \
"v_mfma_f32_16x16x32_bf16 v[16:19], v[208:211], a[48:51], v[16:19]\n" \
"ds_read_b64_tr_b16 v[204:205], v233 offset:512\n" \
"v_cvt_pk_bf16_f32 v174, v92, v93\n" \
"v_mfma_f32_16x16x32_bf16 v[20:23], v[208:211], a[52:55], v[20:23]\n" \
"ds_read_b64_tr_b16 v[206:207], v233 offset:768\n" \
"v_cvt_pk_bf16_f32 v175, v94, v95\n" \
"v_mfma_f32_16x16x32_bf16 v[24:27], v[208:211], a[56:59], v[24:27]\n" \
"buffer_load_dwordx4 v225, %[ca], s84 offen offset:1664 lds\n" \
"v_mfma_f32_16x16x32_bf16 v[28:31], v[208:211], a[60:63], v[28:31]\n" \
"v_mul_f32_e32 v176, s77, v176\n" \
"v_mul_f32_e32 v177, s77, v177\n" \
"v_mfma_f32_16x16x32_bf16 v[48:51], v[212:215], a[48:51], v[48:51]\n" \
"ds_read_b64_tr_b16 v[208:209], v230 offset:8192\n" \
"v_mul_f32_e32 v178, s77, v178\n" \
"v_mfma_f32_16x16x32_bf16 v[52:55], v[212:215], a[52:55], v[52:55]\n" \
"ds_read_b64_tr_b16 v[210:211], v230 offset:8448\n" \
"v_mul_f32_e32 v179, s77, v179\n" \
"v_mfma_f32_16x16x32_bf16 v[56:59], v[212:215], a[56:59], v[56:59]\n" \
"buffer_load_dwordx4 v224, %[ca], s85 offen offset:2688 lds\n" \
"v_mfma_f32_16x16x32_bf16 v[60:63], v[212:215], a[60:63], v[60:63]\n" \
"s_waitcnt lgkmcnt(6)\n" \
"v_cvt_pk_bf16_f32 v176, v176, v177\n" \
"v_cvt_pk_bf16_f32 v177, v178, v179\n" \
"v_mfma_f32_16x16x32_bf16 v[0:3], v[192:195], a[64:67], v[0:3]\n" \
"ds_read_b64_tr_b16 v[212:213], v233 offset:8192\n" \
"v_mul_f32_e32 v180, s77, v180\n" \
"v_mfma_f32_16x16x32_bf16 v[4:7], v[192:195], a[68:71], v[4:7]\n" \
"ds_read_b64_tr_b16 v[214:215], v233 offset:8448\n" \
"v_mul_f32_e32 v181, s77, v181\n" \
"v_mfma_f32_16x16x32_bf16 v[8:11], v[192:195], a[72:75], v[8:11]\n" \
"buffer_load_dwordx4 v225, %[ca], s86 offen offset:3712 lds\n" \
"v_mfma_f32_16x16x32_bf16 v[12:15], v[192:195], a[76:79], v[12:15]\n" \
"s_waitcnt vmcnt(16)\n" \
"v_mul_f32_e32 v182, s77, v182\n" \
"v_mul_f32_e32 v183, s77, v183\n" \
"v_mfma_f32_16x16x32_bf16 v[44:47], v[196:199], a[76:79], v[44:47]\n" \
"ds_read_b64_tr_b16 v[192:193], v230 offset:8704\n" \
"ds_read_b128 a[96:99], v238 offset:0\n" \
"v_mfma_f32_16x16x32_bf16 v[40:43], v[196:199], a[72:75], v[40:43]\n" \
"s_add_u32 m0, s64, 68992\n" \
"ds_read_b64_tr_b16 v[194:195], v230 offset:8960\n" \
"ds_read_b128 a[100:103], v238 offset:2048\n" \
"v_mfma_f32_16x16x32_bf16 v[36:39], v[196:199], a[68:71], v[36:39]\n" \
"buffer_load_dwordx4 v224, %[cb], 0 offen offset:640 lds\n" \
"v_mfma_f32_16x16x32_bf16 v[32:35], v[196:199], a[64:67], v[32:35]\n" \
"s_waitcnt lgkmcnt(8)\n" \
"v_cvt_pk_bf16_f32 v180, v180, v181\n" \
"v_cvt_pk_bf16_f32 v181, v182, v183\n" \
"v_mfma_f32_16x16x32_bf16 v[16:19], v[200:203], a[64:67], v[16:19]\n" \
"ds_read_b64_tr_b16 v[196:197], v233 offset:8704\n" \
"ds_read_b128 a[104:107], v238 offset:4096\n" \
"v_mfma_f32_16x16x32_bf16 v[20:23], v[200:203], a[68:71], v[20:23]\n" \
"ds_read_b64_tr_b16 v[198:199], v233 offset:8960\n" \
"ds_read_b128 a[108:111], v238 offset:6144\n" \
"v_mfma_f32_16x16x32_bf16 v[24:27], v[200:203], a[72:75], v[24:27]\n" \
"buffer_load_dwordx4 v225, %[cb], s84 offen offset:1664 lds\n" \
"v_mfma_f32_16x16x32_bf16 v[28:31], v[200:203], a[76:79], v[28:31]\n" \
"v_mul_f32_e32 v184, s77, v184\n" \
"v_mul_f32_e32 v185, s77, v185\n" \
"v_mfma_f32_16x16x32_bf16 v[60:63], v[204:207], a[76:79], v[60:63]\n" \
"ds_read_b64_tr_b16 v[200:201], v228 offset:1024\n" \
"ds_read_b128 a[112:115], v239 offset:0\n" \
"v_mfma_f32_16x16x32_bf16 v[56:59], v[204:207], a[72:75], v[56:59]\n" \
"ds_read_b64_tr_b16 v[202:203], v228 offset:1280\n" \
"ds_read_b128 a[116:119], v239 offset:2048\n" \
"v_mfma_f32_16x16x32_bf16 v[52:55], v[204:207], a[68:71], v[52:55]\n" \
"buffer_load_dwordx4 v224, %[cb], s85 offen offset:2688 lds\n" \
"v_mfma_f32_16x16x32_bf16 v[48:51], v[204:207], a[64:67], v[48:51]\n" \
"s_waitcnt lgkmcnt(12)\n" \
"v_mul_f32_e32 v186, s77, v186\n" \
"v_mul_f32_e32 v187, s77, v187\n" \
"v_mfma_f32_16x16x32_bf16 v[0:3], v[208:211], a[80:83], v[0:3]\n" \
"ds_read_b64_tr_b16 v[204:205], v231 offset:1024\n" \
"ds_read_b128 a[120:123], v239 offset:4096\n" \
"v_mfma_f32_16x16x32_bf16 v[4:7], v[208:211], a[84:87], v[4:7]\n" \
"ds_read_b64_tr_b16 v[206:207], v231 offset:1280\n" \
"ds_read_b128 a[124:127], v239 offset:6144\n" \
"v_mfma_f32_16x16x32_bf16 v[8:11], v[208:211], a[88:91], v[8:11]\n" \
"buffer_load_dwordx4 v225, %[cb], s86 offen offset:3712 lds\n" \
"v_mfma_f32_16x16x32_bf16 v[12:15], v[208:211], a[92:95], v[12:15]\n" \
"v_cvt_pk_bf16_f32 v184, v184, v185\n" \
"v_cvt_pk_bf16_f32 v185, v186, v187\n" \
"v_mfma_f32_16x16x32_bf16 v[44:47], v[212:215], a[92:95], v[44:47]\n" \
"ds_read_b64_tr_b16 v[208:209], v228 offset:1536\n" \
"v_mul_f32_e32 v188, s77, v188\n" \
"v_mfma_f32_16x16x32_bf16 v[40:43], v[212:215], a[88:91], v[40:43]\n" \
"ds_read_b64_tr_b16 v[210:211], v228 offset:1792\n" \
"v_mul_f32_e32 v189, s77, v189\n" \
"v_mfma_f32_16x16x32_bf16 v[36:39], v[212:215], a[84:87], v[36:39]\n" \
"buffer_load_dword v247, v218, %[wo], 0 offen nt\n" \
"v_mfma_f32_16x16x32_bf16 v[32:35], v[212:215], a[80:83], v[32:35]\n" \
"s_waitcnt lgkmcnt(11)\n" \
"v_mul_f32_e32 v190, s77, v190\n" \
"v_mul_f32_e32 v191, s77, v191\n" \
"v_mfma_f32_16x16x32_bf16 v[16:19], v[192:195], a[80:83], v[16:19]\n" \
"ds_read_b64_tr_b16 v[212:213], v231 offset:1536\n" \
"v_cvt_pk_bf16_f32 v188, v188, v189\n" \
"v_mfma_f32_16x16x32_bf16 v[20:23], v[192:195], a[84:87], v[20:23]\n" \
"ds_read_b64_tr_b16 v[214:215], v231 offset:1792\n" \
"v_cvt_pk_bf16_f32 v189, v190, v191\n" \
"v_mfma_f32_16x16x32_bf16 v[24:27], v[192:195], a[88:91], v[24:27]\n" \
"buffer_load_dword v247, v218, %[wo], 0 offen\n" \
"v_mfma_f32_16x16x32_bf16 v[28:31], v[192:195], a[92:95], v[28:31]\n" \
"v_mul_f32_e32 v112, s77, v112\n" \
"v_mul_f32_e32 v113, s77, v113\n" \
"v_mfma_f32_16x16x32_bf16 v[60:63], v[196:199], a[92:95], v[60:63]\n" \
"ds_read_b64_tr_b16 v[192:193], v228 offset:9216\n" \
"v_mul_f32_e32 v114, s77, v114\n" \
"v_mfma_f32_16x16x32_bf16 v[56:59], v[196:199], a[88:91], v[56:59]\n" \
"ds_read_b64_tr_b16 v[194:195], v228 offset:9472\n" \
"v_mul_f32_e32 v115, s77, v115\n" \
"v_mfma_f32_16x16x32_bf16 v[52:55], v[196:199], a[84:87], v[52:55]\n" \
"v_cvt_pk_bf16_f32 v178, v112, v113\n" \
"v_cvt_pk_bf16_f32 v179, v114, v115\n" \
"v_mfma_f32_16x16x32_bf16 v[48:51], v[196:199], a[80:83], v[48:51]\n" \
"s_waitcnt lgkmcnt(6)\n" \
"v_mul_f32_e32 v116, s77, v116\n" \
"v_mul_f32_e32 v117, s77, v117\n" \
"v_mfma_f32_16x16x32_bf16 v[64:67], v[200:203], a[0:3], 0\n" \
"ds_read_b64_tr_b16 v[196:197], v231 offset:9216\n" \
"v_mul_f32_e32 v118, s77, v118\n" \
"v_mfma_f32_16x16x32_bf16 v[68:71], v[200:203], a[4:7], 0\n" \
"ds_read_b64_tr_b16 v[198:199], v231 offset:9472\n" \
"v_mul_f32_e32 v119, s77, v119\n" \
"v_mfma_f32_16x16x32_bf16 v[72:75], v[200:203], a[8:11], 0\n" \
"v_cvt_pk_bf16_f32 v182, v116, v117\n" \
"v_cvt_pk_bf16_f32 v183, v118, v119\n" \
"v_mfma_f32_16x16x32_bf16 v[76:79], v[200:203], a[12:15], 0\n" \
"v_mul_f32_e32 v120, s77, v120\n" \
"v_mul_f32_e32 v121, s77, v121\n" \
"v_mfma_f32_16x16x32_bf16 v[108:111], v[204:207], a[12:15], 0\n" \
"ds_read_b64_tr_b16 v[200:201], v228 offset:9728\n" \
"v_mul_f32_e32 v122, s77, v122\n" \
"v_mfma_f32_16x16x32_bf16 v[104:107], v[204:207], a[8:11], 0\n" \
"ds_read_b64_tr_b16 v[202:203], v228 offset:9984\n" \
"v_mul_f32_e32 v123, s77, v123\n" \
"v_mfma_f32_16x16x32_bf16 v[100:103], v[204:207], a[4:7], 0\n" \
"v_cvt_pk_bf16_f32 v186, v120, v121\n" \
"v_cvt_pk_bf16_f32 v187, v122, v123\n" \
"v_mfma_f32_16x16x32_bf16 v[96:99], v[204:207], a[0:3], 0\n" \
"s_waitcnt lgkmcnt(6)\n" \
"v_mul_f32_e32 v124, s77, v124\n" \
"v_mul_f32_e32 v125, s77, v125\n" \
"v_mfma_f32_16x16x32_bf16 v[80:83], v[208:211], a[0:3], 0\n" \
"ds_read_b64_tr_b16 v[204:205], v231 offset:9728\n" \
"v_mul_f32_e32 v126, s77, v126\n" \
"v_mfma_f32_16x16x32_bf16 v[84:87], v[208:211], a[4:7], 0\n" \
"ds_read_b64_tr_b16 v[206:207], v231 offset:9984\n" \
"v_mul_f32_e32 v127, s77, v127\n" \
"v_mfma_f32_16x16x32_bf16 v[88:91], v[208:211], a[8:11], 0\n" \
"v_cvt_pk_bf16_f32 v190, v124, v125\n" \
"v_cvt_pk_bf16_f32 v191, v126, v127\n" \
"v_mfma_f32_16x16x32_bf16 v[92:95], v[208:211], a[12:15], 0\n" \
"v_mfma_f32_16x16x32_bf16 v[124:127], v[212:215], a[12:15], 0\n" \
"ds_read_b64_tr_b16 v[208:209], v228 offset:17408\n" \
"v_mfma_f32_16x16x32_bf16 v[120:123], v[212:215], a[8:11], 0\n" \
"ds_read_b64_tr_b16 v[210:211], v228 offset:17664\n" \
"v_mfma_f32_16x16x32_bf16 v[116:119], v[212:215], a[4:7], 0\n" \
"v_mfma_f32_16x16x32_bf16 v[112:115], v[212:215], a[0:3], 0\n" \
"s_waitcnt lgkmcnt(6)\n" \
"v_mfma_f32_16x16x32_bf16 v[64:67], v[192:195], a[16:19], v[64:67]\n" \
"ds_read_b64_tr_b16 v[212:213], v231 offset:17408\n" \
"v_mfma_f32_16x16x32_bf16 v[68:71], v[192:195], a[20:23], v[68:71]\n" \
"ds_read_b64_tr_b16 v[214:215], v231 offset:17664\n" \
"v_mfma_f32_16x16x32_bf16 v[72:75], v[192:195], a[24:27], v[72:75]\n" \
"v_mfma_f32_16x16x32_bf16 v[76:79], v[192:195], a[28:31], v[76:79]\n" \
"s_waitcnt vmcnt(10)\n" \
"v_mfma_f32_16x16x32_bf16 v[108:111], v[196:199], a[28:31], v[108:111]\n" \
"ds_read_b64_tr_b16 v[192:193], v228 offset:17920\n" \
"ds_read_b128 a[128:131], v234 offset:0\n" \
"v_mfma_f32_16x16x32_bf16 v[104:107], v[196:199], a[24:27], v[104:107]\n" \
"ds_read_b64_tr_b16 v[194:195], v228 offset:18176\n" \
"ds_read_b128 a[132:135], v234 offset:2048\n" \
"v_mfma_f32_16x16x32_bf16 v[100:103], v[196:199], a[20:23], v[100:103]\n" \
"v_mfma_f32_16x16x32_bf16 v[96:99], v[196:199], a[16:19], v[96:99]\n" \
"s_waitcnt lgkmcnt(8)\n" \
"v_mfma_f32_16x16x32_bf16 v[80:83], v[200:203], a[16:19], v[80:83]\n" \
"ds_read_b64_tr_b16 v[196:197], v231 offset:17920\n" \
"ds_read_b128 a[136:139], v234 offset:4096\n" \
"v_mfma_f32_16x16x32_bf16 v[84:87], v[200:203], a[20:23], v[84:87]\n" \
"ds_read_b64_tr_b16 v[198:199], v231 offset:18176\n" \
"ds_read_b128 a[140:143], v234 offset:6144\n" \
"v_mfma_f32_16x16x32_bf16 v[88:91], v[200:203], a[24:27], v[88:91]\n" \
"v_mfma_f32_16x16x32_bf16 v[92:95], v[200:203], a[28:31], v[92:95]\n" \
"s_barrier\n" \
"v_mfma_f32_16x16x32_bf16 v[124:127], v[204:207], a[28:31], v[124:127]\n" \
"ds_read_b64_tr_b16 v[200:201], v228 offset:25600\n" \
"ds_read_b128 a[144:147], v235 offset:0\n" \
"v_mfma_f32_16x16x32_bf16 v[120:123], v[204:207], a[24:27], v[120:123]\n" \
"ds_read_b64_tr_b16 v[202:203], v228 offset:25856\n" \
"ds_read_b128 a[148:151], v235 offset:2048\n" \
"v_mfma_f32_16x16x32_bf16 v[116:119], v[204:207], a[20:23], v[116:119]\n" \
"v_mfma_f32_16x16x32_bf16 v[112:115], v[204:207], a[16:19], v[112:115]\n" \
"s_waitcnt lgkmcnt(12)\n" \
"v_mfma_f32_16x16x32_bf16 v[64:67], v[208:211], a[32:35], v[64:67]\n" \
"s_add_u32 m0, %[w4k], 0\n" \
"ds_read_b64_tr_b16 v[204:205], v231 offset:25600\n" \
"ds_read_b128 a[152:155], v235 offset:4096\n" \
"v_mfma_f32_16x16x32_bf16 v[68:71], v[208:211], a[36:39], v[68:71]\n" \
"s_mov_b32 s79, 2097152\n" \
"ds_read_b64_tr_b16 v[206:207], v231 offset:25856\n" \
"ds_read_b128 a[156:159], v235 offset:6144\n" \
"v_mfma_f32_16x16x32_bf16 v[72:75], v[208:211], a[40:43], v[72:75]\n" \
"buffer_load_dwordx4 v226, %[wk], s79 offen offset:0 lds\n" \
"v_mfma_f32_16x16x32_bf16 v[76:79], v[208:211], a[44:47], v[76:79]\n" \
"v_mfma_f32_16x16x32_bf16 v[108:111], v[212:215], a[44:47], v[108:111]\n" \
"ds_read_b64_tr_b16 v[208:209], v228 offset:26112\n" \
"v_mfma_f32_16x16x32_bf16 v[104:107], v[212:215], a[40:43], v[104:107]\n" \
"ds_read_b64_tr_b16 v[210:211], v228 offset:26368\n" \
"v_mfma_f32_16x16x32_bf16 v[100:103], v[212:215], a[36:39], v[100:103]\n" \
"buffer_load_dwordx4 v227, %[wk], s79 offen offset:1024 lds\n" \
"v_mfma_f32_16x16x32_bf16 v[96:99], v[212:215], a[32:35], v[96:99]\n" \
"s_waitcnt lgkmcnt(11)\n" \
"v_mfma_f32_16x16x32_bf16 v[80:83], v[192:195], a[32:35], v[80:83]\n" \
"ds_read_b64_tr_b16 v[212:213], v231 offset:26112\n" \
"v_mfma_f32_16x16x32_bf16 v[84:87], v[192:195], a[36:39], v[84:87]\n" \
"ds_read_b64_tr_b16 v[214:215], v231 offset:26368\n" \
"v_mfma_f32_16x16x32_bf16 v[88:91], v[192:195], a[40:43], v[88:91]\n" \
"buffer_load_dwordx4 v254, %[wk], s79 offen offset:2048 lds\n" \
"v_mfma_f32_16x16x32_bf16 v[92:95], v[192:195], a[44:47], v[92:95]\n" \
"v_mfma_f32_16x16x32_bf16 v[124:127], v[196:199], a[44:47], v[124:127]\n" \
"ds_read_b64_tr_b16 v[192:193], v230 offset:1024\n" \
"v_mfma_f32_16x16x32_bf16 v[120:123], v[196:199], a[40:43], v[120:123]\n" \
"ds_read_b64_tr_b16 v[194:195], v230 offset:1280\n" \
"v_mfma_f32_16x16x32_bf16 v[116:119], v[196:199], a[36:39], v[116:119]\n" \
"buffer_load_dwordx4 v236, %[wk], s79 offen offset:3072 lds\n" \
"v_mfma_f32_16x16x32_bf16 v[112:115], v[196:199], a[32:35], v[112:115]\n" \
"s_waitcnt lgkmcnt(6)\n" \
"v_mfma_f32_16x16x32_bf16 v[64:67], v[200:203], a[48:51], v[64:67]\n" \
"ds_read_b64_tr_b16 v[196:197], v233 offset:1024\n" \
"v_mfma_f32_16x16x32_bf16 v[68:71], v[200:203], a[52:55], v[68:71]\n" \
"s_add_u32 m0, s64, 31872\n" \
"ds_read_b64_tr_b16 v[198:199], v233 offset:1280\n" \
"v_mfma_f32_16x16x32_bf16 v[72:75], v[200:203], a[56:59], v[72:75]\n" \
"buffer_load_dwordx4 v224, %[ca], 0 offen offset:896 lds\n" \
"v_mfma_f32_16x16x32_bf16 v[76:79], v[200:203], a[60:63], v[76:79]\n" \
"s_waitcnt vmcnt(7)\n" \
"v_mfma_f32_16x16x32_bf16 v[108:111], v[204:207], a[60:63], v[108:111]\n" \
"ds_read_b64_tr_b16 v[200:201], v230 offset:1536\n" \
"ds_read_b128 a[160:163], v234 offset:32768\n" \
"v_mfma_f32_16x16x32_bf16 v[104:107], v[204:207], a[56:59], v[104:107]\n" \
"ds_read_b64_tr_b16 v[202:203], v230 offset:1792\n" \
"ds_read_b128 a[164:167], v234 offset:34816\n" \
"v_mfma_f32_16x16x32_bf16 v[100:103], v[204:207], a[52:55], v[100:103]\n" \
"buffer_load_dwordx4 v225, %[ca], s84 offen offset:1920 lds\n" \
"v_mfma_f32_16x16x32_bf16 v[96:99], v[204:207], a[48:51], v[96:99]\n" \
"s_waitcnt lgkmcnt(8)\n" \
"v_mfma_f32_16x16x32_bf16 v[80:83], v[208:211], a[48:51], v[80:83]\n" \
"ds_read_b64_tr_b16 v[204:205], v233 offset:1536\n" \
"ds_read_b128 a[168:171], v234 offset:36864\n" \
"v_mfma_f32_16x16x32_bf16 v[84:87], v[208:211], a[52:55], v[84:87]\n" \
"ds_read_b64_tr_b16 v[206:207], v233 offset:1792\n" \
"ds_read_b128 a[172:175], v234 offset:38912\n" \
"v_mfma_f32_16x16x32_bf16 v[88:91], v[208:211], a[56:59], v[88:91]\n" \
"buffer_load_dwordx4 v224, %[ca], s85 offen offset:2944 lds\n" \
"v_mfma_f32_16x16x32_bf16 v[92:95], v[208:211], a[60:63], v[92:95]\n" \
"v_mfma_f32_16x16x32_bf16 v[124:127], v[212:215], a[60:63], v[124:127]\n" \
"ds_read_b64_tr_b16 v[208:209], v230 offset:9216\n" \
"ds_read_b128 a[176:179], v235 offset:32768\n" \
"v_mfma_f32_16x16x32_bf16 v[120:123], v[212:215], a[56:59], v[120:123]\n" \
"ds_read_b64_tr_b16 v[210:211], v230 offset:9472\n" \
"ds_read_b128 a[180:183], v235 offset:34816\n" \
"v_mfma_f32_16x16x32_bf16 v[116:119], v[212:215], a[52:55], v[116:119]\n" \
"buffer_load_dwordx4 v225, %[ca], s86 offen offset:3968 lds\n" \
"v_mfma_f32_16x16x32_bf16 v[112:115], v[212:215], a[48:51], v[112:115]\n" \
"s_waitcnt lgkmcnt(12)\n" \
"v_mfma_f32_16x16x32_bf16 v[64:67], v[192:195], a[64:67], v[64:67]\n" \
"ds_read_b64_tr_b16 v[212:213], v233 offset:9216\n" \
"ds_read_b128 a[184:187], v235 offset:36864\n" \
"v_mfma_f32_16x16x32_bf16 v[68:71], v[192:195], a[68:71], v[68:71]\n" \
"s_add_u32 m0, s64, 35968\n" \
"ds_read_b64_tr_b16 v[214:215], v233 offset:9472\n" \
"ds_read_b128 a[188:191], v235 offset:38912\n" \
"v_mfma_f32_16x16x32_bf16 v[72:75], v[192:195], a[72:75], v[72:75]\n" \
"buffer_load_dwordx4 v224, %[cb], 0 offen offset:896 lds\n" \
"v_mfma_f32_16x16x32_bf16 v[76:79], v[192:195], a[76:79], v[76:79]\n" \
"v_mfma_f32_16x16x32_bf16 v[108:111], v[196:199], a[76:79], v[108:111]\n" \
"ds_read_b64_tr_b16 v[192:193], v230 offset:9728\n" \
"v_mfma_f32_16x16x32_bf16 v[104:107], v[196:199], a[72:75], v[104:107]\n" \
"ds_read_b64_tr_b16 v[194:195], v230 offset:9984\n" \
"v_mfma_f32_16x16x32_bf16 v[100:103], v[196:199], a[68:71], v[100:103]\n" \
"buffer_load_dwordx4 v225, %[cb], s84 offen offset:1920 lds\n" \
"v_mfma_f32_16x16x32_bf16 v[96:99], v[196:199], a[64:67], v[96:99]\n" \
"s_waitcnt lgkmcnt(11)\n" \
"v_mfma_f32_16x16x32_bf16 v[80:83], v[200:203], a[64:67], v[80:83]\n" \
"ds_read_b64_tr_b16 v[196:197], v233 offset:9728\n" \
"v_mfma_f32_16x16x32_bf16 v[84:87], v[200:203], a[68:71], v[84:87]\n" \
"ds_read_b64_tr_b16 v[198:199], v233 offset:9984\n" \
"v_mfma_f32_16x16x32_bf16 v[88:91], v[200:203], a[72:75], v[88:91]\n" \
"buffer_load_dwordx4 v224, %[cb], s85 offen offset:2944 lds\n" \
"v_mfma_f32_16x16x32_bf16 v[92:95], v[200:203], a[76:79], v[92:95]\n" \
"s_barrier\n" \
"v_mfma_f32_16x16x32_bf16 v[124:127], v[204:207], a[76:79], v[124:127]\n" \
"ds_read_b64_tr_b16 v[200:201], v230 offset:16384\n" \
"v_mfma_f32_16x16x32_bf16 v[120:123], v[204:207], a[72:75], v[120:123]\n" \
"ds_read_b64_tr_b16 v[202:203], v230 offset:16640\n" \
"v_mfma_f32_16x16x32_bf16 v[116:119], v[204:207], a[68:71], v[116:119]\n" \
"buffer_load_dwordx4 v225, %[cb], s86 offen offset:3968 lds\n" \
"v_mfma_f32_16x16x32_bf16 v[112:115], v[204:207], a[64:67], v[112:115]\n" \
"s_waitcnt lgkmcnt(6)\n" \
"v_mfma_f32_16x16x32_bf16 v[64:67], v[208:211], a[80:83], v[64:67]\n" \
"s_add_u32 m0, %[w4k], 16384\n" \
"ds_read_b64_tr_b16 v[204:205], v233 offset:16384\n" \
"v_mfma_f32_16x16x32_bf16 v[68:71], v[208:211], a[84:87], v[68:71]\n" \
"s_mov_b32 s79, 2621440\n" \
"ds_read_b64_tr_b16 v[206:207], v233 offset:16640\n" \
"v_mfma_f32_16x16x32_bf16 v[72:75], v[208:211], a[88:91], v[72:75]\n" \
"buffer_load_dwordx4 v226, %[wk], s79 offen offset:0 lds\n" \
"v_mfma_f32_16x16x32_bf16 v[76:79], v[208:211], a[92:95], v[76:79]\n" \
"v_mfma_f32_16x16x32_bf16 v[108:111], v[212:215], a[92:95], v[108:111]\n" \
"ds_read_b64_tr_b16 v[208:209], v230 offset:16896\n" \
"v_mfma_f32_16x16x32_bf16 v[104:107], v[212:215], a[88:91], v[104:107]\n" \
"ds_read_b64_tr_b16 v[210:211], v230 offset:17152\n" \
"v_mfma_f32_16x16x32_bf16 v[100:103], v[212:215], a[84:87], v[100:103]\n" \
"buffer_load_dwordx4 v227, %[wk], s79 offen offset:1024 lds\n" \
"v_mfma_f32_16x16x32_bf16 v[96:99], v[212:215], a[80:83], v[96:99]\n" \
"s_waitcnt lgkmcnt(6)\n" \
"v_mfma_f32_16x16x32_bf16 v[80:83], v[192:195], a[80:83], v[80:83]\n" \
"ds_read_b64_tr_b16 v[212:213], v233 offset:16896\n" \
"v_mfma_f32_16x16x32_bf16 v[84:87], v[192:195], a[84:87], v[84:87]\n" \
"ds_read_b64_tr_b16 v[214:215], v233 offset:17152\n" \
"v_mfma_f32_16x16x32_bf16 v[88:91], v[192:195], a[88:91], v[88:91]\n" \
"buffer_load_dwordx4 v254, %[wk], s79 offen offset:2048 lds\n" \
"v_mfma_f32_16x16x32_bf16 v[92:95], v[192:195], a[92:95], v[92:95]\n" \
"s_barrier\n" \
"v_mfma_f32_16x16x32_bf16 v[124:127], v[196:199], a[92:95], v[124:127]\n" \
"ds_read_b64_tr_b16 v[192:193], v230 offset:17408\n" \
"v_mfma_f32_16x16x32_bf16 v[120:123], v[196:199], a[88:91], v[120:123]\n" \
"ds_read_b64_tr_b16 v[194:195], v230 offset:17664\n" \
"v_mfma_f32_16x16x32_bf16 v[116:119], v[196:199], a[84:87], v[116:119]\n" \
"buffer_load_dwordx4 v236, %[wk], s79 offen offset:3072 lds\n" \
"v_mfma_f32_16x16x32_bf16 v[112:115], v[196:199], a[80:83], v[112:115]\n" \
"s_waitcnt lgkmcnt(6)\n" \
"v_mfma_f32_16x16x32_bf16 v[0:3], v[200:203], a[96:99], v[0:3]\n" \
"ds_read_b64_tr_b16 v[196:197], v233 offset:17408\n" \
"v_mfma_f32_16x16x32_bf16 v[4:7], v[200:203], a[100:103], v[4:7]\n" \
"s_add_u32 m0, s64, 97536\n" \
"ds_read_b64_tr_b16 v[198:199], v233 offset:17664\n" \
"v_mfma_f32_16x16x32_bf16 v[8:11], v[200:203], a[104:107], v[8:11]\n" \
"buffer_load_dwordx4 v224, %[ca], 0 offen offset:768 lds\n" \
"v_mfma_f32_16x16x32_bf16 v[12:15], v[200:203], a[108:111], v[12:15]\n" \
"v_mfma_f32_16x16x32_bf16 v[44:47], v[204:207], a[108:111], v[44:47]\n" \
"ds_read_b64_tr_b16 v[200:201], v230 offset:17920\n" \
"v_mfma_f32_16x16x32_bf16 v[40:43], v[204:207], a[104:107], v[40:43]\n" \
"ds_read_b64_tr_b16 v[202:203], v230 offset:18176\n" \
"v_mfma_f32_16x16x32_bf16 v[36:39], v[204:207], a[100:103], v[36:39]\n" \
"buffer_load_dwordx4 v225, %[ca], s84 offen offset:1792 lds\n" \
"v_mfma_f32_16x16x32_bf16 v[32:35], v[204:207], a[96:99], v[32:35]\n" \
"s_waitcnt lgkmcnt(6)\n" \
"v_mfma_f32_16x16x32_bf16 v[16:19], v[208:211], a[96:99], v[16:19]\n" \
"ds_read_b64_tr_b16 v[204:205], v233 offset:17920\n" \
"v_mfma_f32_16x16x32_bf16 v[20:23], v[208:211], a[100:103], v[20:23]\n" \
"ds_read_b64_tr_b16 v[206:207], v233 offset:18176\n" \
"v_mfma_f32_16x16x32_bf16 v[24:27], v[208:211], a[104:107], v[24:27]\n" \
"buffer_load_dwordx4 v224, %[ca], s85 offen offset:2816 lds\n" \
"v_mfma_f32_16x16x32_bf16 v[28:31], v[208:211], a[108:111], v[28:31]\n" \
"v_mfma_f32_16x16x32_bf16 v[60:63], v[212:215], a[108:111], v[60:63]\n" \
"ds_read_b64_tr_b16 v[208:209], v230 offset:24576\n" \
"v_mfma_f32_16x16x32_bf16 v[56:59], v[212:215], a[104:107], v[56:59]\n" \
"ds_read_b64_tr_b16 v[210:211], v230 offset:24832\n" \
"v_mfma_f32_16x16x32_bf16 v[52:55], v[212:215], a[100:103], v[52:55]\n" \
"buffer_load_dwordx4 v225, %[ca], s86 offen offset:3840 lds\n" \
"v_mfma_f32_16x16x32_bf16 v[48:51], v[212:215], a[96:99], v[48:51]\n" \
"s_waitcnt lgkmcnt(6)\n" \
"v_mfma_f32_16x16x32_bf16 v[64:67], v[192:195], a[96:99], v[64:67]\n" \
"ds_read_b64_tr_b16 v[212:213], v233 offset:24576\n" \
"v_mfma_f32_16x16x32_bf16 v[68:71], v[192:195], a[100:103], v[68:71]\n" \
"s_add_u32 m0, s64, 101632\n" \
"ds_read_b64_tr_b16 v[214:215], v233 offset:24832\n" \
"v_mfma_f32_16x16x32_bf16 v[72:75], v[192:195], a[104:107], v[72:75]\n" \
"buffer_load_dwordx4 v224, %[cb], 0 offen offset:768 lds\n" \
"v_mfma_f32_16x16x32_bf16 v[76:79], v[192:195], a[108:111], v[76:79]\n" \
"v_mfma_f32_16x16x32_bf16 v[108:111], v[196:199], a[108:111], v[108:111]\n" \
"ds_read_b64_tr_b16 v[192:193], v230 offset:25088\n" \
"v_mfma_f32_16x16x32_bf16 v[104:107], v[196:199], a[104:107], v[104:107]\n" \
"ds_read_b64_tr_b16 v[194:195], v230 offset:25344\n" \
"v_mfma_f32_16x16x32_bf16 v[100:103], v[196:199], a[100:103], v[100:103]\n" \
"buffer_load_dwordx4 v225, %[cb], s84 offen offset:1792 lds\n" \
"v_mfma_f32_16x16x32_bf16 v[96:99], v[196:199], a[96:99], v[96:99]\n" \
"s_waitcnt lgkmcnt(6)\n" \
"v_mfma_f32_16x16x32_bf16 v[80:83], v[200:203], a[96:99], v[80:83]\n" \
"ds_read_b64_tr_b16 v[196:197], v233 offset:25088\n" \
"v_mfma_f32_16x16x32_bf16 v[84:87], v[200:203], a[100:103], v[84:87]\n" \
"ds_read_b64_tr_b16 v[198:199], v233 offset:25344\n" \
"v_mfma_f32_16x16x32_bf16 v[88:91], v[200:203], a[104:107], v[88:91]\n" \
"buffer_load_dwordx4 v224, %[cb], s85 offen offset:2816 lds\n" \
"v_mfma_f32_16x16x32_bf16 v[92:95], v[200:203], a[108:111], v[92:95]\n" \
"v_mfma_f32_16x16x32_bf16 v[124:127], v[204:207], a[108:111], v[124:127]\n" \
"ds_read_b64_tr_b16 v[200:201], v230 offset:25600\n" \
"v_mfma_f32_16x16x32_bf16 v[120:123], v[204:207], a[104:107], v[120:123]\n" \
"ds_read_b64_tr_b16 v[202:203], v230 offset:25856\n" \
"v_mfma_f32_16x16x32_bf16 v[116:119], v[204:207], a[100:103], v[116:119]\n" \
"buffer_load_dwordx4 v225, %[cb], s86 offen offset:3840 lds\n" \
"v_mfma_f32_16x16x32_bf16 v[112:115], v[204:207], a[96:99], v[112:115]\n" \
"s_waitcnt lgkmcnt(6)\n" \
"v_mfma_f32_16x16x32_bf16 v[0:3], v[208:211], a[112:115], v[0:3]\n" \
"s_add_u32 m0, %[w4k], 131072\n" \
"ds_read_b64_tr_b16 v[204:205], v233 offset:25600\n" \
"v_mfma_f32_16x16x32_bf16 v[4:7], v[208:211], a[116:119], v[4:7]\n" \
"s_mov_b32 s79, 3145728\n" \
"ds_read_b64_tr_b16 v[206:207], v233 offset:25856\n" \
"v_mfma_f32_16x16x32_bf16 v[8:11], v[208:211], a[120:123], v[8:11]\n" \
"buffer_load_dwordx4 v226, %[wk], s79 offen offset:0 lds\n" \
"v_mfma_f32_16x16x32_bf16 v[12:15], v[208:211], a[124:127], v[12:15]\n" \
"v_mfma_f32_16x16x32_bf16 v[44:47], v[212:215], a[124:127], v[44:47]\n" \
"ds_read_b64_tr_b16 v[208:209], v230 offset:26112\n" \
"v_mfma_f32_16x16x32_bf16 v[40:43], v[212:215], a[120:123], v[40:43]\n" \
"ds_read_b64_tr_b16 v[210:211], v230 offset:26368\n" \
"v_mfma_f32_16x16x32_bf16 v[36:39], v[212:215], a[116:119], v[36:39]\n" \
"buffer_load_dwordx4 v227, %[wk], s79 offen offset:1024 lds\n" \
"v_mfma_f32_16x16x32_bf16 v[32:35], v[212:215], a[112:115], v[32:35]\n" \
"s_waitcnt lgkmcnt(6)\n" \
"v_mfma_f32_16x16x32_bf16 v[16:19], v[192:195], a[112:115], v[16:19]\n" \
"ds_read_b64_tr_b16 v[212:213], v233 offset:26112\n" \
"v_mfma_f32_16x16x32_bf16 v[20:23], v[192:195], a[116:119], v[20:23]\n" \
"ds_read_b64_tr_b16 v[214:215], v233 offset:26368\n" \
"v_mfma_f32_16x16x32_bf16 v[24:27], v[192:195], a[120:123], v[24:27]\n" \
"buffer_load_dwordx4 v254, %[wk], s79 offen offset:2048 lds\n" \
"v_mfma_f32_16x16x32_bf16 v[28:31], v[192:195], a[124:127], v[28:31]\n" \
"s_waitcnt vmcnt(23)\n" \
"s_barrier\n" \
"v_mfma_f32_16x16x32_bf16 v[60:63], v[196:199], a[124:127], v[60:63]\n" \
"ds_read_b64_tr_b16 v[192:193], v228 offset:0\n" \
"v_mfma_f32_16x16x32_bf16 v[56:59], v[196:199], a[120:123], v[56:59]\n" \
"ds_read_b64_tr_b16 v[194:195], v228 offset:256\n" \
"v_mfma_f32_16x16x32_bf16 v[52:55], v[196:199], a[116:119], v[52:55]\n" \
"buffer_load_dwordx4 v236, %[wk], s79 offen offset:3072 lds\n" \
"v_mfma_f32_16x16x32_bf16 v[48:51], v[196:199], a[112:115], v[48:51]\n" \
"s_waitcnt lgkmcnt(6)\n" \
"s_waitcnt vmcnt(16)\n" \
"v_mfma_f32_16x16x32_bf16 v[64:67], v[200:203], a[112:115], v[64:67]\n" \
"ds_read_b64_tr_b16 v[196:197], v231 offset:0\n" \
"ds_read_b128 a[224:227], v234 offset:0\n" \
"v_mfma_f32_16x16x32_bf16 v[68:71], v[200:203], a[116:119], v[68:71]\n" \
"ds_read_b64_tr_b16 v[198:199], v231 offset:256\n" \
"ds_read_b128 a[228:231], v234 offset:2048\n" \
"v_mfma_f32_16x16x32_bf16 v[72:75], v[200:203], a[120:123], v[72:75]\n" \
"buffer_load_dword v247, v216, %[wv], s62 offen nt\n" \
"v_mfma_f32_16x16x32_bf16 v[76:79], v[200:203], a[124:127], v[76:79]\n" \
"v_mfma_f32_16x16x32_bf16 v[108:111], v[204:207], a[124:127], v[108:111]\n" \
"ds_read_b64_tr_b16 v[200:201], v228 offset:512\n" \
"ds_read_b128 a[232:235], v234 offset:4096\n" \
"v_mfma_f32_16x16x32_bf16 v[104:107], v[204:207], a[120:123], v[104:107]\n" \
"ds_read_b64_tr_b16 v[202:203], v228 offset:768\n" \
"ds_read_b128 a[236:239], v234 offset:6144\n" \
"v_mfma_f32_16x16x32_bf16 v[100:103], v[204:207], a[116:119], v[100:103]\n" \
"buffer_load_dword v247, v216, %[wv], s62 offen\n" \
"v_mfma_f32_16x16x32_bf16 v[96:99], v[204:207], a[112:115], v[96:99]\n" \
"s_waitcnt lgkmcnt(10)\n" \
"v_mfma_f32_16x16x32_bf16 v[80:83], v[208:211], a[112:115], v[80:83]\n" \
"ds_read_b64_tr_b16 v[204:205], v231 offset:512\n" \
"ds_read_b128 a[240:243], v235 offset:0\n" \
"v_mfma_f32_16x16x32_bf16 v[84:87], v[208:211], a[116:119], v[84:87]\n" \
"ds_read_b64_tr_b16 v[206:207], v231 offset:768\n" \
"ds_read_b128 a[244:247], v235 offset:2048\n" \
"v_mfma_f32_16x16x32_bf16 v[88:91], v[208:211], a[120:123], v[88:91]\n" \
"buffer_load_dword v247, v242, %[wsr], s80 offen offset:256 sc1\n" \
"v_mfma_f32_16x16x32_bf16 v[92:95], v[208:211], a[124:127], v[92:95]\n" \
"s_barrier\n" \
"v_mfma_f32_16x16x32_bf16 v[124:127], v[212:215], a[124:127], v[124:127]\n" \
"ds_read_b64_tr_b16 v[208:209], v228 offset:1024\n" \
"ds_read_b128 a[248:251], v235 offset:4096\n" \
"v_mfma_f32_16x16x32_bf16 v[120:123], v[212:215], a[120:123], v[120:123]\n" \
"ds_read_b64_tr_b16 v[210:211], v228 offset:1280\n" \
"ds_read_b128 a[252:255], v235 offset:6144\n" \
"v_mfma_f32_16x16x32_bf16 v[116:119], v[212:215], a[116:119], v[116:119]\n" \
"v_mfma_f32_16x16x32_bf16 v[112:115], v[212:215], a[112:115], v[112:115]\n" \
"s_waitcnt lgkmcnt(13)\n" \
"v_mfma_f32_16x16x32_bf16 v[0:3], v[192:195], a[128:131], v[0:3]\n" \
"ds_read_b64_tr_b16 v[212:213], v231 offset:1024\n" \
"v_mfma_f32_16x16x32_bf16 v[4:7], v[192:195], a[132:135], v[4:7]\n" \
"ds_read_b64_tr_b16 v[214:215], v231 offset:1280\n" \
"v_mfma_f32_16x16x32_bf16 v[8:11], v[192:195], a[136:139], v[8:11]\n" \
"v_mfma_f32_16x16x32_bf16 v[12:15], v[192:195], a[140:143], v[12:15]\n" \
"v_mfma_f32_16x16x32_bf16 v[44:47], v[196:199], a[140:143], v[44:47]\n" \
"ds_read_b64_tr_b16 v[192:193], v228 offset:1536\n" \
"v_mfma_f32_16x16x32_bf16 v[40:43], v[196:199], a[136:139], v[40:43]\n" \
"ds_read_b64_tr_b16 v[194:195], v228 offset:1792\n" \
"v_mfma_f32_16x16x32_bf16 v[36:39], v[196:199], a[132:135], v[36:39]\n" \
"v_mfma_f32_16x16x32_bf16 v[32:35], v[196:199], a[128:131], v[32:35]\n" \
"s_waitcnt lgkmcnt(4)\n" \
"v_mfma_f32_16x16x32_bf16 v[16:19], v[200:203], a[128:131], v[16:19]\n" \
"ds_read_b64_tr_b16 v[196:197], v231 offset:1536\n" \
"v_mfma_f32_16x16x32_bf16 v[20:23], v[200:203], a[132:135], v[20:23]\n" \
"ds_read_b64_tr_b16 v[198:199], v231 offset:1792\n" \
"v_mfma_f32_16x16x32_bf16 v[24:27], v[200:203], a[136:139], v[24:27]\n" \
"v_mfma_f32_16x16x32_bf16 v[28:31], v[200:203], a[140:143], v[28:31]\n" \
"v_mfma_f32_16x16x32_bf16 v[60:63], v[204:207], a[140:143], v[60:63]\n" \
"ds_read_b64_tr_b16 v[200:201], v228 offset:8192\n" \
"v_mfma_f32_16x16x32_bf16 v[56:59], v[204:207], a[136:139], v[56:59]\n" \
"ds_read_b64_tr_b16 v[202:203], v228 offset:8448\n" \
"v_mfma_f32_16x16x32_bf16 v[52:55], v[204:207], a[132:135], v[52:55]\n" \
"v_mfma_f32_16x16x32_bf16 v[48:51], v[204:207], a[128:131], v[48:51]\n" \
"s_waitcnt lgkmcnt(6)\n" \
"v_mfma_f32_16x16x32_bf16 v[64:67], v[208:211], a[128:131], v[64:67]\n" \
"ds_read_b64_tr_b16 v[204:205], v231 offset:8192\n" \
"v_mfma_f32_16x16x32_bf16 v[68:71], v[208:211], a[132:135], v[68:71]\n" \
"ds_read_b64_tr_b16 v[206:207], v231 offset:8448\n" \
"v_mfma_f32_16x16x32_bf16 v[72:75], v[208:211], a[136:139], v[72:75]\n" \
"v_mfma_f32_16x16x32_bf16 v[76:79], v[208:211], a[140:143], v[76:79]\n" \
"v_mfma_f32_16x16x32_bf16 v[108:111], v[212:215], a[140:143], v[108:111]\n" \
"ds_read_b64_tr_b16 v[208:209], v228 offset:8704\n" \
"v_mfma_f32_16x16x32_bf16 v[104:107], v[212:215], a[136:139], v[104:107]\n" \
"ds_read_b64_tr_b16 v[210:211], v228 offset:8960\n" \
"v_mfma_f32_16x16x32_bf16 v[100:103], v[212:215], a[132:135], v[100:103]\n" \
"v_mfma_f32_16x16x32_bf16 v[96:99], v[212:215], a[128:131], v[96:99]\n" \
"s_waitcnt lgkmcnt(6)\n" \
"v_mfma_f32_16x16x32_bf16 v[80:83], v[192:195], a[128:131], v[80:83]\n" \
"ds_read_b64_tr_b16 v[212:213], v231 offset:8704\n" \
"v_mfma_f32_16x16x32_bf16 v[84:87], v[192:195], a[132:135], v[84:87]\n" \
"ds_read_b64_tr_b16 v[214:215], v231 offset:8960\n" \
"v_mfma_f32_16x16x32_bf16 v[88:91], v[192:195], a[136:139], v[88:91]\n" \
"v_mfma_f32_16x16x32_bf16 v[92:95], v[192:195], a[140:143], v[92:95]\n" \
"v_mfma_f32_16x16x32_bf16 v[124:127], v[196:199], a[140:143], v[124:127]\n" \
"s_andn2_b32 s91, %[pair], 31\n" \
"ds_read_b64_tr_b16 v[192:193], v228 offset:9216\n" \
"s_lshl_b32 s91, s91, 10\n" \
"s_add_u32 m0, %[w4k], 147456\n" \
"v_mfma_f32_16x16x32_bf16 v[120:123], v[196:199], a[136:139], v[120:123]\n" \
"s_lshl1_add_u32 s91, %[w4k], s91\n" \
"ds_read_b64_tr_b16 v[194:195], v228 offset:9472\n" \
"s_and_b32 s90, %[pair], 31\n" \
"s_mov_b32 s79, 3670016\n" \
"v_mfma_f32_16x16x32_bf16 v[116:119], v[196:199], a[132:135], v[116:119]\n" \
"buffer_load_dwordx4 v226, %[wk], s79 offen offset:0 lds\n" \
"v_mfma_f32_16x16x32_bf16 v[112:115], v[196:199], a[128:131], v[112:115]\n" \
"s_waitcnt lgkmcnt(6)\n" \
"s_lshl2_add_u32 s91, s90, s91\n" \
"s_waitcnt vmcnt(8)\n" \
"v_mfma_f32_16x16x32_bf16 v[0:3], v[200:203], a[144:147], v[0:3]\n" \
"ds_read_b64_tr_b16 v[196:197], v231 offset:9216\n" \
"ds_read_b128 a[192:195], v238 offset:0\n" \
"v_mfma_f32_16x16x32_bf16 v[4:7], v[200:203], a[148:151], v[4:7]\n" \
"ds_read_b64_tr_b16 v[198:199], v231 offset:9472\n" \
"ds_read_b128 a[196:199], v238 offset:2048\n" \
"v_mfma_f32_16x16x32_bf16 v[8:11], v[200:203], a[152:155], v[8:11]\n" \
"buffer_load_dwordx4 v227, %[wk], s79 offen offset:1024 lds\n" \
"v_mfma_f32_16x16x32_bf16 v[12:15], v[200:203], a[156:159], v[12:15]\n" \
"v_mfma_f32_16x16x32_bf16 v[44:47], v[204:207], a[156:159], v[44:47]\n" \
"ds_read_b64_tr_b16 v[200:201], v228 offset:9728\n" \
"ds_read_b128 a[200:203], v238 offset:4096\n" \
"v_mfma_f32_16x16x32_bf16 v[40:43], v[204:207], a[152:155], v[40:43]\n" \
"ds_read_b64_tr_b16 v[202:203], v228 offset:9984\n" \
"ds_read_b128 a[204:207], v238 offset:6144\n" \
"v_mfma_f32_16x16x32_bf16 v[36:39], v[204:207], a[148:151], v[36:39]\n" \
"buffer_load_dwordx4 v254, %[wk], s79 offen offset:2048 lds\n" \
"v_mfma_f32_16x16x32_bf16 v[32:35], v[204:207], a[144:147], v[32:35]\n" \
"s_waitcnt lgkmcnt(10)\n" \
"v_mfma_f32_16x16x32_bf16 v[16:19], v[208:211], a[144:147], v[16:19]\n" \
"ds_read_b64_tr_b16 v[204:205], v231 offset:9728\n" \
"ds_read_b128 a[208:211], v239 offset:0\n" \
"v_mfma_f32_16x16x32_bf16 v[20:23], v[208:211], a[148:151], v[20:23]\n" \
"ds_read_b64_tr_b16 v[206:207], v231 offset:9984\n" \
"ds_read_b128 a[212:215], v239 offset:2048\n" \
"v_mfma_f32_16x16x32_bf16 v[24:27], v[208:211], a[152:155], v[24:27]\n" \
"buffer_load_dwordx4 v236, %[wk], s79 offen offset:3072 lds\n" \
"v_mfma_f32_16x16x32_bf16 v[28:31], v[208:211], a[156:159], v[28:31]\n" \
"s_barrier\n" \
"v_mfma_f32_16x16x32_bf16 v[60:63], v[212:215], a[156:159], v[60:63]\n" \
"ds_read_b64_tr_b16 v[208:209], v228 offset:16384\n" \
"ds_read_b128 a[216:219], v239 offset:4096\n" \
"v_mfma_f32_16x16x32_bf16 v[56:59], v[212:215], a[152:155], v[56:59]\n" \
"s_add_u32 m0, %[w4k], 49152\n" \
"ds_read_b64_tr_b16 v[210:211], v228 offset:16640\n" \
"ds_read_b128 a[220:223], v239 offset:6144\n" \
"v_mfma_f32_16x16x32_bf16 v[52:55], v[212:215], a[148:151], v[52:55]\n" \
"buffer_load_dwordx4 v248, %[wo], 0 offen offset:0 lds\n" \
"v_mfma_f32_16x16x32_bf16 v[48:51], v[212:215], a[144:147], v[48:51]\n" \
"s_waitcnt lgkmcnt(13)\n" \
"v_mfma_f32_16x16x32_bf16 v[64:67], v[192:195], a[144:147], v[64:67]\n" \
"ds_read_b64_tr_b16 v[212:213], v231 offset:16384\n" \
"v_mfma_f32_16x16x32_bf16 v[68:71], v[192:195], a[148:151], v[68:71]\n" \
"ds_read_b64_tr_b16 v[214:215], v231 offset:16640\n" \
"v_mfma_f32_16x16x32_bf16 v[72:75], v[192:195], a[152:155], v[72:75]\n" \
"buffer_load_dwordx4 v248, %[wo], s84 offen offset:1024 lds\n" \
"v_mfma_f32_16x16x32_bf16 v[76:79], v[192:195], a[156:159], v[76:79]\n" \
"v_mfma_f32_16x16x32_bf16 v[108:111], v[196:199], a[156:159], v[108:111]\n" \
"ds_read_b64_tr_b16 v[192:193], v228 offset:16896\n" \
"v_mfma_f32_16x16x32_bf16 v[104:107], v[196:199], a[152:155], v[104:107]\n" \
"ds_read_b64_tr_b16 v[194:195], v228 offset:17152\n" \
"v_mfma_f32_16x16x32_bf16 v[100:103], v[196:199], a[148:151], v[100:103]\n" \
"buffer_load_dwordx4 v248, %[wo], s85 offen offset:2048 lds\n" \
"v_mfma_f32_16x16x32_bf16 v[96:99], v[196:199], a[144:147], v[96:99]\n" \
"s_waitcnt lgkmcnt(4)\n" \
"v_mfma_f32_16x16x32_bf16 v[80:83], v[200:203], a[144:147], v[80:83]\n" \
"ds_read_b64_tr_b16 v[196:197], v231 offset:16896\n" \
"v_mfma_f32_16x16x32_bf16 v[84:87], v[200:203], a[148:151], v[84:87]\n" \
"ds_read_b64_tr_b16 v[198:199], v231 offset:17152\n" \
"v_mfma_f32_16x16x32_bf16 v[88:91], v[200:203], a[152:155], v[88:91]\n" \
"buffer_load_dwordx4 v248, %[wo], s86 offen offset:3072 lds\n" \
"v_mfma_f32_16x16x32_bf16 v[92:95], v[200:203], a[156:159], v[92:95]\n" \
"s_barrier\n" \
"v_mfma_f32_16x16x32_bf16 v[124:127], v[204:207], a[156:159], v[124:127]\n" \
"s_add_u32 m0, %[w4k], 65536\n" \
"ds_read_b64_tr_b16 v[200:201], v228 offset:17408\n" \
"v_mfma_f32_16x16x32_bf16 v[120:123], v[204:207], a[152:155], v[120:123]\n" \
"s_mov_b32 s79, 0\n" \
"ds_read_b64_tr_b16 v[202:203], v228 offset:17664\n" \
"v_mfma_f32_16x16x32_bf16 v[116:119], v[204:207], a[148:151], v[116:119]\n" \
"buffer_load_dwordx4 v226, %[wv], s79 offen offset:0 lds\n" \
"v_mfma_f32_16x16x32_bf16 v[112:115], v[204:207], a[144:147], v[112:115]\n" \
"s_waitcnt lgkmcnt(6)\n" \
"v_mfma_f32_16x16x32_bf16 v[0:3], v[208:211], a[160:163], v[0:3]\n" \
"ds_read_b64_tr_b16 v[204:205], v231 offset:17408\n" \
"s_waitcnt vmcnt(9)\n" \
"v_mfma_f32_16x16x32_bf16 v[4:7], v[208:211], a[164:167], v[4:7]\n" \
"ds_read_b64_tr_b16 v[206:207], v231 offset:17664\n" \
"v_cmp_eq_u32_e32 vcc, v244, v247\n" \
"v_mfma_f32_16x16x32_bf16 v[8:11], v[208:211], a[168:171], v[8:11]\n" \
"buffer_load_dwordx4 v254, %[wv], s79 offen offset:2048 lds\n" \
"v_mfma_f32_16x16x32_bf16 v[12:15], v[208:211], a[172:175], v[12:15]\n" \
"s_and_b32 s88, vcc_lo, -1\n" \
"s_cbranch_scc1 .LATLsu\n" \
".LATLsub:\n" \
"v_mfma_f32_16x16x32_bf16 v[44:47], v[212:215], a[172:175], v[44:47]\n" \
"ds_read_b64_tr_b16 v[208:209], v228 offset:17920\n" \
"v_mfma_f32_16x16x32_bf16 v[40:43], v[212:215], a[168:171], v[40:43]\n" \
"s_mov_b32 s79, 524160\n" \
"ds_read_b64_tr_b16 v[210:211], v228 offset:18176\n" \
"v_mfma_f32_16x16x32_bf16 v[36:39], v[212:215], a[164:167], v[36:39]\n" \
"buffer_load_dwordx4 v227, %[wv], s79 offen offset:1024 lds\n" \
"v_mfma_f32_16x16x32_bf16 v[32:35], v[212:215], a[160:163], v[32:35]\n" \
"s_waitcnt lgkmcnt(6)\n" \
"v_mfma_f32_16x16x32_bf16 v[16:19], v[192:195], a[160:163], v[16:19]\n" \
"ds_read_b64_tr_b16 v[212:213], v231 offset:17920\n" \
"v_mfma_f32_16x16x32_bf16 v[20:23], v[192:195], a[164:167], v[20:23]\n" \
"ds_read_b64_tr_b16 v[214:215], v231 offset:18176\n" \
"v_mfma_f32_16x16x32_bf16 v[24:27], v[192:195], a[168:171], v[24:27]\n" \
"buffer_load_dwordx4 v236, %[wv], s79 offen offset:3072 lds\n" \
"v_mfma_f32_16x16x32_bf16 v[28:31], v[192:195], a[172:175], v[28:31]\n" \
"v_mfma_f32_16x16x32_bf16 v[60:63], v[196:199], a[172:175], v[60:63]\n" \
"s_add_u32 m0, %[w4k], 81920\n" \
"ds_read_b64_tr_b16 v[192:193], v228 offset:24576\n" \
"v_mfma_f32_16x16x32_bf16 v[56:59], v[196:199], a[168:171], v[56:59]\n" \
"s_mov_b32 s79, 1048576\n" \
"ds_read_b64_tr_b16 v[194:195], v228 offset:24832\n" \
"v_mfma_f32_16x16x32_bf16 v[52:55], v[196:199], a[164:167], v[52:55]\n" \
"buffer_load_dwordx4 v226, %[wv], s79 offen offset:0 lds\n" \
"v_mfma_f32_16x16x32_bf16 v[48:51], v[196:199], a[160:163], v[48:51]\n" \
"s_waitcnt lgkmcnt(6)\n" \
"v_mfma_f32_16x16x32_bf16 v[64:67], v[200:203], a[160:163], v[64:67]\n" \
"ds_read_b64_tr_b16 v[196:197], v231 offset:24576\n" \
"v_mfma_f32_16x16x32_bf16 v[68:71], v[200:203], a[164:167], v[68:71]\n" \
"ds_read_b64_tr_b16 v[198:199], v231 offset:24832\n" \
"v_mfma_f32_16x16x32_bf16 v[72:75], v[200:203], a[168:171], v[72:75]\n" \
"buffer_load_dwordx4 v254, %[wv], s79 offen offset:2048 lds\n" \
"v_mfma_f32_16x16x32_bf16 v[76:79], v[200:203], a[172:175], v[76:79]\n" \
"v_mfma_f32_16x16x32_bf16 v[108:111], v[204:207], a[172:175], v[108:111]\n" \
"ds_read_b64_tr_b16 v[200:201], v228 offset:25088\n" \
"v_mfma_f32_16x16x32_bf16 v[104:107], v[204:207], a[168:171], v[104:107]\n" \
"s_mov_b32 s79, 1572736\n" \
"ds_read_b64_tr_b16 v[202:203], v228 offset:25344\n" \
"v_mfma_f32_16x16x32_bf16 v[100:103], v[204:207], a[164:167], v[100:103]\n" \
"buffer_load_dwordx4 v227, %[wv], s79 offen offset:1024 lds\n" \
"v_mfma_f32_16x16x32_bf16 v[96:99], v[204:207], a[160:163], v[96:99]\n" \
"s_waitcnt lgkmcnt(6)\n" \
"v_mfma_f32_16x16x32_bf16 v[80:83], v[208:211], a[160:163], v[80:83]\n" \
"ds_read_b64_tr_b16 v[204:205], v231 offset:25088\n" \
"v_mfma_f32_16x16x32_bf16 v[84:87], v[208:211], a[164:167], v[84:87]\n" \
"ds_read_b64_tr_b16 v[206:207], v231 offset:25344\n" \
"v_mfma_f32_16x16x32_bf16 v[88:91], v[208:211], a[168:171], v[88:91]\n" \
"buffer_load_dwordx4 v236, %[wv], s79 offen offset:3072 lds\n" \
"v_mfma_f32_16x16x32_bf16 v[92:95], v[208:211], a[172:175], v[92:95]\n" \
"v_mfma_f32_16x16x32_bf16 v[124:127], v[212:215], a[172:175], v[124:127]\n" \
"s_add_u32 m0, %[w4k], 98304\n" \
"ds_read_b64_tr_b16 v[208:209], v228 offset:25600\n" \
"v_mfma_f32_16x16x32_bf16 v[120:123], v[212:215], a[168:171], v[120:123]\n" \
"s_mov_b32 s79, 2097152\n" \
"ds_read_b64_tr_b16 v[210:211], v228 offset:25856\n" \
"v_mfma_f32_16x16x32_bf16 v[116:119], v[212:215], a[164:167], v[116:119]\n" \
"buffer_load_dwordx4 v226, %[wv], s79 offen offset:0 lds\n" \
"v_mfma_f32_16x16x32_bf16 v[112:115], v[212:215], a[160:163], v[112:115]\n" \
"s_waitcnt lgkmcnt(6)\n" \
"v_mfma_f32_16x16x32_bf16 v[0:3], v[192:195], a[176:179], v[0:3]\n" \
"ds_read_b64_tr_b16 v[212:213], v231 offset:25600\n" \
"v_mfma_f32_16x16x32_bf16 v[4:7], v[192:195], a[180:183], v[4:7]\n" \
"ds_read_b64_tr_b16 v[214:215], v231 offset:25856\n" \
"v_mfma_f32_16x16x32_bf16 v[8:11], v[192:195], a[184:187], v[8:11]\n" \
"buffer_load_dwordx4 v254, %[wv], s79 offen offset:2048 lds\n" \
"v_mfma_f32_16x16x32_bf16 v[12:15], v[192:195], a[188:191], v[12:15]\n" \
"v_mfma_f32_16x16x32_bf16 v[44:47], v[196:199], a[188:191], v[44:47]\n" \
"ds_read_b64_tr_b16 v[192:193], v228 offset:26112\n" \
"v_mfma_f32_16x16x32_bf16 v[40:43], v[196:199], a[184:187], v[40:43]\n" \
"s_mov_b32 s79, 2621312\n" \
"ds_read_b64_tr_b16 v[194:195], v228 offset:26368\n" \
"v_mfma_f32_16x16x32_bf16 v[36:39], v[196:199], a[180:183], v[36:39]\n" \
"buffer_load_dwordx4 v227, %[wv], s79 offen offset:1024 lds\n" \
"v_mfma_f32_16x16x32_bf16 v[32:35], v[196:199], a[176:179], v[32:35]\n" \
"s_waitcnt lgkmcnt(6)\n" \
"v_mfma_f32_16x16x32_bf16 v[16:19], v[200:203], a[176:179], v[16:19]\n" \
"ds_read_b64_tr_b16 v[196:197], v231 offset:26112\n" \
"v_mfma_f32_16x16x32_bf16 v[20:23], v[200:203], a[180:183], v[20:23]\n" \
"ds_read_b64_tr_b16 v[198:199], v231 offset:26368\n" \
"v_mfma_f32_16x16x32_bf16 v[24:27], v[200:203], a[184:187], v[24:27]\n" \
"buffer_load_dwordx4 v236, %[wv], s79 offen offset:3072 lds\n" \
"v_mfma_f32_16x16x32_bf16 v[28:31], v[200:203], a[188:191], v[28:31]\n" \
"s_barrier\n" \
"v_mfma_f32_16x16x32_bf16 v[60:63], v[204:207], a[188:191], v[60:63]\n" \
"s_add_u32 m0, %[w4k], 114688\n" \
"ds_read_b64_tr_b16 v[200:201], v230 offset:0\n" \
"v_mfma_f32_16x16x32_bf16 v[56:59], v[204:207], a[184:187], v[56:59]\n" \
"s_mov_b32 s79, 3145728\n" \
"ds_read_b64_tr_b16 v[202:203], v230 offset:256\n" \
"v_mfma_f32_16x16x32_bf16 v[52:55], v[204:207], a[180:183], v[52:55]\n" \
"buffer_load_dwordx4 v226, %[wv], s79 offen offset:0 lds\n" \
"v_mfma_f32_16x16x32_bf16 v[48:51], v[204:207], a[176:179], v[48:51]\n" \
"s_waitcnt lgkmcnt(6)\n" \
"v_mfma_f32_16x16x32_bf16 v[64:67], v[208:211], a[176:179], v[64:67]\n" \
"ds_read_b64_tr_b16 v[204:205], v233 offset:0\n" \
"v_mfma_f32_16x16x32_bf16 v[68:71], v[208:211], a[180:183], v[68:71]\n" \
"ds_read_b64_tr_b16 v[206:207], v233 offset:256\n" \
"v_mfma_f32_16x16x32_bf16 v[72:75], v[208:211], a[184:187], v[72:75]\n" \
"buffer_load_dwordx4 v254, %[wv], s79 offen offset:2048 lds\n" \
"v_mfma_f32_16x16x32_bf16 v[76:79], v[208:211], a[188:191], v[76:79]\n" \
"v_mfma_f32_16x16x32_bf16 v[108:111], v[212:215], a[188:191], v[108:111]\n" \
"ds_read_b64_tr_b16 v[208:209], v230 offset:512\n" \
"v_mfma_f32_16x16x32_bf16 v[104:107], v[212:215], a[184:187], v[104:107]\n" \
"s_mov_b32 s79, 3669888\n" \
"ds_read_b64_tr_b16 v[210:211], v230 offset:768\n" \
"v_mfma_f32_16x16x32_bf16 v[100:103], v[212:215], a[180:183], v[100:103]\n" \
"buffer_load_dwordx4 v227, %[wv], s79 offen offset:1024 lds\n" \
"v_mfma_f32_16x16x32_bf16 v[96:99], v[212:215], a[176:179], v[96:99]\n" \
"s_waitcnt lgkmcnt(6)\n" \
"v_mfma_f32_16x16x32_bf16 v[80:83], v[192:195], a[176:179], v[80:83]\n" \
"ds_read_b64_tr_b16 v[212:213], v233 offset:512\n" \
"v_mfma_f32_16x16x32_bf16 v[84:87], v[192:195], a[180:183], v[84:87]\n" \
"ds_read_b64_tr_b16 v[214:215], v233 offset:768\n" \
"v_mfma_f32_16x16x32_bf16 v[88:91], v[192:195], a[184:187], v[88:91]\n" \
"buffer_load_dwordx4 v236, %[wv], s79 offen offset:3072 lds\n" \
"v_mfma_f32_16x16x32_bf16 v[92:95], v[192:195], a[188:191], v[92:95]\n" \
"s_barrier\n" \
"v_mfma_f32_16x16x32_bf16 v[124:127], v[196:199], a[188:191], v[124:127]\n" \
"s_add_u32 m0, %[w4k], 0\n" \
"ds_read_b64_tr_b16 v[192:193], v230 offset:1024\n" \
"v_mfma_f32_16x16x32_bf16 v[120:123], v[196:199], a[184:187], v[120:123]\n" \
"s_mov_b32 s79, 128\n" \
"ds_read_b64_tr_b16 v[194:195], v230 offset:1280\n" \
"v_mfma_f32_16x16x32_bf16 v[116:119], v[196:199], a[180:183], v[116:119]\n" \
"buffer_load_dwordx4 v226, %[wv], s79 offen offset:0 lds\n" \
"v_mfma_f32_16x16x32_bf16 v[112:115], v[196:199], a[176:179], v[112:115]\n" \
"s_waitcnt lgkmcnt(6)\n" \
"v_mfma_f32_16x16x32_bf16 v[0:3], v[200:203], a[192:195], v[0:3]\n" \
"ds_read_b64_tr_b16 v[196:197], v233 offset:1024\n" \
"v_mfma_f32_16x16x32_bf16 v[4:7], v[200:203], a[196:199], v[4:7]\n" \
"ds_read_b64_tr_b16 v[198:199], v233 offset:1280\n" \
"v_mfma_f32_16x16x32_bf16 v[8:11], v[200:203], a[200:203], v[8:11]\n" \
"buffer_load_dwordx4 v254, %[wv], s79 offen offset:2048 lds\n" \
"v_mfma_f32_16x16x32_bf16 v[12:15], v[200:203], a[204:207], v[12:15]\n" \
"v_mfma_f32_16x16x32_bf16 v[44:47], v[204:207], a[204:207], v[44:47]\n" \
"ds_read_b64_tr_b16 v[200:201], v230 offset:1536\n" \
"v_mfma_f32_16x16x32_bf16 v[40:43], v[204:207], a[200:203], v[40:43]\n" \
"s_mov_b32 s79, 524288\n" \
"ds_read_b64_tr_b16 v[202:203], v230 offset:1792\n" \
"v_mfma_f32_16x16x32_bf16 v[36:39], v[204:207], a[196:199], v[36:39]\n" \
"buffer_load_dwordx4 v227, %[wv], s79 offen offset:1024 lds\n" \
"v_mfma_f32_16x16x32_bf16 v[32:35], v[204:207], a[192:195], v[32:35]\n" \
"s_waitcnt lgkmcnt(6)\n" \
"v_mfma_f32_16x16x32_bf16 v[16:19], v[208:211], a[192:195], v[16:19]\n" \
"ds_read_b64_tr_b16 v[204:205], v233 offset:1536\n" \
"v_mfma_f32_16x16x32_bf16 v[20:23], v[208:211], a[196:199], v[20:23]\n" \
"ds_read_b64_tr_b16 v[206:207], v233 offset:1792\n" \
"v_mfma_f32_16x16x32_bf16 v[24:27], v[208:211], a[200:203], v[24:27]\n" \
"buffer_load_dwordx4 v236, %[wv], s79 offen offset:3072 lds\n" \
"v_mfma_f32_16x16x32_bf16 v[28:31], v[208:211], a[204:207], v[28:31]\n" \
"v_mfma_f32_16x16x32_bf16 v[60:63], v[212:215], a[204:207], v[60:63]\n" \
"s_add_u32 m0, %[w4k], 16384\n" \
"ds_read_b64_tr_b16 v[208:209], v230 offset:8192\n" \
"v_mfma_f32_16x16x32_bf16 v[56:59], v[212:215], a[200:203], v[56:59]\n" \
"s_mov_b32 s79, 1048704\n" \
"ds_read_b64_tr_b16 v[210:211], v230 offset:8448\n" \
"v_mfma_f32_16x16x32_bf16 v[52:55], v[212:215], a[196:199], v[52:55]\n" \
"buffer_load_dwordx4 v226, %[wv], s79 offen offset:0 lds\n" \
"v_mfma_f32_16x16x32_bf16 v[48:51], v[212:215], a[192:195], v[48:51]\n" \
"s_waitcnt lgkmcnt(6)\n" \
"v_mfma_f32_16x16x32_bf16 v[64:67], v[192:195], a[192:195], v[64:67]\n" \
"ds_read_b64_tr_b16 v[212:213], v233 offset:8192\n" \
"v_mfma_f32_16x16x32_bf16 v[68:71], v[192:195], a[196:199], v[68:71]\n" \
"ds_read_b64_tr_b16 v[214:215], v233 offset:8448\n" \
"v_mfma_f32_16x16x32_bf16 v[72:75], v[192:195], a[200:203], v[72:75]\n" \
"buffer_load_dwordx4 v254, %[wv], s79 offen offset:2048 lds\n" \
"v_mfma_f32_16x16x32_bf16 v[76:79], v[192:195], a[204:207], v[76:79]\n" \
"v_mfma_f32_16x16x32_bf16 v[108:111], v[196:199], a[204:207], v[108:111]\n" \
"ds_read_b64_tr_b16 v[192:193], v230 offset:8704\n" \
"v_mfma_f32_16x16x32_bf16 v[104:107], v[196:199], a[200:203], v[104:107]\n" \
"s_mov_b32 s79, 1572864\n" \
"ds_read_b64_tr_b16 v[194:195], v230 offset:8960\n" \
"v_mfma_f32_16x16x32_bf16 v[100:103], v[196:199], a[196:199], v[100:103]\n" \
"buffer_load_dwordx4 v227, %[wv], s79 offen offset:1024 lds\n" \
"v_mfma_f32_16x16x32_bf16 v[96:99], v[196:199], a[192:195], v[96:99]\n" \
"s_waitcnt lgkmcnt(6)\n" \
"v_mfma_f32_16x16x32_bf16 v[80:83], v[200:203], a[192:195], v[80:83]\n" \
"ds_read_b64_tr_b16 v[196:197], v233 offset:8704\n" \
"v_mfma_f32_16x16x32_bf16 v[84:87], v[200:203], a[196:199], v[84:87]\n" \
"ds_read_b64_tr_b16 v[198:199], v233 offset:8960\n" \
"v_mfma_f32_16x16x32_bf16 v[88:91], v[200:203], a[200:203], v[88:91]\n" \
"buffer_load_dwordx4 v236, %[wv], s79 offen offset:3072 lds\n" \
"v_mfma_f32_16x16x32_bf16 v[92:95], v[200:203], a[204:207], v[92:95]\n" \
"v_mfma_f32_16x16x32_bf16 v[124:127], v[204:207], a[204:207], v[124:127]\n" \
"s_add_u32 m0, %[w4k], 32768\n" \
"ds_read_b64_tr_b16 v[200:201], v230 offset:9216\n" \
"v_lshrrev_b32_e32 v250, 2, v240\n" \
"v_mfma_f32_16x16x32_bf16 v[120:123], v[204:207], a[200:203], v[120:123]\n" \
"s_mov_b32 s79, 2097280\n" \
"ds_read_b64_tr_b16 v[202:203], v230 offset:9472\n" \
"v_and_b32_e32 v251, 3, v240\n" \
"v_mfma_f32_16x16x32_bf16 v[116:119], v[204:207], a[196:199], v[116:119]\n" \
"buffer_load_dwordx4 v226, %[wv], s79 offen offset:0 lds\n" \
"v_mfma_f32_16x16x32_bf16 v[112:115], v[204:207], a[192:195], v[112:115]\n" \
"s_waitcnt lgkmcnt(6)\n" \
"v_lshlrev_b32_e32 v250, 7, v250\n" \
"v_lshl_add_u32 v250, v251, 3, v250\n" \
"v_mfma_f32_16x16x32_bf16 v[0:3], v[208:211], a[208:211], v[0:3]\n" \
"s_lshl_b32 s63, s73, 19\n" \
"ds_read_b64_tr_b16 v[204:205], v233 offset:9216\n" \
"v_add_u32_e32 v250, %[w4k], v250\n" \
"v_mfma_f32_16x16x32_bf16 v[4:7], v[208:211], a[212:215], v[4:7]\n" \
"s_mul_i32 s61, %[h], 0x20800\n" \
"ds_read_b64_tr_b16 v[206:207], v233 offset:9472\n" \
"s_and_b32 s61, s61, 0x6e000\n" \
"v_mfma_f32_16x16x32_bf16 v[8:11], v[208:211], a[216:219], v[8:11]\n" \
"buffer_load_dwordx4 v254, %[wv], s79 offen offset:2048 lds\n" \
"v_mfma_f32_16x16x32_bf16 v[12:15], v[208:211], a[220:223], v[12:15]\n" \
"s_add_u32 s63, s63, s61\n" \
"s_lshr_b32 s61, %[w4k], 1\n" \
"s_add_u32 s63, s63, s61\n" \
"v_mfma_f32_16x16x32_bf16 v[44:47], v[212:215], a[220:223], v[44:47]\n" \
"s_add_u32 s63, s63, 67108864\n" \
"ds_read_b64_tr_b16 v[208:209], v230 offset:9728\n" \
"s_waitcnt vmcnt(26)\n" \
"v_mfma_f32_16x16x32_bf16 v[40:43], v[212:215], a[216:219], v[40:43]\n" \
"s_mov_b32 s79, 2621440\n" \
"ds_read_b64_tr_b16 v[210:211], v230 offset:9984\n" \
"ds_read_b64_tr_b16 v[216:217], v250 offset:49152\n" \
"v_mfma_f32_16x16x32_bf16 v[36:39], v[212:215], a[212:215], v[36:39]\n" \
"buffer_load_dwordx4 v227, %[wv], s79 offen offset:1024 lds\n" \
"v_mfma_f32_16x16x32_bf16 v[32:35], v[212:215], a[208:211], v[32:35]\n" \
"s_waitcnt lgkmcnt(7)\n" \
"ds_read_b64_tr_b16 v[218:219], v250 offset:51200\n" \
"ds_read_b64_tr_b16 v[220:221], v250 offset:49184\n" \
"v_mfma_f32_16x16x32_bf16 v[16:19], v[192:195], a[208:211], v[16:19]\n" \
"ds_read_b64_tr_b16 v[212:213], v233 offset:9728\n" \
"ds_read_b64_tr_b16 v[222:223], v250 offset:51232\n" \
"v_mfma_f32_16x16x32_bf16 v[20:23], v[192:195], a[212:215], v[20:23]\n" \
"ds_read_b64_tr_b16 v[214:215], v233 offset:9984\n" \
"v_mfma_f32_16x16x32_bf16 v[24:27], v[192:195], a[216:219], v[24:27]\n" \
"buffer_atomic_add v252, v253, %[wsr], s91 offen offset:256 sc1\n" \
"v_mfma_f32_16x16x32_bf16 v[28:31], v[192:195], a[220:223], v[28:31]\n" \
"s_barrier\n" \
"v_mfma_f32_16x16x32_bf16 v[60:63], v[196:199], a[220:223], v[60:63]\n" \
"ds_read_b64_tr_b16 v[192:193], v230 offset:16384\n" \
"v_mfma_f32_16x16x32_bf16 v[56:59], v[196:199], a[216:219], v[56:59]\n" \
"ds_read_b64_tr_b16 v[194:195], v230 offset:16640\n" \
"v_mfma_f32_16x16x32_bf16 v[52:55], v[196:199], a[212:215], v[52:55]\n" \
"buffer_load_dwordx4 v236, %[wv], s79 offen offset:3072 lds\n" \
"v_mfma_f32_16x16x32_bf16 v[48:51], v[196:199], a[208:211], v[48:51]\n" \
"s_waitcnt lgkmcnt(10)\n" \
"v_mfma_f32_16x16x32_bf16 v[64:67], v[200:203], a[208:211], v[64:67]\n" \
"ds_read_b64_tr_b16 v[196:197], v230 offset:24576\n" \
"v_mfma_f32_16x16x32_bf16 v[68:71], v[200:203], a[212:215], v[68:71]\n" \
"ds_read_b64_tr_b16 v[198:199], v230 offset:24832\n" \
"v_mfma_f32_16x16x32_bf16 v[72:75], v[200:203], a[216:219], v[72:75]\n" \
"v_mfma_f32_16x16x32_bf16 v[76:79], v[200:203], a[220:223], v[76:79]\n" \
"v_mfma_f32_16x16x32_bf16 v[108:111], v[204:207], a[220:223], v[108:111]\n" \
"ds_read_b64_tr_b16 v[200:201], v233 offset:16384\n" \
"v_mfma_f32_16x16x32_bf16 v[104:107], v[204:207], a[216:219], v[104:107]\n" \
"ds_read_b64_tr_b16 v[202:203], v233 offset:16640\n" \
"v_mfma_f32_16x16x32_bf16 v[100:103], v[204:207], a[212:215], v[100:103]\n" \
"v_mfma_f32_16x16x32_bf16 v[96:99], v[204:207], a[208:211], v[96:99]\n" \
"s_waitcnt lgkmcnt(6)\n" \
"v_mfma_f32_16x16x32_bf16 v[80:83], v[208:211], a[208:211], v[80:83]\n" \
"ds_read_b64_tr_b16 v[204:205], v233 offset:24576\n" \
"v_mfma_f32_16x16x32_bf16 v[84:87], v[208:211], a[212:215], v[84:87]\n" \
"ds_read_b64_tr_b16 v[206:207], v233 offset:24832\n" \
"v_mfma_f32_16x16x32_bf16 v[88:91], v[208:211], a[216:219], v[88:91]\n" \
"buffer_store_dwordx4 v[216:219], v241, %[wsr], s63 offen offset:0 sc1\n" \
"v_mfma_f32_16x16x32_bf16 v[92:95], v[208:211], a[220:223], v[92:95]\n" \
"s_barrier\n" \
"v_mfma_f32_16x16x32_bf16 v[124:127], v[212:215], a[220:223], v[124:127]\n" \
"ds_read_b64_tr_b16 v[208:209], v230 offset:16896\n" \
"v_mfma_f32_16x16x32_bf16 v[120:123], v[212:215], a[216:219], v[120:123]\n" \
"ds_read_b64_tr_b16 v[210:211], v230 offset:17152\n" \
"v_mfma_f32_16x16x32_bf16 v[116:119], v[212:215], a[212:215], v[116:119]\n" \
"buffer_store_dwordx4 v[220:223], v241, %[wsr], s63 offen offset:1024 sc1\n" \
"v_mfma_f32_16x16x32_bf16 v[112:115], v[212:215], a[208:211], v[112:115]\n" \
"s_waitcnt lgkmcnt(6)\n" \
"ds_read_b64_tr_b16 v[216:217], v250 offset:49216\n" \
"ds_read_b64_tr_b16 v[218:219], v250 offset:51264\n" \
"v_mfma_f32_16x16x32_bf16 v[0:3], v[192:195], a[224:227], v[0:3]\n" \
"ds_read_b64_tr_b16 v[212:213], v230 offset:25088\n" \
"ds_read_b64_tr_b16 v[220:221], v250 offset:49248\n" \
"v_mfma_f32_16x16x32_bf16 v[4:7], v[192:195], a[228:231], v[4:7]\n" \
"ds_read_b64_tr_b16 v[214:215], v230 offset:25344\n" \
"ds_read_b64_tr_b16 v[222:223], v250 offset:51296\n" \
"v_mfma_f32_16x16x32_bf16 v[8:11], v[192:195], a[232:235], v[8:11]\n" \
"v_mfma_f32_16x16x32_bf16 v[12:15], v[192:195], a[236:239], v[12:15]\n" \
"v_mfma_f32_16x16x32_bf16 v[0:3], v[196:199], a[240:243], v[0:3]\n" \
"ds_read_b64_tr_b16 v[192:193], v233 offset:16896\n" \
"v_mfma_f32_16x16x32_bf16 v[4:7], v[196:199], a[244:247], v[4:7]\n" \
"ds_read_b64_tr_b16 v[194:195], v233 offset:17152\n" \
"v_mfma_f32_16x16x32_bf16 v[8:11], v[196:199], a[248:251], v[8:11]\n" \
"v_mfma_f32_16x16x32_bf16 v[12:15], v[196:199], a[252:255], v[12:15]\n" \
"s_waitcnt lgkmcnt(10)\n" \
"v_mfma_f32_16x16x32_bf16 v[32:35], v[200:203], a[224:227], v[32:35]\n" \
"ds_read_b64_tr_b16 v[196:197], v233 offset:25088\n" \
"v_mfma_f32_16x16x32_bf16 v[36:39], v[200:203], a[228:231], v[36:39]\n" \
"ds_read_b64_tr_b16 v[198:199], v233 offset:25344\n" \
"v_mfma_f32_16x16x32_bf16 v[40:43], v[200:203], a[232:235], v[40:43]\n" \
"v_mfma_f32_16x16x32_bf16 v[44:47], v[200:203], a[236:239], v[44:47]\n" \
"s_lshr_b32 s61, %[w4k], 1\n" \
"s_lshl_b32 s62, %[pair], 13\n" \
"s_add_u32 s62, s62, s61\n" \
"v_mfma_f32_16x16x32_bf16 v[32:35], v[204:207], a[240:243], v[32:35]\n" \
"s_add_u32 s62, s62, 4194304\n" \
"ds_read_b64_tr_b16 v[200:201], v230 offset:17408\n" \
"v_mfma_f32_16x16x32_bf16 v[36:39], v[204:207], a[244:247], v[36:39]\n" \
"ds_read_b64_tr_b16 v[202:203], v230 offset:17664\n" \
"v_mfma_f32_16x16x32_bf16 v[40:43], v[204:207], a[248:251], v[40:43]\n" \
"v_mfma_f32_16x16x32_bf16 v[44:47], v[204:207], a[252:255], v[44:47]\n" \
"s_waitcnt lgkmcnt(6)\n" \
"v_cvt_pk_bf16_f32 v0, v0, v1\n" \
"v_cvt_pk_bf16_f32 v1, v2, v3\n" \
"v_mfma_f32_16x16x32_bf16 v[16:19], v[208:211], a[224:227], v[16:19]\n" \
"ds_read_b64_tr_b16 v[204:205], v230 offset:25600\n" \
"v_cvt_pk_bf16_f32 v2, v32, v33\n" \
"v_mfma_f32_16x16x32_bf16 v[20:23], v[208:211], a[228:231], v[20:23]\n" \
"s_add_u32 s63, s63, 0x10000\n" \
"ds_read_b64_tr_b16 v[206:207], v230 offset:25856\n" \
"v_cvt_pk_bf16_f32 v3, v34, v35\n" \
"v_mfma_f32_16x16x32_bf16 v[24:27], v[208:211], a[232:235], v[24:27]\n" \
"buffer_store_dwordx4 v[216:219], v241, %[wsr], s63 offen offset:0 sc1\n" \
"v_cvt_pk_bf16_f32 v8, v8, v9\n" \
"v_mfma_f32_16x16x32_bf16 v[28:31], v[208:211], a[236:239], v[28:31]\n" \
"v_cvt_pk_bf16_f32 v9, v10, v11\n" \
"v_cvt_pk_bf16_f32 v10, v40, v41\n" \
"v_mfma_f32_16x16x32_bf16 v[16:19], v[212:215], a[240:243], v[16:19]\n" \
"ds_read_b64_tr_b16 v[208:209], v233 offset:17408\n" \
"v_cvt_pk_bf16_f32 v11, v42, v43\n" \
"v_mfma_f32_16x16x32_bf16 v[20:23], v[212:215], a[244:247], v[20:23]\n" \
"ds_read_b64_tr_b16 v[210:211], v233 offset:17664\n" \
"v_cvt_pk_bf16_f32 v4, v4, v5\n" \
"v_mfma_f32_16x16x32_bf16 v[24:27], v[212:215], a[248:251], v[24:27]\n" \
"buffer_store_dwordx4 v[220:223], v241, %[wsr], s63 offen offset:1024 sc1\n" \
"v_cvt_pk_bf16_f32 v5, v6, v7\n" \
"v_mfma_f32_16x16x32_bf16 v[28:31], v[212:215], a[252:255], v[28:31]\n" \
"s_waitcnt lgkmcnt(6)\n" \
"v_cvt_pk_bf16_f32 v6, v36, v37\n" \
"v_cvt_pk_bf16_f32 v7, v38, v39\n" \
"v_mfma_f32_16x16x32_bf16 v[48:51], v[192:195], a[224:227], v[48:51]\n" \
"ds_read_b64_tr_b16 v[212:213], v233 offset:25600\n" \
"v_cvt_pk_bf16_f32 v12, v12, v13\n" \
"v_mfma_f32_16x16x32_bf16 v[52:55], v[192:195], a[228:231], v[52:55]\n" \
"ds_read_b64_tr_b16 v[214:215], v233 offset:25856\n" \
"v_cvt_pk_bf16_f32 v13, v14, v15\n" \
"v_mfma_f32_16x16x32_bf16 v[56:59], v[192:195], a[232:235], v[56:59]\n" \
"buffer_load_dwordx4 v[216:219], v241, %[wsr], s62 offen offset:0 sc1\n" \
"v_mfma_f32_16x16x32_bf16 v[60:63], v[192:195], a[236:239], v[60:63]\n" \
"v_cvt_pk_bf16_f32 v14, v44, v45\n" \
"v_cvt_pk_bf16_f32 v15, v46, v47\n" \
"v_mfma_f32_16x16x32_bf16 v[48:51], v[196:199], a[240:243], v[48:51]\n" \
"ds_read_b64_tr_b16 v[192:193], v230 offset:17920\n" \
"v_mfma_f32_16x16x32_bf16 v[52:55], v[196:199], a[244:247], v[52:55]\n" \
"ds_read_b64_tr_b16 v[194:195], v230 offset:18176\n" \
"v_mfma_f32_16x16x32_bf16 v[56:59], v[196:199], a[248:251], v[56:59]\n" \
"buffer_load_dwordx4 v[220:223], v241, %[wsr], s62 offen offset:1024 sc1\n" \
"v_mfma_f32_16x16x32_bf16 v[60:63], v[196:199], a[252:255], v[60:63]\n" \
"s_waitcnt lgkmcnt(6)\n" \
"v_cvt_pk_bf16_f32 v16, v16, v17\n" \
"v_cvt_pk_bf16_f32 v17, v18, v19\n" \
"v_mfma_f32_16x16x32_bf16 v[64:67], v[200:203], a[224:227], v[64:67]\n" \
"s_add_u32 m0, %[w4k], 49152\n" \
"ds_read_b64_tr_b16 v[196:197], v230 offset:26112\n" \
"v_cvt_pk_bf16_f32 v18, v48, v49\n" \
"v_mfma_f32_16x16x32_bf16 v[68:71], v[200:203], a[228:231], v[68:71]\n" \
"s_mov_b32 s79, 3145856\n" \
"ds_read_b64_tr_b16 v[198:199], v230 offset:26368\n" \
"v_cvt_pk_bf16_f32 v19, v50, v51\n" \
"v_mfma_f32_16x16x32_bf16 v[72:75], v[200:203], a[232:235], v[72:75]\n" \
"buffer_load_dwordx4 v226, %[wv], s79 offen offset:0 lds\n" \
"v_mfma_f32_16x16x32_bf16 v[76:79], v[200:203], a[236:239], v[76:79]\n" \
"v_cvt_pk_bf16_f32 v24, v24, v25\n" \
"v_cvt_pk_bf16_f32 v25, v26, v27\n" \
"v_mfma_f32_16x16x32_bf16 v[64:67], v[204:207], a[240:243], v[64:67]\n" \
"ds_read_b64_tr_b16 v[200:201], v233 offset:17920\n" \
"v_cvt_pk_bf16_f32 v26, v56, v57\n" \
"v_mfma_f32_16x16x32_bf16 v[68:71], v[204:207], a[244:247], v[68:71]\n" \
"ds_read_b64_tr_b16 v[202:203], v233 offset:18176\n" \
"v_cvt_pk_bf16_f32 v27, v58, v59\n" \
"v_mfma_f32_16x16x32_bf16 v[72:75], v[204:207], a[248:251], v[72:75]\n" \
"buffer_load_dwordx4 v254, %[wv], s79 offen offset:2048 lds\n" \
"v_mfma_f32_16x16x32_bf16 v[76:79], v[204:207], a[252:255], v[76:79]\n" \
"s_waitcnt lgkmcnt(6)\n" \
"v_cvt_pk_bf16_f32 v20, v20, v21\n" \
"v_cvt_pk_bf16_f32 v21, v22, v23\n" \
"v_mfma_f32_16x16x32_bf16 v[96:99], v[208:211], a[224:227], v[96:99]\n" \
"ds_read_b64_tr_b16 v[204:205], v233 offset:26112\n" \
"v_cvt_pk_bf16_f32 v22, v52, v53\n" \
".LATLws:\n" \
"v_mfma_f32_16x16x32_bf16 v[100:103], v[208:211], a[228:231], v[100:103]\n" \
"s_mov_b32 s79, 3670016\n" \
"ds_read_b64_tr_b16 v[206:207], v233 offset:26368\n" \
"v_cvt_pk_bf16_f32 v23, v54, v55\n" \
"v_mfma_f32_16x16x32_bf16 v[104:107], v[208:211], a[232:235], v[104:107]\n" \
"buffer_load_dwordx4 v227, %[wv], s79 offen offset:1024 lds\n" \
"v_mfma_f32_16x16x32_bf16 v[108:111], v[208:211], a[236:239], v[108:111]\n" \
"s_waitcnt vmcnt(30)\n" \
"s_barrier\n" \
"v_mfma_f32_16x16x32_bf16 v[96:99], v[212:215], a[240:243], v[96:99]\n" \
"ds_read_b64_tr_b16 v[208:209], v229 offset:0\n" \
"v_cvt_pk_bf16_f32 v28, v28, v29\n" \
"v_mfma_f32_16x16x32_bf16 v[100:103], v[212:215], a[244:247], v[100:103]\n" \
"ds_read_b64_tr_b16 v[210:211], v229 offset:256\n" \
"v_cvt_pk_bf16_f32 v29, v30, v31\n" \
"v_mfma_f32_16x16x32_bf16 v[104:107], v[212:215], a[248:251], v[104:107]\n" \
"buffer_load_dwordx4 v236, %[wv], s79 offen offset:3072 lds\n" \
"v_mfma_f32_16x16x32_bf16 v[108:111], v[212:215], a[252:255], v[108:111]\n" \
"s_waitcnt lgkmcnt(6)\n" \
"v_cvt_pk_bf16_f32 v30, v60, v61\n" \
"v_cvt_pk_bf16_f32 v31, v62, v63\n" \
"v_mfma_f32_16x16x32_bf16 v[80:83], v[192:195], a[224:227], v[80:83]\n" \
"ds_read_b64_tr_b16 v[212:213], v232 offset:0\n" \
"v_cvt_pk_bf16_f32 v64, v64, v65\n" \
"v_mfma_f32_16x16x32_bf16 v[84:87], v[192:195], a[228:231], v[84:87]\n" \
"ds_read_b64_tr_b16 v[214:215], v232 offset:256\n" \
"v_cvt_pk_bf16_f32 v65, v66, v67\n" \
"v_mfma_f32_16x16x32_bf16 v[88:91], v[192:195], a[232:235], v[88:91]\n" \
"v_cvt_pk_bf16_f32 v66, v96, v97\n" \
"v_cvt_pk_bf16_f32 v67, v98, v99\n" \
"v_mfma_f32_16x16x32_bf16 v[92:95], v[192:195], a[236:239], v[92:95]\n" \
"v_cvt_pk_bf16_f32 v72, v72, v73\n" \
"v_cvt_pk_bf16_f32 v73, v74, v75\n" \
"v_mfma_f32_16x16x32_bf16 v[80:83], v[196:199], a[240:243], v[80:83]\n" \
"ds_read_b64_tr_b16 v[192:193], v229 offset:512\n" \
"v_cvt_pk_bf16_f32 v74, v104, v105\n" \
"v_mfma_f32_16x16x32_bf16 v[84:87], v[196:199], a[244:247], v[84:87]\n" \
"ds_read_b64_tr_b16 v[194:195], v229 offset:768\n" \
"v_cvt_pk_bf16_f32 v75, v106, v107\n" \
"v_mfma_f32_16x16x32_bf16 v[88:91], v[196:199], a[248:251], v[88:91]\n" \
"v_cvt_pk_bf16_f32 v68, v68, v69\n" \
"v_cvt_pk_bf16_f32 v69, v70, v71\n" \
"v_mfma_f32_16x16x32_bf16 v[92:95], v[196:199], a[252:255], v[92:95]\n" \
"s_waitcnt lgkmcnt(6)\n" \
"v_cvt_pk_bf16_f32 v70, v100, v101\n" \
"v_cvt_pk_bf16_f32 v71, v102, v103\n" \
"v_mfma_f32_16x16x32_bf16 v[112:115], v[200:203], a[224:227], v[112:115]\n" \
"ds_read_b64_tr_b16 v[196:197], v232 offset:512\n" \
"v_cvt_pk_bf16_f32 v76, v76, v77\n" \
"v_mfma_f32_16x16x32_bf16 v[116:119], v[200:203], a[228:231], v[116:119]\n" \
"ds_read_b64_tr_b16 v[198:199], v232 offset:768\n" \
"v_cvt_pk_bf16_f32 v77, v78, v79\n" \
"v_mfma_f32_16x16x32_bf16 v[120:123], v[200:203], a[232:235], v[120:123]\n" \
"v_cvt_pk_bf16_f32 v78, v108, v109\n" \
"v_cvt_pk_bf16_f32 v79, v110, v111\n" \
"v_mfma_f32_16x16x32_bf16 v[124:127], v[200:203], a[236:239], v[124:127]\n" \
"s_barrier\n" \
"v_mfma_f32_16x16x32_bf16 v[112:115], v[204:207], a[240:243], v[112:115]\n" \
"ds_read_b64_tr_b16 v[200:201], v229 offset:8192\n" \
"v_mfma_f32_16x16x32_bf16 v[116:119], v[204:207], a[244:247], v[116:119]\n" \
"ds_read_b64_tr_b16 v[202:203], v229 offset:8448\n" \
"v_mfma_f32_16x16x32_bf16 v[120:123], v[204:207], a[248:251], v[120:123]\n" \
"v_mfma_f32_16x16x32_bf16 v[124:127], v[204:207], a[252:255], v[124:127]\n" \
"s_waitcnt lgkmcnt(6)\n" \
"v_cvt_pk_bf16_f32 v80, v80, v81\n" \
"v_cvt_pk_bf16_f32 v81, v82, v83\n" \
"v_mfma_f32_16x16x32_bf16 v[32:35], a[0:3], v[208:211], 0\n" \
"ds_read_b64_tr_b16 v[204:205], v232 offset:8192\n" \
"v_cvt_pk_bf16_f32 v82, v112, v113\n" \
"v_mfma_f32_16x16x32_bf16 v[40:43], a[4:7], v[208:211], 0\n" \
"s_add_u32 s60, s67, 0\n" \
"ds_read_b64_tr_b16 v[206:207], v232 offset:8448\n" \
"v_cvt_pk_bf16_f32 v83, v114, v115\n" \
"v_mfma_f32_16x16x32_bf16 v[36:39], a[8:11], v[208:211], 0\n" \
"buffer_store_dwordx4 v[0:3], v241, %[wsr], s60 offen offset:0 sc1\n" \
"v_cvt_pk_bf16_f32 v88, v88, v89\n" \
"v_mfma_f32_16x16x32_bf16 v[44:47], a[12:15], v[208:211], 0\n" \
"v_cvt_pk_bf16_f32 v89, v90, v91\n" \
"v_cvt_pk_bf16_f32 v90, v120, v121\n" \
"v_mfma_f32_16x16x32_bf16 v[48:51], a[0:3], v[212:215], 0\n" \
"ds_read_b64_tr_b16 v[208:209], v229 offset:8704\n" \
"v_cvt_pk_bf16_f32 v91, v122, v123\n" \
"v_mfma_f32_16x16x32_bf16 v[56:59], a[4:7], v[212:215], 0\n" \
"ds_read_b64_tr_b16 v[210:211], v229 offset:8960\n" \
"v_cvt_pk_bf16_f32 v84, v84, v85\n" \
"v_mfma_f32_16x16x32_bf16 v[52:55], a[8:11], v[212:215], 0\n" \
"v_cvt_pk_bf16_f32 v85, v86, v87\n" \
"v_cvt_pk_bf16_f32 v86, v116, v117\n" \
"v_mfma_f32_16x16x32_bf16 v[60:63], a[12:15], v[212:215], 0\n" \
"s_waitcnt lgkmcnt(6)\n" \
"v_cvt_pk_bf16_f32 v87, v118, v119\n" \
"v_cvt_pk_bf16_f32 v92, v92, v93\n" \
"v_mfma_f32_16x16x32_bf16 v[96:99], a[0:3], v[192:195], 0\n" \
"ds_read_b64_tr_b16 v[212:213], v232 offset:8704\n" \
"v_cvt_pk_bf16_f32 v93, v94, v95\n" \
"v_mfma_f32_16x16x32_bf16 v[104:107], a[4:7], v[192:195], 0\n" \
"ds_read_b64_tr_b16 v[214:215], v232 offset:8960\n" \
"v_cvt_pk_bf16_f32 v94, v124, v125\n" \
"v_mfma_f32_16x16x32_bf16 v[100:103], a[8:11], v[192:195], 0\n" \
"s_lshr_b32 s61, %[w4k], 2\n" \
"v_cvt_pk_bf16_f32 v95, v126, v127\n" \
"s_add_u32 m0, s61, 157696\n" \
"v_mfma_f32_16x16x32_bf16 v[108:111], a[12:15], v[192:195], 0\n" \
"s_lshr_b32 s62, %[w4k], 12\n" \
"v_mfma_f32_16x16x32_bf16 v[112:115], a[0:3], v[196:199], 0\n" \
"ds_read_b64_tr_b16 v[192:193], v229 offset:1024\n" \
"v_mfma_f32_16x16x32_bf16 v[120:123], a[4:7], v[196:199], 0\n" \
"s_add_u32 s60, s67, 16384\n" \
"ds_read_b64_tr_b16 v[194:195], v229 offset:1280\n" \
"v_mfma_f32_16x16x32_bf16 v[116:119], a[8:11], v[196:199], 0\n" \
"buffer_store_dwordx4 v[8:11], v241, %[wsr], s60 offen offset:0 sc1\n" \
"v_mfma_f32_16x16x32_bf16 v[124:127], a[12:15], v[196:199], 0\n" \
"s_waitcnt lgkmcnt(6)\n" \
"v_mfma_f32_16x16x32_bf16 v[32:35], a[16:19], v[200:203], v[32:35]\n" \
"ds_read_b64_tr_b16 v[196:197], v232 offset:1024\n" \
"v_mfma_f32_16x16x32_bf16 v[40:43], a[20:23], v[200:203], v[40:43]\n" \
"ds_read_b64_tr_b16 v[198:199], v232 offset:1280\n" \
"v_mfma_f32_16x16x32_bf16 v[36:39], a[24:27], v[200:203], v[36:39]\n" \
"buffer_load_dwordx4 v241, %[wsr], s82 offen lds\n" \
"v_mfma_f32_16x16x32_bf16 v[44:47], a[28:31], v[200:203], v[44:47]\n" \
"s_mul_i32 s62, s62, 4160\n" \
"s_add_u32 m0, s62, 131072\n" \
"s_nop 0\n" \
"v_mfma_f32_16x16x32_bf16 v[60:63], a[28:31], v[204:207], v[60:63]\n" \
"ds_read_b64_tr_b16 v[200:201], v229 offset:1536\n" \
"v_mfma_f32_16x16x32_bf16 v[52:55], a[24:27], v[204:207], v[52:55]\n" \
"ds_read_b64_tr_b16 v[202:203], v229 offset:1792\n" \
"v_mfma_f32_16x16x32_bf16 v[56:59], a[20:23], v[204:207], v[56:59]\n" \
"global_load_lds_dwordx4 v241, %[tab] offset:0\n" \
"v_mfma_f32_16x16x32_bf16 v[48:51], a[16:19], v[204:207], v[48:51]\n" \
"s_waitcnt lgkmcnt(6)\n" \
"v_mfma_f32_16x16x32_bf16 v[96:99], a[16:19], v[208:211], v[96:99]\n" \
"ds_read_b64_tr_b16 v[204:205], v232 offset:1536\n" \
"v_mfma_f32_16x16x32_bf16 v[104:107], a[20:23], v[208:211], v[104:107]\n" \
"ds_read_b64_tr_b16 v[206:207], v232 offset:1792\n" \
"v_mfma_f32_16x16x32_bf16 v[100:103], a[24:27], v[208:211], v[100:103]\n" \
"global_load_lds_dwordx4 v241, %[tab] offset:1024\n" \
"v_mfma_f32_16x16x32_bf16 v[108:111], a[28:31], v[208:211], v[108:111]\n" \
"global_load_lds_dwordx4 v241, %[tab] offset:2048\n" \
"v_mfma_f32_16x16x32_bf16 v[124:127], a[28:31], v[212:215], v[124:127]\n" \
"ds_read_b64_tr_b16 v[208:209], v229 offset:9216\n" \
"v_mfma_f32_16x16x32_bf16 v[116:119], a[24:27], v[212:215], v[116:119]\n" \
"s_add_u32 s60, s67, 0\n" \
"ds_read_b64_tr_b16 v[210:211], v229 offset:9472\n" \
"v_mfma_f32_16x16x32_bf16 v[120:123], a[20:23], v[212:215], v[120:123]\n" \
"buffer_store_dwordx4 v[4:7], v241, %[wsr], s60 offen offset:1024 sc1\n" \
"v_mfma_f32_16x16x32_bf16 v[112:115], a[16:19], v[212:215], v[112:115]\n" \
"s_waitcnt lgkmcnt(6)\n" \
"v_mfma_f32_16x16x32_bf16 v[32:35], a[32:35], v[192:195], v[32:35]\n" \
"ds_read_b64_tr_b16 v[212:213], v232 offset:9216\n" \
"v_mfma_f32_16x16x32_bf16 v[40:43], a[36:39], v[192:195], v[40:43]\n" \
"ds_read_b64_tr_b16 v[214:215], v232 offset:9472\n" \
"v_mfma_f32_16x16x32_bf16 v[36:39], a[40:43], v[192:195], v[36:39]\n" \
"global_load_lds_dwordx4 v241, %[tab] offset:3072\n" \
"v_mfma_f32_16x16x32_bf16 v[44:47], a[44:47], v[192:195], v[44:47]\n" \
"v_lshrrev_b32_e32 v250, 4, v240\n" \
"v_and_b32_e32 v251, 15, v240\n" \
"v_mfma_f32_16x16x32_bf16 v[60:63], a[44:47], v[196:199], v[60:63]\n" \
"ds_read_b64_tr_b16 v[192:193], v229 offset:9728\n" \
"v_lshlrev_b32_e32 v225, 2, v250\n" \
"v_mfma_f32_16x16x32_bf16 v[52:55], a[40:43], v[196:199], v[52:55]\n" \
"ds_read_b64_tr_b16 v[194:195], v229 offset:9984\n" \
"v_xor_b32_e32 v239, v225, v251\n" \
"v_mfma_f32_16x16x32_bf16 v[56:59], a[36:39], v[196:199], v[56:59]\n" \
"v_sub_u32_e32 v225, v251, v225\n" \
"v_lshlrev_b32_e32 v239, 4, v239\n" \
"v_mfma_f32_16x16x32_bf16 v[48:51], a[32:35], v[196:199], v[48:51]\n" \
"s_waitcnt lgkmcnt(6)\n" \
"v_lshl_add_u32 v239, v250, 8, v239\n" \
"s_add_u32 s61, s64, 131072\n" \
"v_mfma_f32_16x16x32_bf16 v[96:99], a[32:35], v[200:203], v[96:99]\n" \
"ds_read_b64_tr_b16 v[196:197], v232 offset:9728\n" \
"v_add_u32_e32 v239, s61, v239\n" \
"v_mfma_f32_16x16x32_bf16 v[104:107], a[36:39], v[200:203], v[104:107]\n" \
"s_add_u32 s60, s67, 16384\n" \
"ds_read_b64_tr_b16 v[198:199], v232 offset:9984\n" \
"v_lshlrev_b32_e32 v238, 5, v251\n" \
"v_mfma_f32_16x16x32_bf16 v[100:103], a[40:43], v[200:203], v[100:103]\n" \
"buffer_store_dwordx4 v[12:15], v241, %[wsr], s60 offen offset:1024 sc1\n" \
"v_lshl_add_u32 v238, v250, 4, v238\n" \
"v_mfma_f32_16x16x32_bf16 v[108:111], a[44:47], v[200:203], v[108:111]\n" \
"s_lshr_b32 s61, %[w4k], 13\n" \
"s_mul_i32 s63, s61, 4608\n" \
"s_and_b32 s62, %[w4k], 4096\n" \
"v_mfma_f32_16x16x32_bf16 v[124:127], a[44:47], v[204:207], v[124:127]\n" \
"s_lshr_b32 s62, s62, 4\n" \
"ds_read_b64_tr_b16 v[200:201], v229 offset:16384\n" \
"s_add_u32 s63, s63, s62\n" \
"v_mfma_f32_16x16x32_bf16 v[116:119], a[40:43], v[204:207], v[116:119]\n" \
"s_add_u32 s63, s63, 148512\n" \
"ds_read_b64_tr_b16 v[202:203], v229 offset:16640\n" \
"v_add_u32_e32 v238, s63, v238\n" \
"v_mfma_f32_16x16x32_bf16 v[120:123], a[36:39], v[204:207], v[120:123]\n" \
"v_lshlrev_b32_e32 v236, 3, v250\n" \
"v_sub_u32_e32 v236, v236, v251\n" \
"v_mfma_f32_16x16x32_bf16 v[112:115], a[32:35], v[204:207], v[112:115]\n" \
"s_waitcnt lgkmcnt(6)\n" \
"v_add_u32_e32 v236, 1807, v236\n" \
"v_and_b32_e32 v236, -2, v236\n" \
"v_mfma_f32_16x16x32_bf16 v[32:35], a[48:51], v[208:211], v[32:35]\n" \
"ds_read_b64_tr_b16 v[204:205], v232 offset:16384\n" \
"v_and_b32_e32 v251, 1, v251\n" \
"v_mfma_f32_16x16x32_bf16 v[40:43], a[52:55], v[208:211], v[40:43]\n" \
"ds_read_b64_tr_b16 v[206:207], v232 offset:16640\n" \
"v_xor_b32_e32 v251, 1, v251\n" \
"v_mfma_f32_16x16x32_bf16 v[36:39], a[56:59], v[208:211], v[36:39]\n" \
"s_mul_i32 s63, s61, 8320\n" \
"v_mul_u32_u24_e32 v251, 4160, v251\n" \
"v_lshl_add_u32 v236, v236, 1, v251\n" \
"v_mfma_f32_16x16x32_bf16 v[44:47], a[60:63], v[208:211], v[44:47]\n" \
"s_add_u32 s63, s63, s62\n" \
"s_add_u32 s63, s63, 130560\n" \
"v_add_u32_e32 v226, s63, v236\n" \
"v_mfma_f32_16x16x32_bf16 v[60:63], a[60:63], v[212:215], v[60:63]\n" \
"ds_read_b64_tr_b16 v[208:209], v229 offset:16896\n" \
"v_subrev_u32_e32 v227, 1024, v226\n" \
"v_mfma_f32_16x16x32_bf16 v[52:55], a[56:59], v[212:215], v[52:55]\n" \
"ds_read_b64_tr_b16 v[210:211], v229 offset:17152\n" \
"v_subrev_u32_e32 v230, 2048, v226\n" \
"v_mfma_f32_16x16x32_bf16 v[56:59], a[52:55], v[212:215], v[56:59]\n" \
"v_subrev_u32_e32 v233, 3072, v226\n" \
"v_and_b32_e32 v250, 3, v240\n" \
"v_mfma_f32_16x16x32_bf16 v[48:51], a[48:51], v[212:215], v[48:51]\n" \
"s_waitcnt lgkmcnt(6)\n" \
"v_lshrrev_b32_e32 v251, 2, v240\n" \
"v_lshlrev_b32_e32 v254, 2, v250\n" \
"v_mfma_f32_16x16x32_bf16 v[96:99], a[48:51], v[192:195], v[96:99]\n" \
"ds_read_b64_tr_b16 v[212:213], v232 offset:16896\n" \
"v_xor_b32_e32 v254, v254, v251\n" \
"v_mfma_f32_16x16x32_bf16 v[104:107], a[52:55], v[192:195], v[104:107]\n" \
"s_add_u32 s60, s67, 0\n" \
"ds_read_b64_tr_b16 v[214:215], v232 offset:17152\n" \
"v_lshlrev_b32_e32 v254, 4, v254\n" \
"v_mfma_f32_16x16x32_bf16 v[100:103], a[56:59], v[192:195], v[100:103]\n" \
"buffer_store_dwordx4 v[16:19], v241, %[wsr], s60 offen offset:2048 sc1\n" \
"v_lshl_add_u32 v254, v250, 8, v254\n" \
"v_mfma_f32_16x16x32_bf16 v[108:111], a[60:63], v[192:195], v[108:111]\n" \
"s_lshr_b32 s61, %[w4k], 1\n" \
"s_add_u32 s61, s61, 131072\n" \
"v_add_u32_e32 v254, s61, v254\n" \
"v_mfma_f32_16x16x32_bf16 v[124:127], a[60:63], v[196:199], v[124:127]\n" \
"s_lshr_b32 s61, %[w4k], 7\n" \
"ds_read_b64_tr_b16 v[192:193], v229 offset:24576\n" \
"v_lshrrev_b32_e32 v234, 4, v240\n" \
"v_mfma_f32_16x16x32_bf16 v[116:119], a[56:59], v[196:199], v[116:119]\n" \
"s_add_u32 s61, s61, 147712\n" \
"ds_read_b64_tr_b16 v[194:195], v229 offset:24832\n" \
"v_lshl_add_u32 v234, v234, 3, s61\n" \
"v_mfma_f32_16x16x32_bf16 v[120:123], a[52:55], v[196:199], v[120:123]\n" \
"v_and_b32_e32 v235, 15, v240\n" \
"v_lshlrev_b32_e32 v235, 3, v235\n" \
"v_mfma_f32_16x16x32_bf16 v[112:115], a[48:51], v[196:199], v[112:115]\n" \
"s_waitcnt lgkmcnt(6)\n" \
"v_add_u32_e32 v235, 131072, v235\n" \
"s_waitcnt vmcnt(14)\n" \
"v_mfma_f32_16x16x32_bf16 v[32:35], a[64:67], v[200:203], v[32:35]\n" \
"ds_read_b64_tr_b16 v[196:197], v232 offset:24576\n" \
"v_max3_f16 v250, |v216|, |v217|, |v218|\n" \
"v_mfma_f32_16x16x32_bf16 v[40:43], a[68:71], v[200:203], v[40:43]\n" \
"ds_read_b64_tr_b16 v[198:199], v232 offset:24832\n" \
"v_max3_f32 v251, |v216|, |v217|, |v218|\n" \
"v_mfma_f32_16x16x32_bf16 v[36:39], a[72:75], v[200:203], v[36:39]\n" \
"v_max3_f16 v250, |v250|, |v219|, |v220|\n" \
"v_max3_f32 v251, |v251|, |v219|, |v220|\n" \
"v_mfma_f32_16x16x32_bf16 v[44:47], a[76:79], v[200:203], v[44:47]\n" \
"v_max3_f16 v250, |v250|, |v221|, |v222|\n" \
"v_max3_f32 v251, |v251|, |v221|, |v222|\n" \
"v_mfma_f32_16x16x32_bf16 v[60:63], a[76:79], v[204:207], v[60:63]\n" \
"ds_read_b64_tr_b16 v[200:201], v229 offset:25088\n" \
"v_max3_f16 v250, |v250|, |v223|, |v223|\n" \
"v_mfma_f32_16x16x32_bf16 v[52:55], a[72:75], v[204:207], v[52:55]\n" \
"s_add_u32 s60, s67, 16384\n" \
"ds_read_b64_tr_b16 v[202:203], v229 offset:25344\n" \
"v_max3_f32 v251, |v251|, |v223|, |v223|\n" \
"v_mfma_f32_16x16x32_bf16 v[56:59], a[68:71], v[204:207], v[56:59]\n" \
"buffer_store_dwordx4 v[24:27], v241, %[wsr], s60 offen offset:2048 sc1\n" \
"v_lshlrev_b32_e32 v250, 16, v250\n" \
"v_mfma_f32_16x16x32_bf16 v[48:51], a[64:67], v[204:207], v[48:51]\n" \
"s_waitcnt lgkmcnt(6)\n" \
"v_max_u32_dpp v251, v251, v251 row_ror:8 row_mask:0xf bank_mask:0xf\n" \
"v_max_u32_dpp v250, v250, v250 row_ror:8 row_mask:0xf bank_mask:0xf\n" \
"v_mfma_f32_16x16x32_bf16 v[96:99], a[64:67], v[208:211], v[96:99]\n" \
"ds_read_b64_tr_b16 v[204:205], v232 offset:25088\n" \
"v_max_u32_dpp v251, v251, v251 row_ror:4 row_mask:0xf bank_mask:0xf\n" \
"v_mfma_f32_16x16x32_bf16 v[104:107], a[68:71], v[208:211], v[104:107]\n" \
"ds_read_b64_tr_b16 v[206:207], v232 offset:25344\n" \
"v_max_u32_dpp v250, v250, v250 row_ror:4 row_mask:0xf bank_mask:0xf\n" \
"v_mfma_f32_16x16x32_bf16 v[100:103], a[72:75], v[208:211], v[100:103]\n" \
"v_max_u32_dpp v251, v251, v251 row_ror:2 row_mask:0xf bank_mask:0xf\n" \
"v_max_u32_dpp v250, v250, v250 row_ror:2 row_mask:0xf bank_mask:0xf\n" \
"v_mfma_f32_16x16x32_bf16 v[108:111], a[76:79], v[208:211], v[108:111]\n" \
"v_max_u32_dpp v251, v251, v251 row_ror:1 row_mask:0xf bank_mask:0xf\n" \
"v_max_u32_dpp v250, v250, v250 row_ror:1 row_mask:0xf bank_mask:0xf\n" \
"v_mfma_f32_16x16x32_bf16 v[124:127], a[76:79], v[212:215], v[124:127]\n" \
"ds_read_b64_tr_b16 v[208:209], v229 offset:17408\n" \
"v_mfma_f32_16x16x32_bf16 v[116:119], a[72:75], v[212:215], v[116:119]\n" \
"ds_read_b64_tr_b16 v[210:211], v229 offset:17664\n" \
"v_mfma_f32_16x16x32_bf16 v[120:123], a[68:71], v[212:215], v[120:123]\n" \
"ds_write_b64 v234, v[250:251]\n" \
"v_mfma_f32_16x16x32_bf16 v[112:115], a[64:67], v[212:215], v[112:115]\n" \
"s_waitcnt lgkmcnt(7)\n" \
"v_mfma_f32_16x16x32_bf16 v[32:35], a[80:83], v[192:195], v[32:35]\n" \
"ds_read_b64_tr_b16 v[212:213], v232 offset:17408\n" \
"v_mfma_f32_16x16x32_bf16 v[40:43], a[84:87], v[192:195], v[40:43]\n" \
"ds_read_b64_tr_b16 v[214:215], v232 offset:17664\n" \
"v_mfma_f32_16x16x32_bf16 v[36:39], a[88:91], v[192:195], v[36:39]\n" \
"v_mfma_f32_16x16x32_bf16 v[44:47], a[92:95], v[192:195], v[44:47]\n" \
"v_mfma_f32_16x16x32_bf16 v[60:63], a[92:95], v[196:199], v[60:63]\n" \
"ds_read_b64_tr_b16 v[192:193], v229 offset:17920\n" \
"v_mfma_f32_16x16x32_bf16 v[52:55], a[88:91], v[196:199], v[52:55]\n" \
"s_add_u32 s60, s67, 0\n" \
"ds_read_b64_tr_b16 v[194:195], v229 offset:18176\n" \
"v_mfma_f32_16x16x32_bf16 v[56:59], a[84:87], v[196:199], v[56:59]\n" \
"buffer_store_dwordx4 v[20:23], v241, %[wsr], s60 offen offset:3072 sc1\n" \
"v_mfma_f32_16x16x32_bf16 v[48:51], a[80:83], v[196:199], v[48:51]\n" \
"s_waitcnt lgkmcnt(7)\n" \
"v_mfma_f32_16x16x32_bf16 v[96:99], a[80:83], v[200:203], v[96:99]\n" \
"ds_read_b64_tr_b16 v[196:197], v232 offset:17920\n" \
"v_mfma_f32_16x16x32_bf16 v[104:107], a[84:87], v[200:203], v[104:107]\n" \
"ds_read_b64_tr_b16 v[198:199], v232 offset:18176\n" \
"v_mfma_f32_16x16x32_bf16 v[100:103], a[88:91], v[200:203], v[100:103]\n" \
"v_mfma_f32_16x16x32_bf16 v[108:111], a[92:95], v[200:203], v[108:111]\n" \
"v_mfma_f32_16x16x32_bf16 v[124:127], a[92:95], v[204:207], v[124:127]\n" \
"ds_read_b64_tr_b16 v[200:201], v229 offset:25600\n" \
"v_mfma_f32_16x16x32_bf16 v[116:119], a[88:91], v[204:207], v[116:119]\n" \
"ds_read_b64_tr_b16 v[202:203], v229 offset:25856\n" \
"v_mfma_f32_16x16x32_bf16 v[120:123], a[84:87], v[204:207], v[120:123]\n" \
"v_mfma_f32_16x16x32_bf16 v[112:115], a[80:83], v[204:207], v[112:115]\n" \
"s_waitcnt lgkmcnt(6)\n" \
"v_mfma_f32_16x16x32_bf16 v[32:35], a[96:99], v[208:211], v[32:35]\n" \
"ds_read_b64_tr_b16 v[204:205], v232 offset:25600\n" \
"v_mfma_f32_16x16x32_bf16 v[40:43], a[100:103], v[208:211], v[40:43]\n" \
"s_add_u32 s60, s67, 16384\n" \
"ds_read_b64_tr_b16 v[206:207], v232 offset:25856\n" \
"v_mfma_f32_16x16x32_bf16 v[36:39], a[104:107], v[208:211], v[36:39]\n" \
"buffer_store_dwordx4 v[28:31], v241, %[wsr], s60 offen offset:3072 sc1\n" \
"v_mfma_f32_16x16x32_bf16 v[44:47], a[108:111], v[208:211], v[44:47]\n" \
"v_mfma_f32_16x16x32_bf16 v[60:63], a[108:111], v[212:215], v[60:63]\n" \
"ds_read_b64_tr_b16 v[208:209], v229 offset:26112\n" \
"v_mfma_f32_16x16x32_bf16 v[52:55], a[104:107], v[212:215], v[52:55]\n" \
"ds_read_b64_tr_b16 v[210:211], v229 offset:26368\n" \
"v_mfma_f32_16x16x32_bf16 v[56:59], a[100:103], v[212:215], v[56:59]\n" \
"v_mfma_f32_16x16x32_bf16 v[48:51], a[96:99], v[212:215], v[48:51]\n" \
"s_waitcnt lgkmcnt(6)\n" \
"v_mfma_f32_16x16x32_bf16 v[96:99], a[96:99], v[192:195], v[96:99]\n" \
"ds_read_b64_tr_b16 v[212:213], v232 offset:26112\n" \
"v_mfma_f32_16x16x32_bf16 v[104:107], a[100:103], v[192:195], v[104:107]\n" \
"ds_read_b64_tr_b16 v[214:215], v232 offset:26368\n" \
"v_mfma_f32_16x16x32_bf16 v[100:103], a[104:107], v[192:195], v[100:103]\n" \
"v_mfma_f32_16x16x32_bf16 v[108:111], a[108:111], v[192:195], v[108:111]\n" \
"s_waitcnt vmcnt(5)\n" \
"s_barrier\n" \
"v_mfma_f32_16x16x32_bf16 v[124:127], a[108:111], v[196:199], v[124:127]\n" \
"ds_read_b64_tr_b16 v[192:193], v229 offset:32768\n" \
"ds_read_b64 v[250:251], v235 offset:16640\n" \
"v_mfma_f32_16x16x32_bf16 v[116:119], a[104:107], v[196:199], v[116:119]\n" \
"ds_read_b64_tr_b16 v[194:195], v229 offset:33024\n" \
"v_mfma_f32_16x16x32_bf16 v[120:123], a[100:103], v[196:199], v[120:123]\n" \
"buffer_atomic_add v252, v237, %[wsr], s87 offen offset:384 sc1\n" \
"v_mfma_f32_16x16x32_bf16 v[112:115], a[96:99], v[196:199], v[112:115]\n" \
"s_waitcnt lgkmcnt(7)\n" \
"v_mfma_f32_16x16x32_bf16 v[32:35], a[112:115], v[200:203], v[32:35]\n" \
"ds_read_b64_tr_b16 v[196:197], v232 offset:32768\n" \
"v_mfma_f32_16x16x32_bf16 v[40:43], a[116:119], v[200:203], v[40:43]\n" \
"s_add_u32 s60, s67, 4096\n" \
"ds_read_b64_tr_b16 v[198:199], v232 offset:33024\n" \
"s_waitcnt lgkmcnt(3)\n" \
"v_mfma_f32_16x16x32_bf16 v[36:39], a[120:123], v[200:203], v[36:39]\n" \
"buffer_store_dwordx4 v[64:67], v241, %[wsr], s60 offen offset:0 sc1\n" \
"v_max_u32_dpp v251, v251, v251 row_ror:8 row_mask:0xf bank_mask:0xf\n" \
"v_mfma_f32_16x16x32_bf16 v[44:47], a[124:127], v[200:203], v[44:47]\n" \
"v_max_u32_dpp v250, v250, v250 row_ror:8 row_mask:0xf bank_mask:0xf\n" \
"v_mfma_f32_16x16x32_bf16 v[60:63], a[124:127], v[204:207], v[60:63]\n" \
"ds_read_b64_tr_b16 v[200:201], v229 offset:33280\n" \
"v_max_u32_dpp v251, v251, v251 row_ror:4 row_mask:0xf bank_mask:0xf\n" \
"v_mfma_f32_16x16x32_bf16 v[52:55], a[120:123], v[204:207], v[52:55]\n" \
"ds_read_b64_tr_b16 v[202:203], v229 offset:33536\n" \
"v_max_u32_dpp v250, v250, v250 row_ror:4 row_mask:0xf bank_mask:0xf\n" \
"v_mfma_f32_16x16x32_bf16 v[56:59], a[116:119], v[204:207], v[56:59]\n" \
"v_max_u32_dpp v251, v251, v251 row_ror:2 row_mask:0xf bank_mask:0xf\n" \
"v_max_u32_dpp v250, v250, v250 row_ror:2 row_mask:0xf bank_mask:0xf\n" \
"v_mfma_f32_16x16x32_bf16 v[48:51], a[112:115], v[204:207], v[48:51]\n" \
"v_max_u32_dpp v251, v251, v251 row_ror:1 row_mask:0xf bank_mask:0xf\n" \
"v_max_u32_dpp v250, v250, v250 row_ror:1 row_mask:0xf bank_mask:0xf\n" \
"v_mfma_f32_16x16x32_bf16 v[96:99], a[112:115], v[208:211], v[96:99]\n" \
"ds_read_b64_tr_b16 v[204:205], v232 offset:33280\n" \
"v_lshrrev_b32_e32 v250, 23, v250\n" \
"v_mfma_f32_16x16x32_bf16 v[104:107], a[116:119], v[208:211], v[104:107]\n" \
"ds_read_b64_tr_b16 v[206:207], v232 offset:33536\n" \
"v_max_u32_e32 v250, 20, v250\n" \
"v_mfma_f32_16x16x32_bf16 v[100:103], a[120:123], v[208:211], v[100:103]\n" \
"v_min_u32_e32 v250, 230, v250\n" \
"v_sub_u32_e32 v234, 268, v250\n" \
"v_mfma_f32_16x16x32_bf16 v[108:111], a[124:127], v[208:211], v[108:111]\n" \
"v_lshlrev_b32_e32 v234, 23, v234\n" \
"v_subrev_u32_e32 v250, 14, v250\n" \
"v_mfma_f32_16x16x32_bf16 v[124:127], a[124:127], v[212:215], v[124:127]\n" \
"ds_read_b64_tr_b16 v[208:209], v229 offset:40960\n" \
"v_lshlrev_b32_e32 v250, 23, v250\n" \
"v_mfma_f32_16x16x32_bf16 v[116:119], a[120:123], v[212:215], v[116:119]\n" \
"s_add_u32 s60, s67, 20480\n" \
"ds_read_b64_tr_b16 v[210:211], v229 offset:41216\n" \
"v_lshrrev_b32_e32 v251, 23, v251\n" \
"v_mfma_f32_16x16x32_bf16 v[120:123], a[116:119], v[212:215], v[120:123]\n" \
"buffer_store_dwordx4 v[72:75], v241, %[wsr], s60 offen offset:0 sc1\n" \
"v_max_u32_e32 v251, 20, v251\n" \
"v_mfma_f32_16x16x32_bf16 v[112:115], a[112:115], v[212:215], v[112:115]\n" \
"s_waitcnt lgkmcnt(6)\n" \
"v_min_u32_e32 v251, 230, v251\n" \
"v_sub_u32_e32 v235, 268, v251\n" \
"v_mfma_f32_16x16x32_bf16 v[32:35], a[128:131], v[192:195], v[32:35]\n" \
"ds_read_b64_tr_b16 v[212:213], v232 offset:40960\n" \
"v_lshlrev_b32_e32 v235, 23, v235\n" \
"v_mfma_f32_16x16x32_bf16 v[40:43], a[132:135], v[192:195], v[40:43]\n" \
"ds_read_b64_tr_b16 v[214:215], v232 offset:41216\n" \
"v_subrev_u32_e32 v251, 14, v251\n" \
"v_mfma_f32_16x16x32_bf16 v[36:39], a[136:139], v[192:195], v[36:39]\n" \
"s_lshr_b32 s63, %[w4k], 13\n" \
"v_lshlrev_b32_e32 v251, 23, v251\n" \
"s_mul_i32 s63, s63, 2048\n" \
"v_mfma_f32_16x16x32_bf16 v[44:47], a[140:143], v[192:195], v[44:47]\n" \
"s_add_u32 s63, s63, 157696\n" \
"s_lshr_b32 s62, %[w4k], 12\n" \
"s_mul_i32 s62, s62, 4160\n" \
"v_mfma_f32_16x16x32_bf16 v[60:63], a[140:143], v[196:199], v[60:63]\n" \
"s_add_u32 s62, s62, 131072\n" \
"ds_read_b64_tr_b16 v[192:193], v229 offset:41472\n" \
"v_add_u32_e32 v26, s62, v241\n" \
"v_mfma_f32_16x16x32_bf16 v[52:55], a[136:139], v[196:199], v[52:55]\n" \
"ds_read_b64_tr_b16 v[194:195], v229 offset:41728\n" \
"ds_read_b128 v[0:3], v26 offset:0\n" \
"v_mfma_f32_16x16x32_bf16 v[56:59], a[132:135], v[196:199], v[56:59]\n" \
"ds_read_b128 v[4:7], v26 offset:1024\n" \
"ds_read_b128 v[8:11], v26 offset:2048\n" \
"v_mfma_f32_16x16x32_bf16 v[48:51], a[128:131], v[196:199], v[48:51]\n" \
"s_waitcnt lgkmcnt(9)\n" \
"ds_read_b128 v[12:15], v26 offset:3072\n" \
"v_and_b32_e32 v27, 28, v242\n" \
"v_mfma_f32_16x16x32_bf16 v[96:99], a[128:131], v[200:203], v[96:99]\n" \
"ds_read_b64_tr_b16 v[196:197], v232 offset:41472\n" \
"v_add_u32_e32 v27, s62, v27\n" \
"v_mfma_f32_16x16x32_bf16 v[104:107], a[132:135], v[200:203], v[104:107]\n" \
"ds_read_b64_tr_b16 v[198:199], v232 offset:41728\n" \
"v_mov_b32_e32 v28, 0\n" \
"v_mfma_f32_16x16x32_bf16 v[100:103], a[136:139], v[200:203], v[100:103]\n" \
"v_mov_b32_e32 v29, 0\n" \
"v_mfma_f32_16x16x32_bf16 v[108:111], a[140:143], v[200:203], v[108:111]\n" \
"ds_write_b32 v27, v28 offset:4096\n" \
"v_mfma_f32_16x16x32_bf16 v[124:127], a[140:143], v[204:207], v[124:127]\n" \
"s_lshr_b32 s61, %[w4k], 2\n" \
"ds_read_b64_tr_b16 v[200:201], v229 offset:33792\n" \
"v_lshrrev_b32_e32 v224, 1, v241\n" \
"v_mfma_f32_16x16x32_bf16 v[116:119], a[136:139], v[204:207], v[116:119]\n" \
"s_add_u32 s60, s67, 4096\n" \
"ds_read_b64_tr_b16 v[202:203], v229 offset:34048\n" \
"v_add_u32_e32 v224, 148480, v224\n" \
"v_mfma_f32_16x16x32_bf16 v[120:123], a[132:135], v[204:207], v[120:123]\n" \
"buffer_store_dwordx4 v[68:71], v241, %[wsr], s60 offen offset:1024 sc1\n" \
"v_mfma_f32_16x16x32_bf16 v[112:115], a[128:131], v[204:207], v[112:115]\n" \
"s_waitcnt lgkmcnt(11)\n" \
"v_mfma_f32_16x16x32_bf16 v[32:35], a[144:147], v[208:211], v[32:35]\n" \
"ds_read_b64_tr_b16 v[204:205], v232 offset:33792\n" \
"v_mfma_f32_16x16x32_bf16 v[40:43], a[148:151], v[208:211], v[40:43]\n" \
"ds_read_b64_tr_b16 v[206:207], v232 offset:34048\n" \
"v_mfma_f32_16x16x32_bf16 v[36:39], a[152:155], v[208:211], v[36:39]\n" \
"ds_write2st64_b64 v224, v[28:29], v[28:29] offset0:0 offset1:9\n" \
"v_mfma_f32_16x16x32_bf16 v[44:47], a[156:159], v[208:211], v[44:47]\n" \
"s_waitcnt lgkmcnt(8)\n" \
"v_add_u32_e32 v224, s61, v224\n" \
"v_add_u32_sdwa v24, s63, v0 dst_sel:DWORD dst_unused:UNUSED_PAD src0_sel:DWORD src1_sel:WORD_0\n" \
"v_mfma_f32_16x16x32_bf16 v[60:63], a[156:159], v[212:215], v[60:63]\n" \
"ds_read_b64_tr_b16 v[208:209], v229 offset:34304\n" \
"v_add_u32_sdwa v25, s63, v0 dst_sel:DWORD dst_unused:UNUSED_PAD src0_sel:DWORD src1_sel:WORD_1\n" \
"v_mfma_f32_16x16x32_bf16 v[52:55], a[152:155], v[212:215], v[52:55]\n" \
"ds_read_b64_tr_b16 v[210:211], v229 offset:34560\n" \
"ds_read_u16 v0, v24\n" \
"v_mfma_f32_16x16x32_bf16 v[56:59], a[148:151], v[212:215], v[56:59]\n" \
"ds_read_u16 v16, v25\n" \
"v_add_u32_sdwa v24, s63, v1 dst_sel:DWORD dst_unused:UNUSED_PAD src0_sel:DWORD src1_sel:WORD_0\n" \
"v_mfma_f32_16x16x32_bf16 v[48:51], a[144:147], v[212:215], v[48:51]\n" \
"s_waitcnt lgkmcnt(10)\n" \
"v_add_u32_sdwa v25, s63, v1 dst_sel:DWORD dst_unused:UNUSED_PAD src0_sel:DWORD src1_sel:WORD_1\n" \
"v_mfma_f32_16x16x32_bf16 v[96:99], a[144:147], v[192:195], v[96:99]\n" \
"ds_read_b64_tr_b16 v[212:213], v232 offset:34304\n" \
"ds_read_u16 v1, v24\n" \
"v_mfma_f32_16x16x32_bf16 v[104:107], a[148:151], v[192:195], v[104:107]\n" \
"s_add_u32 s60, s67, 20480\n" \
"ds_read_b64_tr_b16 v[214:215], v232 offset:34560\n" \
"ds_read_u16 v17, v25\n" \
"v_mfma_f32_16x16x32_bf16 v[100:103], a[152:155], v[192:195], v[100:103]\n" \
"buffer_store_dwordx4 v[76:79], v241, %[wsr], s60 offen offset:1024 sc1\n" \
"v_add_u32_sdwa v24, s63, v2 dst_sel:DWORD dst_unused:UNUSED_PAD src0_sel:DWORD src1_sel:WORD_0\n" \
"v_mfma_f32_16x16x32_bf16 v[108:111], a[156:159], v[192:195], v[108:111]\n" \
"v_add_u32_sdwa v25, s63, v2 dst_sel:DWORD dst_unused:UNUSED_PAD src0_sel:DWORD src1_sel:WORD_1\n" \
"v_mfma_f32_16x16x32_bf16 v[124:127], a[156:159], v[196:199], v[124:127]\n" \
"ds_read_b64_tr_b16 v[192:193], v229 offset:41984\n" \
"ds_read_u16 v2, v24\n" \
"v_mfma_f32_16x16x32_bf16 v[116:119], a[152:155], v[196:199], v[116:119]\n" \
"ds_read_b64_tr_b16 v[194:195], v229 offset:42240\n" \
"ds_read_u16 v18, v25\n" \
"v_mfma_f32_16x16x32_bf16 v[120:123], a[148:151], v[196:199], v[120:123]\n" \
"v_add_u32_sdwa v24, s63, v3 dst_sel:DWORD dst_unused:UNUSED_PAD src0_sel:DWORD src1_sel:WORD_0\n" \
"v_add_u32_sdwa v25, s63, v3 dst_sel:DWORD dst_unused:UNUSED_PAD src0_sel:DWORD src1_sel:WORD_1\n" \
"v_mfma_f32_16x16x32_bf16 v[112:115], a[144:147], v[196:199], v[112:115]\n" \
"s_waitcnt lgkmcnt(13)\n" \
"ds_read_u16 v3, v24\n" \
"ds_read_u16 v19, v25\n" \
"v_mfma_f32_16x16x32_bf16 v[32:35], a[160:163], v[200:203], v[32:35]\n" \
"ds_read_b64_tr_b16 v[196:197], v232 offset:41984\n" \
"v_add_u32_sdwa v24, s63, v4 dst_sel:DWORD dst_unused:UNUSED_PAD src0_sel:DWORD src1_sel:WORD_0\n" \
"v_mfma_f32_16x16x32_bf16 v[40:43], a[164:167], v[200:203], v[40:43]\n" \
"ds_read_b64_tr_b16 v[198:199], v232 offset:42240\n" \
"v_add_u32_sdwa v25, s63, v4 dst_sel:DWORD dst_unused:UNUSED_PAD src0_sel:DWORD src1_sel:WORD_1\n" \
"v_mfma_f32_16x16x32_bf16 v[36:39], a[168:171], v[200:203], v[36:39]\n" \
"ds_read_u16 v4, v24\n" \
"ds_read_u16 v20, v25\n" \
"v_mfma_f32_16x16x32_bf16 v[44:47], a[172:175], v[200:203], v[44:47]\n" \
"v_add_u32_sdwa v24, s63, v5 dst_sel:DWORD dst_unused:UNUSED_PAD src0_sel:DWORD src1_sel:WORD_0\n" \
"v_add_u32_sdwa v25, s63, v5 dst_sel:DWORD dst_unused:UNUSED_PAD src0_sel:DWORD src1_sel:WORD_1\n" \
"v_mfma_f32_16x16x32_bf16 v[60:63], a[172:175], v[204:207], v[60:63]\n" \
"ds_read_b64_tr_b16 v[200:201], v229 offset:42496\n" \
"ds_read_u16 v5, v24\n" \
"v_mfma_f32_16x16x32_bf16 v[52:55], a[168:171], v[204:207], v[52:55]\n" \
"ds_read_b64_tr_b16 v[202:203], v229 offset:42752\n" \
"ds_read_u16 v21, v25\n" \
"v_mfma_f32_16x16x32_bf16 v[56:59], a[164:167], v[204:207], v[56:59]\n" \
"v_add_u32_sdwa v24, s63, v6 dst_sel:DWORD dst_unused:UNUSED_PAD src0_sel:DWORD src1_sel:WORD_0\n" \
"v_add_u32_sdwa v25, s63, v6 dst_sel:DWORD dst_unused:UNUSED_PAD src0_sel:DWORD src1_sel:WORD_1\n" \
"v_mfma_f32_16x16x32_bf16 v[48:51], a[160:163], v[204:207], v[48:51]\n" \
"s_waitcnt lgkmcnt(15)\n" \
"ds_read_u16 v6, v24\n" \
"ds_read_u16 v22, v25\n" \
"v_mfma_f32_16x16x32_bf16 v[96:99], a[160:163], v[208:211], v[96:99]\n" \
"ds_read_b64_tr_b16 v[204:205], v232 offset:42496\n" \
"v_add_u32_sdwa v24, s63, v7 dst_sel:DWORD dst_unused:UNUSED_PAD src0_sel:DWORD src1_sel:WORD_0\n" \
"v_mfma_f32_16x16x32_bf16 v[104:107], a[164:167], v[208:211], v[104:107]\n" \
"s_add_u32 s60, s67, 4096\n" \
"ds_read_b64_tr_b16 v[206:207], v232 offset:42752\n" \
"v_add_u32_sdwa v25, s63, v7 dst_sel:DWORD dst_unused:UNUSED_PAD src0_sel:DWORD src1_sel:WORD_1\n" \
"v_mfma_f32_16x16x32_bf16 v[100:103], a[168:171], v[208:211], v[100:103]\n" \
"buffer_store_dwordx4 v[80:83], v241, %[wsr], s60 offen offset:2048 sc1\n" \
"v_mfma_f32_16x16x32_bf16 v[108:111], a[172:175], v[208:211], v[108:111]\n" \
"ds_read_u16 v7, v24\n" \
"ds_read_u16 v23, v25\n" \
"v_mfma_f32_16x16x32_bf16 v[124:127], a[172:175], v[212:215], v[124:127]\n" \
"ds_read_b64_tr_b16 v[208:209], v229 offset:49152\n" \
"s_waitcnt lgkmcnt(15)\n" \
"v_mfma_f32_16x16x32_bf16 v[116:119], a[168:171], v[212:215], v[116:119]\n" \
"ds_read_b64_tr_b16 v[210:211], v229 offset:49408\n" \
"v_lshl_or_b32 v0, v16, 16, v0\n" \
"v_mfma_f32_16x16x32_bf16 v[120:123], a[164:167], v[212:215], v[120:123]\n" \
"v_lshl_or_b32 v1, v17, 16, v1\n" \
"v_lshl_or_b32 v2, v18, 16, v2\n" \
"v_mfma_f32_16x16x32_bf16 v[112:115], a[160:163], v[212:215], v[112:115]\n" \
"s_waitcnt lgkmcnt(14)\n" \
"v_lshl_or_b32 v3, v19, 16, v3\n" \
"v_mfma_f32_16x16x32_bf16 v[32:35], a[176:179], v[192:195], v[32:35]\n" \
"ds_read_b64_tr_b16 v[212:213], v232 offset:49152\n" \
"v_mfma_f32_16x16x32_bf16 v[40:43], a[180:183], v[192:195], v[40:43]\n" \
"ds_read_b64_tr_b16 v[214:215], v232 offset:49408\n" \
"v_mfma_f32_16x16x32_bf16 v[36:39], a[184:187], v[192:195], v[36:39]\n" \
"ds_write_b128 v26, v[0:3] offset:0\n" \
"v_mfma_f32_16x16x32_bf16 v[44:47], a[188:191], v[192:195], v[44:47]\n" \
"v_add_u32_sdwa v24, s63, v8 dst_sel:DWORD dst_unused:UNUSED_PAD src0_sel:DWORD src1_sel:WORD_0\n" \
"v_add_u32_sdwa v25, s63, v8 dst_sel:DWORD dst_unused:UNUSED_PAD src0_sel:DWORD src1_sel:WORD_1\n" \
"v_mfma_f32_16x16x32_bf16 v[60:63], a[188:191], v[196:199], v[60:63]\n" \
"ds_read_b64_tr_b16 v[192:193], v229 offset:49664\n" \
"ds_read_u16 v8, v24\n" \
"v_mfma_f32_16x16x32_bf16 v[52:55], a[184:187], v[196:199], v[52:55]\n" \
"s_add_u32 s60, s67, 20480\n" \
"ds_read_b64_tr_b16 v[194:195], v229 offset:49920\n" \
"ds_read_u16 v16, v25\n" \
"v_mfma_f32_16x16x32_bf16 v[56:59], a[180:183], v[196:199], v[56:59]\n" \
"buffer_store_dwordx4 v[88:91], v241, %[wsr], s60 offen offset:2048 sc1\n" \
"v_add_u32_sdwa v24, s63, v9 dst_sel:DWORD dst_unused:UNUSED_PAD src0_sel:DWORD src1_sel:WORD_0\n" \
"v_mfma_f32_16x16x32_bf16 v[48:51], a[176:179], v[196:199], v[48:51]\n" \
"s_waitcnt lgkmcnt(11)\n" \
"v_add_u32_sdwa v25, s63, v9 dst_sel:DWORD dst_unused:UNUSED_PAD src0_sel:DWORD src1_sel:WORD_1\n" \
"v_mfma_f32_16x16x32_bf16 v[96:99], a[176:179], v[200:203], v[96:99]\n" \
"ds_read_b64_tr_b16 v[196:197], v232 offset:49664\n" \
"ds_read_u16 v9, v24\n" \
"v_mfma_f32_16x16x32_bf16 v[104:107], a[180:183], v[200:203], v[104:107]\n" \
"ds_read_b64_tr_b16 v[198:199], v232 offset:49920\n" \
"ds_read_u16 v17, v25\n" \
"v_mfma_f32_16x16x32_bf16 v[100:103], a[184:187], v[200:203], v[100:103]\n" \
"v_add_u32_sdwa v24, s63, v10 dst_sel:DWORD dst_unused:UNUSED_PAD src0_sel:DWORD src1_sel:WORD_0\n" \
"v_add_u32_sdwa v25, s63, v10 dst_sel:DWORD dst_unused:UNUSED_PAD src0_sel:DWORD src1_sel:WORD_1\n" \
"v_mfma_f32_16x16x32_bf16 v[108:111], a[188:191], v[200:203], v[108:111]\n" \
"ds_read_u16 v10, v24\n" \
"ds_read_u16 v18, v25\n" \
"v_mfma_f32_16x16x32_bf16 v[124:127], a[188:191], v[204:207], v[124:127]\n" \
"ds_read_b64_tr_b16 v[200:201], v229 offset:57344\n" \
"v_add_u32_sdwa v24, s63, v11 dst_sel:DWORD dst_unused:UNUSED_PAD src0_sel:DWORD src1_sel:WORD_0\n" \
"v_mfma_f32_16x16x32_bf16 v[116:119], a[184:187], v[204:207], v[116:119]\n" \
"ds_read_b64_tr_b16 v[202:203], v229 offset:57600\n" \
"v_add_u32_sdwa v25, s63, v11 dst_sel:DWORD dst_unused:UNUSED_PAD src0_sel:DWORD src1_sel:WORD_1\n" \
"v_mfma_f32_16x16x32_bf16 v[120:123], a[180:183], v[204:207], v[120:123]\n" \
"ds_read_u16 v11, v24\n" \
"ds_read_u16 v19, v25\n" \
"v_mfma_f32_16x16x32_bf16 v[112:115], a[176:179], v[204:207], v[112:115]\n" \
"s_waitcnt lgkmcnt(15)\n" \
"v_lshl_or_b32 v4, v20, 16, v4\n" \
"v_mfma_f32_16x16x32_bf16 v[32:35], a[192:195], v[208:211], v[32:35]\n" \
"ds_read_b64_tr_b16 v[204:205], v232 offset:57344\n" \
"v_lshl_or_b32 v5, v21, 16, v5\n" \
"v_mfma_f32_16x16x32_bf16 v[40:43], a[196:199], v[208:211], v[40:43]\n" \
"ds_read_b64_tr_b16 v[206:207], v232 offset:57600\n" \
"v_lshl_or_b32 v6, v22, 16, v6\n" \
"v_mfma_f32_16x16x32_bf16 v[36:39], a[200:203], v[208:211], v[36:39]\n" \
"v_lshl_or_b32 v7, v23, 16, v7\n" \
"v_mfma_f32_16x16x32_bf16 v[44:47], a[204:207], v[208:211], v[44:47]\n" \
"ds_write_b128 v26, v[4:7] offset:1024\n" \
"v_mfma_f32_16x16x32_bf16 v[60:63], a[204:207], v[212:215], v[60:63]\n" \
"ds_read_b64_tr_b16 v[208:209], v229 offset:57856\n" \
"v_add_u32_sdwa v24, s63, v12 dst_sel:DWORD dst_unused:UNUSED_PAD src0_sel:DWORD src1_sel:WORD_0\n" \
"v_mfma_f32_16x16x32_bf16 v[52:55], a[200:203], v[212:215], v[52:55]\n" \
"s_add_u32 s60, s67, 4096\n" \
"ds_read_b64_tr_b16 v[210:211], v229 offset:58112\n" \
"v_add_u32_sdwa v25, s63, v12 dst_sel:DWORD dst_unused:UNUSED_PAD src0_sel:DWORD src1_sel:WORD_1\n" \
"v_mfma_f32_16x16x32_bf16 v[56:59], a[196:199], v[212:215], v[56:59]\n" \
"buffer_store_dwordx4 v[84:87], v241, %[wsr], s60 offen offset:3072 sc1\n" \
"v_mfma_f32_16x16x32_bf16 v[48:51], a[192:195], v[212:215], v[48:51]\n" \
"s_waitcnt lgkmcnt(12)\n" \
"ds_read_u16 v12, v24\n" \
"ds_read_u16 v20, v25\n" \
"v_mfma_f32_16x16x32_bf16 v[96:99], a[192:195], v[192:195], v[96:99]\n" \
"ds_read_b64_tr_b16 v[212:213], v232 offset:57856\n" \
"v_add_u32_sdwa v24, s63, v13 dst_sel:DWORD dst_unused:UNUSED_PAD src0_sel:DWORD src1_sel:WORD_0\n" \
"v_mfma_f32_16x16x32_bf16 v[104:107], a[196:199], v[192:195], v[104:107]\n" \
"ds_read_b64_tr_b16 v[214:215], v232 offset:58112\n" \
"v_add_u32_sdwa v25, s63, v13 dst_sel:DWORD dst_unused:UNUSED_PAD src0_sel:DWORD src1_sel:WORD_1\n" \
"v_mfma_f32_16x16x32_bf16 v[100:103], a[200:203], v[192:195], v[100:103]\n" \
"ds_read_u16 v13, v24\n" \
"ds_read_u16 v21, v25\n" \
"v_mfma_f32_16x16x32_bf16 v[108:111], a[204:207], v[192:195], v[108:111]\n" \
"v_add_u32_sdwa v24, s63, v14 dst_sel:DWORD dst_unused:UNUSED_PAD src0_sel:DWORD src1_sel:WORD_0\n" \
"v_add_u32_sdwa v25, s63, v14 dst_sel:DWORD dst_unused:UNUSED_PAD src0_sel:DWORD src1_sel:WORD_1\n" \
"v_mfma_f32_16x16x32_bf16 v[124:127], a[204:207], v[196:199], v[124:127]\n" \
"ds_read_b64_tr_b16 v[192:193], v229 offset:50176\n" \
"ds_read_u16 v14, v24\n" \
"v_mfma_f32_16x16x32_bf16 v[116:119], a[200:203], v[196:199], v[116:119]\n" \
"ds_read_b64_tr_b16 v[194:195], v229 offset:50432\n" \
"ds_read_u16 v22, v25\n" \
"v_mfma_f32_16x16x32_bf16 v[120:123], a[196:199], v[196:199], v[120:123]\n" \
"v_add_u32_sdwa v24, s63, v15 dst_sel:DWORD dst_unused:UNUSED_PAD src0_sel:DWORD src1_sel:WORD_0\n" \
"v_add_u32_sdwa v25, s63, v15 dst_sel:DWORD dst_unused:UNUSED_PAD src0_sel:DWORD src1_sel:WORD_1\n" \
"v_mfma_f32_16x16x32_bf16 v[112:115], a[192:195], v[196:199], v[112:115]\n" \
"s_waitcnt lgkmcnt(13)\n" \
"ds_read_u16 v15, v24\n" \
"ds_read_u16 v23, v25\n" \
"v_mfma_f32_16x16x32_bf16 v[32:35], a[208:211], v[200:203], v[32:35]\n" \
"ds_read_b64_tr_b16 v[196:197], v232 offset:50176\n" \
"v_mfma_f32_16x16x32_bf16 v[40:43], a[212:215], v[200:203], v[40:43]\n" \
"s_add_u32 s60, s67, 20480\n" \
"ds_read_b64_tr_b16 v[198:199], v232 offset:50432\n" \
"v_lshl_or_b32 v8, v16, 16, v8\n" \
"v_mfma_f32_16x16x32_bf16 v[36:39], a[216:219], v[200:203], v[36:39]\n" \
"buffer_store_dwordx4 v[92:95], v241, %[wsr], s60 offen offset:3072 sc1\n" \
"v_lshl_or_b32 v9, v17, 16, v9\n" \
"v_mfma_f32_16x16x32_bf16 v[44:47], a[220:223], v[200:203], v[44:47]\n" \
"v_lshl_or_b32 v10, v18, 16, v10\n" \
"v_lshl_or_b32 v11, v19, 16, v11\n" \
"v_mfma_f32_16x16x32_bf16 v[60:63], a[220:223], v[204:207], v[60:63]\n" \
"ds_read_b64_tr_b16 v[200:201], v229 offset:50688\n" \
"v_mfma_f32_16x16x32_bf16 v[52:55], a[216:219], v[204:207], v[52:55]\n" \
"ds_read_b64_tr_b16 v[202:203], v229 offset:50944\n" \
"v_mfma_f32_16x16x32_bf16 v[56:59], a[212:215], v[204:207], v[56:59]\n" \
"ds_write_b128 v26, v[8:11] offset:2048\n" \
"v_mfma_f32_16x16x32_bf16 v[48:51], a[208:211], v[204:207], v[48:51]\n" \
"s_waitcnt lgkmcnt(13)\n" \
"s_nop 0\n" \
"s_nop 0\n" \
"v_mfma_f32_16x16x32_bf16 v[96:99], a[208:211], v[208:211], v[96:99]\n" \
"ds_read_b64_tr_b16 v[204:205], v232 offset:50688\n" \
"s_nop 0\n" \
"v_mfma_f32_16x16x32_bf16 v[104:107], a[212:215], v[208:211], v[104:107]\n" \
"ds_read_b64_tr_b16 v[206:207], v232 offset:50944\n" \
"s_waitcnt lgkmcnt(7)\n" \
"v_mfma_f32_16x16x32_bf16 v[100:103], a[216:219], v[208:211], v[100:103]\n" \
"v_lshl_or_b32 v12, v20, 16, v12\n" \
"v_lshl_or_b32 v13, v21, 16, v13\n" \
"v_mfma_f32_16x16x32_bf16 v[108:111], a[220:223], v[208:211], v[108:111]\n" \
"v_lshl_or_b32 v14, v22, 16, v14\n" \
"v_lshl_or_b32 v15, v23, 16, v15\n" \
"v_mfma_f32_16x16x32_bf16 v[124:127], a[220:223], v[212:215], v[124:127]\n" \
"ds_read_b64_tr_b16 v[208:209], v229 offset:58368\n" \
"v_mfma_f32_16x16x32_bf16 v[116:119], a[216:219], v[212:215], v[116:119]\n" \
"ds_read_b64_tr_b16 v[210:211], v229 offset:58624\n" \
"v_mfma_f32_16x16x32_bf16 v[120:123], a[212:215], v[212:215], v[120:123]\n" \
"ds_write_b128 v26, v[12:15] offset:3072\n" \
"v_mfma_f32_16x16x32_bf16 v[112:115], a[208:211], v[212:215], v[112:115]\n" \
"s_waitcnt lgkmcnt(8)\n" \
"v_mfma_f32_16x16x32_bf16 v[32:35], a[224:227], v[192:195], v[32:35]\n" \
"ds_read_b64_tr_b16 v[212:213], v232 offset:58368\n" \
"v_mfma_f32_16x16x32_bf16 v[40:43], a[228:231], v[192:195], v[40:43]\n" \
"ds_read_b64_tr_b16 v[214:215], v232 offset:58624\n" \
"v_mfma_f32_16x16x32_bf16 v[36:39], a[232:235], v[192:195], v[36:39]\n" \
"v_mfma_f32_16x16x32_bf16 v[44:47], a[236:239], v[192:195], v[44:47]\n" \
"v_mfma_f32_16x16x32_bf16 v[60:63], a[236:239], v[196:199], v[60:63]\n" \
"ds_read_b64_tr_b16 v[192:193], v229 offset:58880\n" \
"v_mfma_f32_16x16x32_bf16 v[52:55], a[232:235], v[196:199], v[52:55]\n" \
"ds_read_b64_tr_b16 v[194:195], v229 offset:59136\n" \
"v_mfma_f32_16x16x32_bf16 v[56:59], a[228:231], v[196:199], v[56:59]\n" \
"v_mfma_f32_16x16x32_bf16 v[48:51], a[224:227], v[196:199], v[48:51]\n" \
"s_waitcnt lgkmcnt(7)\n" \
"v_mfma_f32_16x16x32_bf16 v[96:99], a[224:227], v[200:203], v[96:99]\n" \
"ds_read_b64_tr_b16 v[196:197], v232 offset:58880\n" \
"v_mfma_f32_16x16x32_bf16 v[104:107], a[228:231], v[200:203], v[104:107]\n" \
"ds_read_b64_tr_b16 v[198:199], v232 offset:59136\n" \
"v_mfma_f32_16x16x32_bf16 v[100:103], a[232:235], v[200:203], v[100:103]\n" \
"v_mfma_f32_16x16x32_bf16 v[108:111], a[236:239], v[200:203], v[108:111]\n" \
"s_barrier\n" \
"v_mfma_f32_16x16x32_bf16 v[124:127], a[236:239], v[204:207], v[124:127]\n" \
"ds_read_b64_tr_b16 v[200:201], v228 offset:0\n" \
"v_mfma_f32_16x16x32_bf16 v[116:119], a[232:235], v[204:207], v[116:119]\n" \
"ds_read_b64_tr_b16 v[202:203], v228 offset:256\n" \
"v_mfma_f32_16x16x32_bf16 v[120:123], a[228:231], v[204:207], v[120:123]\n" \
"v_mfma_f32_16x16x32_bf16 v[112:115], a[224:227], v[204:207], v[112:115]\n" \
"s_waitcnt lgkmcnt(6)\n" \
"v_mfma_f32_16x16x32_bf16 v[32:35], a[240:243], v[208:211], v[32:35]\n" \
"ds_read_b64_tr_b16 v[204:205], v231 offset:0\n" \
"v_mfma_f32_16x16x32_bf16 v[40:43], a[244:247], v[208:211], v[40:43]\n" \
"ds_read_b64_tr_b16 v[206:207], v231 offset:256\n" \
"v_mfma_f32_16x16x32_bf16 v[36:39], a[248:251], v[208:211], v[36:39]\n" \
"v_mfma_f32_16x16x32_bf16 v[44:47], a[252:255], v[208:211], v[44:47]\n" \
"v_mfma_f32_16x16x32_bf16 v[48:51], a[240:243], v[212:215], v[48:51]\n" \
"ds_read_b64_tr_b16 v[208:209], v228 offset:512\n" \
"v_mfma_f32_16x16x32_bf16 v[56:59], a[244:247], v[212:215], v[56:59]\n" \
"ds_read_b64_tr_b16 v[210:211], v228 offset:768\n" \
"v_mfma_f32_16x16x32_bf16 v[52:55], a[248:251], v[212:215], v[52:55]\n" \
"v_mfma_f32_16x16x32_bf16 v[60:63], a[252:255], v[212:215], v[60:63]\n" \
"s_waitcnt lgkmcnt(6)\n" \
"v_mfma_f32_16x16x32_bf16 v[96:99], a[240:243], v[192:195], v[96:99]\n" \
"ds_read_b64_tr_b16 v[212:213], v231 offset:512\n" \
"v_mfma_f32_16x16x32_bf16 v[104:107], a[244:247], v[192:195], v[104:107]\n" \
"ds_read_b64_tr_b16 v[214:215], v231 offset:768\n" \
"v_mfma_f32_16x16x32_bf16 v[100:103], a[248:251], v[192:195], v[100:103]\n" \
"v_mfma_f32_16x16x32_bf16 v[108:111], a[252:255], v[192:195], v[108:111]\n" \
"v_mfma_f32_16x16x32_bf16 v[112:115], a[240:243], v[196:199], v[112:115]\n" \
"ds_read_b64_tr_b16 v[192:193], v228 offset:8192\n" \
"v_mfma_f32_16x16x32_bf16 v[120:123], a[244:247], v[196:199], v[120:123]\n" \
"ds_read_b64_tr_b16 v[194:195], v228 offset:8448\n" \
"v_mfma_f32_16x16x32_bf16 v[116:119], a[248:251], v[196:199], v[116:119]\n" \
"v_mfma_f32_16x16x32_bf16 v[124:127], a[252:255], v[196:199], v[124:127]\n" \
"s_waitcnt lgkmcnt(6)\n" \
"v_mfma_f32_16x16x32_bf16 v[0:3], a[0:3], v[200:203], 0\n" \
"ds_read_b64_tr_b16 v[196:197], v231 offset:8192\n" \
"v_mfma_f32_16x16x32_bf16 v[8:11], a[4:7], v[200:203], 0\n" \
"ds_read_b64_tr_b16 v[198:199], v231 offset:8448\n" \
"v_mfma_f32_16x16x32_bf16 v[4:7], a[8:11], v[200:203], 0\n" \
"v_cvt_pk_bf16_f32 v32, v32, v33\n" \
"v_cvt_pk_bf16_f32 v33, v34, v35\n" \
"v_mfma_f32_16x16x32_bf16 v[12:15], a[12:15], v[200:203], 0\n" \
"v_cvt_pk_bf16_f32 v34, v40, v41\n" \
"v_cvt_pk_bf16_f32 v35, v42, v43\n" \
"v_mfma_f32_16x16x32_bf16 v[16:19], a[0:3], v[204:207], 0\n" \
"ds_read_b64_tr_b16 v[200:201], v228 offset:8704\n" \
"v_cvt_pk_bf16_f32 v36, v36, v37\n" \
"v_mfma_f32_16x16x32_bf16 v[24:27], a[4:7], v[204:207], 0\n" \
"ds_read_b64_tr_b16 v[202:203], v228 offset:8960\n" \
"v_cvt_pk_bf16_f32 v37, v38, v39\n" \
"v_mfma_f32_16x16x32_bf16 v[20:23], a[8:11], v[204:207], 0\n" \
"v_cvt_pk_bf16_f32 v38, v44, v45\n" \
"v_cvt_pk_bf16_f32 v39, v46, v47\n" \
"v_mfma_f32_16x16x32_bf16 v[28:31], a[12:15], v[204:207], 0\n" \
"s_waitcnt lgkmcnt(6)\n" \
"v_cvt_pk_bf16_f32 v48, v48, v49\n" \
"v_cvt_pk_bf16_f32 v49, v50, v51\n" \
"v_mfma_f32_16x16x32_bf16 v[64:67], a[0:3], v[208:211], 0\n" \
"ds_read_b64_tr_b16 v[204:205], v231 offset:8704\n" \
"v_cvt_pk_bf16_f32 v50, v56, v57\n" \
"v_mfma_f32_16x16x32_bf16 v[72:75], a[4:7], v[208:211], 0\n" \
"s_add_u32 s60, s67, 8192\n" \
"ds_read_b64_tr_b16 v[206:207], v231 offset:8960\n" \
"v_cvt_pk_bf16_f32 v51, v58, v59\n" \
"v_mfma_f32_16x16x32_bf16 v[68:71], a[8:11], v[208:211], 0\n" \
"buffer_store_dwordx4 v[32:35], v241, %[wsr], s60 offen offset:0 sc1\n" \
"v_cvt_pk_bf16_f32 v52, v52, v53\n" \
"v_mfma_f32_16x16x32_bf16 v[76:79], a[12:15], v[208:211], 0\n" \
"v_cvt_pk_bf16_f32 v53, v54, v55\n" \
"v_cvt_pk_bf16_f32 v54, v60, v61\n" \
"v_mfma_f32_16x16x32_bf16 v[80:83], a[0:3], v[212:215], 0\n" \
"ds_read_b64_tr_b16 v[208:209], v228 offset:1024\n" \
"v_cvt_pk_bf16_f32 v55, v62, v63\n" \
"v_mfma_f32_16x16x32_bf16 v[88:91], a[4:7], v[212:215], 0\n" \
"ds_read_b64_tr_b16 v[210:211], v228 offset:1280\n" \
"v_cvt_pk_bf16_f32 v96, v96, v97\n" \
"v_mfma_f32_16x16x32_bf16 v[84:87], a[8:11], v[212:215], 0\n" \
"v_cvt_pk_bf16_f32 v97, v98, v99\n" \
"v_cvt_pk_bf16_f32 v98, v104, v105\n" \
"v_mfma_f32_16x16x32_bf16 v[92:95], a[12:15], v[212:215], 0\n" \
"s_waitcnt lgkmcnt(6)\n" \
"v_cvt_pk_bf16_f32 v99, v106, v107\n" \
"v_cvt_pk_bf16_f32 v100, v100, v101\n" \
"v_mfma_f32_16x16x32_bf16 v[0:3], a[16:19], v[192:195], v[0:3]\n" \
"ds_read_b64_tr_b16 v[212:213], v231 offset:1024\n" \
"v_cvt_pk_bf16_f32 v101, v102, v103\n" \
"v_mfma_f32_16x16x32_bf16 v[8:11], a[20:23], v[192:195], v[8:11]\n" \
"ds_read_b64_tr_b16 v[214:215], v231 offset:1280\n" \
"v_cvt_pk_bf16_f32 v102, v108, v109\n" \
"v_mfma_f32_16x16x32_bf16 v[4:7], a[24:27], v[192:195], v[4:7]\n" \
"v_cvt_pk_bf16_f32 v103, v110, v111\n" \
"v_cvt_pk_bf16_f32 v112, v112, v113\n" \
"v_mfma_f32_16x16x32_bf16 v[12:15], a[28:31], v[192:195], v[12:15]\n" \
"v_cvt_pk_bf16_f32 v113, v114, v115\n" \
"v_cvt_pk_bf16_f32 v114, v120, v121\n" \
"v_mfma_f32_16x16x32_bf16 v[28:31], a[28:31], v[196:199], v[28:31]\n" \
"ds_read_b64_tr_b16 v[192:193], v228 offset:1536\n" \
"v_cvt_pk_bf16_f32 v115, v122, v123\n" \
"v_mfma_f32_16x16x32_bf16 v[20:23], a[24:27], v[196:199], v[20:23]\n" \
"ds_read_b64_tr_b16 v[194:195], v228 offset:1792\n" \
"v_cvt_pk_bf16_f32 v116, v116, v117\n" \
"v_mfma_f32_16x16x32_bf16 v[24:27], a[20:23], v[196:199], v[24:27]\n" \
"v_cvt_pk_bf16_f32 v117, v118, v119\n" \
"v_cvt_pk_bf16_f32 v118, v124, v125\n" \
"v_mfma_f32_16x16x32_bf16 v[16:19], a[16:19], v[196:199], v[16:19]\n" \
"s_waitcnt lgkmcnt(6)\n" \
"v_cvt_pk_bf16_f32 v119, v126, v127\n" \
"v_lshlrev_b32_e32 v40, 16, v216\n" \
"v_mfma_f32_16x16x32_bf16 v[64:67], a[16:19], v[200:203], v[64:67]\n" \
"ds_read_b64_tr_b16 v[196:197], v231 offset:1536\n" \
"v_and_b32_e32 v44, 0xffff0000, v216\n" \
"v_mfma_f32_16x16x32_bf16 v[72:75], a[20:23], v[200:203], v[72:75]\n" \
"ds_read_b64_tr_b16 v[198:199], v231 offset:1792\n" \
"v_lshlrev_b32_e32 v41, 16, v217\n" \
"v_mfma_f32_16x16x32_bf16 v[68:71], a[24:27], v[200:203], v[68:71]\n" \
"v_and_b32_e32 v45, 0xffff0000, v217\n" \
"v_lshlrev_b32_e32 v42, 16, v218\n" \
"v_mfma_f32_16x16x32_bf16 v[76:79], a[28:31], v[200:203], v[76:79]\n" \
"v_and_b32_e32 v46, 0xffff0000, v218\n" \
"v_lshlrev_b32_e32 v43, 16, v219\n" \
"v_mfma_f32_16x16x32_bf16 v[92:95], a[28:31], v[204:207], v[92:95]\n" \
"ds_read_b64_tr_b16 v[200:201], v228 offset:9216\n" \
"v_and_b32_e32 v47, 0xffff0000, v219\n" \
"v_mfma_f32_16x16x32_bf16 v[84:87], a[24:27], v[204:207], v[84:87]\n" \
"ds_read_b64_tr_b16 v[202:203], v228 offset:9472\n" \
"v_mul_f32_e32 v40, v234, v40\n" \
"v_mfma_f32_16x16x32_bf16 v[88:91], a[20:23], v[204:207], v[88:91]\n" \
"v_mul_f32_e32 v41, v234, v41\n" \
"v_mul_f32_e32 v42, v234, v42\n" \
"v_mfma_f32_16x16x32_bf16 v[80:83], a[16:19], v[204:207], v[80:83]\n" \
"s_waitcnt lgkmcnt(6)\n" \
"v_mul_f32_e32 v43, v234, v43\n" \
"v_mul_f32_e32 v44, v235, v44\n" \
"v_mfma_f32_16x16x32_bf16 v[0:3], a[32:35], v[208:211], v[0:3]\n" \
"ds_read_b64_tr_b16 v[204:205], v231 offset:9216\n" \
"v_mul_f32_e32 v45, v235, v45\n" \
"v_mfma_f32_16x16x32_bf16 v[8:11], a[36:39], v[208:211], v[8:11]\n" \
"s_add_u32 s60, s67, 24576\n" \
"ds_read_b64_tr_b16 v[206:207], v231 offset:9472\n" \
"v_mul_f32_e32 v46, v235, v46\n" \
"v_mfma_f32_16x16x32_bf16 v[4:7], a[40:43], v[208:211], v[4:7]\n" \
"buffer_store_dwordx4 v[36:39], v241, %[wsr], s60 offen offset:0 sc1\n" \
"v_mul_f32_e32 v47, v235, v47\n" \
"v_mfma_f32_16x16x32_bf16 v[12:15], a[44:47], v[208:211], v[12:15]\n" \
"v_cvt_pkrtz_f16_f32 v40, v40, v41\n" \
"v_cvt_pkrtz_f16_f32 v41, v42, v43\n" \
"v_mfma_f32_16x16x32_bf16 v[28:31], a[44:47], v[212:215], v[28:31]\n" \
"ds_read_b64_tr_b16 v[208:209], v228 offset:9728\n" \
"v_cvt_pkrtz_f16_f32 v42, v44, v45\n" \
"v_mfma_f32_16x16x32_bf16 v[20:23], a[40:43], v[212:215], v[20:23]\n" \
"ds_read_b64_tr_b16 v[210:211], v228 offset:9984\n" \
"v_cvt_pkrtz_f16_f32 v43, v46, v47\n" \
"v_mfma_f32_16x16x32_bf16 v[24:27], a[36:39], v[212:215], v[24:27]\n" \
"ds_write2st64_b64 v224, v[40:41], v[42:43] offset0:1 offset1:10\n" \
"v_mfma_f32_16x16x32_bf16 v[16:19], a[32:35], v[212:215], v[16:19]\n" \
"s_waitcnt lgkmcnt(7)\n" \
"v_lshlrev_b32_e32 v56, 16, v220\n" \
"v_and_b32_e32 v60, 0xffff0000, v220\n" \
"v_mfma_f32_16x16x32_bf16 v[64:67], a[32:35], v[192:195], v[64:67]\n" \
"ds_read_b64_tr_b16 v[212:213], v231 offset:9728\n" \
"v_lshlrev_b32_e32 v57, 16, v221\n" \
"v_mfma_f32_16x16x32_bf16 v[72:75], a[36:39], v[192:195], v[72:75]\n" \
"ds_read_b64_tr_b16 v[214:215], v231 offset:9984\n" \
"v_and_b32_e32 v61, 0xffff0000, v221\n" \
"v_mfma_f32_16x16x32_bf16 v[68:71], a[40:43], v[192:195], v[68:71]\n" \
"buffer_load_dword v247, v242, %[wsr], s80 offen offset:256 sc1\n" \
"v_mfma_f32_16x16x32_bf16 v[76:79], a[44:47], v[192:195], v[76:79]\n" \
"v_lshlrev_b32_e32 v58, 16, v222\n" \
"v_and_b32_e32 v62, 0xffff0000, v222\n" \
"v_mfma_f32_16x16x32_bf16 v[92:95], a[44:47], v[196:199], v[92:95]\n" \
"ds_read_b64_tr_b16 v[192:193], v228 offset:16384\n" \
"v_lshlrev_b32_e32 v59, 16, v223\n" \
"v_mfma_f32_16x16x32_bf16 v[84:87], a[40:43], v[196:199], v[84:87]\n" \
"ds_read_b64_tr_b16 v[194:195], v228 offset:16640\n" \
"v_and_b32_e32 v63, 0xffff0000, v223\n" \
"v_mfma_f32_16x16x32_bf16 v[88:91], a[36:39], v[196:199], v[88:91]\n" \
"v_mul_f32_e32 v56, v234, v56\n" \
"v_mul_f32_e32 v57, v234, v57\n" \
"v_mfma_f32_16x16x32_bf16 v[80:83], a[32:35], v[196:199], v[80:83]\n" \
"s_waitcnt lgkmcnt(7)\n" \
"v_mul_f32_e32 v58, v234, v58\n" \
"v_mul_f32_e32 v59, v234, v59\n" \
"v_mfma_f32_16x16x32_bf16 v[0:3], a[48:51], v[200:203], v[0:3]\n" \
"ds_read_b64_tr_b16 v[196:197], v231 offset:16384\n" \
"v_mul_f32_e32 v60, v235, v60\n" \
"v_mfma_f32_16x16x32_bf16 v[8:11], a[52:55], v[200:203], v[8:11]\n" \
"ds_read_b64_tr_b16 v[198:199], v231 offset:16640\n" \
"v_mul_f32_e32 v61, v235, v61\n" \
"v_mfma_f32_16x16x32_bf16 v[4:7], a[56:59], v[200:203], v[4:7]\n" \
"v_mul_f32_e32 v62, v235, v62\n" \
"v_mul_f32_e32 v63, v235, v63\n" \
"v_mfma_f32_16x16x32_bf16 v[12:15], a[60:63], v[200:203], v[12:15]\n" \
"v_cvt_pkrtz_f16_f32 v56, v56, v57\n" \
"v_cvt_pkrtz_f16_f32 v57, v58, v59\n" \
"v_mfma_f32_16x16x32_bf16 v[28:31], a[60:63], v[204:207], v[28:31]\n" \
"ds_read_b64_tr_b16 v[200:201], v228 offset:16896\n" \
"v_cvt_pkrtz_f16_f32 v58, v60, v61\n" \
"v_mfma_f32_16x16x32_bf16 v[20:23], a[56:59], v[204:207], v[20:23]\n" \
"ds_read_b64_tr_b16 v[202:203], v228 offset:17152\n" \
"v_cvt_pkrtz_f16_f32 v59, v62, v63\n" \
"v_mfma_f32_16x16x32_bf16 v[24:27], a[52:55], v[204:207], v[24:27]\n" \
"ds_write2st64_b64 v224, v[56:57], v[58:59] offset0:2 offset1:11\n" \
"v_mfma_f32_16x16x32_bf16 v[16:19], a[48:51], v[204:207], v[16:19]\n" \
"s_waitcnt lgkmcnt(7)\n" \
"v_add_u32_e32 v224, %[w4k], v241\n" \
"v_mov_b32_e32 v234, v241\n" \
"v_mfma_f32_16x16x32_bf16 v[64:67], a[48:51], v[208:211], v[64:67]\n" \
"ds_read_b64_tr_b16 v[204:205], v231 offset:16896\n" \
"v_add_u32_e32 v235, 0x10000, v241\n" \
"v_mfma_f32_16x16x32_bf16 v[72:75], a[52:55], v[208:211], v[72:75]\n" \
"s_add_u32 s60, s67, 8192\n" \
"ds_read_b64_tr_b16 v[206:207], v231 offset:17152\n" \
"v_mfma_f32_16x16x32_bf16 v[68:71], a[56:59], v[208:211], v[68:71]\n" \
"buffer_store_dwordx4 v[48:51], v241, %[wsr], s60 offen offset:1024 sc1\n" \
"v_mfma_f32_16x16x32_bf16 v[76:79], a[60:63], v[208:211], v[76:79]\n" \
"v_mfma_f32_16x16x32_bf16 v[92:95], a[60:63], v[212:215], v[92:95]\n" \
"ds_read_b64_tr_b16 v[208:209], v228 offset:24576\n" \
"v_mfma_f32_16x16x32_bf16 v[84:87], a[56:59], v[212:215], v[84:87]\n" \
"ds_read_b64_tr_b16 v[210:211], v228 offset:24832\n" \
"s_waitcnt vmcnt(4)\n" \
"v_mfma_f32_16x16x32_bf16 v[88:91], a[52:55], v[212:215], v[88:91]\n" \
"buffer_atomic_add v252, v253, %[wsr], s83 offen offset:0 sc1\n" \
"v_mfma_f32_16x16x32_bf16 v[80:83], a[48:51], v[212:215], v[80:83]\n" \
"s_waitcnt lgkmcnt(7)\n" \
"v_mfma_f32_16x16x32_bf16 v[0:3], a[64:67], v[192:195], v[0:3]\n" \
"ds_read_b64_tr_b16 v[212:213], v231 offset:24576\n" \
"v_mfma_f32_16x16x32_bf16 v[8:11], a[68:71], v[192:195], v[8:11]\n" \
"ds_read_b64_tr_b16 v[214:215], v231 offset:24832\n" \
"v_mfma_f32_16x16x32_bf16 v[4:7], a[72:75], v[192:195], v[4:7]\n" \
"v_mfma_f32_16x16x32_bf16 v[12:15], a[76:79], v[192:195], v[12:15]\n" \
"v_mfma_f32_16x16x32_bf16 v[28:31], a[76:79], v[196:199], v[28:31]\n" \
"ds_read_b64_tr_b16 v[192:193], v228 offset:25088\n" \
"v_mfma_f32_16x16x32_bf16 v[20:23], a[72:75], v[196:199], v[20:23]\n" \
"ds_read_b64_tr_b16 v[194:195], v228 offset:25344\n" \
"v_mfma_f32_16x16x32_bf16 v[24:27], a[68:71], v[196:199], v[24:27]\n" \
"v_mfma_f32_16x16x32_bf16 v[16:19], a[64:67], v[196:199], v[16:19]\n" \
"s_waitcnt lgkmcnt(6)\n" \
"v_mfma_f32_16x16x32_bf16 v[64:67], a[64:67], v[200:203], v[64:67]\n" \
"ds_read_b64_tr_b16 v[196:197], v231 offset:25088\n" \
"v_mfma_f32_16x16x32_bf16 v[72:75], a[68:71], v[200:203], v[72:75]\n" \
"ds_read_b64_tr_b16 v[198:199], v231 offset:25344\n" \
"v_mfma_f32_16x16x32_bf16 v[68:71], a[72:75], v[200:203], v[68:71]\n" \
"v_mfma_f32_16x16x32_bf16 v[76:79], a[76:79], v[200:203], v[76:79]\n" \
"v_mfma_f32_16x16x32_bf16 v[92:95], a[76:79], v[204:207], v[92:95]\n" \
"ds_read_b64_tr_b16 v[200:201], v228 offset:17408\n" \
"v_mfma_f32_16x16x32_bf16 v[84:87], a[72:75], v[204:207], v[84:87]\n" \
"ds_read_b64_tr_b16 v[202:203], v228 offset:17664\n" \
"v_mfma_f32_16x16x32_bf16 v[88:91], a[68:71], v[204:207], v[88:91]\n" \
"v_mfma_f32_16x16x32_bf16 v[80:83], a[64:67], v[204:207], v[80:83]\n" \
"s_waitcnt lgkmcnt(6)\n" \
"v_mfma_f32_16x16x32_bf16 v[0:3], a[80:83], v[208:211], v[0:3]\n" \
"ds_read_b64_tr_b16 v[204:205], v231 offset:17408\n" \
"v_mfma_f32_16x16x32_bf16 v[8:11], a[84:87], v[208:211], v[8:11]\n" \
"s_add_u32 s60, s67, 24576\n" \
"ds_read_b64_tr_b16 v[206:207], v231 offset:17664\n" \
"v_mfma_f32_16x16x32_bf16 v[4:7], a[88:91], v[208:211], v[4:7]\n" \
"buffer_store_dwordx4 v[52:55], v241, %[wsr], s60 offen offset:1024 sc1\n" \
"v_mfma_f32_16x16x32_bf16 v[12:15], a[92:95], v[208:211], v[12:15]\n" \
"v_mfma_f32_16x16x32_bf16 v[28:31], a[92:95], v[212:215], v[28:31]\n" \
"ds_read_b64_tr_b16 v[208:209], v228 offset:17920\n" \
"v_mfma_f32_16x16x32_bf16 v[20:23], a[88:91], v[212:215], v[20:23]\n" \
"ds_read_b64_tr_b16 v[210:211], v228 offset:18176\n" \
"v_mfma_f32_16x16x32_bf16 v[24:27], a[84:87], v[212:215], v[24:27]\n" \
"v_mfma_f32_16x16x32_bf16 v[16:19], a[80:83], v[212:215], v[16:19]\n" \
"s_waitcnt lgkmcnt(6)\n" \
"v_mfma_f32_16x16x32_bf16 v[64:67], a[80:83], v[192:195], v[64:67]\n" \
"ds_read_b64_tr_b16 v[212:213], v231 offset:17920\n" \
"v_mfma_f32_16x16x32_bf16 v[72:75], a[84:87], v[192:195], v[72:75]\n" \
"ds_read_b64_tr_b16 v[214:215], v231 offset:18176\n" \
"v_mfma_f32_16x16x32_bf16 v[68:71], a[88:91], v[192:195], v[68:71]\n" \
"v_mfma_f32_16x16x32_bf16 v[76:79], a[92:95], v[192:195], v[76:79]\n" \
"v_mfma_f32_16x16x32_bf16 v[92:95], a[92:95], v[196:199], v[92:95]\n" \
"ds_read_b64_tr_b16 v[192:193], v228 offset:25600\n" \
"v_mfma_f32_16x16x32_bf16 v[84:87], a[88:91], v[196:199], v[84:87]\n" \
"ds_read_b64_tr_b16 v[194:195], v228 offset:25856\n" \
"v_mfma_f32_16x16x32_bf16 v[88:91], a[84:87], v[196:199], v[88:91]\n" \
"v_mfma_f32_16x16x32_bf16 v[80:83], a[80:83], v[196:199], v[80:83]\n" \
"s_waitcnt lgkmcnt(6)\n" \
"v_mfma_f32_16x16x32_bf16 v[0:3], a[96:99], v[200:203], v[0:3]\n" \
"ds_read_b64_tr_b16 v[196:197], v231 offset:25600\n" \
"v_mfma_f32_16x16x32_bf16 v[8:11], a[100:103], v[200:203], v[8:11]\n" \
"ds_read_b64_tr_b16 v[198:199], v231 offset:25856\n" \
"v_mfma_f32_16x16x32_bf16 v[4:7], a[104:107], v[200:203], v[4:7]\n" \
"v_mfma_f32_16x16x32_bf16 v[12:15], a[108:111], v[200:203], v[12:15]\n" \
"v_mfma_f32_16x16x32_bf16 v[28:31], a[108:111], v[204:207], v[28:31]\n" \
"ds_read_b64_tr_b16 v[200:201], v228 offset:26112\n" \
"v_mfma_f32_16x16x32_bf16 v[20:23], a[104:107], v[204:207], v[20:23]\n" \
"ds_read_b64_tr_b16 v[202:203], v228 offset:26368\n" \
"v_mfma_f32_16x16x32_bf16 v[24:27], a[100:103], v[204:207], v[24:27]\n" \
"v_mfma_f32_16x16x32_bf16 v[16:19], a[96:99], v[204:207], v[16:19]\n" \
"s_waitcnt lgkmcnt(6)\n" \
"v_mfma_f32_16x16x32_bf16 v[64:67], a[96:99], v[208:211], v[64:67]\n" \
"ds_read_b64_tr_b16 v[204:205], v231 offset:26112\n" \
"v_mfma_f32_16x16x32_bf16 v[72:75], a[100:103], v[208:211], v[72:75]\n" \
"s_add_u32 s60, s67, 8192\n" \
"ds_read_b64_tr_b16 v[206:207], v231 offset:26368\n" \
"v_mfma_f32_16x16x32_bf16 v[68:71], a[104:107], v[208:211], v[68:71]\n" \
"buffer_store_dwordx4 v[96:99], v241, %[wsr], s60 offen offset:2048 sc1\n" \
"v_mfma_f32_16x16x32_bf16 v[76:79], a[108:111], v[208:211], v[76:79]\n" \
"s_barrier\n" \
"v_mfma_f32_16x16x32_bf16 v[92:95], a[108:111], v[212:215], v[92:95]\n" \
"ds_read_b64_tr_b16 v[208:209], v228 offset:32768\n" \
"v_mfma_f32_16x16x32_bf16 v[84:87], a[104:107], v[212:215], v[84:87]\n" \
"ds_read_b64_tr_b16 v[210:211], v228 offset:33024\n" \
"v_mfma_f32_16x16x32_bf16 v[88:91], a[100:103], v[212:215], v[88:91]\n" \
"v_mfma_f32_16x16x32_bf16 v[80:83], a[96:99], v[212:215], v[80:83]\n" \
"s_waitcnt lgkmcnt(6)\n" \
"v_mfma_f32_16x16x32_bf16 v[0:3], a[112:115], v[192:195], v[0:3]\n" \
"ds_read_b64_tr_b16 v[212:213], v231 offset:32768\n" \
"v_mfma_f32_16x16x32_bf16 v[8:11], a[116:119], v[192:195], v[8:11]\n" \
"ds_read_b64_tr_b16 v[214:215], v231 offset:33024\n" \
"v_mfma_f32_16x16x32_bf16 v[4:7], a[120:123], v[192:195], v[4:7]\n" \
"v_mfma_f32_16x16x32_bf16 v[12:15], a[124:127], v[192:195], v[12:15]\n" \
"v_mfma_f32_16x16x32_bf16 v[28:31], a[124:127], v[196:199], v[28:31]\n" \
"ds_read_b64_tr_b16 v[192:193], v228 offset:33280\n" \
"v_mfma_f32_16x16x32_bf16 v[20:23], a[120:123], v[196:199], v[20:23]\n" \
"ds_read_b64_tr_b16 v[194:195], v228 offset:33536\n" \
"v_mfma_f32_16x16x32_bf16 v[24:27], a[116:119], v[196:199], v[24:27]\n" \
"v_mfma_f32_16x16x32_bf16 v[16:19], a[112:115], v[196:199], v[16:19]\n" \
"s_waitcnt lgkmcnt(6)\n" \
"v_mfma_f32_16x16x32_bf16 v[64:67], a[112:115], v[200:203], v[64:67]\n" \
"ds_read_b64_tr_b16 v[196:197], v231 offset:33280\n" \
"v_mfma_f32_16x16x32_bf16 v[72:75], a[116:119], v[200:203], v[72:75]\n" \
"ds_read_b64_tr_b16 v[198:199], v231 offset:33536\n" \
"v_mfma_f32_16x16x32_bf16 v[68:71], a[120:123], v[200:203], v[68:71]\n" \
"v_mfma_f32_16x16x32_bf16 v[76:79], a[124:127], v[200:203], v[76:79]\n" \
"v_mfma_f32_16x16x32_bf16 v[92:95], a[124:127], v[204:207], v[92:95]\n" \
"ds_read_b64_tr_b16 v[200:201], v228 offset:40960\n" \
"v_mfma_f32_16x16x32_bf16 v[84:87], a[120:123], v[204:207], v[84:87]\n" \
"ds_read_b64_tr_b16 v[202:203], v228 offset:41216\n" \
"v_mfma_f32_16x16x32_bf16 v[88:91], a[116:119], v[204:207], v[88:91]\n" \
"v_mfma_f32_16x16x32_bf16 v[80:83], a[112:115], v[204:207], v[80:83]\n" \
"s_waitcnt lgkmcnt(6)\n" \
"v_mfma_f32_16x16x32_bf16 v[0:3], a[128:131], v[208:211], v[0:3]\n" \
"ds_read_b64_tr_b16 v[204:205], v231 offset:40960\n" \
"s_waitcnt vmcnt(4)\n" \
"v_mfma_f32_16x16x32_bf16 v[8:11], a[132:135], v[208:211], v[8:11]\n" \
"s_add_u32 s60, s67, 24576\n" \
"ds_read_b64_tr_b16 v[206:207], v231 offset:41216\n" \
"v_cmp_eq_u32_e32 vcc, v244, v247\n" \
"v_mfma_f32_16x16x32_bf16 v[4:7], a[136:139], v[208:211], v[4:7]\n" \
"buffer_store_dwordx4 v[100:103], v241, %[wsr], s60 offen offset:2048 sc1\n" \
"v_mfma_f32_16x16x32_bf16 v[12:15], a[140:143], v[208:211], v[12:15]\n" \
"s_barrier\n" \
"s_and_b32 s88, vcc_hi, -1\n" \
"s_cbranch_scc1 .LATLsw\n" \
".LATLswb:\n" \
"v_mfma_f32_16x16x32_bf16 v[28:31], a[140:143], v[212:215], v[28:31]\n" \
"ds_read_b64_tr_b16 v[208:209], v228 offset:41472\n" \
"v_mfma_f32_16x16x32_bf16 v[20:23], a[136:139], v[212:215], v[20:23]\n" \
"ds_read_b64_tr_b16 v[210:211], v228 offset:41728\n" \
"v_mfma_f32_16x16x32_bf16 v[24:27], a[132:135], v[212:215], v[24:27]\n" \
"buffer_atomic_add v252, v237, %[wsr], s87 offen offset:384 sc1\n" \
"v_mfma_f32_16x16x32_bf16 v[16:19], a[128:131], v[212:215], v[16:19]\n" \
"s_waitcnt lgkmcnt(6)\n" \
"v_mov_b32_e32 v236, 158732\n" \
"v_mfma_f32_16x16x32_bf16 v[64:67], a[128:131], v[192:195], v[64:67]\n" \
"ds_read_b64_tr_b16 v[212:213], v231 offset:41472\n" \
"ds_read2st64_b32 v[236:237], v236 offset0:0 offset1:8\n" \
"v_mfma_f32_16x16x32_bf16 v[72:75], a[132:135], v[192:195], v[72:75]\n" \
"ds_read_b64_tr_b16 v[214:215], v231 offset:41728\n" \
"v_accvgpr_write_b32 a0, v128\n" \
"v_mfma_f32_16x16x32_bf16 v[68:71], a[136:139], v[192:195], v[68:71]\n" \
"v_accvgpr_write_b32 a1, v129\n" \
"v_accvgpr_write_b32 a2, v130\n" \
"v_mfma_f32_16x16x32_bf16 v[76:79], a[140:143], v[192:195], v[76:79]\n" \
"v_accvgpr_write_b32 a3, v131\n" \
"v_accvgpr_write_b32 a4, v144\n" \
"v_mfma_f32_16x16x32_bf16 v[92:95], a[140:143], v[196:199], v[92:95]\n" \
"ds_read_b64_tr_b16 v[192:193], v228 offset:33792\n" \
"v_accvgpr_write_b32 a5, v145\n" \
"v_mfma_f32_16x16x32_bf16 v[84:87], a[136:139], v[196:199], v[84:87]\n" \
"ds_read_b64_tr_b16 v[194:195], v228 offset:34048\n" \
"v_accvgpr_write_b32 a6, v146\n" \
"v_mfma_f32_16x16x32_bf16 v[88:91], a[132:135], v[196:199], v[88:91]\n" \
"s_waitcnt lgkmcnt(3)\n" \
"v_accvgpr_write_b32 a7, v147\n" \
"v_mul_f32_e32 v236, v236, v250\n" \
"v_mfma_f32_16x16x32_bf16 v[80:83], a[128:131], v[196:199], v[80:83]\n" \
"v_mul_f32_e32 v237, v237, v251\n" \
"v_accvgpr_write_b32 a8, v160\n" \
"v_mfma_f32_16x16x32_bf16 v[0:3], a[144:147], v[200:203], v[0:3]\n" \
"ds_read_b64_tr_b16 v[196:197], v231 offset:33792\n" \
"v_accvgpr_write_b32 a9, v161\n" \
"v_mfma_f32_16x16x32_bf16 v[8:11], a[148:151], v[200:203], v[8:11]\n" \
"ds_read_b64_tr_b16 v[198:199], v231 offset:34048\n" \
"v_accvgpr_write_b32 a10, v162\n" \
"v_mfma_f32_16x16x32_bf16 v[4:7], a[152:155], v[200:203], v[4:7]\n" \
"v_accvgpr_write_b32 a11, v163\n" \
"v_accvgpr_write_b32 a12, v176\n" \
"v_mfma_f32_16x16x32_bf16 v[12:15], a[156:159], v[200:203], v[12:15]\n" \
"v_accvgpr_write_b32 a13, v177\n" \
"v_accvgpr_write_b32 a14, v178\n" \
"v_mfma_f32_16x16x32_bf16 v[28:31], a[156:159], v[204:207], v[28:31]\n" \
"ds_read_b64_tr_b16 v[200:201], v228 offset:34304\n" \
"v_accvgpr_write_b32 a15, v179\n" \
"v_mfma_f32_16x16x32_bf16 v[20:23], a[152:155], v[204:207], v[20:23]\n" \
"ds_read_b64_tr_b16 v[202:203], v228 offset:34560\n" \
"v_accvgpr_write_b32 a16, v132\n" \
"v_mfma_f32_16x16x32_bf16 v[24:27], a[148:151], v[204:207], v[24:27]\n" \
"buffer_load_dword v247, v242, %[wsr], s80 offen offset:0 sc1\n" \
"v_mfma_f32_16x16x32_bf16 v[16:19], a[144:147], v[204:207], v[16:19]\n" \
"s_waitcnt lgkmcnt(6)\n" \
"v_accvgpr_write_b32 a17, v133\n" \
"v_accvgpr_write_b32 a18, v134\n" \
"v_mfma_f32_16x16x32_bf16 v[64:67], a[144:147], v[208:211], v[64:67]\n" \
"ds_read_b64_tr_b16 v[204:205], v231 offset:34304\n" \
"v_accvgpr_write_b32 a19, v135\n" \
"v_mfma_f32_16x16x32_bf16 v[72:75], a[148:151], v[208:211], v[72:75]\n" \
"s_add_u32 s60, s67, 8192\n" \
"ds_read_b64_tr_b16 v[206:207], v231 offset:34560\n" \
"v_accvgpr_write_b32 a20, v148\n" \
"v_mfma_f32_16x16x32_bf16 v[68:71], a[152:155], v[208:211], v[68:71]\n" \
"buffer_store_dwordx4 v[112:115], v241, %[wsr], s60 offen offset:3072 sc1\n" \
"v_accvgpr_write_b32 a21, v149\n" \
"v_mfma_f32_16x16x32_bf16 v[76:79], a[156:159], v[208:211], v[76:79]\n" \
"v_accvgpr_write_b32 a22, v150\n" \
"v_accvgpr_write_b32 a23, v151\n" \
"v_mfma_f32_16x16x32_bf16 v[92:95], a[156:159], v[212:215], v[92:95]\n" \
"ds_read_b64_tr_b16 v[208:209], v228 offset:41984\n" \
"v_accvgpr_write_b32 a24, v164\n" \
"v_mfma_f32_16x16x32_bf16 v[84:87], a[152:155], v[212:215], v[84:87]\n" \
"ds_read_b64_tr_b16 v[210:211], v228 offset:42240\n" \
"v_accvgpr_write_b32 a25, v165\n" \
"v_mfma_f32_16x16x32_bf16 v[88:91], a[148:151], v[212:215], v[88:91]\n" \
"v_accvgpr_write_b32 a26, v166\n" \
"v_accvgpr_write_b32 a27, v167\n" \
"v_mfma_f32_16x16x32_bf16 v[80:83], a[144:147], v[212:215], v[80:83]\n" \
"s_waitcnt lgkmcnt(6)\n" \
"v_accvgpr_write_b32 a28, v180\n" \
"v_accvgpr_write_b32 a29, v181\n" \
"v_mfma_f32_16x16x32_bf16 v[0:3], a[160:163], v[192:195], v[0:3]\n" \
"ds_read_b64_tr_b16 v[212:213], v231 offset:41984\n" \
"v_accvgpr_write_b32 a30, v182\n" \
"v_mfma_f32_16x16x32_bf16 v[8:11], a[164:167], v[192:195], v[8:11]\n" \
"ds_read_b64_tr_b16 v[214:215], v231 offset:42240\n" \
"ds_read2_b32 a[128:129], v226 offset0:128 offset1:129\n" \
"v_mfma_f32_16x16x32_bf16 v[4:7], a[168:171], v[192:195], v[4:7]\n" \
"ds_read2_b32 a[130:131], v226 offset0:130 offset1:131\n" \
"ds_read_b128 v[32:35], v238 offset:0\n" \
"v_mfma_f32_16x16x32_bf16 v[12:15], a[172:175], v[192:195], v[12:15]\n" \
"ds_read2_b32 a[132:133], v226 offset0:0 offset1:1\n" \
"ds_read2_b32 a[134:135], v226 offset0:2 offset1:3\n" \
"v_mfma_f32_16x16x32_bf16 v[28:31], a[172:175], v[196:199], v[28:31]\n" \
"ds_read_b64_tr_b16 v[192:193], v228 offset:42496\n" \
"ds_read_b128 v[36:39], v238 offset:512\n" \
"v_mfma_f32_16x16x32_bf16 v[20:23], a[168:171], v[196:199], v[20:23]\n" \
"ds_read_b64_tr_b16 v[194:195], v228 offset:42752\n" \
"ds_read2_b32 a[136:137], v227 offset0:128 offset1:129\n" \
"v_mfma_f32_16x16x32_bf16 v[24:27], a[164:167], v[196:199], v[24:27]\n" \
"ds_read2_b32 a[138:139], v227 offset0:130 offset1:131\n" \
"ds_read_b128 v[40:43], v238 offset:1024\n" \
"v_mfma_f32_16x16x32_bf16 v[16:19], a[160:163], v[196:199], v[16:19]\n" \
"s_waitcnt lgkmcnt(15)\n" \
"ds_read2_b32 a[140:141], v227 offset0:0 offset1:1\n" \
"ds_read2_b32 a[142:143], v227 offset0:2 offset1:3\n" \
"v_mfma_f32_16x16x32_bf16 v[64:67], a[160:163], v[200:203], v[64:67]\n" \
"ds_read_b64_tr_b16 v[196:197], v231 offset:42496\n" \
"ds_read_b128 v[44:47], v238 offset:1536\n" \
"v_mfma_f32_16x16x32_bf16 v[72:75], a[164:167], v[200:203], v[72:75]\n" \
"ds_read_b64_tr_b16 v[198:199], v231 offset:42752\n" \
"ds_read2_b32 a[144:145], v230 offset0:128 offset1:129\n" \
"v_mfma_f32_16x16x32_bf16 v[68:71], a[168:171], v[200:203], v[68:71]\n" \
"ds_read2_b32 a[146:147], v230 offset0:130 offset1:131\n" \
"ds_read_b128 v[48:51], v238 offset:2048\n" \
"v_mfma_f32_16x16x32_bf16 v[76:79], a[172:175], v[200:203], v[76:79]\n" \
"ds_read2_b32 a[148:149], v230 offset0:0 offset1:1\n" \
"ds_read2_b32 a[150:151], v230 offset0:2 offset1:3\n" \
"v_mfma_f32_16x16x32_bf16 v[92:95], a[172:175], v[204:207], v[92:95]\n" \
"ds_read_b64_tr_b16 v[200:201], v228 offset:49152\n" \
"ds_read_b128 v[52:55], v238 offset:2560\n" \
"v_mfma_f32_16x16x32_bf16 v[84:87], a[168:171], v[204:207], v[84:87]\n" \
"ds_read_b64_tr_b16 v[202:203], v228 offset:49408\n" \
"ds_read2_b32 a[152:153], v233 offset0:128 offset1:129\n" \
"v_mfma_f32_16x16x32_bf16 v[88:91], a[164:167], v[204:207], v[88:91]\n" \
"ds_read2_b32 a[154:155], v233 offset0:130 offset1:131\n" \
"ds_read_b128 v[56:59], v238 offset:3072\n" \
"v_mfma_f32_16x16x32_bf16 v[80:83], a[160:163], v[204:207], v[80:83]\n" \
"s_waitcnt lgkmcnt(15)\n" \
"ds_read2_b32 a[156:157], v233 offset0:0 offset1:1\n" \
"ds_read2_b32 a[158:159], v233 offset0:2 offset1:3\n" \
"v_mfma_f32_16x16x32_bf16 v[0:3], a[176:179], v[208:211], v[0:3]\n" \
"ds_read_b64_tr_b16 v[204:205], v228 offset:57344\n" \
"ds_read_b128 v[60:63], v238 offset:3584\n" \
"v_mfma_f32_16x16x32_bf16 v[8:11], a[180:183], v[208:211], v[8:11]\n" \
"s_add_u32 s60, s67, 24576\n" \
"ds_read_b64_tr_b16 v[206:207], v228 offset:57600\n" \
"v_accvgpr_write_b32 a31, v183\n" \
"v_mfma_f32_16x16x32_bf16 v[4:7], a[184:187], v[208:211], v[4:7]\n" \
"buffer_store_dwordx4 v[116:119], v241, %[wsr], s60 offen offset:3072 sc1\n" \
"v_accvgpr_write_b32 a32, v136\n" \
"v_mfma_f32_16x16x32_bf16 v[12:15], a[188:191], v[208:211], v[12:15]\n" \
"v_accvgpr_write_b32 a33, v137\n" \
"v_accvgpr_write_b32 a34, v138\n" \
"v_mfma_f32_16x16x32_bf16 v[28:31], a[188:191], v[212:215], v[28:31]\n" \
"ds_read_b64_tr_b16 v[208:209], v228 offset:50176\n" \
"v_accvgpr_write_b32 a35, v139\n" \
"v_mfma_f32_16x16x32_bf16 v[20:23], a[184:187], v[212:215], v[20:23]\n" \
"ds_read_b64_tr_b16 v[210:211], v228 offset:50432\n" \
"v_accvgpr_write_b32 a36, v152\n" \
"v_mfma_f32_16x16x32_bf16 v[24:27], a[180:183], v[212:215], v[24:27]\n" \
"v_accvgpr_write_b32 a37, v153\n" \
"v_accvgpr_write_b32 a38, v154\n" \
"v_mfma_f32_16x16x32_bf16 v[16:19], a[176:179], v[212:215], v[16:19]\n" \
"s_waitcnt lgkmcnt(15)\n" \
"v_accvgpr_write_b32 a39, v155\n" \
"v_accvgpr_write_b32 a40, v168\n" \
"v_mfma_f32_16x16x32_bf16 v[64:67], a[176:179], v[192:195], v[64:67]\n" \
"ds_read_b64_tr_b16 v[212:213], v228 offset:58368\n" \
"v_accvgpr_write_b32 a41, v169\n" \
"v_mfma_f32_16x16x32_bf16 v[72:75], a[180:183], v[192:195], v[72:75]\n" \
"ds_read_b64_tr_b16 v[214:215], v228 offset:58624\n" \
"v_accvgpr_write_b32 a42, v170\n" \
"v_mfma_f32_16x16x32_bf16 v[68:71], a[184:187], v[192:195], v[68:71]\n" \
"v_accvgpr_write_b32 a43, v171\n" \
"v_accvgpr_write_b32 a44, v184\n" \
"v_mfma_f32_16x16x32_bf16 v[76:79], a[188:191], v[192:195], v[76:79]\n" \
"v_accvgpr_write_b32 a45, v185\n" \
"v_accvgpr_write_b32 a46, v186\n" \
"v_mfma_f32_16x16x32_bf16 v[92:95], a[188:191], v[196:199], v[92:95]\n" \
"ds_read_b64_tr_b16 v[192:193], v231 offset:49152\n" \
"s_waitcnt vmcnt(2)\n" \
"v_mfma_f32_16x16x32_bf16 v[84:87], a[184:187], v[196:199], v[84:87]\n" \
"ds_read_b64_tr_b16 v[194:195], v231 offset:49408\n" \
"v_cmp_eq_u32_e32 vcc, v243, v247\n" \
"v_mfma_f32_16x16x32_bf16 v[88:91], a[180:183], v[196:199], v[88:91]\n" \
"v_accvgpr_write_b32 a47, v187\n" \
"v_accvgpr_write_b32 a48, v140\n" \
"v_mfma_f32_16x16x32_bf16 v[80:83], a[176:179], v[196:199], v[80:83]\n" \
"s_waitcnt lgkmcnt(6)\n" \
"s_and_b32 s88, vcc_lo, -1\n" \
"s_cbranch_scc1 .LATLsk\n" \
".LATLskb:\n" \
"v_mfma_f32_16x16x32_bf16 v[0:3], a[192:195], v[200:203], v[0:3]\n" \
"s_add_u32 m0, s66, 0\n" \
"ds_read_b64_tr_b16 v[196:197], v231 offset:57344\n" \
"v_accvgpr_write_b32 a49, v141\n" \
"v_mfma_f32_16x16x32_bf16 v[8:11], a[196:199], v[200:203], v[8:11]\n" \
"s_add_u32 s79, s65, 0\n" \
"ds_read_b64_tr_b16 v[198:199], v231 offset:57600\n" \
"ds_read2_b32 a[160:161], v226 offset0:144 offset1:145\n" \
"v_mfma_f32_16x16x32_bf16 v[4:7], a[200:203], v[200:203], v[4:7]\n" \
"buffer_load_dwordx4 v224, %[kvs], s79 offen offset:0 sc1 lds\n" \
"v_mfma_f32_16x16x32_bf16 v[12:15], a[204:207], v[200:203], v[12:15]\n" \
"ds_read2_b32 a[162:163], v226 offset0:146 offset1:147\n" \
"ds_read_b128 v[96:99], v238 offset:64\n" \
"v_mfma_f32_16x16x32_bf16 v[0:3], a[208:211], v[204:207], v[0:3]\n" \
"ds_read_b64_tr_b16 v[200:201], v231 offset:50176\n" \
"ds_read2_b32 a[164:165], v226 offset0:16 offset1:17\n" \
"v_mfma_f32_16x16x32_bf16 v[8:11], a[212:215], v[204:207], v[8:11]\n" \
"ds_read_b64_tr_b16 v[202:203], v231 offset:50432\n" \
"ds_read2_b32 a[166:167], v226 offset0:18 offset1:19\n" \
"v_mfma_f32_16x16x32_bf16 v[4:7], a[216:219], v[204:207], v[4:7]\n" \
"buffer_load_dwordx4 v224, %[kvs], s79 offen offset:1024 sc1 lds\n" \
"v_mfma_f32_16x16x32_bf16 v[12:15], a[220:223], v[204:207], v[12:15]\n" \
"s_waitcnt lgkmcnt(11)\n" \
"ds_read_b128 v[100:103], v238 offset:576\n" \
"ds_read2_b32 a[168:169], v227 offset0:144 offset1:145\n" \
"v_mfma_f32_16x16x32_bf16 v[0:3], a[224:227], v[208:211], v[0:3]\n" \
"ds_read_b64_tr_b16 v[204:205], v231 offset:58368\n" \
"ds_read2_b32 a[170:171], v227 offset0:146 offset1:147\n" \
"v_mfma_f32_16x16x32_bf16 v[8:11], a[228:231], v[208:211], v[8:11]\n" \
"ds_read_b64_tr_b16 v[206:207], v231 offset:58624\n" \
"ds_read_b128 v[104:107], v238 offset:1088\n" \
"v_mfma_f32_16x16x32_bf16 v[4:7], a[232:235], v[208:211], v[4:7]\n" \
"buffer_load_dwordx4 v224, %[kvs], s79 offen offset:2048 sc1 lds\n" \
"v_mfma_f32_16x16x32_bf16 v[12:15], a[236:239], v[208:211], v[12:15]\n" \
"ds_read2_b32 a[172:173], v227 offset0:16 offset1:17\n" \
"ds_read2_b32 a[174:175], v227 offset0:18 offset1:19\n" \
"v_mfma_f32_16x16x32_bf16 v[0:3], a[240:243], v[212:215], v[0:3]\n" \
"ds_read_b64_tr_b16 v[208:209], v228 offset:49664\n" \
"ds_read_b128 v[108:111], v238 offset:1600\n" \
"v_mfma_f32_16x16x32_bf16 v[8:11], a[244:247], v[212:215], v[8:11]\n" \
"ds_read_b64_tr_b16 v[210:211], v228 offset:49920\n" \
"ds_read2_b32 a[176:177], v230 offset0:144 offset1:145\n" \
"v_mfma_f32_16x16x32_bf16 v[4:7], a[248:251], v[212:215], v[4:7]\n" \
"buffer_load_dwordx4 v224, %[kvs], s79 offen offset:3072 sc1 lds\n" \
"v_mfma_f32_16x16x32_bf16 v[12:15], a[252:255], v[212:215], v[12:15]\n" \
"s_waitcnt lgkmcnt(15)\n" \
"ds_read2_b32 a[178:179], v230 offset0:146 offset1:147\n" \
"ds_read_b128 v[112:115], v238 offset:2112\n" \
"v_mfma_f32_16x16x32_bf16 v[16:19], a[192:195], v[192:195], v[16:19]\n" \
"ds_read_b64_tr_b16 v[212:213], v228 offset:57856\n" \
"ds_read2_b32 a[180:181], v230 offset0:16 offset1:17\n" \
"v_mfma_f32_16x16x32_bf16 v[24:27], a[196:199], v[192:195], v[24:27]\n" \
"ds_read_b64_tr_b16 v[214:215], v228 offset:58112\n" \
"v_cvt_pk_bf16_f32 v0, v0, v1\n" \
"v_mfma_f32_16x16x32_bf16 v[20:23], a[200:203], v[192:195], v[20:23]\n" \
"v_cvt_pk_bf16_f32 v1, v2, v3\n" \
"v_cvt_pk_bf16_f32 v2, v8, v9\n" \
"v_mfma_f32_16x16x32_bf16 v[28:31], a[204:207], v[192:195], v[28:31]\n" \
"v_cvt_pk_bf16_f32 v3, v10, v11\n" \
"v_cvt_pk_bf16_f32 v4, v4, v5\n" \
"v_mfma_f32_16x16x32_bf16 v[16:19], a[208:211], v[196:199], v[16:19]\n" \
"ds_read_b64_tr_b16 v[192:193], v228 offset:50688\n" \
"v_cvt_pk_bf16_f32 v5, v6, v7\n" \
"v_mfma_f32_16x16x32_bf16 v[24:27], a[212:215], v[196:199], v[24:27]\n" \
"s_add_u32 s60, s67, 12288\n" \
"ds_read_b64_tr_b16 v[194:195], v228 offset:50944\n" \
"v_cvt_pk_bf16_f32 v6, v12, v13\n" \
"v_mfma_f32_16x16x32_bf16 v[20:23], a[216:219], v[196:199], v[20:23]\n" \
"buffer_store_dwordx4 v[0:3], v241, %[wsr], s60 offen offset:0 sc1\n" \
"v_cvt_pk_bf16_f32 v7, v14, v15\n" \
"v_mfma_f32_16x16x32_bf16 v[28:31], a[220:223], v[196:199], v[28:31]\n" \
"s_waitcnt lgkmcnt(14)\n" \
"ds_read2_b32 a[182:183], v230 offset0:18 offset1:19\n" \
"ds_read_b128 v[116:119], v238 offset:2624\n" \
"v_mfma_f32_16x16x32_bf16 v[16:19], a[224:227], v[200:203], v[16:19]\n" \
"ds_read_b64_tr_b16 v[196:197], v228 offset:58880\n" \
"ds_read2_b32 a[184:185], v233 offset0:144 offset1:145\n" \
"v_mfma_f32_16x16x32_bf16 v[24:27], a[228:231], v[200:203], v[24:27]\n" \
"s_add_u32 s60, s67, 28672\n" \
"ds_read_b64_tr_b16 v[198:199], v228 offset:59136\n" \
"ds_read2_b32 a[186:187], v233 offset0:146 offset1:147\n" \
"v_mfma_f32_16x16x32_bf16 v[20:23], a[232:235], v[200:203], v[20:23]\n" \
"buffer_store_dwordx4 v[4:7], v241, %[wsr], s60 offen offset:0 sc1\n" \
"v_accvgpr_write_b32 a50, v142\n" \
"v_mfma_f32_16x16x32_bf16 v[28:31], a[236:239], v[200:203], v[28:31]\n" \
"ds_read_b128 v[120:123], v238 offset:3136\n" \
"ds_read2_b32 a[188:189], v233 offset0:16 offset1:17\n" \
"v_mfma_f32_16x16x32_bf16 v[16:19], a[240:243], v[204:207], v[16:19]\n" \
"ds_read_b64_tr_b16 v[200:201], v231 offset:49664\n" \
"ds_read2_b32 a[190:191], v233 offset0:18 offset1:19\n" \
"v_mfma_f32_16x16x32_bf16 v[24:27], a[244:247], v[204:207], v[24:27]\n" \
"ds_read_b64_tr_b16 v[202:203], v231 offset:49920\n" \
"ds_read_b128 v[124:127], v238 offset:3648\n" \
"v_mfma_f32_16x16x32_bf16 v[20:23], a[248:251], v[204:207], v[20:23]\n" \
"v_accvgpr_write_b32 a51, v143\n" \
"v_accvgpr_write_b32 a52, v156\n" \
"v_mfma_f32_16x16x32_bf16 v[28:31], a[252:255], v[204:207], v[28:31]\n" \
"s_waitcnt lgkmcnt(14)\n" \
"v_accvgpr_write_b32 a53, v157\n" \
"v_accvgpr_write_b32 a54, v158\n" \
"v_mfma_f32_16x16x32_bf16 v[64:67], a[192:195], v[208:211], v[64:67]\n" \
"ds_read_b64_tr_b16 v[204:205], v231 offset:57856\n" \
"v_accvgpr_write_b32 a55, v159\n" \
"v_mfma_f32_16x16x32_bf16 v[72:75], a[196:199], v[208:211], v[72:75]\n" \
"ds_read_b64_tr_b16 v[206:207], v231 offset:58112\n" \
"v_cvt_pk_bf16_f32 v16, v16, v17\n" \
"v_mfma_f32_16x16x32_bf16 v[68:71], a[200:203], v[208:211], v[68:71]\n" \
"v_cvt_pk_bf16_f32 v17, v18, v19\n" \
"v_cvt_pk_bf16_f32 v18, v24, v25\n" \
"v_mfma_f32_16x16x32_bf16 v[76:79], a[204:207], v[208:211], v[76:79]\n" \
"v_cvt_pk_bf16_f32 v19, v26, v27\n" \
"v_cvt_pk_bf16_f32 v20, v20, v21\n" \
"v_mfma_f32_16x16x32_bf16 v[64:67], a[208:211], v[212:215], v[64:67]\n" \
"ds_read_b64_tr_b16 v[208:209], v231 offset:50688\n" \
"v_cvt_pk_bf16_f32 v21, v22, v23\n" \
"v_mfma_f32_16x16x32_bf16 v[72:75], a[212:215], v[212:215], v[72:75]\n" \
"s_add_u32 s60, s67, 12288\n" \
"ds_read_b64_tr_b16 v[210:211], v231 offset:50944\n" \
"v_cvt_pk_bf16_f32 v22, v28, v29\n" \
"v_mfma_f32_16x16x32_bf16 v[68:71], a[216:219], v[212:215], v[68:71]\n" \
"buffer_store_dwordx4 v[16:19], v241, %[wsr], s60 offen offset:1024 sc1\n" \
"v_cvt_pk_bf16_f32 v23, v30, v31\n" \
"v_mfma_f32_16x16x32_bf16 v[76:79], a[220:223], v[212:215], v[76:79]\n" \
"s_waitcnt lgkmcnt(11)\n" \
"v_accvgpr_write_b32 a56, v172\n" \
"v_accvgpr_write_b32 a57, v173\n" \
"v_mfma_f32_16x16x32_bf16 v[64:67], a[224:227], v[192:195], v[64:67]\n" \
"ds_read_b64_tr_b16 v[212:213], v231 offset:58880\n" \
"v_accvgpr_write_b32 a58, v174\n" \
"v_mfma_f32_16x16x32_bf16 v[72:75], a[228:231], v[192:195], v[72:75]\n" \
"s_add_u32 s60, s67, 28672\n" \
"ds_read_b64_tr_b16 v[214:215], v231 offset:59136\n" \
"v_accvgpr_write_b32 a59, v175\n" \
"v_mfma_f32_16x16x32_bf16 v[68:71], a[232:235], v[192:195], v[68:71]\n" \
"buffer_store_dwordx4 v[20:23], v241, %[wsr], s60 offen offset:1024 sc1\n" \
"v_accvgpr_write_b32 a60, v188\n" \
"v_mfma_f32_16x16x32_bf16 v[76:79], a[236:239], v[192:195], v[76:79]\n" \
"v_accvgpr_write_b32 a61, v189\n" \
"v_accvgpr_write_b32 a62, v190\n" \
"v_mfma_f32_16x16x32_bf16 v[64:67], a[240:243], v[196:199], v[64:67]\n" \
"v_accvgpr_write_b32 a63, v191\n" \
"v_mfma_f32_16x16x32_bf16 v[72:75], a[244:247], v[196:199], v[72:75]\n" \
"v_mfma_f32_16x16x32_bf16 v[68:71], a[248:251], v[196:199], v[68:71]\n" \
"v_mfma_f32_16x16x32_bf16 v[76:79], a[252:255], v[196:199], v[76:79]\n" \
"s_waitcnt lgkmcnt(4)\n" \
"v_mfma_f32_16x16x32_bf16 v[80:83], a[192:195], v[200:203], v[80:83]\n" \
"v_mfma_f32_16x16x32_bf16 v[88:91], a[196:199], v[200:203], v[88:91]\n" \
"v_mfma_f32_16x16x32_bf16 v[84:87], a[200:203], v[200:203], v[84:87]\n" \
"v_mfma_f32_16x16x32_bf16 v[92:95], a[204:207], v[200:203], v[92:95]\n" \
"v_mfma_f32_16x16x32_bf16 v[80:83], a[208:211], v[204:207], v[80:83]\n" \
"v_cvt_pk_bf16_f32 v64, v64, v65\n" \
"v_cvt_pk_bf16_f32 v65, v66, v67\n" \
"v_mfma_f32_16x16x32_bf16 v[88:91], a[212:215], v[204:207], v[88:91]\n" \
"s_add_u32 s60, s67, 12288\n" \
"v_cvt_pk_bf16_f32 v66, v72, v73\n" \
"v_cvt_pk_bf16_f32 v67, v74, v75\n" \
"v_mfma_f32_16x16x32_bf16 v[84:87], a[216:219], v[204:207], v[84:87]\n" \
"buffer_store_dwordx4 v[64:67], v241, %[wsr], s60 offen offset:2048 sc1\n" \
"v_cvt_pk_bf16_f32 v68, v68, v69\n" \
"v_mfma_f32_16x16x32_bf16 v[92:95], a[220:223], v[204:207], v[92:95]\n" \
"s_waitcnt lgkmcnt(0)\n" \
"v_cvt_pk_bf16_f32 v69, v70, v71\n" \
"v_cvt_pk_bf16_f32 v70, v76, v77\n" \
"v_mfma_f32_16x16x32_bf16 v[80:83], a[224:227], v[208:211], v[80:83]\n" \
"v_cvt_pk_bf16_f32 v71, v78, v79\n" \
"v_mfma_f32_16x16x32_bf16 v[88:91], a[228:231], v[208:211], v[88:91]\n" \
"s_add_u32 s60, s67, 28672\n" \
"v_mfma_f32_16x16x32_bf16 v[84:87], a[232:235], v[208:211], v[84:87]\n" \
"buffer_store_dwordx4 v[68:71], v241, %[wsr], s60 offen offset:2048 sc1\n" \
"v_mfma_f32_16x16x32_bf16 v[92:95], a[236:239], v[208:211], v[92:95]\n" \
"v_mfma_f32_16x16x32_bf16 v[80:83], a[240:243], v[212:215], v[80:83]\n" \
"v_mfma_f32_16x16x32_bf16 v[88:91], a[244:247], v[212:215], v[88:91]\n" \
"v_mfma_f32_16x16x32_bf16 v[84:87], a[248:251], v[212:215], v[84:87]\n" \
"v_mfma_f32_16x16x32_bf16 v[92:95], a[252:255], v[212:215], v[92:95]\n" \
"v_mfma_f32_16x16x32_f16 a[192:195], a[128:131], v[32:35], 0\n" \
"v_mfma_f32_16x16x32_f16 a[196:199], a[132:135], v[32:35], 0\n" \
"v_mfma_f32_16x16x32_f16 a[196:199], a[128:131], v[36:39], a[196:199]\n" \
"v_mfma_f32_16x16x32_f16 a[200:203], a[132:135], v[36:39], 0\n" \
"v_mfma_f32_16x16x32_f16 a[200:203], a[136:139], v[32:35], a[200:203]\n" \
"v_cvt_pk_bf16_f32 v80, v80, v81\n" \
"v_cvt_pk_bf16_f32 v81, v82, v83\n" \
"v_mfma_f32_16x16x32_f16 a[200:203], a[128:131], v[40:43], a[200:203]\n" \
"s_add_u32 s60, s67, 12288\n" \
"v_cvt_pk_bf16_f32 v82, v88, v89\n" \
"v_cvt_pk_bf16_f32 v83, v90, v91\n" \
"v_mfma_f32_16x16x32_f16 a[204:207], a[136:139], v[36:39], 0\n" \
"buffer_store_dwordx4 v[80:83], v241, %[wsr], s60 offen offset:3072 sc1\n" \
"v_cvt_pk_bf16_f32 v84, v84, v85\n" \
"v_mfma_f32_16x16x32_f16 a[204:207], a[132:135], v[40:43], a[204:207]\n" \
"v_cvt_pk_bf16_f32 v85, v86, v87\n" \
"v_cvt_pk_bf16_f32 v86, v92, v93\n" \
"v_mfma_f32_16x16x32_f16 a[208:211], a[136:139], v[40:43], 0\n" \
"v_cvt_pk_bf16_f32 v87, v94, v95\n" \
"v_lshrrev_b32_e32 v212, 4, v240\n" \
"v_mfma_f32_16x16x32_f16 a[204:207], a[140:143], v[32:35], a[204:207]\n" \
"s_add_u32 s60, s67, 28672\n" \
"v_xor_b32_e32 v212, v212, v240\n" \
"v_bfe_i32 v212, v212, 1, 1\n" \
"v_mfma_f32_16x16x32_f16 a[204:207], a[128:131], v[44:47], a[204:207]\n" \
"buffer_store_dwordx4 v[84:87], v241, %[wsr], s60 offen offset:3072 sc1\n" \
"v_not_b32_e32 v212, v212\n" \
"v_mfma_f32_16x16x32_f16 a[208:211], a[140:143], v[36:39], a[208:211]\n" \
"s_and_b32 s64, %[pair], 15\n" \
"v_and_b32_e32 v212, 0x3f803f80, v212\n" \
"v_accvgpr_write_b32 a236, v212\n" \
"v_mfma_f32_16x16x32_f16 a[208:211], a[132:135], v[44:47], a[208:211]\n" \
"s_mul_i32 s64, s64, 0x88000\n" \
"v_accvgpr_write_b32 a237, v212\n" \
"v_accvgpr_write_b32 a238, v212\n" \
"v_mfma_f32_16x16x32_f16 a[212:215], a[140:143], v[40:43], 0\n" \
"s_and_b32 s64, s64, 0x3c0000\n" \
"v_accvgpr_write_b32 a239, v212\n" \
"v_bfe_i32 v248, v240, 0, 1\n" \
"v_mfma_f32_16x16x32_f16 a[212:215], a[136:139], v[44:47], a[212:215]\n" \
"s_lshl4_add_u32 s64, %[w4k], s64\n" \
"v_and_b32_e32 v248, 0xaaaaaaaa, v248\n" \
"v_or_b32_e32 v248, 0x44444444, v248\n" \
"v_mfma_f32_16x16x32_f16 a[216:219], a[140:143], v[44:47], 0\n" \
"v_accvgpr_write_b32 a240, 0\n" \
"v_accvgpr_write_b32 a241, 0\n" \
"v_mfma_f32_16x16x32_f16 a[208:211], a[144:147], v[32:35], a[208:211]\n" \
"s_add_u32 s64, s64, 67108864\n" \
"v_accvgpr_write_b32 a242, 0\n" \
"v_accvgpr_write_b32 a243, 0\n" \
"v_mfma_f32_16x16x32_f16 a[208:211], a[128:131], v[48:51], a[208:211]\n" \
"s_lshl_b32 s61, %[db], 14\n" \
"v_accvgpr_write_b32 a248, 0\n" \
"v_accvgpr_write_b32 a249, 0\n" \
"v_mfma_f32_16x16x32_f16 a[212:215], a[144:147], v[36:39], a[212:215]\n" \
"s_andn2_b32 s61, 0x10000, s61\n" \
"v_accvgpr_write_b32 a250, 0\n" \
"v_accvgpr_write_b32 a251, 0\n" \
"v_mfma_f32_16x16x32_f16 a[212:215], a[132:135], v[48:51], a[212:215]\n" \
"s_add_u32 s82, %[w4k], s61\n" \
"v_accvgpr_write_b32 a252, -1\n" \
"v_accvgpr_write_b32 a253, -1\n" \
"v_mfma_f32_16x16x32_f16 a[216:219], a[144:147], v[40:43], a[216:219]\n" \
"s_xor_b32 s67, s82, 0x10000\n" \
"v_accvgpr_write_b32 a254, -1\n" \
"v_accvgpr_write_b32 a255, -1\n" \
"v_mfma_f32_16x16x32_f16 a[216:219], a[136:139], v[48:51], a[216:219]\n" \
"v_mfma_f32_16x16x32_f16 a[220:223], a[144:147], v[44:47], 0\n" \
"s_waitcnt vmcnt(8)\n" \
"s_barrier\n" \
"v_mfma_f32_16x16x32_f16 a[220:223], a[140:143], v[48:51], a[220:223]\n" \
"ds_read_b128 a[64:67], v241 offset:0\n" \
"ds_read_b128 a[68:71], v241 offset:1024\n" \
"v_mfma_f32_16x16x32_f16 a[212:215], a[148:151], v[32:35], a[212:215]\n" \
"ds_read_b128 a[72:75], v241 offset:2048\n" \
"ds_read_b128 a[76:79], v241 offset:3072\n" \
"v_mfma_f32_16x16x32_f16 a[212:215], a[128:131], v[52:55], a[212:215]\n" \
"ds_read_b128 a[80:83], v241 offset:4096\n" \
"ds_read_b128 a[84:87], v241 offset:5120\n" \
"v_mfma_f32_16x16x32_f16 a[216:219], a[148:151], v[36:39], a[216:219]\n" \
"ds_read_b128 a[88:91], v241 offset:6144\n" \
"ds_read_b128 a[92:95], v241 offset:7168\n" \
"v_mfma_f32_16x16x32_f16 a[216:219], a[132:135], v[52:55], a[216:219]\n" \
"v_mfma_f32_16x16x32_f16 a[220:223], a[148:151], v[40:43], a[220:223]\n" \
"v_mfma_f32_16x16x32_f16 a[220:223], a[136:139], v[52:55], a[220:223]\n" \
"v_mfma_f32_16x16x32_f16 a[216:219], a[152:155], v[32:35], a[216:219]\n" \
"v_mfma_f32_16x16x32_f16 a[216:219], a[128:131], v[56:59], a[216:219]\n" \
"v_mfma_f32_16x16x32_f16 a[220:223], a[152:155], v[36:39], a[220:223]\n" \
"v_mfma_f32_16x16x32_f16 a[220:223], a[132:135], v[56:59], a[220:223]\n" \
"v_mfma_f32_16x16x32_f16 a[220:223], a[156:159], v[32:35], a[220:223]\n" \
"v_mfma_f32_16x16x32_f16 a[220:223], a[128:131], v[60:63], a[220:223]\n" \
"s_waitcnt lgkmcnt(0)\n" \
"v_mfma_f32_16x16x32_bf16 v[128:131], a[64:67], a[0:3], 0\n" \
"ds_read2_b32 a[128:129], v226 offset0:160 offset1:161\n" \
"v_mfma_f32_16x16x32_bf16 v[136:139], a[64:67], a[16:19], 0\n" \
"ds_read2_b32 a[130:131], v226 offset0:162 offset1:163\n" \
"v_mfma_f32_16x16x32_bf16 v[140:143], a[68:71], a[16:19], 0\n" \
"ds_read_b128 v[32:35], v238 offset:128\n" \
"v_mfma_f32_16x16x32_bf16 v[132:135], a[68:71], a[0:3], 0\n" \
"ds_read2_b32 a[132:133], v226 offset0:32 offset1:33\n" \
"v_mfma_f32_16x16x32_bf16 v[128:131], a[72:75], a[4:7], v[128:131]\n" \
"ds_read2_b32 a[134:135], v226 offset0:34 offset1:35\n" \
"v_mfma_f32_16x16x32_bf16 v[136:139], a[72:75], a[20:23], v[136:139]\n" \
"ds_read_b128 v[36:39], v238 offset:640\n" \
"v_mfma_f32_16x16x32_bf16 v[140:143], a[76:79], a[20:23], v[140:143]\n" \
"ds_read2_b32 a[136:137], v227 offset0:160 offset1:161\n" \
"v_mfma_f32_16x16x32_bf16 v[132:135], a[76:79], a[4:7], v[132:135]\n" \
"ds_read2_b32 a[138:139], v227 offset0:162 offset1:163\n" \
"v_mfma_f32_16x16x32_bf16 v[128:131], a[80:83], a[8:11], v[128:131]\n" \
"ds_read_b128 v[40:43], v238 offset:1152\n" \
"v_mfma_f32_16x16x32_bf16 v[136:139], a[80:83], a[24:27], v[136:139]\n" \
"ds_read2_b32 a[140:141], v227 offset0:32 offset1:33\n" \
"v_mfma_f32_16x16x32_bf16 v[140:143], a[84:87], a[24:27], v[140:143]\n" \
"ds_read2_b32 a[142:143], v227 offset0:34 offset1:35\n" \
"v_mfma_f32_16x16x32_bf16 v[132:135], a[84:87], a[8:11], v[132:135]\n" \
"ds_read_b128 v[44:47], v238 offset:1664\n" \
"v_mfma_f32_16x16x32_bf16 v[128:131], a[88:91], a[12:15], v[128:131]\n" \
"s_add_u32 m0, s66, 32768\n" \
"ds_read2_b32 a[144:145], v230 offset0:160 offset1:161\n" \
"v_mfma_f32_16x16x32_bf16 v[136:139], a[88:91], a[28:31], v[136:139]\n" \
"s_add_u32 s79, s65, 32768\n" \
"ds_read2_b32 a[146:147], v230 offset0:162 offset1:163\n" \
"v_mfma_f32_16x16x32_bf16 v[140:143], a[92:95], a[28:31], v[140:143]\n" \
"buffer_load_dwordx4 v224, %[kvs], s79 offen offset:0 sc1 lds\n" \
"v_mfma_f32_16x16x32_bf16 v[132:135], a[92:95], a[12:15], v[132:135]\n" \
"ds_read_b128 v[48:51], v238 offset:2176\n" \
"v_mfma_f32_16x16x32_bf16 v[144:147], a[64:67], a[32:35], 0\n" \
"ds_read2_b32 a[148:149], v230 offset0:32 offset1:33\n" \
"v_mfma_f32_16x16x32_bf16 v[152:155], a[64:67], a[48:51], 0\n" \
"ds_read_b128 a[64:67], v241 offset:16384\n" \
"v_mfma_f32_16x16x32_bf16 v[156:159], a[68:71], a[48:51], 0\n" \
"buffer_load_dwordx4 v224, %[kvs], s79 offen offset:1024 sc1 lds\n" \
"v_mfma_f32_16x16x32_bf16 v[148:151], a[68:71], a[32:35], 0\n" \
"ds_read_b128 a[68:71], v241 offset:17408\n" \
"v_mfma_f32_16x16x32_bf16 v[144:147], a[72:75], a[36:39], v[144:147]\n" \
"ds_read2_b32 a[150:151], v230 offset0:34 offset1:35\n" \
"s_cmp_eq_u32 %[da], 0\n" \
"s_cbranch_scc1 .LATLpz\n" \
".LATLpzb:\n" \
"v_mfma_f32_16x16x32_bf16 v[152:155], a[72:75], a[52:55], v[152:155]\n" \
"ds_read_b128 a[72:75], v241 offset:18432\n" \
"v_max3_f32 v208, v128, v129, v130\n" \
"v_mfma_f32_16x16x32_bf16 v[156:159], a[76:79], a[52:55], v[156:159]\n" \
"buffer_load_dwordx4 v224, %[kvs], s79 offen offset:2048 sc1 lds\n" \
"v_mfma_f32_16x16x32_bf16 v[148:151], a[76:79], a[36:39], v[148:151]\n" \
"ds_read_b128 a[76:79], v241 offset:19456\n" \
"v_max3_f32 v210, v131, v132, v133\n" \
"v_mfma_f32_16x16x32_bf16 v[144:147], a[80:83], a[40:43], v[144:147]\n" \
"ds_read_b128 v[52:55], v238 offset:2688\n" \
"v_max3_f32 v209, v136, v137, v138\n" \
"v_mfma_f32_16x16x32_bf16 v[152:155], a[80:83], a[56:59], v[152:155]\n" \
"s_waitcnt vmcnt(3)\n" \
"ds_read_b128 a[80:83], v241 offset:20480\n" \
"v_max3_f32 v211, v139, v140, v141\n" \
"v_mfma_f32_16x16x32_bf16 v[156:159], a[84:87], a[56:59], v[156:159]\n" \
"buffer_atomic_add v252, v253, %[wsr], s83 offen offset:128 sc1\n" \
"v_mfma_f32_16x16x32_bf16 v[148:151], a[84:87], a[40:43], v[148:151]\n" \
"ds_read_b128 a[84:87], v241 offset:21504\n" \
"v_max3_f32 v208, v208, v134, v135\n" \
"v_mfma_f32_16x16x32_bf16 v[144:147], a[88:91], a[44:47], v[144:147]\n" \
"ds_read2_b32 a[152:153], v233 offset0:160 offset1:161\n" \
"v_max3_f32 v209, v209, v142, v143\n" \
"v_mfma_f32_16x16x32_bf16 v[152:155], a[88:91], a[60:63], v[152:155]\n" \
"ds_read_b128 a[88:91], v241 offset:22528\n" \
"v_max_f32_e32 v208, v208, v210\n" \
"v_mfma_f32_16x16x32_bf16 v[156:159], a[92:95], a[60:63], v[156:159]\n" \
"buffer_load_dwordx4 v224, %[kvs], s79 offen offset:3072 sc1 lds\n" \
"v_mfma_f32_16x16x32_bf16 v[148:151], a[92:95], a[44:47], v[148:151]\n" \
"ds_read_b128 a[92:95], v241 offset:23552\n" \
"v_max_f32_e32 v209, v209, v211\n" \
"v_mfma_f32_16x16x32_f16 a[192:195], a[160:163], v[96:99], a[192:195]\n" \
"s_add_u32 m0, s66, 65536\n" \
"ds_read2_b32 a[154:155], v233 offset0:162 offset1:163\n" \
"v_mov_b32_e32 v210, v208\n" \
"v_mfma_f32_16x16x32_f16 a[196:199], a[164:167], v[96:99], a[196:199]\n" \
"s_add_u32 s79, s65, 65536\n" \
"ds_read_b128 v[56:59], v238 offset:3200\n" \
"v_mov_b32_e32 v211, v209\n" \
"v_mfma_f32_16x16x32_f16 a[196:199], a[160:163], v[100:103], a[196:199]\n" \
"buffer_load_dwordx4 v224, %[kvs], s79 offen offset:0 sc1 lds\n" \
"v_mfma_f32_16x16x32_f16 a[200:203], a[164:167], v[100:103], a[200:203]\n" \
"ds_read2_b32 a[156:157], v233 offset0:32 offset1:33\n" \
"v_mfma_f32_16x16x32_f16 a[200:203], a[168:171], v[96:99], a[200:203]\n" \
"ds_read2_b32 a[158:159], v233 offset0:34 offset1:35\n" \
"v_mfma_f32_16x16x32_f16 a[200:203], a[160:163], v[104:107], a[200:203]\n" \
"ds_read_b128 v[60:63], v238 offset:3712\n" \
"v_mfma_f32_16x16x32_f16 a[204:207], a[168:171], v[100:103], a[204:207]\n" \
"buffer_load_dwordx4 v224, %[kvs], s79 offen offset:1024 sc1 lds\n" \
"v_mfma_f32_16x16x32_f16 a[204:207], a[164:167], v[104:107], a[204:207]\n" \
"v_permlane32_swap_b32_e32 v208, v210\n" \
"v_mfma_f32_16x16x32_f16 a[208:211], a[168:171], v[104:107], a[208:211]\n" \
"v_permlane32_swap_b32_e32 v209, v211\n" \
"v_mfma_f32_16x16x32_f16 a[204:207], a[172:175], v[96:99], a[204:207]\n" \
"v_max_f32_e32 v208, v208, v210\n" \
"v_max_f32_e32 v209, v209, v211\n" \
"v_mfma_f32_16x16x32_f16 a[204:207], a[160:163], v[108:111], a[204:207]\n" \
"buffer_load_dwordx4 v224, %[kvs], s79 offen offset:2048 sc1 lds\n" \
"v_mfma_f32_16x16x32_f16 a[208:211], a[172:175], v[100:103], a[208:211]\n" \
"v_mov_b32_e32 v210, v208\n" \
"v_mov_b32_e32 v211, v209\n" \
"v_mfma_f32_16x16x32_f16 a[208:211], a[164:167], v[108:111], a[208:211]\n" \
"v_permlane16_swap_b32_e32 v208, v210\n" \
"v_mfma_f32_16x16x32_f16 a[212:215], a[172:175], v[104:107], a[212:215]\n" \
"v_permlane16_swap_b32_e32 v209, v211\n" \
"v_mfma_f32_16x16x32_f16 a[212:215], a[168:171], v[108:111], a[212:215]\n" \
"buffer_load_dwordx4 v224, %[kvs], s79 offen offset:3072 sc1 lds\n" \
"v_mfma_f32_16x16x32_f16 a[216:219], a[172:175], v[108:111], a[216:219]\n" \
"v_max_f32_e32 v208, v208, v210\n" \
"v_max_f32_e32 v209, v209, v211\n" \
"v_mfma_f32_16x16x32_f16 a[208:211], a[176:179], v[96:99], a[208:211]\n" \
"s_add_u32 m0, s66, 98304\n" \
"v_min_f32_e32 v210, v208, v209\n" \
"v_cmp_nlt_f32_e64 s[84:85], s76, v210\n" \
"v_mfma_f32_16x16x32_f16 a[208:211], a[160:163], v[112:115], a[208:211]\n" \
"s_add_u32 s79, s65, 98304\n" \
"v_sub_f32_e32 v128, v128, v208\n" \
"v_sub_f32_e32 v129, v129, v208\n" \
"v_mfma_f32_16x16x32_f16 a[212:215], a[176:179], v[100:103], a[212:215]\n" \
"buffer_load_dwordx4 v224, %[kvs], s79 offen offset:0 sc1 lds\n" \
"v_mfma_f32_16x16x32_f16 a[212:215], a[164:167], v[112:115], a[212:215]\n" \
"v_sub_f32_e32 v130, v130, v208\n" \
"v_sub_f32_e32 v131, v131, v208\n" \
"v_mfma_f32_16x16x32_f16 a[216:219], a[176:179], v[104:107], a[216:219]\n" \
"v_sub_f32_e32 v132, v132, v208\n" \
"v_sub_f32_e32 v133, v133, v208\n" \
"v_mfma_f32_16x16x32_f16 a[216:219], a[168:171], v[112:115], a[216:219]\n" \
"v_sub_f32_e32 v134, v134, v208\n" \
"v_sub_f32_e32 v135, v135, v208\n" \
"v_mfma_f32_16x16x32_f16 a[220:223], a[176:179], v[108:111], a[220:223]\n" \
"buffer_load_dwordx4 v224, %[kvs], s79 offen offset:1024 sc1 lds\n" \
"v_mfma_f32_16x16x32_f16 a[220:223], a[172:175], v[112:115], a[220:223]\n" \
"v_sub_f32_e32 v136, v136, v209\n" \
"v_sub_f32_e32 v137, v137, v209\n" \
"v_mfma_f32_16x16x32_f16 a[212:215], a[180:183], v[96:99], a[212:215]\n" \
"v_sub_f32_e32 v138, v138, v209\n" \
"v_sub_f32_e32 v139, v139, v209\n" \
"v_mfma_f32_16x16x32_f16 a[212:215], a[160:163], v[116:119], a[212:215]\n" \
"v_sub_f32_e32 v140, v140, v209\n" \
"v_sub_f32_e32 v141, v141, v209\n" \
"v_mfma_f32_16x16x32_f16 a[216:219], a[180:183], v[100:103], a[216:219]\n" \
"buffer_load_dwordx4 v224, %[kvs], s79 offen offset:2048 sc1 lds\n" \
"v_mfma_f32_16x16x32_f16 a[216:219], a[164:167], v[116:119], a[216:219]\n" \
"v_sub_f32_e32 v142, v142, v209\n" \
"v_sub_f32_e32 v143, v143, v209\n" \
"v_mfma_f32_16x16x32_f16 a[220:223], a[180:183], v[104:107], a[220:223]\n" \
"v_max3_f32 v212, v144, v145, v146\n" \
"v_max3_f32 v214, v147, v148, v149\n" \
"v_exp_f32_e32 v128, v128\n" \
"v_exp_f32_e32 v129, v129\n" \
"v_exp_f32_e32 v130, v130\n" \
"v_exp_f32_e32 v131, v131\n" \
"v_mfma_f32_16x16x32_f16 a[220:223], a[168:171], v[116:119], a[220:223]\n" \
"v_max3_f32 v213, v152, v153, v154\n" \
"v_max3_f32 v215, v155, v156, v157\n" \
"v_mfma_f32_16x16x32_f16 a[216:219], a[184:187], v[96:99], a[216:219]\n" \
"buffer_load_dwordx4 v224, %[kvs], s79 offen offset:3072 sc1 lds\n" \
"v_mfma_f32_16x16x32_f16 a[216:219], a[160:163], v[120:123], a[216:219]\n" \
"v_max3_f32 v212, v212, v150, v151\n" \
"v_max3_f32 v213, v213, v158, v159\n" \
"v_mfma_f32_16x16x32_f16 a[220:223], a[184:187], v[100:103], a[220:223]\n" \
"v_max_f32_e32 v212, v212, v214\n" \
"v_max_f32_e32 v213, v213, v215\n" \
"v_mfma_f32_16x16x32_f16 a[220:223], a[164:167], v[120:123], a[220:223]\n" \
"v_mov_b32_e32 v214, v212\n" \
"v_mov_b32_e32 v215, v213\n" \
"v_mfma_f32_16x16x32_f16 a[220:223], a[188:191], v[96:99], a[220:223]\n" \
"v_permlane32_swap_b32_e32 v212, v214\n" \
"v_mfma_f32_16x16x32_f16 a[220:223], a[160:163], v[124:127], a[220:223]\n" \
"s_waitcnt lgkmcnt(15)\n" \
"v_permlane32_swap_b32_e32 v213, v215\n" \
"v_mfma_f32_16x16x32_f16 a[192:195], a[128:131], v[32:35], a[192:195]\n" \
"ds_read2_b32 a[160:161], v226 offset0:176 offset1:177\n" \
"v_max_f32_e32 v212, v212, v214\n" \
"v_mfma_f32_16x16x32_f16 a[196:199], a[132:135], v[32:35], a[196:199]\n" \
"ds_read2_b32 a[162:163], v226 offset0:178 offset1:179\n" \
"v_max_f32_e32 v213, v213, v215\n" \
"v_mfma_f32_16x16x32_f16 a[196:199], a[128:131], v[36:39], a[196:199]\n" \
"ds_read_b128 v[96:99], v238 offset:192\n" \
"v_mov_b32_e32 v214, v212\n" \
"v_mfma_f32_16x16x32_f16 a[200:203], a[132:135], v[36:39], a[200:203]\n" \
"ds_read2_b32 a[164:165], v226 offset0:48 offset1:49\n" \
"v_mov_b32_e32 v215, v213\n" \
"v_mfma_f32_16x16x32_f16 a[200:203], a[136:139], v[32:35], a[200:203]\n" \
"ds_read2_b32 a[166:167], v226 offset0:50 offset1:51\n" \
"v_cvt_pk_bf16_f32 v192, v128, v129\n" \
"v_mfma_f32_16x16x32_f16 a[200:203], a[128:131], v[40:43], a[200:203]\n" \
"ds_read_b128 v[100:103], v238 offset:704\n" \
"v_cvt_pk_bf16_f32 v193, v130, v131\n" \
"v_mfma_f32_16x16x32_f16 a[204:207], a[136:139], v[36:39], a[204:207]\n" \
"ds_read2_b32 a[168:169], v227 offset0:176 offset1:177\n" \
"v_exp_f32_e32 v132, v132\n" \
"v_mfma_f32_16x16x32_f16 a[204:207], a[132:135], v[40:43], a[204:207]\n" \
"ds_read2_b32 a[170:171], v227 offset0:178 offset1:179\n" \
"v_exp_f32_e32 v133, v133\n" \
"v_mfma_f32_16x16x32_f16 a[208:211], a[136:139], v[40:43], a[208:211]\n" \
"ds_read_b128 v[104:107], v238 offset:1216\n" \
"v_exp_f32_e32 v134, v134\n" \
"v_mfma_f32_16x16x32_f16 a[204:207], a[140:143], v[32:35], a[204:207]\n" \
"ds_read2_b32 a[172:173], v227 offset0:48 offset1:49\n" \
"v_exp_f32_e32 v135, v135\n" \
"v_mfma_f32_16x16x32_f16 a[204:207], a[128:131], v[44:47], a[204:207]\n" \
"ds_read2_b32 a[174:175], v227 offset0:50 offset1:51\n" \
"v_cvt_pk_bf16_f32 v194, v132, v133\n" \
"v_mfma_f32_16x16x32_f16 a[208:211], a[140:143], v[36:39], a[208:211]\n" \
"ds_read_b128 v[108:111], v238 offset:1728\n" \
"v_cvt_pk_bf16_f32 v195, v134, v135\n" \
"v_mfma_f32_16x16x32_f16 a[208:211], a[132:135], v[44:47], a[208:211]\n" \
"ds_read2_b32 a[176:177], v230 offset0:176 offset1:177\n" \
"v_exp_f32_e32 v136, v136\n" \
"v_mfma_f32_16x16x32_f16 a[212:215], a[140:143], v[40:43], a[212:215]\n" \
"ds_read2_b32 a[178:179], v230 offset0:178 offset1:179\n" \
"v_exp_f32_e32 v137, v137\n" \
"v_mfma_f32_16x16x32_f16 a[212:215], a[136:139], v[44:47], a[212:215]\n" \
"ds_read_b128 v[112:115], v238 offset:2240\n" \
"v_exp_f32_e32 v138, v138\n" \
"v_mfma_f32_16x16x32_f16 a[216:219], a[140:143], v[44:47], a[216:219]\n" \
"s_waitcnt lgkmcnt(15)\n" \
"ds_read2_b32 a[180:181], v230 offset0:48 offset1:49\n" \
"v_exp_f32_e32 v139, v139\n" \
"v_mfma_f32_16x16x32_f16 a[208:211], a[144:147], v[32:35], a[208:211]\n" \
"ds_read2_b32 a[182:183], v230 offset0:50 offset1:51\n" \
"v_cvt_pk_bf16_f32 v196, v136, v137\n" \
"v_mfma_f32_16x16x32_f16 a[208:211], a[128:131], v[48:51], a[208:211]\n" \
"ds_read_b128 v[116:119], v238 offset:2752\n" \
"v_cvt_pk_bf16_f32 v197, v138, v139\n" \
"v_mfma_f32_16x16x32_f16 a[212:215], a[144:147], v[36:39], a[212:215]\n" \
"ds_read2_b32 a[184:185], v233 offset0:176 offset1:177\n" \
"v_exp_f32_e32 v140, v140\n" \
"v_mfma_f32_16x16x32_f16 a[212:215], a[132:135], v[48:51], a[212:215]\n" \
"ds_read2_b32 a[186:187], v233 offset0:178 offset1:179\n" \
"v_exp_f32_e32 v141, v141\n" \
"v_mfma_f32_16x16x32_f16 a[216:219], a[144:147], v[40:43], a[216:219]\n" \
"ds_read_b128 v[120:123], v238 offset:3264\n" \
"v_exp_f32_e32 v142, v142\n" \
"v_mfma_f32_16x16x32_f16 a[216:219], a[136:139], v[48:51], a[216:219]\n" \
"ds_read2_b32 a[188:189], v233 offset0:48 offset1:49\n" \
"v_exp_f32_e32 v143, v143\n" \
"v_mfma_f32_16x16x32_f16 a[220:223], a[144:147], v[44:47], a[220:223]\n" \
"ds_read2_b32 a[190:191], v233 offset0:50 offset1:51\n" \
"v_cvt_pk_bf16_f32 v198, v140, v141\n" \
"v_mfma_f32_16x16x32_f16 a[220:223], a[140:143], v[48:51], a[220:223]\n" \
"ds_read_b128 v[124:127], v238 offset:3776\n" \
"v_cvt_pk_bf16_f32 v199, v142, v143\n" \
"v_mfma_f32_16x16x32_f16 a[212:215], a[148:151], v[32:35], a[212:215]\n" \
"v_permlane16_swap_b32_e32 v212, v214\n" \
"v_mfma_f32_16x16x32_f16 a[212:215], a[128:131], v[52:55], a[212:215]\n" \
"v_permlane16_swap_b32_e32 v213, v215\n" \
"v_mfma_f32_16x16x32_f16 a[216:219], a[148:151], v[36:39], a[216:219]\n" \
"v_max_f32_e32 v212, v212, v214\n" \
"v_max_f32_e32 v213, v213, v215\n" \
"v_mfma_f32_16x16x32_f16 a[216:219], a[132:135], v[52:55], a[216:219]\n" \
"v_min_f32_e32 v214, v212, v213\n" \
"v_cmp_nlt_f32_e64 s[86:87], s76, v214\n" \
"v_mfma_f32_16x16x32_f16 a[220:223], a[148:151], v[40:43], a[220:223]\n" \
"v_sub_f32_e32 v144, v144, v212\n" \
"v_sub_f32_e32 v145, v145, v212\n" \
"v_mfma_f32_16x16x32_f16 a[220:223], a[136:139], v[52:55], a[220:223]\n" \
"v_sub_f32_e32 v146, v146, v212\n" \
"v_sub_f32_e32 v147, v147, v212\n" \
"v_mfma_f32_16x16x32_f16 a[216:219], a[152:155], v[32:35], a[216:219]\n" \
"v_sub_f32_e32 v148, v148, v212\n" \
"v_sub_f32_e32 v149, v149, v212\n" \
"v_mfma_f32_16x16x32_f16 a[216:219], a[128:131], v[56:59], a[216:219]\n" \
"v_sub_f32_e32 v150, v150, v212\n" \
"v_sub_f32_e32 v151, v151, v212\n" \
"v_mfma_f32_16x16x32_f16 a[220:223], a[152:155], v[36:39], a[220:223]\n" \
"v_sub_f32_e32 v152, v152, v213\n" \
"v_sub_f32_e32 v153, v153, v213\n" \
"v_mfma_f32_16x16x32_f16 a[220:223], a[132:135], v[56:59], a[220:223]\n" \
"v_sub_f32_e32 v154, v154, v213\n" \
"v_sub_f32_e32 v155, v155, v213\n" \
"v_mfma_f32_16x16x32_f16 a[220:223], a[156:159], v[32:35], a[220:223]\n" \
"buffer_load_dword v247, v242, %[wsr], s80 offen offset:0 sc1\n" \
"v_mfma_f32_16x16x32_f16 a[220:223], a[128:131], v[60:63], a[220:223]\n" \
"s_waitcnt lgkmcnt(12)\n" \
"v_sub_f32_e32 v156, v156, v213\n" \
"v_sub_f32_e32 v157, v157, v213\n" \
"v_mfma_f32_16x16x32_f16 a[192:195], a[160:163], v[96:99], a[192:195]\n" \
"v_sub_f32_e32 v158, v158, v213\n" \
"v_sub_f32_e32 v159, v159, v213\n" \
"v_mfma_f32_16x16x32_f16 a[196:199], a[164:167], v[96:99], a[196:199]\n" \
"v_sub_f32_e32 v160, 0, v208\n" \
"v_sub_f32_e32 v161, 0, v208\n" \
"v_exp_f32_e32 v144, v144\n" \
"v_exp_f32_e32 v145, v145\n" \
"v_exp_f32_e32 v146, v146\n" \
"v_exp_f32_e32 v147, v147\n" \
"v_mfma_f32_16x16x32_f16 a[196:199], a[160:163], v[100:103], a[196:199]\n" \
"v_sub_f32_e32 v162, 0, v208\n" \
"v_sub_f32_e32 v163, 0, v208\n" \
"v_cvt_pk_bf16_f32 v200, v144, v145\n" \
"v_cvt_pk_bf16_f32 v201, v146, v147\n" \
"v_mfma_f32_16x16x32_f16 a[200:203], a[164:167], v[100:103], a[200:203]\n" \
"v_sub_f32_e32 v164, 0, v208\n" \
"v_sub_f32_e32 v165, 0, v208\n" \
"v_mfma_f32_16x16x32_f16 a[200:203], a[168:171], v[96:99], a[200:203]\n" \
"v_sub_f32_e32 v166, 0, v208\n" \
"v_sub_f32_e32 v167, 0, v208\n" \
"v_mfma_f32_16x16x32_f16 a[200:203], a[160:163], v[104:107], a[200:203]\n" \
"v_sub_f32_e32 v168, 0, v209\n" \
"v_sub_f32_e32 v169, 0, v209\n" \
"v_mfma_f32_16x16x32_f16 a[204:207], a[168:171], v[100:103], a[204:207]\n" \
"v_sub_f32_e32 v170, 0, v209\n" \
"v_sub_f32_e32 v171, 0, v209\n" \
"v_mfma_f32_16x16x32_f16 a[204:207], a[164:167], v[104:107], a[204:207]\n" \
"v_sub_f32_e32 v172, 0, v209\n" \
"v_sub_f32_e32 v173, 0, v209\n" \
"v_mfma_f32_16x16x32_f16 a[208:211], a[168:171], v[104:107], a[208:211]\n" \
"v_sub_f32_e32 v174, 0, v209\n" \
"v_sub_f32_e32 v175, 0, v209\n" \
"v_mfma_f32_16x16x32_f16 a[204:207], a[172:175], v[96:99], a[204:207]\n" \
"v_sub_f32_e32 v176, 0, v212\n" \
"v_sub_f32_e32 v177, 0, v212\n" \
"v_mfma_f32_16x16x32_f16 a[204:207], a[160:163], v[108:111], a[204:207]\n" \
"v_sub_f32_e32 v178, 0, v212\n" \
"v_sub_f32_e32 v179, 0, v212\n" \
"v_mfma_f32_16x16x32_f16 a[208:211], a[172:175], v[100:103], a[208:211]\n" \
"v_sub_f32_e32 v180, 0, v212\n" \
"v_sub_f32_e32 v181, 0, v212\n" \
"v_mfma_f32_16x16x32_f16 a[208:211], a[164:167], v[108:111], a[208:211]\n" \
"v_sub_f32_e32 v182, 0, v212\n" \
"v_sub_f32_e32 v183, 0, v212\n" \
"v_mfma_f32_16x16x32_f16 a[212:215], a[172:175], v[104:107], a[212:215]\n" \
"v_sub_f32_e32 v184, 0, v213\n" \
"v_sub_f32_e32 v185, 0, v213\n" \
"v_mfma_f32_16x16x32_f16 a[212:215], a[168:171], v[108:111], a[212:215]\n" \
"v_sub_f32_e32 v186, 0, v213\n" \
"v_sub_f32_e32 v187, 0, v213\n" \
"v_mfma_f32_16x16x32_f16 a[216:219], a[172:175], v[108:111], a[216:219]\n" \
"s_waitcnt lgkmcnt(0)\n" \
"v_sub_f32_e32 v188, 0, v213\n" \
"v_sub_f32_e32 v189, 0, v213\n" \
"v_mfma_f32_16x16x32_f16 a[208:211], a[176:179], v[96:99], a[208:211]\n" \
"v_sub_f32_e32 v190, 0, v213\n" \
"v_sub_f32_e32 v191, 0, v213\n" \
"v_mfma_f32_16x16x32_f16 a[208:211], a[160:163], v[112:115], a[208:211]\n" \
"s_waitcnt vmcnt(1)\n" \
"s_barrier\n" \
"v_mfma_f32_16x16x32_f16 a[212:215], a[176:179], v[100:103], a[212:215]\n" \
"ds_write_b128 v239, a[192:195] offset:0\n" \
"v_mfma_f32_16x16x32_f16 a[212:215], a[164:167], v[112:115], a[212:215]\n" \
"v_exp_f32_e32 v148, v148\n" \
"v_mfma_f32_16x16x32_f16 a[216:219], a[176:179], v[104:107], a[216:219]\n" \
"v_exp_f32_e32 v149, v149\n" \
"v_mfma_f32_16x16x32_f16 a[216:219], a[168:171], v[112:115], a[216:219]\n" \
"ds_write_b128 v239, a[196:199] offset:1024\n" \
"v_mfma_f32_16x16x32_f16 a[220:223], a[176:179], v[108:111], a[220:223]\n" \
"v_exp_f32_e32 v150, v150\n" \
"v_mfma_f32_16x16x32_f16 a[220:223], a[172:175], v[112:115], a[220:223]\n" \
"v_exp_f32_e32 v151, v151\n" \
"v_mfma_f32_16x16x32_f16 a[212:215], a[180:183], v[96:99], a[212:215]\n" \
"s_waitcnt vmcnt(0)\n" \
"s_add_u32 m0, s66, 8192\n" \
"s_add_u32 s79, s65, 8192\n" \
"v_cvt_pk_bf16_f32 v202, v148, v149\n" \
"v_mfma_f32_16x16x32_f16 a[212:215], a[160:163], v[116:119], a[212:215]\n" \
"v_cmp_eq_u32_e32 vcc, v243, v247\n" \
"v_cvt_pk_bf16_f32 v203, v150, v151\n" \
"v_mfma_f32_16x16x32_f16 a[216:219], a[180:183], v[100:103], a[216:219]\n" \
"ds_write_b128 v239, a[200:203] offset:2048\n" \
"v_mfma_f32_16x16x32_f16 a[216:219], a[164:167], v[116:119], a[216:219]\n" \
"s_and_b32 s88, vcc_hi, -1\n" \
"s_cbranch_scc1 .LATLsv\n" \
".LATLsvb:\n" \
"v_mfma_f32_16x16x32_f16 a[220:223], a[180:183], v[104:107], a[220:223]\n" \
"v_exp_f32_e32 v152, v152\n" \
"v_mfma_f32_16x16x32_f16 a[220:223], a[168:171], v[116:119], a[220:223]\n" \
"buffer_load_dwordx4 v224, %[kvs], s79 offen offset:0 sc1 lds\n" \
"v_mfma_f32_16x16x32_f16 a[216:219], a[184:187], v[96:99], a[216:219]\n" \
"ds_write_b128 v239, a[204:207] offset:3072\n" \
"v_mfma_f32_16x16x32_f16 a[216:219], a[160:163], v[120:123], a[216:219]\n" \
"buffer_load_dwordx4 v224, %[kvs], s79 offen offset:1024 sc1 lds\n" \
"v_mfma_f32_16x16x32_f16 a[220:223], a[184:187], v[100:103], a[220:223]\n" \
"v_exp_f32_e32 v153, v153\n" \
"v_mfma_f32_16x16x32_f16 a[220:223], a[164:167], v[120:123], a[220:223]\n" \
"buffer_load_dwordx4 v224, %[kvs], s79 offen offset:2048 sc1 lds\n" \
"v_mfma_f32_16x16x32_f16 a[220:223], a[188:191], v[96:99], a[220:223]\n" \
"ds_write_b128 v239, a[208:211] offset:4096\n" \
"v_mfma_f32_16x16x32_f16 a[220:223], a[160:163], v[124:127], a[220:223]\n" \
"buffer_load_dwordx4 v224, %[kvs], s79 offen offset:3072 sc1 lds\n" \
"s_nop 1\n" \
"s_cmp_ge_i32 s74, -1\n" \
"s_cbranch_scc1 .LATLmk0\n" \
".LATLmb0:\n" \
"v_mfma_f32_16x16x32_bf16 v[128:131], a[64:67], a[0:3], v[160:163]\n" \
"s_add_u32 m0, s66, 40960\n" \
"s_add_u32 s79, s65, 40960\n" \
"v_exp_f32_e32 v154, v154\n" \
"v_mfma_f32_16x16x32_bf16 v[136:139], a[64:67], a[16:19], v[168:171]\n" \
"buffer_load_dwordx4 v224, %[kvs], s79 offen offset:0 sc1 lds\n" \
"v_mfma_f32_16x16x32_bf16 v[140:143], a[68:71], a[16:19], v[172:175]\n" \
"s_add_u32 s60, s64, 0\n" \
"ds_write_b128 v239, a[212:215] offset:5120\n" \
"v_mfma_f32_16x16x32_bf16 v[132:135], a[68:71], a[0:3], v[164:167]\n" \
"buffer_load_dwordx4 a[128:131], v241, %[wsr], s60 offen offset:0 sc1\n" \
"v_exp_f32_e32 v155, v155\n" \
"v_mfma_f32_16x16x32_bf16 v[128:131], a[72:75], a[4:7], v[128:131]\n" \
"v_cvt_pk_bf16_f32 v204, v152, v153\n" \
"v_cvt_pk_bf16_f32 v205, v154, v155\n" \
"v_mfma_f32_16x16x32_bf16 v[136:139], a[72:75], a[20:23], v[136:139]\n" \
"buffer_load_dwordx4 v224, %[kvs], s79 offen offset:1024 sc1 lds\n" \
"v_mfma_f32_16x16x32_bf16 v[140:143], a[76:79], a[20:23], v[140:143]\n" \
"ds_write_b128 v239, a[216:219] offset:6144\n" \
"v_mfma_f32_16x16x32_bf16 v[132:135], a[76:79], a[4:7], v[132:135]\n" \
"buffer_load_dwordx4 a[132:135], v241, %[wsr], s60 offen offset:1024 sc1\n" \
"v_exp_f32_e32 v156, v156\n" \
"v_mfma_f32_16x16x32_bf16 v[128:131], a[80:83], a[8:11], v[128:131]\n" \
"v_exp_f32_e32 v157, v157\n" \
"v_mfma_f32_16x16x32_bf16 v[136:139], a[80:83], a[24:27], v[136:139]\n" \
"buffer_load_dwordx4 v224, %[kvs], s79 offen offset:2048 sc1 lds\n" \
"v_mfma_f32_16x16x32_bf16 v[140:143], a[84:87], a[24:27], v[140:143]\n" \
"ds_write_b128 v239, a[220:223] offset:7168\n" \
"v_mfma_f32_16x16x32_bf16 v[132:135], a[84:87], a[8:11], v[132:135]\n" \
"v_exp_f32_e32 v158, v158\n" \
"v_mfma_f32_16x16x32_bf16 v[128:131], a[88:91], a[12:15], v[128:131]\n" \
"v_exp_f32_e32 v159, v159\n" \
"v_mfma_f32_16x16x32_bf16 v[136:139], a[88:91], a[28:31], v[136:139]\n" \
"buffer_load_dwordx4 v224, %[kvs], s79 offen offset:3072 sc1 lds\n" \
"v_mfma_f32_16x16x32_bf16 v[140:143], a[92:95], a[28:31], v[140:143]\n" \
"v_cvt_pk_bf16_f32 v206, v156, v157\n" \
"v_cvt_pk_bf16_f32 v207, v158, v159\n" \
"v_mfma_f32_16x16x32_bf16 v[132:135], a[92:95], a[12:15], v[132:135]\n" \
"buffer_load_dwordx4 a[136:139], v241, %[wsr], s60 offen offset:2048 sc1\n" \
"v_mfma_f32_16x16x32_bf16 v[144:147], a[64:67], a[32:35], v[176:179]\n" \
"s_add_u32 m0, s66, 73728\n" \
"s_add_u32 s79, s65, 73728\n" \
"v_mfma_f32_16x16x32_bf16 v[152:155], a[64:67], a[48:51], v[184:187]\n" \
"buffer_load_dwordx4 v224, %[kvs], s79 offen offset:0 sc1 lds\n" \
"ds_read_b128 a[64:67], v234 offset:32768\n" \
"v_mfma_f32_16x16x32_bf16 v[156:159], a[68:71], a[48:51], v[188:191]\n" \
"v_mfma_f32_16x16x32_bf16 v[148:151], a[68:71], a[32:35], v[180:183]\n" \
"ds_read_b128 a[68:71], v234 offset:33792\n" \
"buffer_load_dwordx4 a[140:143], v241, %[wsr], s60 offen offset:3072 sc1\n" \
"v_mfma_f32_16x16x32_bf16 v[144:147], a[72:75], a[36:39], v[144:147]\n" \
"v_mfma_f32_16x16x32_bf16 v[152:155], a[72:75], a[52:55], v[152:155]\n" \
"buffer_load_dwordx4 v224, %[kvs], s79 offen offset:1024 sc1 lds\n" \
"ds_read_b128 a[72:75], v234 offset:34816\n" \
"v_mfma_f32_16x16x32_bf16 v[156:159], a[76:79], a[52:55], v[156:159]\n" \
"s_waitcnt vmcnt(10) lgkmcnt(3)\n" \
"s_barrier\n" \
"v_mfma_f32_16x16x32_bf16 v[148:151], a[76:79], a[36:39], v[148:151]\n" \
"ds_read_b128 a[76:79], v234 offset:35840\n" \
"ds_read_b128 a[96:99], v241 offset:8192\n" \
"buffer_load_dwordx4 a[144:147], v249, %[wsr], s60 offen offset:0 sc1\n" \
"v_mfma_f32_16x16x32_bf16 v[144:147], a[80:83], a[40:43], v[144:147]\n" \
"ds_read_b128 a[100:103], v241 offset:9216\n" \
"ds_read_b128 a[104:107], v241 offset:10240\n" \
"v_mfma_f32_16x16x32_bf16 v[152:155], a[80:83], a[56:59], v[152:155]\n" \
"buffer_load_dwordx4 v224, %[kvs], s79 offen offset:2048 sc1 lds\n" \
"ds_read_b128 a[80:83], v234 offset:36864\n" \
"v_mfma_f32_16x16x32_bf16 v[156:159], a[84:87], a[56:59], v[156:159]\n" \
"ds_read_b128 a[108:111], v241 offset:11264\n" \
"ds_read_b128 a[112:115], v241 offset:12288\n" \
"v_mfma_f32_16x16x32_bf16 v[148:151], a[84:87], a[40:43], v[148:151]\n" \
"ds_read_b128 a[84:87], v234 offset:37888\n" \
"ds_read_b128 a[116:119], v241 offset:13312\n" \
"v_mfma_f32_16x16x32_bf16 v[144:147], a[88:91], a[44:47], v[144:147]\n" \
"ds_read_b128 a[120:123], v241 offset:14336\n" \
"ds_read_b128 a[124:127], v241 offset:15360\n" \
"v_mfma_f32_16x16x32_bf16 v[152:155], a[88:91], a[60:63], v[152:155]\n" \
"buffer_load_dwordx4 v224, %[kvs], s79 offen offset:3072 sc1 lds\n" \
"ds_read_b128 a[88:91], v234 offset:38912\n" \
"v_mfma_f32_16x16x32_bf16 v[156:159], a[92:95], a[60:63], v[156:159]\n" \
"v_exp_f32_e32 v128, v128\n" \
"v_mfma_f32_16x16x32_bf16 v[148:151], a[92:95], a[44:47], v[148:151]\n" \
"s_waitcnt lgkmcnt(1)\n" \
"ds_read_b128 a[92:95], v234 offset:39936\n" \
"buffer_load_dwordx4 a[148:151], v249, %[wsr], s60 offen offset:1024 sc1\n" \
"v_mfma_f32_16x16x32_bf16 v[0:3], a[96:99], v[192:195], 0\n" \
"v_exp_f32_e32 v129, v129\n" \
"v_mfma_f32_16x16x32_bf16 v[32:35], a[96:99], v[196:199], 0\n" \
"s_cmp_ge_i32 s74, -2\n" \
"s_cbranch_scc1 .LATLmk1\n" \
".LATLmb1:\n" \
"v_mfma_f32_16x16x32_bf16 v[36:39], a[100:103], v[196:199], 0\n" \
"s_add_u32 s68, s68, 0x4000\n" \
"v_exp_f32_e32 v130, v130\n" \
"v_mfma_f32_16x16x32_bf16 v[4:7], a[100:103], v[192:195], 0\n" \
"buffer_load_dwordx4 a[152:155], v249, %[wsr], s60 offen offset:2048 sc1\n" \
"v_exp_f32_e32 v131, v131\n" \
"v_mfma_f32_16x16x32_bf16 v[8:11], a[104:107], v[192:195], 0\n" \
"s_mov_b32 m0, s69\n" \
"v_exp_f32_e32 v132, v132\n" \
"v_mfma_f32_16x16x32_bf16 v[40:43], a[104:107], v[196:199], 0\n" \
"buffer_load_dwordx4 v224, %[kvs], s68 offen offset:0 sc1 lds\n" \
"v_mfma_f32_16x16x32_bf16 v[44:47], a[108:111], v[196:199], 0\n" \
"s_xor_b32 s72, s72, 0x10000\n" \
"v_exp_f32_e32 v133, v133\n" \
"v_mfma_f32_16x16x32_bf16 v[12:15], a[108:111], v[192:195], 0\n" \
"s_lshl_b32 s61, %[h], 13\n" \
"v_exp_f32_e32 v134, v134\n" \
"v_mfma_f32_16x16x32_bf16 v[16:19], a[112:115], v[192:195], 0\n" \
"s_add_u32 s61, s61, 50331648\n" \
"v_exp_f32_e32 v135, v135\n" \
"v_mfma_f32_16x16x32_bf16 v[48:51], a[112:115], v[196:199], 0\n" \
"buffer_load_dwordx4 v224, %[kvs], s68 offen offset:1024 sc1 lds\n" \
"v_mfma_f32_16x16x32_bf16 v[52:55], a[116:119], v[196:199], 0\n" \
"s_lshl_b32 s65, %[da], 18\n" \
"v_exp_f32_e32 v136, v136\n" \
"v_mfma_f32_16x16x32_bf16 v[20:23], a[116:119], v[192:195], 0\n" \
"buffer_load_dwordx4 a[156:159], v249, %[wsr], s60 offen offset:3072 sc1\n" \
"v_exp_f32_e32 v137, v137\n" \
"v_mfma_f32_16x16x32_bf16 v[24:27], a[120:123], v[192:195], 0\n" \
"s_add_u32 s65, s65, s61\n" \
"v_exp_f32_e32 v138, v138\n" \
"v_mfma_f32_16x16x32_bf16 v[56:59], a[120:123], v[196:199], 0\n" \
"buffer_load_dwordx4 v224, %[kvs], s68 offen offset:2048 sc1 lds\n" \
"v_mfma_f32_16x16x32_bf16 v[60:63], a[124:127], v[196:199], 0\n" \
"s_waitcnt lgkmcnt(0)\n" \
"v_exp_f32_e32 v139, v139\n" \
"v_mfma_f32_16x16x32_bf16 v[28:31], a[124:127], v[192:195], 0\n" \
"s_add_u32 s60, s64, 8192\n" \
"v_exp_f32_e32 v140, v140\n" \
"v_smfmac_f32_16x16x64_bf16 a[240:243], a[236:239], v[192:199], v248\n" \
"s_lshl_b32 s66, %[db], 18\n" \
"v_exp_f32_e32 v141, v141\n" \
"v_mfma_f32_16x16x32_bf16 v[64:67], a[96:99], v[200:203], 0\n" \
"buffer_load_dwordx4 v224, %[kvs], s68 offen offset:3072 sc1 lds\n" \
"v_mfma_f32_16x16x32_bf16 v[96:99], a[96:99], v[204:207], 0\n" \
"s_add_u32 s66, s66, s61\n" \
"ds_read_b128 a[96:99], v234 offset:24576\n" \
"s_mov_b32 s76, 0xfab44000\n" \
"v_mfma_f32_16x16x32_bf16 v[100:103], a[100:103], v[204:207], 0\n" \
"s_and_b32 s61, %[pair], 3\n" \
"v_exp_f32_e32 v142, v142\n" \
"v_mfma_f32_16x16x32_bf16 v[68:71], a[100:103], v[200:203], 0\n" \
"s_mul_i32 s62, s61, 0x21000\n" \
"ds_read_b128 a[100:103], v234 offset:25600\n" \
"buffer_load_dwordx4 a[160:163], v241, %[wsr], s60 offen offset:0 sc1\n" \
"v_mfma_f32_16x16x32_bf16 v[72:75], a[104:107], v[200:203], 0\n" \
"s_and_b32 s62, s62, 0x41000\n" \
"v_exp_f32_e32 v143, v143\n" \
"v_mfma_f32_16x16x32_bf16 v[104:107], a[104:107], v[204:207], 0\n" \
"s_lshl_b32 s61, s61, 16\n" \
"ds_read_b128 a[104:107], v234 offset:26624\n" \
"v_cvt_pk_bf16_f32 v192, v128, v129\n" \
"v_mfma_f32_16x16x32_bf16 v[108:111], a[108:111], v[204:207], 0\n" \
"s_or_b32 s62, s62, s61\n" \
"v_cvt_pk_bf16_f32 v193, v130, v131\n" \
"v_cvt_pk_bf16_f32 v194, v132, v133\n" \
"v_mfma_f32_16x16x32_bf16 v[76:79], a[108:111], v[200:203], 0\n" \
"s_andn2_b32 s61, %[h], 1\n" \
"ds_read_b128 a[108:111], v234 offset:27648\n" \
"v_cvt_pk_bf16_f32 v195, v134, v135\n" \
"v_mfma_f32_16x16x32_bf16 v[80:83], a[112:115], v[200:203], 0\n" \
"s_lshl_b32 s61, s61, 18\n" \
"v_cvt_pk_bf16_f32 v196, v136, v137\n" \
"v_cvt_pk_bf16_f32 v197, v138, v139\n" \
"v_mfma_f32_16x16x32_bf16 v[112:115], a[112:115], v[204:207], 0\n" \
"s_add_u32 s62, s62, s61\n" \
"ds_read_b128 a[112:115], v234 offset:28672\n" \
"v_cvt_pk_bf16_f32 v198, v140, v141\n" \
"v_mfma_f32_16x16x32_bf16 v[116:119], a[116:119], v[204:207], 0\n" \
"s_lshl1_add_u32 s62, %[w4k], s62\n" \
"v_cvt_pk_bf16_f32 v199, v142, v143\n" \
"s_add_u32 s77, s62, 50331648\n" \
"v_mfma_f32_16x16x32_bf16 v[84:87], a[116:119], v[200:203], 0\n" \
"s_lshl_b32 s62, %[dd], 16\n" \
"ds_read_b128 a[116:119], v234 offset:29696\n" \
"s_and_b32 s63, %[dd], 0xffff0000\n" \
"v_mfma_f32_16x16x32_bf16 v[88:91], a[120:123], v[200:203], 0\n" \
"buffer_load_dwordx4 a[164:167], v241, %[wsr], s60 offen offset:1024 sc1\n" \
"v_mfma_f32_16x16x32_bf16 v[120:123], a[120:123], v[204:207], 0\n" \
"ds_read_b128 a[120:123], v234 offset:30720\n" \
"ds_read_b128 v[212:215], v254 offset:8192\n" \
"v_mfma_f32_16x16x32_bf16 v[124:127], a[124:127], v[204:207], 0\n" \
"buffer_load_dwordx4 a[168:171], v241, %[wsr], s60 offen offset:2048 sc1\n" \
"v_mfma_f32_16x16x32_bf16 v[92:95], a[124:127], v[200:203], 0\n" \
"ds_read_b128 a[124:127], v234 offset:31744\n" \
"ds_read_b128 v[208:211], v254 offset:0\n" \
"v_smfmac_f32_16x16x64_bf16 a[248:251], a[236:239], v[200:207], v248\n" \
"buffer_load_dwordx4 a[172:175], v241, %[wsr], s60 offen offset:3072 sc1\n" \
"v_mfma_f32_16x16x32_bf16 v[128:131], a[64:67], a[0:3], v[160:163]\n" \
"v_exp_f32_e32 v144, v144\n" \
"v_mfma_f32_16x16x32_bf16 v[136:139], a[64:67], a[16:19], v[168:171]\n" \
"ds_read_b128 v[226:229], v254 offset:16384\n" \
"ds_read_b128 v[230:233], v254 offset:24576\n" \
"v_mfma_f32_16x16x32_bf16 v[140:143], a[68:71], a[16:19], v[172:175]\n" \
"s_add_u32 s68, s68, 0x4000\n" \
"v_exp_f32_e32 v145, v145\n" \
"v_mfma_f32_16x16x32_bf16 v[132:135], a[68:71], a[0:3], v[164:167]\n" \
"v_cvt_pk_bf16_f32 v200, v144, v145\n" \
"v_mfma_f32_16x16x32_bf16 v[128:131], a[72:75], a[4:7], v[128:131]\n" \
"s_mov_b32 m0, s70\n" \
"v_exp_f32_e32 v146, v146\n" \
"v_mfma_f32_16x16x32_bf16 v[136:139], a[72:75], a[20:23], v[136:139]\n" \
"buffer_load_dwordx4 v224, %[kvs], s68 offen offset:0 sc1 lds\n" \
"v_mfma_f32_16x16x32_bf16 v[140:143], a[76:79], a[20:23], v[140:143]\n" \
"s_xor_b32 s69, s69, 0x10000\n" \
"v_exp_f32_e32 v147, v147\n" \
"v_mfma_f32_16x16x32_bf16 v[132:135], a[76:79], a[4:7], v[132:135]\n" \
"buffer_load_dwordx4 a[176:179], v249, %[wsr], s60 offen offset:0 sc1\n" \
"v_cvt_pk_bf16_f32 v201, v146, v147\n" \
"v_mfma_f32_16x16x32_bf16 v[128:131], a[80:83], a[8:11], v[128:131]\n" \
"s_waitcnt lgkmcnt(0)\n" \
"v_exp_f32_e32 v148, v148\n" \
"v_add_f32_e32 v208, v208, v212\n" \
"v_add_f32_e32 v209, v209, v213\n" \
"v_add_f32_e32 v210, v210, v214\n" \
"v_add_f32_e32 v211, v211, v215\n" \
"v_mfma_f32_16x16x32_bf16 v[136:139], a[80:83], a[24:27], v[136:139]\n" \
"buffer_load_dwordx4 v224, %[kvs], s68 offen offset:1024 sc1 lds\n" \
"v_lshlrev_b32_e32 v212, 16, v216\n" \
"v_lshlrev_b32_e32 v213, 16, v217\n" \
"v_lshlrev_b32_e32 v214, 16, v218\n" \
"v_lshlrev_b32_e32 v215, 16, v219\n" \
"v_mul_f32_e32 v208, v236, v208\n" \
"v_mfma_f32_16x16x32_bf16 v[140:143], a[84:87], a[24:27], v[140:143]\n" \
"v_exp_f32_e32 v149, v149\n" \
"v_mul_f32_e32 v209, v236, v209\n" \
"v_mul_f32_e32 v210, v236, v210\n" \
"v_mul_f32_e32 v211, v236, v211\n" \
"v_fmac_f32_e32 v208, s62, v212\n" \
"v_fmac_f32_e32 v209, s62, v213\n" \
"v_mfma_f32_16x16x32_bf16 v[132:135], a[84:87], a[8:11], v[132:135]\n" \
"buffer_load_dwordx4 a[180:183], v249, %[wsr], s60 offen offset:1024 sc1\n" \
"v_cvt_pk_bf16_f32 v202, v148, v149\n" \
"v_fmac_f32_e32 v210, s62, v214\n" \
"v_fmac_f32_e32 v211, s62, v215\n" \
"v_add_f32_e32 v226, v226, v230\n" \
"v_add_f32_e32 v227, v227, v231\n" \
"v_add_f32_e32 v228, v228, v232\n" \
"v_mfma_f32_16x16x32_bf16 v[128:131], a[88:91], a[12:15], v[128:131]\n" \
"v_exp_f32_e32 v150, v150\n" \
"v_add_f32_e32 v229, v229, v233\n" \
"v_and_b32_e32 v230, 0xffff0000, v216\n" \
"v_and_b32_e32 v231, 0xffff0000, v217\n" \
"v_and_b32_e32 v232, 0xffff0000, v218\n" \
"v_and_b32_e32 v233, 0xffff0000, v219\n" \
"v_mfma_f32_16x16x32_bf16 v[136:139], a[88:91], a[28:31], v[136:139]\n" \
"s_cmp_ge_i32 s74, -3\n" \
"s_cbranch_scc1 .LATLmk2\n" \
".LATLmb2:\n" \
"v_mul_f32_e32 v226, v237, v226\n" \
"v_mul_f32_e32 v227, v237, v227\n" \
"v_mul_f32_e32 v228, v237, v228\n" \
"v_mul_f32_e32 v229, v237, v229\n" \
"v_fmac_f32_e32 v226, s63, v230\n" \
"v_mfma_f32_16x16x32_bf16 v[140:143], a[92:95], a[28:31], v[140:143]\n" \
"v_exp_f32_e32 v151, v151\n" \
"v_fmac_f32_e32 v227, s63, v231\n" \
"v_fmac_f32_e32 v228, s63, v232\n" \
"v_mfma_f32_16x16x32_bf16 v[132:135], a[92:95], a[12:15], v[132:135]\n" \
"s_waitcnt vmcnt(22)\n" \
"s_barrier\n" \
"v_mfma_f32_16x16x32_bf16 v[0:3], a[96:99], v[192:195], v[0:3]\n" \
"v_exp_f32_e32 v152, v152\n" \
"v_mfma_f32_16x16x32_bf16 v[32:35], a[96:99], v[196:199], v[32:35]\n" \
"buffer_load_dwordx4 a[184:187], v249, %[wsr], s60 offen offset:2048 sc1\n" \
"v_cvt_pk_bf16_f32 v203, v150, v151\n" \
"v_mfma_f32_16x16x32_bf16 v[36:39], a[100:103], v[196:199], v[36:39]\n" \
"v_exp_f32_e32 v153, v153\n" \
"v_mfma_f32_16x16x32_bf16 v[4:7], a[100:103], v[192:195], v[4:7]\n" \
"v_cvt_pk_bf16_f32 v204, v152, v153\n" \
"v_fmac_f32_e32 v229, s63, v233\n" \
"v_mfma_f32_16x16x32_bf16 v[8:11], a[104:107], v[192:195], v[8:11]\n" \
"v_exp_f32_e32 v154, v154\n" \
"v_mfma_f32_16x16x32_bf16 v[40:43], a[104:107], v[196:199], v[40:43]\n" \
"buffer_load_dwordx4 v224, %[kvs], s68 offen offset:2048 sc1 lds\n" \
"v_mfma_f32_16x16x32_bf16 v[44:47], a[108:111], v[196:199], v[44:47]\n" \
"v_exp_f32_e32 v155, v155\n" \
"v_mfma_f32_16x16x32_bf16 v[12:15], a[108:111], v[192:195], v[12:15]\n" \
"v_cvt_pk_bf16_f32 v205, v154, v155\n" \
"v_cvt_pk_bf16_f32 v208, v208, v226\n" \
"v_mfma_f32_16x16x32_bf16 v[16:19], a[112:115], v[192:195], v[16:19]\n" \
"v_exp_f32_e32 v156, v156\n" \
"v_mfma_f32_16x16x32_bf16 v[48:51], a[112:115], v[196:199], v[48:51]\n" \
"buffer_load_dwordx4 a[188:191], v249, %[wsr], s60 offen offset:3072 sc1\n" \
"v_mfma_f32_16x16x32_bf16 v[52:55], a[116:119], v[196:199], v[52:55]\n" \
"v_exp_f32_e32 v157, v157\n" \
"v_mfma_f32_16x16x32_bf16 v[20:23], a[116:119], v[192:195], v[20:23]\n" \
"v_cvt_pk_bf16_f32 v206, v156, v157\n" \
"v_cvt_pk_bf16_f32 v209, v209, v227\n" \
"v_mfma_f32_16x16x32_bf16 v[24:27], a[120:123], v[192:195], v[24:27]\n" \
"v_exp_f32_e32 v158, v158\n" \
"v_mfma_f32_16x16x32_bf16 v[56:59], a[120:123], v[196:199], v[56:59]\n" \
"buffer_load_dwordx4 v224, %[kvs], s68 offen offset:3072 sc1 lds\n" \
"v_mfma_f32_16x16x32_bf16 v[60:63], a[124:127], v[196:199], v[60:63]\n" \
"s_add_u32 s60, s64, 16384\n" \
"v_exp_f32_e32 v159, v159\n" \
"v_mfma_f32_16x16x32_bf16 v[28:31], a[124:127], v[192:195], v[28:31]\n" \
"v_cvt_pk_bf16_f32 v207, v158, v159\n" \
"v_cvt_pk_bf16_f32 v210, v210, v228\n" \
"v_smfmac_f32_16x16x64_bf16 a[240:243], a[236:239], v[192:199], v248\n" \
"buffer_load_dwordx4 a[192:195], v241, %[wsr], s60 offen offset:0 sc1\n" \
"v_mfma_f32_16x16x32_bf16 v[144:147], a[64:67], a[32:35], v[176:179]\n" \
"v_exp_f32_e32 v128, v128\n" \
"v_mfma_f32_16x16x32_bf16 v[152:155], a[64:67], a[48:51], v[184:187]\n" \
"s_lshl_b32 s79, %[pair], 13\n" \
"ds_read_b128 a[64:67], v234 offset:49152\n" \
"v_cvt_pk_bf16_f32 v211, v211, v229\n" \
"v_mfma_f32_16x16x32_bf16 v[156:159], a[68:71], a[48:51], v[188:191]\n" \
"s_lshr_b32 s61, %[w4k], 1\n" \
"v_exp_f32_e32 v129, v129\n" \
"v_mfma_f32_16x16x32_bf16 v[148:151], a[68:71], a[32:35], v[180:183]\n" \
"s_add_u32 s79, s79, s61\n" \
"ds_read_b128 a[68:71], v234 offset:50176\n" \
"v_cvt_pk_bf16_f32 v192, v128, v129\n" \
"v_mfma_f32_16x16x32_bf16 v[144:147], a[72:75], a[36:39], v[144:147]\n" \
"s_add_u32 s79, s79, 2097152\n" \
"v_exp_f32_e32 v130, v130\n" \
"v_mfma_f32_16x16x32_bf16 v[152:155], a[72:75], a[52:55], v[152:155]\n" \
"ds_read_b128 a[72:75], v234 offset:51200\n" \
"buffer_load_dwordx4 a[196:199], v241, %[wsr], s60 offen offset:1024 sc1\n" \
"v_mfma_f32_16x16x32_bf16 v[156:159], a[76:79], a[52:55], v[156:159]\n" \
"v_exp_f32_e32 v131, v131\n" \
"v_mfma_f32_16x16x32_bf16 v[148:151], a[76:79], a[36:39], v[148:151]\n" \
"ds_read_b128 a[76:79], v234 offset:52224\n" \
"buffer_store_dwordx4 v[208:211], v241, %[wsr], s79 offen offset:0 sc1\n" \
"v_cvt_pk_bf16_f32 v193, v130, v131\n" \
"v_mfma_f32_16x16x32_bf16 v[144:147], a[80:83], a[40:43], v[144:147]\n" \
"ds_read_b128 v[212:215], v254 offset:9216\n" \
"ds_read_b128 v[208:211], v254 offset:1024\n" \
"ds_read_b128 v[226:229], v254 offset:17408\n" \
"v_exp_f32_e32 v132, v132\n" \
"v_mfma_f32_16x16x32_bf16 v[152:155], a[80:83], a[56:59], v[152:155]\n" \
"ds_read_b128 a[80:83], v234 offset:53248\n" \
"buffer_load_dwordx4 a[200:203], v241, %[wsr], s60 offen offset:2048 sc1\n" \
"v_mfma_f32_16x16x32_bf16 v[156:159], a[84:87], a[56:59], v[156:159]\n" \
"v_exp_f32_e32 v133, v133\n" \
"v_mfma_f32_16x16x32_bf16 v[148:151], a[84:87], a[40:43], v[148:151]\n" \
"ds_read_b128 a[84:87], v234 offset:54272\n" \
"v_cvt_pk_bf16_f32 v194, v132, v133\n" \
"v_mfma_f32_16x16x32_bf16 v[144:147], a[88:91], a[44:47], v[144:147]\n" \
"v_exp_f32_e32 v134, v134\n" \
"v_mfma_f32_16x16x32_bf16 v[152:155], a[88:91], a[60:63], v[152:155]\n" \
"ds_read_b128 a[88:91], v234 offset:55296\n" \
"ds_read_b128 v[230:233], v254 offset:25600\n" \
"v_mfma_f32_16x16x32_bf16 v[156:159], a[92:95], a[60:63], v[156:159]\n" \
"v_exp_f32_e32 v135, v135\n" \
"v_mfma_f32_16x16x32_bf16 v[148:151], a[92:95], a[44:47], v[148:151]\n" \
"ds_read_b128 a[92:95], v234 offset:56320\n" \
"buffer_load_dwordx4 a[204:207], v241, %[wsr], s60 offen offset:3072 sc1\n" \
"v_mfma_f32_16x16x32_bf16 v[64:67], a[96:99], v[200:203], v[64:67]\n" \
"v_exp_f32_e32 v136, v136\n" \
"v_mfma_f32_16x16x32_bf16 v[96:99], a[96:99], v[204:207], v[96:99]\n" \
"ds_read_b128 a[96:99], v234 offset:40960\n" \
"v_cvt_pk_bf16_f32 v195, v134, v135\n" \
"v_mfma_f32_16x16x32_bf16 v[100:103], a[100:103], v[204:207], v[100:103]\n" \
"v_exp_f32_e32 v137, v137\n" \
"v_mfma_f32_16x16x32_bf16 v[68:71], a[100:103], v[200:203], v[68:71]\n" \
"ds_read_b128 a[100:103], v234 offset:41984\n" \
"v_cvt_pk_bf16_f32 v196, v136, v137\n" \
"v_mfma_f32_16x16x32_bf16 v[72:75], a[104:107], v[200:203], v[72:75]\n" \
"s_waitcnt lgkmcnt(3)\n" \
"v_exp_f32_e32 v138, v138\n" \
"v_add_f32_e32 v208, v208, v212\n" \
"v_add_f32_e32 v209, v209, v213\n" \
"v_add_f32_e32 v210, v210, v214\n" \
"v_add_f32_e32 v211, v211, v215\n" \
"v_mfma_f32_16x16x32_bf16 v[104:107], a[104:107], v[204:207], v[104:107]\n" \
"ds_read_b128 a[104:107], v234 offset:43008\n" \
"buffer_load_dwordx4 a[208:211], v249, %[wsr], s60 offen offset:0 sc1\n" \
"v_lshlrev_b32_e32 v212, 16, v220\n" \
"v_lshlrev_b32_e32 v213, 16, v221\n" \
"v_lshlrev_b32_e32 v214, 16, v222\n" \
"v_lshlrev_b32_e32 v215, 16, v223\n" \
"v_mul_f32_e32 v208, v236, v208\n" \
"v_mfma_f32_16x16x32_bf16 v[108:111], a[108:111], v[204:207], v[108:111]\n" \
"v_exp_f32_e32 v139, v139\n" \
"v_mul_f32_e32 v209, v236, v209\n" \
"v_mul_f32_e32 v210, v236, v210\n" \
"v_mul_f32_e32 v211, v236, v211\n" \
"v_fmac_f32_e32 v208, s62, v212\n" \
"v_fmac_f32_e32 v209, s62, v213\n" \
"v_mfma_f32_16x16x32_bf16 v[76:79], a[108:111], v[200:203], v[76:79]\n" \
"ds_read_b128 a[108:111], v234 offset:44032\n" \
"v_cvt_pk_bf16_f32 v197, v138, v139\n" \
"v_fmac_f32_e32 v210, s62, v214\n" \
"v_fmac_f32_e32 v211, s62, v215\n" \
"v_add_f32_e32 v226, v226, v230\n" \
"v_add_f32_e32 v227, v227, v231\n" \
"v_add_f32_e32 v228, v228, v232\n" \
"v_mfma_f32_16x16x32_bf16 v[80:83], a[112:115], v[200:203], v[80:83]\n" \
"s_waitcnt lgkmcnt(4)\n" \
"v_exp_f32_e32 v140, v140\n" \
"v_add_f32_e32 v229, v229, v233\n" \
"v_and_b32_e32 v230, 0xffff0000, v220\n" \
"v_mfma_f32_16x16x32_bf16 v[112:115], a[112:115], v[204:207], v[112:115]\n" \
"ds_read_b128 a[112:115], v234 offset:45056\n" \
"v_and_b32_e32 v231, 0xffff0000, v221\n" \
"v_mfma_f32_16x16x32_bf16 v[116:119], a[116:119], v[204:207], v[116:119]\n" \
"v_exp_f32_e32 v141, v141\n" \
"v_mfma_f32_16x16x32_bf16 v[84:87], a[116:119], v[200:203], v[84:87]\n" \
"ds_read_b128 a[116:119], v234 offset:46080\n" \
"v_cvt_pk_bf16_f32 v198, v140, v141\n" \
"v_mfma_f32_16x16x32_bf16 v[88:91], a[120:123], v[200:203], v[88:91]\n" \
"v_exp_f32_e32 v142, v142\n" \
"v_mfma_f32_16x16x32_bf16 v[120:123], a[120:123], v[204:207], v[120:123]\n" \
"ds_read_b128 a[120:123], v234 offset:47104\n" \
"v_and_b32_e32 v232, 0xffff0000, v222\n" \
"v_mfma_f32_16x16x32_bf16 v[124:127], a[124:127], v[204:207], v[124:127]\n" \
"v_exp_f32_e32 v143, v143\n" \
"v_mfma_f32_16x16x32_bf16 v[92:95], a[124:127], v[200:203], v[92:95]\n" \
"ds_read_b128 a[124:127], v234 offset:48128\n" \
"v_cvt_pk_bf16_f32 v199, v142, v143\n" \
"v_smfmac_f32_16x16x64_bf16 a[248:251], a[236:239], v[200:207], v248\n" \
"buffer_load_dwordx4 a[212:215], v249, %[wsr], s60 offen offset:1024 sc1\n" \
"v_mfma_f32_16x16x32_bf16 v[128:131], a[64:67], a[0:3], v[160:163]\n" \
"v_exp_f32_e32 v144, v144\n" \
"v_mfma_f32_16x16x32_bf16 v[136:139], a[64:67], a[16:19], v[168:171]\n" \
"buffer_load_dwordx4 a[216:219], v249, %[wsr], s60 offen offset:2048 sc1\n" \
"v_mfma_f32_16x16x32_bf16 v[140:143], a[68:71], a[16:19], v[172:175]\n" \
"s_add_u32 s68, s68, 0x4000\n" \
"v_exp_f32_e32 v145, v145\n" \
"v_mfma_f32_16x16x32_bf16 v[132:135], a[68:71], a[0:3], v[164:167]\n" \
"v_cvt_pk_bf16_f32 v200, v144, v145\n" \
"v_and_b32_e32 v233, 0xffff0000, v223\n" \
"v_mfma_f32_16x16x32_bf16 v[128:131], a[72:75], a[4:7], v[128:131]\n" \
"s_mov_b32 m0, s71\n" \
"v_exp_f32_e32 v146, v146\n" \
"v_mfma_f32_16x16x32_bf16 v[136:139], a[72:75], a[20:23], v[136:139]\n" \
"buffer_load_dwordx4 a[220:223], v249, %[wsr], s60 offen offset:3072 sc1\n" \
"v_mfma_f32_16x16x32_bf16 v[140:143], a[76:79], a[20:23], v[140:143]\n" \
"s_xor_b32 s70, s70, 0x10000\n" \
"v_exp_f32_e32 v147, v147\n" \
"v_mfma_f32_16x16x32_bf16 v[132:135], a[76:79], a[4:7], v[132:135]\n" \
"v_cvt_pk_bf16_f32 v201, v146, v147\n" \
"v_mul_f32_e32 v226, v237, v226\n" \
"v_mfma_f32_16x16x32_bf16 v[128:131], a[80:83], a[8:11], v[128:131]\n" \
"v_exp_f32_e32 v148, v148\n" \
"v_mfma_f32_16x16x32_bf16 v[136:139], a[80:83], a[24:27], v[136:139]\n" \
"v_mul_f32_e32 v227, v237, v227\n" \
"v_mul_f32_e32 v228, v237, v228\n" \
"v_mfma_f32_16x16x32_bf16 v[140:143], a[84:87], a[24:27], v[140:143]\n" \
"v_exp_f32_e32 v149, v149\n" \
"v_mfma_f32_16x16x32_bf16 v[132:135], a[84:87], a[8:11], v[132:135]\n" \
"s_add_u32 s60, s64, 24576\n" \
"v_cvt_pk_bf16_f32 v202, v148, v149\n" \
"v_mul_f32_e32 v229, v237, v229\n" \
"v_mfma_f32_16x16x32_bf16 v[128:131], a[88:91], a[12:15], v[128:131]\n" \
"v_exp_f32_e32 v150, v150\n" \
"v_mfma_f32_16x16x32_bf16 v[136:139], a[88:91], a[28:31], v[136:139]\n" \
"s_cmp_eq_i32 s74, -4\n" \
"s_cbranch_scc1 .LATLmk3\n" \
".LATLmb3:\n" \
"v_mfma_f32_16x16x32_bf16 v[140:143], a[92:95], a[28:31], v[140:143]\n" \
"v_exp_f32_e32 v151, v151\n" \
"v_mfma_f32_16x16x32_bf16 v[132:135], a[92:95], a[12:15], v[132:135]\n" \
"s_waitcnt vmcnt(28) lgkmcnt(0)\n" \
"s_barrier\n" \
"v_mfma_f32_16x16x32_bf16 v[0:3], a[96:99], v[192:195], v[0:3]\n" \
"v_exp_f32_e32 v152, v152\n" \
"v_mfma_f32_16x16x32_bf16 v[32:35], a[96:99], v[196:199], v[32:35]\n" \
"buffer_load_dwordx4 a[224:227], v241, %[wsr], s60 offen offset:0 sc1\n" \
"v_cvt_pk_bf16_f32 v203, v150, v151\n" \
"v_mfma_f32_16x16x32_bf16 v[36:39], a[100:103], v[196:199], v[36:39]\n" \
"v_exp_f32_e32 v153, v153\n" \
"v_mfma_f32_16x16x32_bf16 v[4:7], a[100:103], v[192:195], v[4:7]\n" \
"buffer_load_dwordx4 a[228:231], v241, %[wsr], s60 offen offset:1024 sc1\n" \
"v_cvt_pk_bf16_f32 v204, v152, v153\n" \
"v_mfma_f32_16x16x32_bf16 v[8:11], a[104:107], v[192:195], v[8:11]\n" \
"v_exp_f32_e32 v154, v154\n" \
"v_mfma_f32_16x16x32_bf16 v[40:43], a[104:107], v[196:199], v[40:43]\n" \
"buffer_load_dwordx4 v224, %[kvs], s68 offen offset:0 sc1 lds\n" \
"v_mfma_f32_16x16x32_bf16 v[44:47], a[108:111], v[196:199], v[44:47]\n" \
"v_exp_f32_e32 v155, v155\n" \
"v_mfma_f32_16x16x32_bf16 v[12:15], a[108:111], v[192:195], v[12:15]\n" \
"buffer_load_dwordx4 a[232:235], v241, %[wsr], s60 offen offset:2048 sc1\n" \
"v_cvt_pk_bf16_f32 v205, v154, v155\n" \
"v_mfma_f32_16x16x32_bf16 v[16:19], a[112:115], v[192:195], v[16:19]\n" \
"v_exp_f32_e32 v156, v156\n" \
"v_mfma_f32_16x16x32_bf16 v[48:51], a[112:115], v[196:199], v[48:51]\n" \
"buffer_load_dwordx4 v224, %[kvs], s68 offen offset:1024 sc1 lds\n" \
"v_mfma_f32_16x16x32_bf16 v[52:55], a[116:119], v[196:199], v[52:55]\n" \
"v_exp_f32_e32 v157, v157\n" \
"v_mfma_f32_16x16x32_bf16 v[20:23], a[116:119], v[192:195], v[20:23]\n" \
"v_cvt_pk_bf16_f32 v206, v156, v157\n" \
"v_fmac_f32_e32 v226, s63, v230\n" \
"v_mfma_f32_16x16x32_bf16 v[24:27], a[120:123], v[192:195], v[24:27]\n" \
"v_exp_f32_e32 v158, v158\n" \
"v_mfma_f32_16x16x32_bf16 v[56:59], a[120:123], v[196:199], v[56:59]\n" \
"buffer_load_dwordx4 v224, %[kvs], s68 offen offset:2048 sc1 lds\n" \
"v_mfma_f32_16x16x32_bf16 v[60:63], a[124:127], v[196:199], v[60:63]\n" \
"v_exp_f32_e32 v159, v159\n" \
"v_mfma_f32_16x16x32_bf16 v[28:31], a[124:127], v[192:195], v[28:31]\n" \
"v_cvt_pk_bf16_f32 v207, v158, v159\n" \
"v_fmac_f32_e32 v227, s63, v231\n" \
"v_smfmac_f32_16x16x64_bf16 a[240:243], a[236:239], v[192:199], v248\n" \
"buffer_load_dwordx4 v224, %[kvs], s68 offen offset:3072 sc1 lds\n" \
"v_mfma_f32_16x16x32_bf16 v[144:147], a[64:67], a[32:35], v[176:179]\n" \
"v_exp_f32_e32 v128, v128\n" \
"v_mfma_f32_16x16x32_bf16 v[152:155], a[64:67], a[48:51], v[184:187]\n" \
"ds_read_b128 a[64:67], v235 offset:0\n" \
"v_fmac_f32_e32 v228, s63, v232\n" \
"v_mfma_f32_16x16x32_bf16 v[156:159], a[68:71], a[48:51], v[188:191]\n" \
"v_exp_f32_e32 v129, v129\n" \
"v_mfma_f32_16x16x32_bf16 v[148:151], a[68:71], a[32:35], v[180:183]\n" \
"ds_read_b128 a[68:71], v235 offset:1024\n" \
"v_cvt_pk_bf16_f32 v192, v128, v129\n" \
"v_mfma_f32_16x16x32_bf16 v[144:147], a[72:75], a[36:39], v[144:147]\n" \
"v_exp_f32_e32 v130, v130\n" \
"v_mfma_f32_16x16x32_bf16 v[152:155], a[72:75], a[52:55], v[152:155]\n" \
"ds_read_b128 a[72:75], v235 offset:2048\n" \
"v_fmac_f32_e32 v229, s63, v233\n" \
"v_mfma_f32_16x16x32_bf16 v[156:159], a[76:79], a[52:55], v[156:159]\n" \
"v_exp_f32_e32 v131, v131\n" \
"v_mfma_f32_16x16x32_bf16 v[148:151], a[76:79], a[36:39], v[148:151]\n" \
"ds_read_b128 a[76:79], v235 offset:3072\n" \
"v_cvt_pk_bf16_f32 v193, v130, v131\n" \
"v_mfma_f32_16x16x32_bf16 v[144:147], a[80:83], a[40:43], v[144:147]\n" \
"v_exp_f32_e32 v132, v132\n" \
"v_mfma_f32_16x16x32_bf16 v[152:155], a[80:83], a[56:59], v[152:155]\n" \
"ds_read_b128 a[80:83], v235 offset:4096\n" \
"v_cvt_pk_bf16_f32 v208, v208, v226\n" \
"v_mfma_f32_16x16x32_bf16 v[156:159], a[84:87], a[56:59], v[156:159]\n" \
"s_lshl1_add_u32 m0, %[w4k], 138240\n" \
"v_exp_f32_e32 v133, v133\n" \
"v_mfma_f32_16x16x32_bf16 v[148:151], a[84:87], a[40:43], v[148:151]\n" \
"ds_read_b128 a[84:87], v235 offset:5120\n" \
"v_cvt_pk_bf16_f32 v194, v132, v133\n" \
"v_mfma_f32_16x16x32_bf16 v[144:147], a[88:91], a[44:47], v[144:147]\n" \
"s_add_u32 s83, s64, 56320\n" \
"v_exp_f32_e32 v134, v134\n" \
"v_mfma_f32_16x16x32_bf16 v[152:155], a[88:91], a[60:63], v[152:155]\n" \
"ds_read_b128 a[88:91], v235 offset:6144\n" \
"buffer_load_dwordx4 v241, %[wsr], s83 offen sc1 lds\n" \
"v_mfma_f32_16x16x32_bf16 v[156:159], a[92:95], a[60:63], v[156:159]\n" \
"v_exp_f32_e32 v135, v135\n" \
"v_mfma_f32_16x16x32_bf16 v[148:151], a[92:95], a[44:47], v[148:151]\n" \
"ds_read_b128 a[92:95], v235 offset:7168\n" \
"v_cvt_pk_bf16_f32 v209, v209, v227\n" \
"v_mfma_f32_16x16x32_bf16 v[64:67], a[96:99], v[200:203], v[64:67]\n" \
"v_exp_f32_e32 v136, v136\n" \
"v_mfma_f32_16x16x32_bf16 v[96:99], a[96:99], v[204:207], v[96:99]\n" \
"ds_read_b128 a[96:99], v234 offset:57344\n" \
"v_xor_b32_e32 v235, 0x10000, v235\n" \
"v_cvt_pk_bf16_f32 v195, v134, v135\n" \
"v_mfma_f32_16x16x32_bf16 v[100:103], a[100:103], v[204:207], v[100:103]\n" \
"v_exp_f32_e32 v137, v137\n" \
"v_mfma_f32_16x16x32_bf16 v[68:71], a[100:103], v[200:203], v[68:71]\n" \
"ds_read_b128 a[100:103], v234 offset:58368\n" \
"v_cvt_pk_bf16_f32 v196, v136, v137\n" \
"v_mfma_f32_16x16x32_bf16 v[72:75], a[104:107], v[200:203], v[72:75]\n" \
"v_exp_f32_e32 v138, v138\n" \
"v_mfma_f32_16x16x32_bf16 v[104:107], a[104:107], v[204:207], v[104:107]\n" \
"ds_read_b128 a[104:107], v234 offset:59392\n" \
"v_cvt_pk_bf16_f32 v210, v210, v228\n" \
"v_mfma_f32_16x16x32_bf16 v[108:111], a[108:111], v[204:207], v[108:111]\n" \
"v_exp_f32_e32 v139, v139\n" \
"v_mfma_f32_16x16x32_bf16 v[76:79], a[108:111], v[200:203], v[76:79]\n" \
"ds_read_b128 a[108:111], v234 offset:60416\n" \
"v_cvt_pk_bf16_f32 v197, v138, v139\n" \
"v_mfma_f32_16x16x32_bf16 v[80:83], a[112:115], v[200:203], v[80:83]\n" \
"s_waitcnt lgkmcnt(4)\n" \
"v_exp_f32_e32 v140, v140\n" \
"v_mfma_f32_16x16x32_bf16 v[112:115], a[112:115], v[204:207], v[112:115]\n" \
"ds_read_b128 a[112:115], v234 offset:61440\n" \
"v_cvt_pk_bf16_f32 v211, v211, v229\n" \
"v_mfma_f32_16x16x32_bf16 v[116:119], a[116:119], v[204:207], v[116:119]\n" \
"buffer_store_dwordx4 v[208:211], v241, %[wsr], s79 offen offset:1024 sc1\n" \
"v_exp_f32_e32 v141, v141\n" \
"v_mfma_f32_16x16x32_bf16 v[84:87], a[116:119], v[200:203], v[84:87]\n" \
"s_andn2_b32 s79, %[pair], 31\n" \
"ds_read_b128 a[116:119], v234 offset:62464\n" \
"v_cvt_pk_bf16_f32 v198, v140, v141\n" \
"v_mfma_f32_16x16x32_bf16 v[88:91], a[120:123], v[200:203], v[88:91]\n" \
"s_lshl_b32 s79, s79, 10\n" \
"v_exp_f32_e32 v142, v142\n" \
"v_mfma_f32_16x16x32_bf16 v[120:123], a[120:123], v[204:207], v[120:123]\n" \
"s_lshl1_add_u32 s79, %[w4k], s79\n" \
"ds_read_b128 a[120:123], v234 offset:63488\n" \
"s_and_b32 s61, %[pair], 31\n" \
"v_mfma_f32_16x16x32_bf16 v[124:127], a[124:127], v[204:207], v[124:127]\n" \
"s_lshl2_add_u32 s79, s61, s79\n" \
"v_exp_f32_e32 v143, v143\n" \
"v_mfma_f32_16x16x32_bf16 v[92:95], a[124:127], v[200:203], v[92:95]\n" \
"ds_read_b128 a[124:127], v234 offset:64512\n" \
"v_cvt_pk_bf16_f32 v199, v142, v143\n" \
"v_smfmac_f32_16x16x64_bf16 a[248:251], a[236:239], v[200:207], v248\n" \
"v_xor_b32_e32 v234, 0x10000, v234\n" \
"s_sub_u32 s73, s73, 1\n" \
"s_cbranch_scc1 .LATLt0\n" \
"v_mfma_f32_16x16x32_bf16 v[128:131], a[64:67], a[0:3], v[160:163]\n" \
"s_add_u32 s60, s64, 57344\n" \
"v_exp_f32_e32 v144, v144\n" \
"v_mfma_f32_16x16x32_bf16 v[136:139], a[64:67], a[16:19], v[168:171]\n" \
"buffer_load_dwordx4 v[226:229], v241, %[wsr], s60 offen offset:0 sc1\n" \
"v_mfma_f32_16x16x32_bf16 v[140:143], a[68:71], a[16:19], v[172:175]\n" \
"s_add_u32 s68, s68, 0x4000\n" \
"v_exp_f32_e32 v145, v145\n" \
"v_mfma_f32_16x16x32_bf16 v[132:135], a[68:71], a[0:3], v[164:167]\n" \
"buffer_load_dwordx4 v[230:233], v241, %[wsr], s60 offen offset:1024 sc1\n" \
"v_cvt_pk_bf16_f32 v200, v144, v145\n" \
"v_mfma_f32_16x16x32_bf16 v[128:131], a[72:75], a[4:7], v[128:131]\n" \
"s_mov_b32 m0, s72\n" \
"v_exp_f32_e32 v146, v146\n" \
"v_mfma_f32_16x16x32_bf16 v[136:139], a[72:75], a[20:23], v[136:139]\n" \
"buffer_load_dwordx4 v224, %[kvs], s68 offen offset:0 sc1 lds\n" \
"v_mfma_f32_16x16x32_bf16 v[140:143], a[76:79], a[20:23], v[140:143]\n" \
"s_xor_b32 s71, s71, 0x10000\n" \
"v_exp_f32_e32 v147, v147\n" \
"v_mfma_f32_16x16x32_bf16 v[132:135], a[76:79], a[4:7], v[132:135]\n" \
"buffer_load_dwordx4 v[236:239], v241, %[wsr], s60 offen offset:2048 sc1\n" \
"v_cvt_pk_bf16_f32 v201, v146, v147\n" \
"v_mfma_f32_16x16x32_bf16 v[128:131], a[80:83], a[8:11], v[128:131]\n" \
"s_add_i32 s74, s74, 4\n" \
"v_exp_f32_e32 v148, v148\n" \
"v_mfma_f32_16x16x32_bf16 v[136:139], a[80:83], a[24:27], v[136:139]\n" \
"buffer_load_dwordx4 v224, %[kvs], s68 offen offset:1024 sc1 lds\n" \
"v_mfma_f32_16x16x32_bf16 v[140:143], a[84:87], a[24:27], v[140:143]\n" \
"v_exp_f32_e32 v149, v149\n" \
"v_mfma_f32_16x16x32_bf16 v[132:135], a[84:87], a[8:11], v[132:135]\n" \
"buffer_load_dwordx4 a[244:247], v241, %[wsr], s60 offen offset:3072 sc1\n" \
"v_cvt_pk_bf16_f32 v202, v148, v149\n" \
"v_mfma_f32_16x16x32_bf16 v[128:131], a[88:91], a[12:15], v[128:131]\n" \
"v_exp_f32_e32 v150, v150\n" \
"v_mfma_f32_16x16x32_bf16 v[136:139], a[88:91], a[28:31], v[136:139]\n" \
"s_cmp_ge_i32 s74, -1\n" \
"s_cbranch_scc1 .LATLmk4\n" \
".LATLmb4:\n" \
"v_mfma_f32_16x16x32_bf16 v[140:143], a[92:95], a[28:31], v[140:143]\n" \
"v_exp_f32_e32 v151, v151\n" \
"v_mfma_f32_16x16x32_bf16 v[132:135], a[92:95], a[12:15], v[132:135]\n" \
"s_waitcnt lgkmcnt(0)\n" \
"v_mfma_f32_16x16x32_bf16 v[0:3], a[96:99], v[192:195], v[0:3]\n" \
"v_exp_f32_e32 v152, v152\n" \
"v_mfma_f32_16x16x32_bf16 v[32:35], a[96:99], v[196:199], v[32:35]\n" \
"buffer_load_dwordx4 v[208:211], v249, %[wsr], s60 offen offset:0 sc1\n" \
"v_cvt_pk_bf16_f32 v203, v150, v151\n" \
"v_mfma_f32_16x16x32_bf16 v[36:39], a[100:103], v[196:199], v[36:39]\n" \
"v_exp_f32_e32 v153, v153\n" \
"v_mfma_f32_16x16x32_bf16 v[4:7], a[100:103], v[192:195], v[4:7]\n" \
"buffer_load_dwordx4 v[212:215], v249, %[wsr], s60 offen offset:1024 sc1\n" \
"v_cvt_pk_bf16_f32 v204, v152, v153\n" \
"v_mfma_f32_16x16x32_bf16 v[8:11], a[104:107], v[192:195], v[8:11]\n" \
"v_exp_f32_e32 v154, v154\n" \
"v_mfma_f32_16x16x32_bf16 v[40:43], a[104:107], v[196:199], v[40:43]\n" \
"buffer_load_dwordx4 v224, %[kvs], s68 offen offset:2048 sc1 lds\n" \
"v_mfma_f32_16x16x32_bf16 v[44:47], a[108:111], v[196:199], v[44:47]\n" \
"v_exp_f32_e32 v155, v155\n" \
"v_mfma_f32_16x16x32_bf16 v[12:15], a[108:111], v[192:195], v[12:15]\n" \
"buffer_load_dwordx4 v[216:219], v249, %[wsr], s60 offen offset:2048 sc1\n" \
"v_cvt_pk_bf16_f32 v205, v154, v155\n" \
"v_mfma_f32_16x16x32_bf16 v[16:19], a[112:115], v[192:195], v[16:19]\n" \
"v_exp_f32_e32 v156, v156\n" \
"v_mfma_f32_16x16x32_bf16 v[48:51], a[112:115], v[196:199], v[48:51]\n" \
"buffer_load_dwordx4 v[220:223], v249, %[wsr], s60 offen offset:3072 sc1\n" \
"v_mfma_f32_16x16x32_bf16 v[52:55], a[116:119], v[196:199], v[52:55]\n" \
"v_exp_f32_e32 v157, v157\n" \
"v_mfma_f32_16x16x32_bf16 v[20:23], a[116:119], v[192:195], v[20:23]\n" \
"v_cvt_pk_bf16_f32 v206, v156, v157\n" \
"v_mfma_f32_16x16x32_bf16 v[24:27], a[120:123], v[192:195], v[24:27]\n" \
"v_exp_f32_e32 v158, v158\n" \
"v_mfma_f32_16x16x32_bf16 v[56:59], a[120:123], v[196:199], v[56:59]\n" \
"buffer_load_dwordx4 v224, %[kvs], s68 offen offset:3072 sc1 lds\n" \
"v_mfma_f32_16x16x32_bf16 v[60:63], a[124:127], v[196:199], v[60:63]\n" \
"v_exp_f32_e32 v159, v159\n" \
"v_mfma_f32_16x16x32_bf16 v[28:31], a[124:127], v[192:195], v[28:31]\n" \
"v_cvt_pk_bf16_f32 v207, v158, v159\n" \
"v_smfmac_f32_16x16x64_bf16 a[240:243], a[236:239], v[192:199], v248\n" \
".p2align 6\n" \
".LATLp1:\n" \
"v_mfma_f32_16x16x32_bf16 v[144:147], a[64:67], a[32:35], v[176:179]\n" \
"v_exp_f32_e32 v128, v128\n" \
"v_mfma_f32_16x16x32_bf16 v[152:155], a[64:67], a[48:51], v[184:187]\n" \
"ds_read_b128 a[64:67], v234 offset:16384\n" \
"v_mfma_f32_16x16x32_bf16 v[156:159], a[68:71], a[48:51], v[188:191]\n" \
"v_exp_f32_e32 v129, v129\n" \
"v_mfma_f32_16x16x32_bf16 v[148:151], a[68:71], a[32:35], v[180:183]\n" \
"ds_read_b128 a[68:71], v234 offset:17408\n" \
"v_cvt_pk_bf16_f32 v192, v128, v129\n" \
"v_mfma_f32_16x16x32_bf16 v[144:147], a[72:75], a[36:39], v[144:147]\n" \
"v_exp_f32_e32 v130, v130\n" \
"v_mfma_f32_16x16x32_bf16 v[152:155], a[72:75], a[52:55], v[152:155]\n" \
"ds_read_b128 a[72:75], v234 offset:18432\n" \
"v_mfma_f32_16x16x32_bf16 v[156:159], a[76:79], a[52:55], v[156:159]\n" \
"v_exp_f32_e32 v131, v131\n" \
"v_mfma_f32_16x16x32_bf16 v[148:151], a[76:79], a[36:39], v[148:151]\n" \
"ds_read_b128 a[76:79], v234 offset:19456\n" \
"v_cvt_pk_bf16_f32 v193, v130, v131\n" \
"v_mfma_f32_16x16x32_bf16 v[144:147], a[80:83], a[40:43], v[144:147]\n" \
"s_and_b32 s83, s76, 114688\n" \
"v_exp_f32_e32 v132, v132\n" \
"v_mfma_f32_16x16x32_bf16 v[152:155], a[80:83], a[56:59], v[152:155]\n" \
"ds_read_b128 a[80:83], v234 offset:20480\n" \
"v_mfma_f32_16x16x32_bf16 v[156:159], a[84:87], a[56:59], v[156:159]\n" \
"s_lshr_b32 s76, s76, 3\n" \
"v_exp_f32_e32 v133, v133\n" \
"v_mfma_f32_16x16x32_bf16 v[148:151], a[84:87], a[40:43], v[148:151]\n" \
"ds_read_b128 a[84:87], v234 offset:21504\n" \
"v_cvt_pk_bf16_f32 v194, v132, v133\n" \
"v_mfma_f32_16x16x32_bf16 v[144:147], a[88:91], a[44:47], v[144:147]\n" \
"s_xor_b32 s83, %[po], s83\n" \
"v_exp_f32_e32 v134, v134\n" \
"v_mfma_f32_16x16x32_bf16 v[152:155], a[88:91], a[60:63], v[152:155]\n" \
"ds_read_b128 a[88:91], v234 offset:22528\n" \
"buffer_store_dwordx4 a[252:255], v241, %[wsr], s83 offen sc1\n" \
"v_mfma_f32_16x16x32_bf16 v[156:159], a[92:95], a[60:63], v[156:159]\n" \
"v_exp_f32_e32 v135, v135\n" \
"v_mfma_f32_16x16x32_bf16 v[148:151], a[92:95], a[44:47], v[148:151]\n" \
"ds_read_b128 a[92:95], v234 offset:23552\n" \
"v_mfma_f32_16x16x32_bf16 v[64:67], a[96:99], v[200:203], v[64:67]\n" \
"v_exp_f32_e32 v136, v136\n" \
"v_mfma_f32_16x16x32_bf16 v[96:99], a[96:99], v[204:207], v[96:99]\n" \
"ds_read_b128 a[96:99], v234 offset:8192\n" \
"v_cvt_pk_bf16_f32 v195, v134, v135\n" \
"v_mfma_f32_16x16x32_bf16 v[100:103], a[100:103], v[204:207], v[100:103]\n" \
"v_exp_f32_e32 v137, v137\n" \
"v_mfma_f32_16x16x32_bf16 v[68:71], a[100:103], v[200:203], v[68:71]\n" \
"ds_read_b128 a[100:103], v234 offset:9216\n" \
"v_cvt_pk_bf16_f32 v196, v136, v137\n" \
"v_mfma_f32_16x16x32_bf16 v[72:75], a[104:107], v[200:203], v[72:75]\n" \
"v_exp_f32_e32 v138, v138\n" \
"v_mfma_f32_16x16x32_bf16 v[104:107], a[104:107], v[204:207], v[104:107]\n" \
"ds_read_b128 a[104:107], v234 offset:10240\n" \
"v_mfma_f32_16x16x32_bf16 v[108:111], a[108:111], v[204:207], v[108:111]\n" \
"v_exp_f32_e32 v139, v139\n" \
"v_mfma_f32_16x16x32_bf16 v[76:79], a[108:111], v[200:203], v[76:79]\n" \
"ds_read_b128 a[108:111], v234 offset:11264\n" \
"v_cvt_pk_bf16_f32 v197, v138, v139\n" \
"v_mfma_f32_16x16x32_bf16 v[80:83], a[112:115], v[200:203], v[80:83]\n" \
"s_waitcnt lgkmcnt(4)\n" \
"v_exp_f32_e32 v140, v140\n" \
"v_mfma_f32_16x16x32_bf16 v[112:115], a[112:115], v[204:207], v[112:115]\n" \
"ds_read_b128 a[112:115], v234 offset:12288\n" \
"v_mfma_f32_16x16x32_bf16 v[116:119], a[116:119], v[204:207], v[116:119]\n" \
"v_exp_f32_e32 v141, v141\n" \
"v_mfma_f32_16x16x32_bf16 v[84:87], a[116:119], v[200:203], v[84:87]\n" \
"ds_read_b128 a[116:119], v234 offset:13312\n" \
"v_cvt_pk_bf16_f32 v198, v140, v141\n" \
"v_mfma_f32_16x16x32_bf16 v[88:91], a[120:123], v[200:203], v[88:91]\n" \
"v_exp_f32_e32 v142, v142\n" \
"v_mfma_f32_16x16x32_bf16 v[120:123], a[120:123], v[204:207], v[120:123]\n" \
"ds_read_b128 a[120:123], v234 offset:14336\n" \
"v_mfma_f32_16x16x32_bf16 v[124:127], a[124:127], v[204:207], v[124:127]\n" \
"v_exp_f32_e32 v143, v143\n" \
"v_mfma_f32_16x16x32_bf16 v[92:95], a[124:127], v[200:203], v[92:95]\n" \
"ds_read_b128 a[124:127], v234 offset:15360\n" \
"v_cvt_pk_bf16_f32 v199, v142, v143\n" \
"v_smfmac_f32_16x16x64_bf16 a[248:251], a[236:239], v[200:207], v248\n" \
"v_mfma_f32_16x16x32_bf16 v[128:131], a[64:67], a[0:3], v[160:163]\n" \
"v_exp_f32_e32 v144, v144\n" \
"v_mfma_f32_16x16x32_bf16 v[136:139], a[64:67], a[16:19], v[168:171]\n" \
"v_mfma_f32_16x16x32_bf16 v[140:143], a[68:71], a[16:19], v[172:175]\n" \
"s_add_u32 s68, s68, 0x4000\n" \
"v_exp_f32_e32 v145, v145\n" \
"v_mfma_f32_16x16x32_bf16 v[132:135], a[68:71], a[0:3], v[164:167]\n" \
"v_cvt_pk_bf16_f32 v200, v144, v145\n" \
"v_mfma_f32_16x16x32_bf16 v[128:131], a[72:75], a[4:7], v[128:131]\n" \
"s_mov_b32 m0, s69\n" \
"v_exp_f32_e32 v146, v146\n" \
"v_mfma_f32_16x16x32_bf16 v[136:139], a[72:75], a[20:23], v[136:139]\n" \
"v_mfma_f32_16x16x32_bf16 v[140:143], a[76:79], a[20:23], v[140:143]\n" \
"s_xor_b32 s72, s72, 0x10000\n" \
"v_exp_f32_e32 v147, v147\n" \
"v_mfma_f32_16x16x32_bf16 v[132:135], a[76:79], a[4:7], v[132:135]\n" \
"v_cvt_pk_bf16_f32 v201, v146, v147\n" \
"v_mfma_f32_16x16x32_bf16 v[128:131], a[80:83], a[8:11], v[128:131]\n" \
"v_exp_f32_e32 v148, v148\n" \
"v_mfma_f32_16x16x32_bf16 v[136:139], a[80:83], a[24:27], v[136:139]\n" \
"v_mfma_f32_16x16x32_bf16 v[140:143], a[84:87], a[24:27], v[140:143]\n" \
"s_cmp_ge_i32 s74, -2\n" \
"v_exp_f32_e32 v149, v149\n" \
"v_mfma_f32_16x16x32_bf16 v[132:135], a[84:87], a[8:11], v[132:135]\n" \
"v_cvt_pk_bf16_f32 v202, v148, v149\n" \
"v_mfma_f32_16x16x32_bf16 v[128:131], a[88:91], a[12:15], v[128:131]\n" \
"v_exp_f32_e32 v150, v150\n" \
"v_mfma_f32_16x16x32_bf16 v[136:139], a[88:91], a[28:31], v[136:139]\n" \
"s_cbranch_scc1 .LATLmk5\n" \
".LATLmb5:\n" \
"v_mfma_f32_16x16x32_bf16 v[140:143], a[92:95], a[28:31], v[140:143]\n" \
"v_exp_f32_e32 v151, v151\n" \
"v_mfma_f32_16x16x32_bf16 v[132:135], a[92:95], a[12:15], v[132:135]\n" \
"s_waitcnt vmcnt(13) lgkmcnt(0)\n" \
"s_barrier\n" \
"v_mfma_f32_16x16x32_bf16 v[0:3], a[96:99], v[192:195], v[0:3]\n" \
"v_exp_f32_e32 v152, v152\n" \
"v_mfma_f32_16x16x32_bf16 v[32:35], a[96:99], v[196:199], v[32:35]\n" \
"v_cvt_pk_bf16_f32 v203, v150, v151\n" \
"v_mfma_f32_16x16x32_bf16 v[36:39], a[100:103], v[196:199], v[36:39]\n" \
"v_exp_f32_e32 v153, v153\n" \
"v_mfma_f32_16x16x32_bf16 v[4:7], a[100:103], v[192:195], v[4:7]\n" \
"v_cvt_pk_bf16_f32 v204, v152, v153\n" \
"v_mfma_f32_16x16x32_bf16 v[8:11], a[104:107], v[192:195], v[8:11]\n" \
"v_exp_f32_e32 v154, v154\n" \
"v_mfma_f32_16x16x32_bf16 v[40:43], a[104:107], v[196:199], v[40:43]\n" \
"buffer_load_dwordx4 v224, %[kvs], s68 offen offset:0 sc1 lds\n" \
"v_mfma_f32_16x16x32_bf16 v[44:47], a[108:111], v[196:199], v[44:47]\n" \
"v_exp_f32_e32 v155, v155\n" \
"v_mfma_f32_16x16x32_bf16 v[12:15], a[108:111], v[192:195], v[12:15]\n" \
"v_cvt_pk_bf16_f32 v205, v154, v155\n" \
"v_mfma_f32_16x16x32_bf16 v[16:19], a[112:115], v[192:195], v[16:19]\n" \
"v_exp_f32_e32 v156, v156\n" \
"v_mfma_f32_16x16x32_bf16 v[48:51], a[112:115], v[196:199], v[48:51]\n" \
"buffer_load_dwordx4 v224, %[kvs], s68 offen offset:1024 sc1 lds\n" \
"v_mfma_f32_16x16x32_bf16 v[52:55], a[116:119], v[196:199], v[52:55]\n" \
"v_exp_f32_e32 v157, v157\n" \
"v_mfma_f32_16x16x32_bf16 v[20:23], a[116:119], v[192:195], v[20:23]\n" \
"v_cvt_pk_bf16_f32 v206, v156, v157\n" \
"v_mfma_f32_16x16x32_bf16 v[24:27], a[120:123], v[192:195], v[24:27]\n" \
"v_exp_f32_e32 v158, v158\n" \
"v_mfma_f32_16x16x32_bf16 v[56:59], a[120:123], v[196:199], v[56:59]\n" \
"buffer_load_dwordx4 v224, %[kvs], s68 offen offset:2048 sc1 lds\n" \
"v_mfma_f32_16x16x32_bf16 v[60:63], a[124:127], v[196:199], v[60:63]\n" \
"v_exp_f32_e32 v159, v159\n" \
"v_mfma_f32_16x16x32_bf16 v[28:31], a[124:127], v[192:195], v[28:31]\n" \
"v_cvt_pk_bf16_f32 v207, v158, v159\n" \
"v_smfmac_f32_16x16x64_bf16 a[240:243], a[236:239], v[192:199], v248\n" \
"buffer_load_dwordx4 v224, %[kvs], s68 offen offset:3072 sc1 lds\n" \
"v_mfma_f32_16x16x32_bf16 v[144:147], a[64:67], a[32:35], v[176:179]\n" \
"v_exp_f32_e32 v128, v128\n" \
"v_mfma_f32_16x16x32_bf16 v[152:155], a[64:67], a[48:51], v[184:187]\n" \
"ds_read_b128 a[64:67], v234 offset:32768\n" \
"v_mfma_f32_16x16x32_bf16 v[156:159], a[68:71], a[48:51], v[188:191]\n" \
"v_exp_f32_e32 v129, v129\n" \
"v_mfma_f32_16x16x32_bf16 v[148:151], a[68:71], a[32:35], v[180:183]\n" \
"ds_read_b128 a[68:71], v234 offset:33792\n" \
"v_cvt_pk_bf16_f32 v192, v128, v129\n" \
"v_mfma_f32_16x16x32_bf16 v[144:147], a[72:75], a[36:39], v[144:147]\n" \
"s_lshl_b32 s83, s73, 10\n" \
"v_exp_f32_e32 v130, v130\n" \
"v_mfma_f32_16x16x32_bf16 v[152:155], a[72:75], a[52:55], v[152:155]\n" \
"ds_read_b128 a[72:75], v234 offset:34816\n" \
"v_mfma_f32_16x16x32_bf16 v[156:159], a[76:79], a[52:55], v[156:159]\n" \
"s_lshl1_add_u32 s60, %[w4k], s83\n" \
"v_exp_f32_e32 v131, v131\n" \
"v_mfma_f32_16x16x32_bf16 v[148:151], a[76:79], a[36:39], v[148:151]\n" \
"ds_read_b128 a[76:79], v234 offset:35840\n" \
"v_cvt_pk_bf16_f32 v193, v130, v131\n" \
"v_mfma_f32_16x16x32_bf16 v[144:147], a[80:83], a[40:43], v[144:147]\n" \
"s_add_u32 m0, s60, 131072\n" \
"v_exp_f32_e32 v132, v132\n" \
"v_mfma_f32_16x16x32_bf16 v[152:155], a[80:83], a[56:59], v[152:155]\n" \
"ds_read_b128 a[80:83], v234 offset:36864\n" \
"v_mfma_f32_16x16x32_bf16 v[156:159], a[84:87], a[56:59], v[156:159]\n" \
"s_add_u32 s83, s83, s64\n" \
"v_exp_f32_e32 v133, v133\n" \
"v_mfma_f32_16x16x32_bf16 v[148:151], a[84:87], a[40:43], v[148:151]\n" \
"ds_read_b128 a[84:87], v234 offset:37888\n" \
"v_cvt_pk_bf16_f32 v194, v132, v133\n" \
"v_mfma_f32_16x16x32_bf16 v[144:147], a[88:91], a[44:47], v[144:147]\n" \
"s_add_u32 s83, s83, 49152\n" \
"v_exp_f32_e32 v134, v134\n" \
"v_mfma_f32_16x16x32_bf16 v[152:155], a[88:91], a[60:63], v[152:155]\n" \
"ds_read_b128 a[88:91], v234 offset:38912\n" \
"buffer_load_dwordx4 v241, %[wsr], s83 offen sc1 lds\n" \
"v_mfma_f32_16x16x32_bf16 v[156:159], a[92:95], a[60:63], v[156:159]\n" \
"v_exp_f32_e32 v135, v135\n" \
"v_mfma_f32_16x16x32_bf16 v[148:151], a[92:95], a[44:47], v[148:151]\n" \
"ds_read_b128 a[92:95], v234 offset:39936\n" \
"v_mfma_f32_16x16x32_bf16 v[64:67], a[96:99], v[200:203], v[64:67]\n" \
"v_exp_f32_e32 v136, v136\n" \
"v_mfma_f32_16x16x32_bf16 v[96:99], a[96:99], v[204:207], v[96:99]\n" \
"ds_read_b128 a[96:99], v234 offset:24576\n" \
"v_cvt_pk_bf16_f32 v195, v134, v135\n" \
"v_mfma_f32_16x16x32_bf16 v[100:103], a[100:103], v[204:207], v[100:103]\n" \
"v_exp_f32_e32 v137, v137\n" \
"v_mfma_f32_16x16x32_bf16 v[68:71], a[100:103], v[200:203], v[68:71]\n" \
"ds_read_b128 a[100:103], v234 offset:25600\n" \
"v_cvt_pk_bf16_f32 v196, v136, v137\n" \
"v_mfma_f32_16x16x32_bf16 v[72:75], a[104:107], v[200:203], v[72:75]\n" \
"v_exp_f32_e32 v138, v138\n" \
"v_mfma_f32_16x16x32_bf16 v[104:107], a[104:107], v[204:207], v[104:107]\n" \
"ds_read_b128 a[104:107], v234 offset:26624\n" \
"v_mfma_f32_16x16x32_bf16 v[108:111], a[108:111], v[204:207], v[108:111]\n" \
"v_exp_f32_e32 v139, v139\n" \
"v_mfma_f32_16x16x32_bf16 v[76:79], a[108:111], v[200:203], v[76:79]\n" \
"ds_read_b128 a[108:111], v234 offset:27648\n" \
"v_cvt_pk_bf16_f32 v197, v138, v139\n" \
"v_mfma_f32_16x16x32_bf16 v[80:83], a[112:115], v[200:203], v[80:83]\n" \
"s_waitcnt lgkmcnt(4)\n" \
"v_exp_f32_e32 v140, v140\n" \
"v_mfma_f32_16x16x32_bf16 v[112:115], a[112:115], v[204:207], v[112:115]\n" \
"ds_read_b128 a[112:115], v234 offset:28672\n" \
"v_mfma_f32_16x16x32_bf16 v[116:119], a[116:119], v[204:207], v[116:119]\n" \
"v_exp_f32_e32 v141, v141\n" \
"v_mfma_f32_16x16x32_bf16 v[84:87], a[116:119], v[200:203], v[84:87]\n" \
"ds_read_b128 a[116:119], v234 offset:29696\n" \
"v_cvt_pk_bf16_f32 v198, v140, v141\n" \
"v_mfma_f32_16x16x32_bf16 v[88:91], a[120:123], v[200:203], v[88:91]\n" \
"v_exp_f32_e32 v142, v142\n" \
"v_mfma_f32_16x16x32_bf16 v[120:123], a[120:123], v[204:207], v[120:123]\n" \
"ds_read_b128 a[120:123], v234 offset:30720\n" \
"v_mfma_f32_16x16x32_bf16 v[124:127], a[124:127], v[204:207], v[124:127]\n" \
"v_exp_f32_e32 v143, v143\n" \
"v_mfma_f32_16x16x32_bf16 v[92:95], a[124:127], v[200:203], v[92:95]\n" \
"ds_read_b128 a[124:127], v234 offset:31744\n" \
"v_cvt_pk_bf16_f32 v199, v142, v143\n" \
"v_smfmac_f32_16x16x64_bf16 a[248:251], a[236:239], v[200:207], v248\n" \
"v_mfma_f32_16x16x32_bf16 v[128:131], a[64:67], a[0:3], v[160:163]\n" \
"v_exp_f32_e32 v144, v144\n" \
"v_mfma_f32_16x16x32_bf16 v[136:139], a[64:67], a[16:19], v[168:171]\n" \
"v_mfma_f32_16x16x32_bf16 v[140:143], a[68:71], a[16:19], v[172:175]\n" \
"s_add_u32 s68, s68, 0x4000\n" \
"v_exp_f32_e32 v145, v145\n" \
"v_mfma_f32_16x16x32_bf16 v[132:135], a[68:71], a[0:3], v[164:167]\n" \
"v_cvt_pk_bf16_f32 v200, v144, v145\n" \
"v_mfma_f32_16x16x32_bf16 v[128:131], a[72:75], a[4:7], v[128:131]\n" \
"s_mov_b32 m0, s70\n" \
"v_exp_f32_e32 v146, v146\n" \
"v_mfma_f32_16x16x32_bf16 v[136:139], a[72:75], a[20:23], v[136:139]\n" \
"buffer_load_dwordx4 v224, %[kvs], s68 offen offset:0 sc1 lds\n" \
"v_mfma_f32_16x16x32_bf16 v[140:143], a[76:79], a[20:23], v[140:143]\n" \
"s_xor_b32 s69, s69, 0x10000\n" \
"v_exp_f32_e32 v147, v147\n" \
"v_mfma_f32_16x16x32_bf16 v[132:135], a[76:79], a[4:7], v[132:135]\n" \
"v_cvt_pk_bf16_f32 v201, v146, v147\n" \
"v_mfma_f32_16x16x32_bf16 v[128:131], a[80:83], a[8:11], v[128:131]\n" \
"v_exp_f32_e32 v148, v148\n" \
"v_mfma_f32_16x16x32_bf16 v[136:139], a[80:83], a[24:27], v[136:139]\n" \
"buffer_load_dwordx4 v224, %[kvs], s68 offen offset:1024 sc1 lds\n" \
"v_mfma_f32_16x16x32_bf16 v[140:143], a[84:87], a[24:27], v[140:143]\n" \
"s_cmp_ge_i32 s74, -3\n" \
"v_exp_f32_e32 v149, v149\n" \
"v_mfma_f32_16x16x32_bf16 v[132:135], a[84:87], a[8:11], v[132:135]\n" \
"v_cvt_pk_bf16_f32 v202, v148, v149\n" \
"v_mfma_f32_16x16x32_bf16 v[128:131], a[88:91], a[12:15], v[128:131]\n" \
"v_exp_f32_e32 v150, v150\n" \
"v_mfma_f32_16x16x32_bf16 v[136:139], a[88:91], a[28:31], v[136:139]\n" \
"s_cbranch_scc1 .LATLmk6\n" \
".LATLmb6:\n" \
"v_mfma_f32_16x16x32_bf16 v[140:143], a[92:95], a[28:31], v[140:143]\n" \
"v_exp_f32_e32 v151, v151\n" \
"v_mfma_f32_16x16x32_bf16 v[132:135], a[92:95], a[12:15], v[132:135]\n" \
"s_waitcnt lgkmcnt(0)\n" \
"v_mfma_f32_16x16x32_bf16 v[0:3], a[96:99], v[192:195], v[0:3]\n" \
"v_exp_f32_e32 v152, v152\n" \
"v_mfma_f32_16x16x32_bf16 v[32:35], a[96:99], v[196:199], v[32:35]\n" \
"v_cvt_pk_bf16_f32 v203, v150, v151\n" \
"v_mfma_f32_16x16x32_bf16 v[36:39], a[100:103], v[196:199], v[36:39]\n" \
"v_exp_f32_e32 v153, v153\n" \
"v_mfma_f32_16x16x32_bf16 v[4:7], a[100:103], v[192:195], v[4:7]\n" \
"v_cvt_pk_bf16_f32 v204, v152, v153\n" \
"v_mfma_f32_16x16x32_bf16 v[8:11], a[104:107], v[192:195], v[8:11]\n" \
"v_exp_f32_e32 v154, v154\n" \
"v_mfma_f32_16x16x32_bf16 v[40:43], a[104:107], v[196:199], v[40:43]\n" \
"buffer_load_dwordx4 v224, %[kvs], s68 offen offset:2048 sc1 lds\n" \
"v_mfma_f32_16x16x32_bf16 v[44:47], a[108:111], v[196:199], v[44:47]\n" \
"v_exp_f32_e32 v155, v155\n" \
"v_mfma_f32_16x16x32_bf16 v[12:15], a[108:111], v[192:195], v[12:15]\n" \
"v_cvt_pk_bf16_f32 v205, v154, v155\n" \
"v_mfma_f32_16x16x32_bf16 v[16:19], a[112:115], v[192:195], v[16:19]\n" \
"v_exp_f32_e32 v156, v156\n" \
"v_mfma_f32_16x16x32_bf16 v[48:51], a[112:115], v[196:199], v[48:51]\n" \
"v_mfma_f32_16x16x32_bf16 v[52:55], a[116:119], v[196:199], v[52:55]\n" \
"v_exp_f32_e32 v157, v157\n" \
"v_mfma_f32_16x16x32_bf16 v[20:23], a[116:119], v[192:195], v[20:23]\n" \
"v_cvt_pk_bf16_f32 v206, v156, v157\n" \
"v_mfma_f32_16x16x32_bf16 v[24:27], a[120:123], v[192:195], v[24:27]\n" \
"v_exp_f32_e32 v158, v158\n" \
"v_mfma_f32_16x16x32_bf16 v[56:59], a[120:123], v[196:199], v[56:59]\n" \
"buffer_load_dwordx4 v224, %[kvs], s68 offen offset:3072 sc1 lds\n" \
"v_mfma_f32_16x16x32_bf16 v[60:63], a[124:127], v[196:199], v[60:63]\n" \
"v_exp_f32_e32 v159, v159\n" \
"v_mfma_f32_16x16x32_bf16 v[28:31], a[124:127], v[192:195], v[28:31]\n" \
"v_cvt_pk_bf16_f32 v207, v158, v159\n" \
"v_smfmac_f32_16x16x64_bf16 a[240:243], a[236:239], v[192:199], v248\n" \
"v_mfma_f32_16x16x32_bf16 v[144:147], a[64:67], a[32:35], v[176:179]\n" \
"v_exp_f32_e32 v128, v128\n" \
"v_mfma_f32_16x16x32_bf16 v[152:155], a[64:67], a[48:51], v[184:187]\n" \
"ds_read_b128 a[64:67], v234 offset:49152\n" \
"v_mfma_f32_16x16x32_bf16 v[156:159], a[68:71], a[48:51], v[188:191]\n" \
"v_exp_f32_e32 v129, v129\n" \
"v_mfma_f32_16x16x32_bf16 v[148:151], a[68:71], a[32:35], v[180:183]\n" \
"ds_read_b128 a[68:71], v234 offset:50176\n" \
"v_cvt_pk_bf16_f32 v192, v128, v129\n" \
"v_mfma_f32_16x16x32_bf16 v[144:147], a[72:75], a[36:39], v[144:147]\n" \
"v_exp_f32_e32 v130, v130\n" \
"v_mfma_f32_16x16x32_bf16 v[152:155], a[72:75], a[52:55], v[152:155]\n" \
"ds_read_b128 a[72:75], v234 offset:51200\n" \
"v_mfma_f32_16x16x32_bf16 v[156:159], a[76:79], a[52:55], v[156:159]\n" \
"v_exp_f32_e32 v131, v131\n" \
"v_mfma_f32_16x16x32_bf16 v[148:151], a[76:79], a[36:39], v[148:151]\n" \
"ds_read_b128 a[76:79], v234 offset:52224\n" \
"v_cvt_pk_bf16_f32 v193, v130, v131\n" \
"v_mfma_f32_16x16x32_bf16 v[144:147], a[80:83], a[40:43], v[144:147]\n" \
"v_exp_f32_e32 v132, v132\n" \
"v_mfma_f32_16x16x32_bf16 v[152:155], a[80:83], a[56:59], v[152:155]\n" \
"ds_read_b128 a[80:83], v234 offset:53248\n" \
"v_mfma_f32_16x16x32_bf16 v[156:159], a[84:87], a[56:59], v[156:159]\n" \
"v_exp_f32_e32 v133, v133\n" \
"v_mfma_f32_16x16x32_bf16 v[148:151], a[84:87], a[40:43], v[148:151]\n" \
"ds_read_b128 a[84:87], v234 offset:54272\n" \
"v_cvt_pk_bf16_f32 v194, v132, v133\n" \
"v_mfma_f32_16x16x32_bf16 v[144:147], a[88:91], a[44:47], v[144:147]\n" \
"v_exp_f32_e32 v134, v134\n" \
"v_mfma_f32_16x16x32_bf16 v[152:155], a[88:91], a[60:63], v[152:155]\n" \
"ds_read_b128 a[88:91], v234 offset:55296\n" \
"v_mfma_f32_16x16x32_bf16 v[156:159], a[92:95], a[60:63], v[156:159]\n" \
"v_exp_f32_e32 v135, v135\n" \
"v_mfma_f32_16x16x32_bf16 v[148:151], a[92:95], a[44:47], v[148:151]\n" \
"ds_read_b128 a[92:95], v234 offset:56320\n" \
"v_mfma_f32_16x16x32_bf16 v[64:67], a[96:99], v[200:203], v[64:67]\n" \
"v_exp_f32_e32 v136, v136\n" \
"v_mfma_f32_16x16x32_bf16 v[96:99], a[96:99], v[204:207], v[96:99]\n" \
"ds_read_b128 a[96:99], v234 offset:40960\n" \
"v_cvt_pk_bf16_f32 v195, v134, v135\n" \
"v_mfma_f32_16x16x32_bf16 v[100:103], a[100:103], v[204:207], v[100:103]\n" \
"v_exp_f32_e32 v137, v137\n" \
"v_mfma_f32_16x16x32_bf16 v[68:71], a[100:103], v[200:203], v[68:71]\n" \
"ds_read_b128 a[100:103], v234 offset:41984\n" \
"v_cvt_pk_bf16_f32 v196, v136, v137\n" \
"v_mfma_f32_16x16x32_bf16 v[72:75], a[104:107], v[200:203], v[72:75]\n" \
"v_exp_f32_e32 v138, v138\n" \
"v_mfma_f32_16x16x32_bf16 v[104:107], a[104:107], v[204:207], v[104:107]\n" \
"ds_read_b128 a[104:107], v234 offset:43008\n" \
"v_mfma_f32_16x16x32_bf16 v[108:111], a[108:111], v[204:207], v[108:111]\n" \
"v_exp_f32_e32 v139, v139\n" \
"v_mfma_f32_16x16x32_bf16 v[76:79], a[108:111], v[200:203], v[76:79]\n" \
"ds_read_b128 a[108:111], v234 offset:44032\n" \
"v_cvt_pk_bf16_f32 v197, v138, v139\n" \
"v_mfma_f32_16x16x32_bf16 v[80:83], a[112:115], v[200:203], v[80:83]\n" \
"s_waitcnt lgkmcnt(4)\n" \
"v_exp_f32_e32 v140, v140\n" \
"v_mfma_f32_16x16x32_bf16 v[112:115], a[112:115], v[204:207], v[112:115]\n" \
"ds_read_b128 a[112:115], v234 offset:45056\n" \
"v_mfma_f32_16x16x32_bf16 v[116:119], a[116:119], v[204:207], v[116:119]\n" \
"v_exp_f32_e32 v141, v141\n" \
"v_mfma_f32_16x16x32_bf16 v[84:87], a[116:119], v[200:203], v[84:87]\n" \
"ds_read_b128 a[116:119], v234 offset:46080\n" \
"v_cvt_pk_bf16_f32 v198, v140, v141\n" \
"v_mfma_f32_16x16x32_bf16 v[88:91], a[120:123], v[200:203], v[88:91]\n" \
"v_exp_f32_e32 v142, v142\n" \
"v_mfma_f32_16x16x32_bf16 v[120:123], a[120:123], v[204:207], v[120:123]\n" \
"ds_read_b128 a[120:123], v234 offset:47104\n" \
"v_mfma_f32_16x16x32_bf16 v[124:127], a[124:127], v[204:207], v[124:127]\n" \
"v_exp_f32_e32 v143, v143\n" \
"v_mfma_f32_16x16x32_bf16 v[92:95], a[124:127], v[200:203], v[92:95]\n" \
"ds_read_b128 a[124:127], v234 offset:48128\n" \
"v_cvt_pk_bf16_f32 v199, v142, v143\n" \
"v_smfmac_f32_16x16x64_bf16 a[248:251], a[236:239], v[200:207], v248\n" \
"v_mfma_f32_16x16x32_bf16 v[128:131], a[64:67], a[0:3], v[160:163]\n" \
"v_exp_f32_e32 v144, v144\n" \
"v_mfma_f32_16x16x32_bf16 v[136:139], a[64:67], a[16:19], v[168:171]\n" \
"v_mfma_f32_16x16x32_bf16 v[140:143], a[68:71], a[16:19], v[172:175]\n" \
"s_add_u32 s68, s68, 0x4000\n" \
"v_exp_f32_e32 v145, v145\n" \
"v_mfma_f32_16x16x32_bf16 v[132:135], a[68:71], a[0:3], v[164:167]\n" \
"v_cvt_pk_bf16_f32 v200, v144, v145\n" \
"v_mfma_f32_16x16x32_bf16 v[128:131], a[72:75], a[4:7], v[128:131]\n" \
"s_mov_b32 m0, s71\n" \
"v_exp_f32_e32 v146, v146\n" \
"v_mfma_f32_16x16x32_bf16 v[136:139], a[72:75], a[20:23], v[136:139]\n" \
"v_mfma_f32_16x16x32_bf16 v[140:143], a[76:79], a[20:23], v[140:143]\n" \
"s_xor_b32 s70, s70, 0x10000\n" \
"v_exp_f32_e32 v147, v147\n" \
"v_mfma_f32_16x16x32_bf16 v[132:135], a[76:79], a[4:7], v[132:135]\n" \
"v_cvt_pk_bf16_f32 v201, v146, v147\n" \
"v_mfma_f32_16x16x32_bf16 v[128:131], a[80:83], a[8:11], v[128:131]\n" \
"v_exp_f32_e32 v148, v148\n" \
"v_mfma_f32_16x16x32_bf16 v[136:139], a[80:83], a[24:27], v[136:139]\n" \
"v_mfma_f32_16x16x32_bf16 v[140:143], a[84:87], a[24:27], v[140:143]\n" \
"s_cmp_eq_i32 s74, -4\n" \
"v_exp_f32_e32 v149, v149\n" \
"v_mfma_f32_16x16x32_bf16 v[132:135], a[84:87], a[8:11], v[132:135]\n" \
"v_cvt_pk_bf16_f32 v202, v148, v149\n" \
"v_mfma_f32_16x16x32_bf16 v[128:131], a[88:91], a[12:15], v[128:131]\n" \
"v_exp_f32_e32 v150, v150\n" \
"v_mfma_f32_16x16x32_bf16 v[136:139], a[88:91], a[28:31], v[136:139]\n" \
"s_cbranch_scc1 .LATLmk7\n" \
".LATLmb7:\n" \
"v_mfma_f32_16x16x32_bf16 v[140:143], a[92:95], a[28:31], v[140:143]\n" \
"v_exp_f32_e32 v151, v151\n" \
"v_mfma_f32_16x16x32_bf16 v[132:135], a[92:95], a[12:15], v[132:135]\n" \
"s_waitcnt vmcnt(13) lgkmcnt(0)\n" \
"s_barrier\n" \
"v_mfma_f32_16x16x32_bf16 v[0:3], a[96:99], v[192:195], v[0:3]\n" \
"v_exp_f32_e32 v152, v152\n" \
"v_mfma_f32_16x16x32_bf16 v[32:35], a[96:99], v[196:199], v[32:35]\n" \
"v_cvt_pk_bf16_f32 v203, v150, v151\n" \
"v_mfma_f32_16x16x32_bf16 v[36:39], a[100:103], v[196:199], v[36:39]\n" \
"v_exp_f32_e32 v153, v153\n" \
"v_mfma_f32_16x16x32_bf16 v[4:7], a[100:103], v[192:195], v[4:7]\n" \
"v_cvt_pk_bf16_f32 v204, v152, v153\n" \
"v_mfma_f32_16x16x32_bf16 v[8:11], a[104:107], v[192:195], v[8:11]\n" \
"v_exp_f32_e32 v154, v154\n" \
"v_mfma_f32_16x16x32_bf16 v[40:43], a[104:107], v[196:199], v[40:43]\n" \
"buffer_load_dwordx4 v224, %[kvs], s68 offen offset:0 sc1 lds\n" \
"v_mfma_f32_16x16x32_bf16 v[44:47], a[108:111], v[196:199], v[44:47]\n" \
"v_exp_f32_e32 v155, v155\n" \
"v_mfma_f32_16x16x32_bf16 v[12:15], a[108:111], v[192:195], v[12:15]\n" \
"v_cvt_pk_bf16_f32 v205, v154, v155\n" \
"v_mfma_f32_16x16x32_bf16 v[16:19], a[112:115], v[192:195], v[16:19]\n" \
"v_exp_f32_e32 v156, v156\n" \
"v_mfma_f32_16x16x32_bf16 v[48:51], a[112:115], v[196:199], v[48:51]\n" \
"buffer_load_dwordx4 v224, %[kvs], s68 offen offset:1024 sc1 lds\n" \
"v_mfma_f32_16x16x32_bf16 v[52:55], a[116:119], v[196:199], v[52:55]\n" \
"v_exp_f32_e32 v157, v157\n" \
"v_mfma_f32_16x16x32_bf16 v[20:23], a[116:119], v[192:195], v[20:23]\n" \
"v_cvt_pk_bf16_f32 v206, v156, v157\n" \
"v_mfma_f32_16x16x32_bf16 v[24:27], a[120:123], v[192:195], v[24:27]\n" \
"v_exp_f32_e32 v158, v158\n" \
"v_mfma_f32_16x16x32_bf16 v[56:59], a[120:123], v[196:199], v[56:59]\n" \
"buffer_load_dwordx4 v224, %[kvs], s68 offen offset:2048 sc1 lds\n" \
"v_mfma_f32_16x16x32_bf16 v[60:63], a[124:127], v[196:199], v[60:63]\n" \
"v_exp_f32_e32 v159, v159\n" \
"v_mfma_f32_16x16x32_bf16 v[28:31], a[124:127], v[192:195], v[28:31]\n" \
"v_cvt_pk_bf16_f32 v207, v158, v159\n" \
"v_smfmac_f32_16x16x64_bf16 a[240:243], a[236:239], v[192:199], v248\n" \
"buffer_load_dwordx4 v224, %[kvs], s68 offen offset:3072 sc1 lds\n" \
"v_mfma_f32_16x16x32_bf16 v[144:147], a[64:67], a[32:35], v[176:179]\n" \
"v_exp_f32_e32 v128, v128\n" \
"v_mfma_f32_16x16x32_bf16 v[152:155], a[64:67], a[48:51], v[184:187]\n" \
"ds_read_b128 a[64:67], v235 offset:0\n" \
"v_mfma_f32_16x16x32_bf16 v[156:159], a[68:71], a[48:51], v[188:191]\n" \
"v_exp_f32_e32 v129, v129\n" \
"v_mfma_f32_16x16x32_bf16 v[148:151], a[68:71], a[32:35], v[180:183]\n" \
"ds_read_b128 a[68:71], v235 offset:1024\n" \
"v_cvt_pk_bf16_f32 v192, v128, v129\n" \
"v_mfma_f32_16x16x32_bf16 v[144:147], a[72:75], a[36:39], v[144:147]\n" \
"v_exp_f32_e32 v130, v130\n" \
"v_mfma_f32_16x16x32_bf16 v[152:155], a[72:75], a[52:55], v[152:155]\n" \
"ds_read_b128 a[72:75], v235 offset:2048\n" \
"v_mfma_f32_16x16x32_bf16 v[156:159], a[76:79], a[52:55], v[156:159]\n" \
"v_exp_f32_e32 v131, v131\n" \
"v_mfma_f32_16x16x32_bf16 v[148:151], a[76:79], a[36:39], v[148:151]\n" \
"ds_read_b128 a[76:79], v235 offset:3072\n" \
"v_cvt_pk_bf16_f32 v193, v130, v131\n" \
"v_mfma_f32_16x16x32_bf16 v[144:147], a[80:83], a[40:43], v[144:147]\n" \
"v_exp_f32_e32 v132, v132\n" \
"v_mfma_f32_16x16x32_bf16 v[152:155], a[80:83], a[56:59], v[152:155]\n" \
"ds_read_b128 a[80:83], v235 offset:4096\n" \
"v_mfma_f32_16x16x32_bf16 v[156:159], a[84:87], a[56:59], v[156:159]\n" \
"v_exp_f32_e32 v133, v133\n" \
"v_mfma_f32_16x16x32_bf16 v[148:151], a[84:87], a[40:43], v[148:151]\n" \
"ds_read_b128 a[84:87], v235 offset:5120\n" \
"v_cvt_pk_bf16_f32 v194, v132, v133\n" \
"v_mfma_f32_16x16x32_bf16 v[144:147], a[88:91], a[44:47], v[144:147]\n" \
"v_exp_f32_e32 v134, v134\n" \
"v_mfma_f32_16x16x32_bf16 v[152:155], a[88:91], a[60:63], v[152:155]\n" \
"ds_read_b128 a[88:91], v235 offset:6144\n" \
"v_mfma_f32_16x16x32_bf16 v[156:159], a[92:95], a[60:63], v[156:159]\n" \
"v_exp_f32_e32 v135, v135\n" \
"v_mfma_f32_16x16x32_bf16 v[148:151], a[92:95], a[44:47], v[148:151]\n" \
"ds_read_b128 a[92:95], v235 offset:7168\n" \
"v_mfma_f32_16x16x32_bf16 v[64:67], a[96:99], v[200:203], v[64:67]\n" \
"v_exp_f32_e32 v136, v136\n" \
"v_mfma_f32_16x16x32_bf16 v[96:99], a[96:99], v[204:207], v[96:99]\n" \
"ds_read_b128 a[96:99], v234 offset:57344\n" \
"v_cvt_pk_bf16_f32 v195, v134, v135\n" \
"v_mfma_f32_16x16x32_bf16 v[100:103], a[100:103], v[204:207], v[100:103]\n" \
"v_exp_f32_e32 v137, v137\n" \
"v_mfma_f32_16x16x32_bf16 v[68:71], a[100:103], v[200:203], v[68:71]\n" \
"ds_read_b128 a[100:103], v234 offset:58368\n" \
"v_cvt_pk_bf16_f32 v196, v136, v137\n" \
"v_mfma_f32_16x16x32_bf16 v[72:75], a[104:107], v[200:203], v[72:75]\n" \
"v_exp_f32_e32 v138, v138\n" \
"v_mfma_f32_16x16x32_bf16 v[104:107], a[104:107], v[204:207], v[104:107]\n" \
"ds_read_b128 a[104:107], v234 offset:59392\n" \
"v_xor_b32_e32 v235, 0x10000, v235\n" \
"v_mfma_f32_16x16x32_bf16 v[108:111], a[108:111], v[204:207], v[108:111]\n" \
"v_exp_f32_e32 v139, v139\n" \
"v_mfma_f32_16x16x32_bf16 v[76:79], a[108:111], v[200:203], v[76:79]\n" \
"ds_read_b128 a[108:111], v234 offset:60416\n" \
"v_cvt_pk_bf16_f32 v197, v138, v139\n" \
"v_mfma_f32_16x16x32_bf16 v[80:83], a[112:115], v[200:203], v[80:83]\n" \
"s_waitcnt lgkmcnt(4)\n" \
"v_exp_f32_e32 v140, v140\n" \
"v_mfma_f32_16x16x32_bf16 v[112:115], a[112:115], v[204:207], v[112:115]\n" \
"ds_read_b128 a[112:115], v234 offset:61440\n" \
"v_mfma_f32_16x16x32_bf16 v[116:119], a[116:119], v[204:207], v[116:119]\n" \
"v_exp_f32_e32 v141, v141\n" \
"v_mfma_f32_16x16x32_bf16 v[84:87], a[116:119], v[200:203], v[84:87]\n" \
"ds_read_b128 a[116:119], v234 offset:62464\n" \
"v_cvt_pk_bf16_f32 v198, v140, v141\n" \
"v_mfma_f32_16x16x32_bf16 v[88:91], a[120:123], v[200:203], v[88:91]\n" \
"s_sub_u32 s73, s73, 1\n" \
"v_exp_f32_e32 v142, v142\n" \
"v_mfma_f32_16x16x32_bf16 v[120:123], a[120:123], v[204:207], v[120:123]\n" \
"ds_read_b128 a[120:123], v234 offset:63488\n" \
"v_mfma_f32_16x16x32_bf16 v[124:127], a[124:127], v[204:207], v[124:127]\n" \
"v_exp_f32_e32 v143, v143\n" \
"v_mfma_f32_16x16x32_bf16 v[92:95], a[124:127], v[200:203], v[92:95]\n" \
"ds_read_b128 a[124:127], v234 offset:64512\n" \
"v_cvt_pk_bf16_f32 v199, v142, v143\n" \
"v_smfmac_f32_16x16x64_bf16 a[248:251], a[236:239], v[200:207], v248\n" \
"s_cbranch_scc1 .LATLtx\n" \
"v_xor_b32_e32 v234, 0x10000, v234\n" \
"v_mfma_f32_16x16x32_bf16 v[128:131], a[64:67], a[0:3], v[160:163]\n" \
"v_exp_f32_e32 v144, v144\n" \
"v_mfma_f32_16x16x32_bf16 v[136:139], a[64:67], a[16:19], v[168:171]\n" \
"v_mfma_f32_16x16x32_bf16 v[140:143], a[68:71], a[16:19], v[172:175]\n" \
"s_add_u32 s68, s68, 0x4000\n" \
"v_exp_f32_e32 v145, v145\n" \
"v_mfma_f32_16x16x32_bf16 v[132:135], a[68:71], a[0:3], v[164:167]\n" \
"v_cvt_pk_bf16_f32 v200, v144, v145\n" \
"v_mfma_f32_16x16x32_bf16 v[128:131], a[72:75], a[4:7], v[128:131]\n" \
"s_mov_b32 m0, s72\n" \
"v_exp_f32_e32 v146, v146\n" \
"v_mfma_f32_16x16x32_bf16 v[136:139], a[72:75], a[20:23], v[136:139]\n" \
"buffer_load_dwordx4 v224, %[kvs], s68 offen offset:0 sc1 lds\n" \
"v_mfma_f32_16x16x32_bf16 v[140:143], a[76:79], a[20:23], v[140:143]\n" \
"s_xor_b32 s71, s71, 0x10000\n" \
"v_exp_f32_e32 v147, v147\n" \
"v_mfma_f32_16x16x32_bf16 v[132:135], a[76:79], a[4:7], v[132:135]\n" \
"v_cvt_pk_bf16_f32 v201, v146, v147\n" \
"v_mfma_f32_16x16x32_bf16 v[128:131], a[80:83], a[8:11], v[128:131]\n" \
"s_add_i32 s74, s74, 4\n" \
"v_exp_f32_e32 v148, v148\n" \
"v_mfma_f32_16x16x32_bf16 v[136:139], a[80:83], a[24:27], v[136:139]\n" \
"buffer_load_dwordx4 v224, %[kvs], s68 offen offset:1024 sc1 lds\n" \
"v_mfma_f32_16x16x32_bf16 v[140:143], a[84:87], a[24:27], v[140:143]\n" \
"s_cmp_ge_i32 s74, -1\n" \
"v_exp_f32_e32 v149, v149\n" \
"v_mfma_f32_16x16x32_bf16 v[132:135], a[84:87], a[8:11], v[132:135]\n" \
"v_cvt_pk_bf16_f32 v202, v148, v149\n" \
"v_mfma_f32_16x16x32_bf16 v[128:131], a[88:91], a[12:15], v[128:131]\n" \
"v_exp_f32_e32 v150, v150\n" \
"v_mfma_f32_16x16x32_bf16 v[136:139], a[88:91], a[28:31], v[136:139]\n" \
"s_cbranch_scc1 .LATLmk8\n" \
".LATLmb8:\n" \
"v_mfma_f32_16x16x32_bf16 v[140:143], a[92:95], a[28:31], v[140:143]\n" \
"v_exp_f32_e32 v151, v151\n" \
"v_mfma_f32_16x16x32_bf16 v[132:135], a[92:95], a[12:15], v[132:135]\n" \
"s_waitcnt lgkmcnt(0)\n" \
"v_mfma_f32_16x16x32_bf16 v[0:3], a[96:99], v[192:195], v[0:3]\n" \
"v_exp_f32_e32 v152, v152\n" \
"v_mfma_f32_16x16x32_bf16 v[32:35], a[96:99], v[196:199], v[32:35]\n" \
"v_cvt_pk_bf16_f32 v203, v150, v151\n" \
"v_mfma_f32_16x16x32_bf16 v[36:39], a[100:103], v[196:199], v[36:39]\n" \
"v_exp_f32_e32 v153, v153\n" \
"v_mfma_f32_16x16x32_bf16 v[4:7], a[100:103], v[192:195], v[4:7]\n" \
"v_cvt_pk_bf16_f32 v204, v152, v153\n" \
"v_mfma_f32_16x16x32_bf16 v[8:11], a[104:107], v[192:195], v[8:11]\n" \
"v_exp_f32_e32 v154, v154\n" \
"v_mfma_f32_16x16x32_bf16 v[40:43], a[104:107], v[196:199], v[40:43]\n" \
"buffer_load_dwordx4 v224, %[kvs], s68 offen offset:2048 sc1 lds\n" \
"v_mfma_f32_16x16x32_bf16 v[44:47], a[108:111], v[196:199], v[44:47]\n" \
"v_exp_f32_e32 v155, v155\n" \
"v_mfma_f32_16x16x32_bf16 v[12:15], a[108:111], v[192:195], v[12:15]\n" \
"v_cvt_pk_bf16_f32 v205, v154, v155\n" \
"v_mfma_f32_16x16x32_bf16 v[16:19], a[112:115], v[192:195], v[16:19]\n" \
"v_exp_f32_e32 v156, v156\n" \
"v_mfma_f32_16x16x32_bf16 v[48:51], a[112:115], v[196:199], v[48:51]\n" \
"v_mfma_f32_16x16x32_bf16 v[52:55], a[116:119], v[196:199], v[52:55]\n" \
"v_exp_f32_e32 v157, v157\n" \
"v_mfma_f32_16x16x32_bf16 v[20:23], a[116:119], v[192:195], v[20:23]\n" \
"v_cvt_pk_bf16_f32 v206, v156, v157\n" \
"v_mfma_f32_16x16x32_bf16 v[24:27], a[120:123], v[192:195], v[24:27]\n" \
"v_exp_f32_e32 v158, v158\n" \
"v_mfma_f32_16x16x32_bf16 v[56:59], a[120:123], v[196:199], v[56:59]\n" \
"buffer_load_dwordx4 v224, %[kvs], s68 offen offset:3072 sc1 lds\n" \
"v_mfma_f32_16x16x32_bf16 v[60:63], a[124:127], v[196:199], v[60:63]\n" \
"v_exp_f32_e32 v159, v159\n" \
"v_mfma_f32_16x16x32_bf16 v[28:31], a[124:127], v[192:195], v[28:31]\n" \
"v_cvt_pk_bf16_f32 v207, v158, v159\n" \
"v_smfmac_f32_16x16x64_bf16 a[240:243], a[236:239], v[192:199], v248\n" \
"s_branch .LATLp1\n" \
".LATLtx:\n" \
"v_xor_b32_e32 v234, 0x10000, v234\n" \
".LATLtr:\n" \
"s_cmp_eq_i32 %[db], 32\n" \
"s_cbranch_scc1 .LATLmk9\n" \
".LATLmb9:\n" \
"s_waitcnt lgkmcnt(8)\n" \
"v_mfma_f32_16x16x32_bf16 v[128:131], a[64:67], a[32:35], v[176:179]\n" \
"v_exp_f32_e32 v144, v144\n" \
"v_mfma_f32_16x16x32_bf16 v[136:139], a[64:67], a[48:51], v[184:187]\n" \
"ds_read_b128 a[64:67], v234 offset:16384\n" \
"s_add_u32 s68, s68, 0x4000\n" \
"v_mfma_f32_16x16x32_bf16 v[140:143], a[68:71], a[48:51], v[188:191]\n" \
"v_exp_f32_e32 v145, v145\n" \
"v_mfma_f32_16x16x32_bf16 v[132:135], a[68:71], a[32:35], v[180:183]\n" \
"ds_read_b128 a[68:71], v234 offset:17408\n" \
"v_cvt_pk_bf16_f32 v200, v144, v145\n" \
"v_mfma_f32_16x16x32_bf16 v[128:131], a[72:75], a[36:39], v[128:131]\n" \
"v_exp_f32_e32 v146, v146\n" \
"v_mfma_f32_16x16x32_bf16 v[136:139], a[72:75], a[52:55], v[136:139]\n" \
"ds_read_b128 a[72:75], v234 offset:18432\n" \
"v_mfma_f32_16x16x32_bf16 v[140:143], a[76:79], a[52:55], v[140:143]\n" \
"v_exp_f32_e32 v147, v147\n" \
"v_mfma_f32_16x16x32_bf16 v[132:135], a[76:79], a[36:39], v[132:135]\n" \
"ds_read_b128 a[76:79], v234 offset:19456\n" \
"v_cvt_pk_bf16_f32 v201, v146, v147\n" \
"v_mfma_f32_16x16x32_bf16 v[128:131], a[80:83], a[40:43], v[128:131]\n" \
"v_exp_f32_e32 v148, v148\n" \
"v_mfma_f32_16x16x32_bf16 v[136:139], a[80:83], a[56:59], v[136:139]\n" \
"s_xor_b32 s71, s71, 0x10000\n" \
"ds_read_b128 a[80:83], v234 offset:20480\n" \
"v_mfma_f32_16x16x32_bf16 v[140:143], a[84:87], a[56:59], v[140:143]\n" \
"v_exp_f32_e32 v149, v149\n" \
"v_mfma_f32_16x16x32_bf16 v[132:135], a[84:87], a[40:43], v[132:135]\n" \
"ds_read_b128 a[84:87], v234 offset:21504\n" \
"v_cvt_pk_bf16_f32 v202, v148, v149\n" \
"v_mfma_f32_16x16x32_bf16 v[128:131], a[88:91], a[44:47], v[128:131]\n" \
"v_exp_f32_e32 v150, v150\n" \
"v_mfma_f32_16x16x32_bf16 v[136:139], a[88:91], a[60:63], v[136:139]\n" \
"s_mov_b32 m0, s72\n" \
"ds_read_b128 a[88:91], v234 offset:22528\n" \
"v_mfma_f32_16x16x32_bf16 v[140:143], a[92:95], a[60:63], v[140:143]\n" \
"v_exp_f32_e32 v151, v151\n" \
"v_mfma_f32_16x16x32_bf16 v[132:135], a[92:95], a[44:47], v[132:135]\n" \
"ds_read_b128 a[92:95], v234 offset:23552\n" \
"s_waitcnt vmcnt(12) lgkmcnt(8)\n" \
"s_barrier\n" \
"v_mfma_f32_16x16x32_bf16 v[0:3], a[96:99], v[192:195], v[0:3]\n" \
"v_exp_f32_e32 v152, v152\n" \
"v_mfma_f32_16x16x32_bf16 v[32:35], a[96:99], v[196:199], v[32:35]\n" \
"v_cvt_pk_bf16_f32 v203, v150, v151\n" \
"v_mfma_f32_16x16x32_bf16 v[36:39], a[100:103], v[196:199], v[36:39]\n" \
"v_exp_f32_e32 v153, v153\n" \
"v_mfma_f32_16x16x32_bf16 v[4:7], a[100:103], v[192:195], v[4:7]\n" \
"v_cvt_pk_bf16_f32 v204, v152, v153\n" \
"v_mfma_f32_16x16x32_bf16 v[8:11], a[104:107], v[192:195], v[8:11]\n" \
"v_exp_f32_e32 v154, v154\n" \
"v_mfma_f32_16x16x32_bf16 v[40:43], a[104:107], v[196:199], v[40:43]\n" \
"buffer_load_dwordx4 v224, %[kvs], s68 offen offset:0 sc1 lds\n" \
"v_mfma_f32_16x16x32_bf16 v[44:47], a[108:111], v[196:199], v[44:47]\n" \
"v_exp_f32_e32 v155, v155\n" \
"v_mfma_f32_16x16x32_bf16 v[12:15], a[108:111], v[192:195], v[12:15]\n" \
"v_cvt_pk_bf16_f32 v205, v154, v155\n" \
"v_mfma_f32_16x16x32_bf16 v[16:19], a[112:115], v[192:195], v[16:19]\n" \
"v_exp_f32_e32 v156, v156\n" \
"v_mfma_f32_16x16x32_bf16 v[48:51], a[112:115], v[196:199], v[48:51]\n" \
"buffer_load_dwordx4 v224, %[kvs], s68 offen offset:1024 sc1 lds\n" \
"v_mfma_f32_16x16x32_bf16 v[52:55], a[116:119], v[196:199], v[52:55]\n" \
"v_exp_f32_e32 v157, v157\n" \
"v_mfma_f32_16x16x32_bf16 v[20:23], a[116:119], v[192:195], v[20:23]\n" \
"v_cvt_pk_bf16_f32 v206, v156, v157\n" \
"v_mfma_f32_16x16x32_bf16 v[24:27], a[120:123], v[192:195], v[24:27]\n" \
"v_exp_f32_e32 v158, v158\n" \
"v_mfma_f32_16x16x32_bf16 v[56:59], a[120:123], v[196:199], v[56:59]\n" \
"buffer_load_dwordx4 v224, %[kvs], s68 offen offset:2048 sc1 lds\n" \
"v_mfma_f32_16x16x32_bf16 v[60:63], a[124:127], v[196:199], v[60:63]\n" \
"v_exp_f32_e32 v159, v159\n" \
"v_mfma_f32_16x16x32_bf16 v[28:31], a[124:127], v[192:195], v[28:31]\n" \
"v_cvt_pk_bf16_f32 v207, v158, v159\n" \
"v_smfmac_f32_16x16x64_bf16 a[240:243], a[236:239], v[192:199], v248\n" \
"buffer_load_dwordx4 v224, %[kvs], s68 offen offset:3072 sc1 lds\n" \
"s_sub_i32 s73, %[db], %[da]\n" \
"s_lshr_b32 s73, s73, 2\n" \
"s_nop 5\n" \
"v_accvgpr_read_b32 v160, a240\n" \
"v_accvgpr_read_b32 v161, a241\n" \
"v_accvgpr_read_b32 v162, a242\n" \
"v_accvgpr_read_b32 v163, a243\n" \
"v_add_f32_e32 v164, v160, v161\n" \
"v_add_f32_e32 v165, v162, v163\n" \
"v_rcp_f32_e32 v160, v164\n" \
"v_rcp_f32_e32 v162, v165\n" \
"v_max_f32_e32 v166, v164, v165\n" \
"v_cmp_nge_f32_e32 vcc, s75, v166\n" \
"s_or_b64 s[84:85], s[84:85], vcc\n" \
"v_cmp_u_f32_e32 vcc, v164, v165\n" \
"s_or_b64 s[84:85], s[84:85], vcc\n" \
"v_mov_b32_e32 v166, 0\n" \
"v_mov_b32_e32 v161, v160\n" \
"v_mov_b32_e32 v163, v162\n" \
"v_pk_mul_f32 v[32:33], v[32:33], v[162:163]\n" \
"v_pk_mul_f32 v[34:35], v[34:35], v[162:163]\n" \
"v_pk_mul_f32 v[36:37], v[36:37], v[162:163]\n" \
"v_pk_mul_f32 v[38:39], v[38:39], v[162:163]\n" \
"v_cvt_pk_bf16_f32 v32, v32, v33\n" \
"v_cvt_pk_bf16_f32 v33, v34, v35\n" \
"v_cvt_pk_bf16_f32 v34, v36, v37\n" \
"v_cvt_pk_bf16_f32 v35, v38, v39\n" \
"buffer_store_dwordx4 v[32:35], v249, %[wsr], s65 offen offset:0 sc1\n" \
"v_pk_maximum3_f16 v166, v166, v32, v33\n" \
"v_pk_maximum3_f16 v166, v166, v34, v35\n" \
"v_pk_mul_f32 v[40:41], v[40:41], v[162:163]\n" \
"v_pk_mul_f32 v[42:43], v[42:43], v[162:163]\n" \
"v_pk_mul_f32 v[44:45], v[44:45], v[162:163]\n" \
"v_pk_mul_f32 v[46:47], v[46:47], v[162:163]\n" \
"v_cvt_pk_bf16_f32 v36, v40, v41\n" \
"v_cvt_pk_bf16_f32 v37, v42, v43\n" \
"v_cvt_pk_bf16_f32 v38, v44, v45\n" \
"v_cvt_pk_bf16_f32 v39, v46, v47\n" \
"buffer_store_dwordx4 v[36:39], v249, %[wsr], s65 offen offset:1024 sc1\n" \
"v_pk_maximum3_f16 v166, v166, v36, v37\n" \
"v_pk_maximum3_f16 v166, v166, v38, v39\n" \
"v_pk_mul_f32 v[48:49], v[48:49], v[162:163]\n" \
"v_pk_mul_f32 v[50:51], v[50:51], v[162:163]\n" \
"v_pk_mul_f32 v[52:53], v[52:53], v[162:163]\n" \
"v_pk_mul_f32 v[54:55], v[54:55], v[162:163]\n" \
"v_cvt_pk_bf16_f32 v40, v48, v49\n" \
"v_cvt_pk_bf16_f32 v41, v50, v51\n" \
"v_cvt_pk_bf16_f32 v42, v52, v53\n" \
"v_cvt_pk_bf16_f32 v43, v54, v55\n" \
"buffer_store_dwordx4 v[40:43], v249, %[wsr], s65 offen offset:2048 sc1\n" \
"v_pk_maximum3_f16 v166, v166, v40, v41\n" \
"v_pk_maximum3_f16 v166, v166, v42, v43\n" \
"v_pk_mul_f32 v[56:57], v[56:57], v[162:163]\n" \
"v_pk_mul_f32 v[58:59], v[58:59], v[162:163]\n" \
"v_pk_mul_f32 v[60:61], v[60:61], v[162:163]\n" \
"v_pk_mul_f32 v[62:63], v[62:63], v[162:163]\n" \
"v_cvt_pk_bf16_f32 v44, v56, v57\n" \
"v_cvt_pk_bf16_f32 v45, v58, v59\n" \
"v_cvt_pk_bf16_f32 v46, v60, v61\n" \
"v_cvt_pk_bf16_f32 v47, v62, v63\n" \
"buffer_store_dwordx4 v[44:47], v249, %[wsr], s65 offen offset:3072 sc1\n" \
"v_pk_maximum3_f16 v166, v166, v44, v45\n" \
"v_pk_maximum3_f16 v166, v166, v46, v47\n" \
"v_pk_mul_f32 v[0:1], v[0:1], v[160:161]\n" \
"v_pk_mul_f32 v[2:3], v[2:3], v[160:161]\n" \
"v_pk_mul_f32 v[4:5], v[4:5], v[160:161]\n" \
"v_pk_mul_f32 v[6:7], v[6:7], v[160:161]\n" \
"v_cvt_pk_bf16_f32 v48, v0, v1\n" \
"v_cvt_pk_bf16_f32 v49, v2, v3\n" \
"v_cvt_pk_bf16_f32 v50, v4, v5\n" \
"v_cvt_pk_bf16_f32 v51, v6, v7\n" \
"buffer_store_dwordx4 v[48:51], v241, %[wsr], s65 offen offset:0 sc1\n" \
"v_pk_maximum3_f16 v166, v166, v48, v49\n" \
"v_pk_maximum3_f16 v166, v166, v50, v51\n" \
"v_pk_mul_f32 v[8:9], v[8:9], v[160:161]\n" \
"v_pk_mul_f32 v[10:11], v[10:11], v[160:161]\n" \
"v_pk_mul_f32 v[12:13], v[12:13], v[160:161]\n" \
"v_pk_mul_f32 v[14:15], v[14:15], v[160:161]\n" \
"v_cvt_pk_bf16_f32 v52, v8, v9\n" \
"v_cvt_pk_bf16_f32 v53, v10, v11\n" \
"v_cvt_pk_bf16_f32 v54, v12, v13\n" \
"v_cvt_pk_bf16_f32 v55, v14, v15\n" \
"buffer_store_dwordx4 v[52:55], v241, %[wsr], s65 offen offset:1024 sc1\n" \
"v_pk_maximum3_f16 v166, v166, v52, v53\n" \
"v_pk_maximum3_f16 v166, v166, v54, v55\n" \
"v_pk_mul_f32 v[16:17], v[16:17], v[160:161]\n" \
"v_pk_mul_f32 v[18:19], v[18:19], v[160:161]\n" \
"v_pk_mul_f32 v[20:21], v[20:21], v[160:161]\n" \
"v_pk_mul_f32 v[22:23], v[22:23], v[160:161]\n" \
"v_cvt_pk_bf16_f32 v56, v16, v17\n" \
"v_cvt_pk_bf16_f32 v57, v18, v19\n" \
"v_cvt_pk_bf16_f32 v58, v20, v21\n" \
"v_cvt_pk_bf16_f32 v59, v22, v23\n" \
"buffer_store_dwordx4 v[56:59], v241, %[wsr], s65 offen offset:2048 sc1\n" \
"v_pk_maximum3_f16 v166, v166, v56, v57\n" \
"v_pk_maximum3_f16 v166, v166, v58, v59\n" \
"v_pk_mul_f32 v[24:25], v[24:25], v[160:161]\n" \
"v_pk_mul_f32 v[26:27], v[26:27], v[160:161]\n" \
"v_pk_mul_f32 v[28:29], v[28:29], v[160:161]\n" \
"v_pk_mul_f32 v[30:31], v[30:31], v[160:161]\n" \
"v_cvt_pk_bf16_f32 v60, v24, v25\n" \
"v_cvt_pk_bf16_f32 v61, v26, v27\n" \
"v_cvt_pk_bf16_f32 v62, v28, v29\n" \
"v_cvt_pk_bf16_f32 v63, v30, v31\n" \
"buffer_store_dwordx4 v[60:63], v241, %[wsr], s65 offen offset:3072 sc1\n" \
"v_pk_maximum3_f16 v166, v166, v60, v61\n" \
"v_pk_maximum3_f16 v166, v166, v62, v63\n" \
"v_cmp_u_f16_sdwa vcc, v166, v166 src0_sel:WORD_0 src1_sel:WORD_1\n" \
"s_or_b64 s[84:85], s[84:85], vcc\n" \
"s_cmp_lg_u64 s[84:85], 0\n" \
"s_cbranch_scc1 .LATLslowA\n" \
".LATLbackA:\n" \
"s_sub_u32 s73, s73, 3\n" \
"s_cbranch_scc1 .LATLn1\n" \
"s_waitcnt lgkmcnt(0)\n" \
"v_mfma_f32_16x16x32_bf16 v[144:147], a[64:67], a[32:35], v[176:179]\n" \
"s_add_u32 s68, s68, 0x4000\n" \
"v_exp_f32_e32 v128, v128\n" \
"v_mfma_f32_16x16x32_bf16 v[152:155], a[64:67], a[48:51], v[184:187]\n" \
"ds_read_b128 a[64:67], v234 offset:32768\n" \
"v_mfma_f32_16x16x32_bf16 v[156:159], a[68:71], a[48:51], v[188:191]\n" \
"v_exp_f32_e32 v129, v129\n" \
"v_mfma_f32_16x16x32_bf16 v[148:151], a[68:71], a[32:35], v[180:183]\n" \
"ds_read_b128 a[68:71], v234 offset:33792\n" \
"v_cvt_pk_bf16_f32 v192, v128, v129\n" \
"v_mfma_f32_16x16x32_bf16 v[144:147], a[72:75], a[36:39], v[144:147]\n" \
"v_exp_f32_e32 v130, v130\n" \
"v_mfma_f32_16x16x32_bf16 v[152:155], a[72:75], a[52:55], v[152:155]\n" \
"ds_read_b128 a[72:75], v234 offset:34816\n" \
"v_mfma_f32_16x16x32_bf16 v[156:159], a[76:79], a[52:55], v[156:159]\n" \
"s_mov_b32 m0, s69\n" \
"v_exp_f32_e32 v131, v131\n" \
"v_mfma_f32_16x16x32_bf16 v[148:151], a[76:79], a[36:39], v[148:151]\n" \
"ds_read_b128 a[76:79], v234 offset:35840\n" \
"v_cvt_pk_bf16_f32 v193, v130, v131\n" \
"v_mfma_f32_16x16x32_bf16 v[144:147], a[80:83], a[40:43], v[144:147]\n" \
"s_waitcnt lgkmcnt(4)\n" \
"v_exp_f32_e32 v132, v132\n" \
"v_mfma_f32_16x16x32_bf16 v[152:155], a[80:83], a[56:59], v[152:155]\n" \
"buffer_load_dwordx4 v224, %[kvs], s68 offen offset:0 sc1 lds\n" \
"v_mfma_f32_16x16x32_bf16 v[156:159], a[84:87], a[56:59], v[156:159]\n" \
"s_add_u32 s60, s64, 24576\n" \
"v_exp_f32_e32 v133, v133\n" \
"v_mfma_f32_16x16x32_bf16 v[148:151], a[84:87], a[40:43], v[148:151]\n" \
"ds_read_b128 a[80:83], v234 offset:36864\n" \
"buffer_load_dwordx4 v[0:3], v241, %[wsr], s60 offen offset:3072 sc1\n" \
"v_cvt_pk_bf16_f32 v194, v132, v133\n" \
"v_mfma_f32_16x16x32_bf16 v[144:147], a[88:91], a[44:47], v[144:147]\n" \
"s_xor_b32 s72, s72, 0x10000\n" \
"v_exp_f32_e32 v134, v134\n" \
"v_mfma_f32_16x16x32_bf16 v[152:155], a[88:91], a[60:63], v[152:155]\n" \
"ds_read_b128 a[84:87], v234 offset:37888\n" \
"ds_read_b128 a[88:91], v234 offset:38912\n" \
"v_mfma_f32_16x16x32_bf16 v[156:159], a[92:95], a[60:63], v[156:159]\n" \
"v_exp_f32_e32 v135, v135\n" \
"v_mfma_f32_16x16x32_bf16 v[148:151], a[92:95], a[44:47], v[148:151]\n" \
"ds_read_b128 a[92:95], v234 offset:39936\n" \
"buffer_load_dwordx4 v[4:7], v249, %[wsr], s60 offen offset:0 sc1\n" \
"v_cvt_pk_bf16_f32 v195, v134, v135\n" \
"v_mfma_f32_16x16x32_bf16 v[64:67], a[96:99], v[200:203], v[64:67]\n" \
"v_exp_f32_e32 v136, v136\n" \
"v_mfma_f32_16x16x32_bf16 v[96:99], a[96:99], v[204:207], v[96:99]\n" \
"buffer_load_dwordx4 v224, %[kvs], s68 offen offset:1024 sc1 lds\n" \
"v_mfma_f32_16x16x32_bf16 v[100:103], a[100:103], v[204:207], v[100:103]\n" \
"v_exp_f32_e32 v137, v137\n" \
"v_mfma_f32_16x16x32_bf16 v[68:71], a[100:103], v[200:203], v[68:71]\n" \
"ds_read_b128 a[96:99], v234 offset:8192\n" \
"v_cvt_pk_bf16_f32 v196, v136, v137\n" \
"v_mfma_f32_16x16x32_bf16 v[72:75], a[104:107], v[200:203], v[72:75]\n" \
"v_exp_f32_e32 v138, v138\n" \
"v_mfma_f32_16x16x32_bf16 v[104:107], a[104:107], v[204:207], v[104:107]\n" \
"ds_read_b128 a[100:103], v234 offset:9216\n" \
"ds_read_b128 a[104:107], v234 offset:10240\n" \
"v_mfma_f32_16x16x32_bf16 v[108:111], a[108:111], v[204:207], v[108:111]\n" \
"v_exp_f32_e32 v139, v139\n" \
"v_mfma_f32_16x16x32_bf16 v[76:79], a[108:111], v[200:203], v[76:79]\n" \
"ds_read_b128 a[108:111], v234 offset:11264\n" \
"buffer_load_dwordx4 v[8:11], v249, %[wsr], s60 offen offset:1024 sc1\n" \
"v_cvt_pk_bf16_f32 v197, v138, v139\n" \
"v_mfma_f32_16x16x32_bf16 v[80:83], a[112:115], v[200:203], v[80:83]\n" \
"s_waitcnt lgkmcnt(4)\n" \
"v_exp_f32_e32 v140, v140\n" \
"v_mfma_f32_16x16x32_bf16 v[112:115], a[112:115], v[204:207], v[112:115]\n" \
"buffer_load_dwordx4 v224, %[kvs], s68 offen offset:2048 sc1 lds\n" \
"v_mfma_f32_16x16x32_bf16 v[116:119], a[116:119], v[204:207], v[116:119]\n" \
"v_exp_f32_e32 v141, v141\n" \
"v_mfma_f32_16x16x32_bf16 v[84:87], a[116:119], v[200:203], v[84:87]\n" \
"ds_read_b128 a[112:115], v234 offset:12288\n" \
"v_cvt_pk_bf16_f32 v198, v140, v141\n" \
"v_mfma_f32_16x16x32_bf16 v[88:91], a[120:123], v[200:203], v[88:91]\n" \
"v_exp_f32_e32 v142, v142\n" \
"v_mfma_f32_16x16x32_bf16 v[120:123], a[120:123], v[204:207], v[120:123]\n" \
"ds_read_b128 a[116:119], v234 offset:13312\n" \
"ds_read_b128 a[120:123], v234 offset:14336\n" \
"v_mfma_f32_16x16x32_bf16 v[124:127], a[124:127], v[204:207], v[124:127]\n" \
"v_exp_f32_e32 v143, v143\n" \
"v_mfma_f32_16x16x32_bf16 v[92:95], a[124:127], v[200:203], v[92:95]\n" \
"ds_read_b128 a[124:127], v234 offset:15360\n" \
"v_cvt_pk_bf16_f32 v199, v142, v143\n" \
"v_smfmac_f32_16x16x64_bf16 a[248:251], a[236:239], v[200:207], v248\n" \
"buffer_load_dwordx4 v224, %[kvs], s68 offen offset:3072 sc1 lds\n" \
"v_mfma_f32_16x16x32_bf16 v[128:131], a[64:67], a[32:35], v[176:179]\n" \
"s_add_u32 s68, s68, 0x4000\n" \
"v_exp_f32_e32 v144, v144\n" \
"v_mfma_f32_16x16x32_bf16 v[136:139], a[64:67], a[48:51], v[184:187]\n" \
"ds_read_b128 a[64:67], v234 offset:49152\n" \
"buffer_load_dwordx4 v[12:15], v249, %[wsr], s60 offen offset:2048 sc1\n" \
"v_mfma_f32_16x16x32_bf16 v[140:143], a[68:71], a[48:51], v[188:191]\n" \
"v_exp_f32_e32 v145, v145\n" \
"v_mfma_f32_16x16x32_bf16 v[132:135], a[68:71], a[32:35], v[180:183]\n" \
"ds_read_b128 a[68:71], v234 offset:50176\n" \
"v_cvt_pk_bf16_f32 v200, v144, v145\n" \
"v_mfma_f32_16x16x32_bf16 v[128:131], a[72:75], a[36:39], v[128:131]\n" \
"v_exp_f32_e32 v146, v146\n" \
"v_mfma_f32_16x16x32_bf16 v[136:139], a[72:75], a[52:55], v[136:139]\n" \
"ds_read_b128 a[72:75], v234 offset:51200\n" \
"buffer_load_dwordx4 v[16:19], v249, %[wsr], s60 offen offset:3072 sc1\n" \
"v_mfma_f32_16x16x32_bf16 v[140:143], a[76:79], a[52:55], v[140:143]\n" \
"s_mov_b32 m0, s70\n" \
"v_exp_f32_e32 v147, v147\n" \
"v_mfma_f32_16x16x32_bf16 v[132:135], a[76:79], a[36:39], v[132:135]\n" \
"s_waitcnt vmcnt(25)\n" \
"ds_read_b128 a[76:79], v234 offset:52224\n" \
"v_cvt_pk_bf16_f32 v201, v146, v147\n" \
"v_mfma_f32_16x16x32_bf16 v[128:131], a[80:83], a[40:43], v[128:131]\n" \
"s_waitcnt lgkmcnt(4)\n" \
"s_barrier\n" \
"v_exp_f32_e32 v148, v148\n" \
"v_mfma_f32_16x16x32_bf16 v[136:139], a[80:83], a[56:59], v[136:139]\n" \
"buffer_load_dwordx4 v224, %[kvs], s68 offen offset:0 sc1 lds\n" \
"v_mfma_f32_16x16x32_bf16 v[140:143], a[84:87], a[56:59], v[140:143]\n" \
"v_exp_f32_e32 v149, v149\n" \
"v_mfma_f32_16x16x32_bf16 v[132:135], a[84:87], a[40:43], v[132:135]\n" \
"ds_read_b128 a[80:83], v234 offset:53248\n" \
"v_cvt_pk_bf16_f32 v202, v148, v149\n" \
"v_mfma_f32_16x16x32_bf16 v[128:131], a[88:91], a[44:47], v[128:131]\n" \
"s_xor_b32 s69, s69, 0x10000\n" \
"v_exp_f32_e32 v150, v150\n" \
"v_mfma_f32_16x16x32_bf16 v[136:139], a[88:91], a[60:63], v[136:139]\n" \
"s_add_u32 s60, s64, 32768\n" \
"ds_read_b128 a[84:87], v234 offset:54272\n" \
"ds_read_b128 a[88:91], v234 offset:55296\n" \
"v_mfma_f32_16x16x32_bf16 v[140:143], a[92:95], a[60:63], v[140:143]\n" \
"buffer_load_dwordx4 v[20:23], v241, %[wsr], s60 offen offset:0 sc1\n" \
"v_exp_f32_e32 v151, v151\n" \
"v_mfma_f32_16x16x32_bf16 v[132:135], a[92:95], a[44:47], v[132:135]\n" \
"ds_read_b128 a[92:95], v234 offset:56320\n" \
"v_cvt_pk_bf16_f32 v203, v150, v151\n" \
"v_mfma_f32_16x16x32_bf16 v[64:67], a[96:99], v[192:195], v[64:67]\n" \
"v_exp_f32_e32 v152, v152\n" \
"v_mfma_f32_16x16x32_bf16 v[96:99], a[96:99], v[196:199], v[96:99]\n" \
"buffer_load_dwordx4 v224, %[kvs], s68 offen offset:1024 sc1 lds\n" \
"v_mfma_f32_16x16x32_bf16 v[100:103], a[100:103], v[196:199], v[100:103]\n" \
"v_exp_f32_e32 v153, v153\n" \
"v_mfma_f32_16x16x32_bf16 v[68:71], a[100:103], v[192:195], v[68:71]\n" \
"ds_read_b128 a[96:99], v234 offset:24576\n" \
"v_cvt_pk_bf16_f32 v204, v152, v153\n" \
"v_mfma_f32_16x16x32_bf16 v[72:75], a[104:107], v[192:195], v[72:75]\n" \
"buffer_load_dwordx4 v[24:27], v241, %[wsr], s60 offen offset:1024 sc1\n" \
"v_exp_f32_e32 v154, v154\n" \
"v_mfma_f32_16x16x32_bf16 v[104:107], a[104:107], v[196:199], v[104:107]\n" \
"ds_read_b128 a[100:103], v234 offset:25600\n" \
"ds_read_b128 a[104:107], v234 offset:26624\n" \
"v_mfma_f32_16x16x32_bf16 v[108:111], a[108:111], v[196:199], v[108:111]\n" \
"v_exp_f32_e32 v155, v155\n" \
"v_mfma_f32_16x16x32_bf16 v[76:79], a[108:111], v[192:195], v[76:79]\n" \
"ds_read_b128 a[108:111], v234 offset:27648\n" \
"v_cvt_pk_bf16_f32 v205, v154, v155\n" \
"v_mfma_f32_16x16x32_bf16 v[80:83], a[112:115], v[192:195], v[80:83]\n" \
"s_waitcnt lgkmcnt(4)\n" \
"v_exp_f32_e32 v156, v156\n" \
"v_mfma_f32_16x16x32_bf16 v[112:115], a[112:115], v[196:199], v[112:115]\n" \
"buffer_load_dwordx4 v224, %[kvs], s68 offen offset:2048 sc1 lds\n" \
"v_mfma_f32_16x16x32_bf16 v[116:119], a[116:119], v[196:199], v[116:119]\n" \
"v_exp_f32_e32 v157, v157\n" \
"v_mfma_f32_16x16x32_bf16 v[84:87], a[116:119], v[192:195], v[84:87]\n" \
"ds_read_b128 a[112:115], v234 offset:28672\n" \
"v_cvt_pk_bf16_f32 v206, v156, v157\n" \
"v_mfma_f32_16x16x32_bf16 v[88:91], a[120:123], v[192:195], v[88:91]\n" \
"v_exp_f32_e32 v158, v158\n" \
"v_mfma_f32_16x16x32_bf16 v[120:123], a[120:123], v[196:199], v[120:123]\n" \
"ds_read_b128 a[116:119], v234 offset:29696\n" \
"ds_read_b128 a[120:123], v234 offset:30720\n" \
"v_mfma_f32_16x16x32_bf16 v[124:127], a[124:127], v[196:199], v[124:127]\n" \
"v_exp_f32_e32 v159, v159\n" \
"v_mfma_f32_16x16x32_bf16 v[92:95], a[124:127], v[192:195], v[92:95]\n" \
"ds_read_b128 a[124:127], v234 offset:31744\n" \
"v_cvt_pk_bf16_f32 v207, v158, v159\n" \
"v_smfmac_f32_16x16x64_bf16 a[248:251], a[236:239], v[192:199], v248\n" \
"buffer_load_dwordx4 v224, %[kvs], s68 offen offset:3072 sc1 lds\n" \
"v_mfma_f32_16x16x32_bf16 v[144:147], a[64:67], a[32:35], v[176:179]\n" \
"s_add_u32 s68, s68, 0x4000\n" \
"v_exp_f32_e32 v128, v128\n" \
"v_mfma_f32_16x16x32_bf16 v[152:155], a[64:67], a[48:51], v[184:187]\n" \
"ds_read_b128 a[64:67], v235 offset:0\n" \
"buffer_load_dwordx4 v[28:31], v241, %[wsr], s60 offen offset:2048 sc1\n" \
"v_mfma_f32_16x16x32_bf16 v[156:159], a[68:71], a[48:51], v[188:191]\n" \
"v_exp_f32_e32 v129, v129\n" \
"v_mfma_f32_16x16x32_bf16 v[148:151], a[68:71], a[32:35], v[180:183]\n" \
"ds_read_b128 a[68:71], v235 offset:1024\n" \
"v_cvt_pk_bf16_f32 v192, v128, v129\n" \
"v_mfma_f32_16x16x32_bf16 v[144:147], a[72:75], a[36:39], v[144:147]\n" \
"v_exp_f32_e32 v130, v130\n" \
"v_mfma_f32_16x16x32_bf16 v[152:155], a[72:75], a[52:55], v[152:155]\n" \
"ds_read_b128 a[72:75], v235 offset:2048\n" \
"buffer_load_dwordx4 v[32:35], v241, %[wsr], s60 offen offset:3072 sc1\n" \
"v_mfma_f32_16x16x32_bf16 v[156:159], a[76:79], a[52:55], v[156:159]\n" \
"s_mov_b32 m0, s71\n" \
"v_exp_f32_e32 v131, v131\n" \
"v_mfma_f32_16x16x32_bf16 v[148:151], a[76:79], a[36:39], v[148:151]\n" \
"ds_read_b128 a[76:79], v235 offset:3072\n" \
"v_cvt_pk_bf16_f32 v193, v130, v131\n" \
"v_mfma_f32_16x16x32_bf16 v[144:147], a[80:83], a[40:43], v[144:147]\n" \
"s_waitcnt lgkmcnt(4)\n" \
"v_exp_f32_e32 v132, v132\n" \
"v_mfma_f32_16x16x32_bf16 v[152:155], a[80:83], a[56:59], v[152:155]\n" \
"buffer_load_dwordx4 v224, %[kvs], s68 offen offset:0 sc1 lds\n" \
"v_mfma_f32_16x16x32_bf16 v[156:159], a[84:87], a[56:59], v[156:159]\n" \
"v_exp_f32_e32 v133, v133\n" \
"v_mfma_f32_16x16x32_bf16 v[148:151], a[84:87], a[40:43], v[148:151]\n" \
"ds_read_b128 a[80:83], v235 offset:4096\n" \
"buffer_load_dwordx4 v[36:39], v249, %[wsr], s60 offen offset:0 sc1\n" \
"v_cvt_pk_bf16_f32 v194, v132, v133\n" \
"v_mfma_f32_16x16x32_bf16 v[144:147], a[88:91], a[44:47], v[144:147]\n" \
"s_xor_b32 s70, s70, 0x10000\n" \
"v_exp_f32_e32 v134, v134\n" \
"v_mfma_f32_16x16x32_bf16 v[152:155], a[88:91], a[60:63], v[152:155]\n" \
"ds_read_b128 a[84:87], v235 offset:5120\n" \
"ds_read_b128 a[88:91], v235 offset:6144\n" \
"v_mfma_f32_16x16x32_bf16 v[156:159], a[92:95], a[60:63], v[156:159]\n" \
"s_mul_i32 s61, %[da], 0x2020\n" \
"v_exp_f32_e32 v135, v135\n" \
"v_mfma_f32_16x16x32_bf16 v[148:151], a[92:95], a[44:47], v[148:151]\n" \
"s_and_b32 s61, s61, 0x3c020\n" \
"ds_read_b128 a[92:95], v235 offset:7168\n" \
"v_cvt_pk_bf16_f32 v195, v134, v135\n" \
"v_mfma_f32_16x16x32_bf16 v[64:67], a[96:99], v[200:203], v[64:67]\n" \
"s_mul_i32 s62, %[h], 0x84\n" \
"v_exp_f32_e32 v136, v136\n" \
"v_mfma_f32_16x16x32_bf16 v[96:99], a[96:99], v[204:207], v[96:99]\n" \
"buffer_load_dwordx4 v224, %[kvs], s68 offen offset:1024 sc1 lds\n" \
"v_mfma_f32_16x16x32_bf16 v[100:103], a[100:103], v[204:207], v[100:103]\n" \
"s_and_b32 s62, s62, 0xc1c\n" \
"v_exp_f32_e32 v137, v137\n" \
"v_mfma_f32_16x16x32_bf16 v[68:71], a[100:103], v[200:203], v[68:71]\n" \
"s_or_b32 s61, s61, s62\n" \
"ds_read_b128 a[96:99], v234 offset:40960\n" \
"buffer_load_dwordx4 v[40:43], v249, %[wsr], s60 offen offset:1024 sc1\n" \
"v_cvt_pk_bf16_f32 v196, v136, v137\n" \
"v_and_b32_e32 v251, 60, v240\n" \
"v_lshlrev_b32_e32 v251, 25, v251\n" \
"v_mfma_f32_16x16x32_bf16 v[72:75], a[104:107], v[200:203], v[72:75]\n" \
"v_exp_f32_e32 v138, v138\n" \
"v_mfma_f32_16x16x32_bf16 v[104:107], a[104:107], v[204:207], v[104:107]\n" \
"ds_read_b128 a[100:103], v234 offset:41984\n" \
"ds_read_b128 a[104:107], v234 offset:43008\n" \
"v_mfma_f32_16x16x32_bf16 v[108:111], a[108:111], v[204:207], v[108:111]\n" \
"v_exp_f32_e32 v139, v139\n" \
"v_mfma_f32_16x16x32_bf16 v[76:79], a[108:111], v[200:203], v[76:79]\n" \
"ds_read_b128 a[108:111], v234 offset:44032\n" \
"v_cvt_pk_bf16_f32 v197, v138, v139\n" \
"v_mfma_f32_16x16x32_bf16 v[80:83], a[112:115], v[200:203], v[80:83]\n" \
"s_waitcnt lgkmcnt(4)\n" \
"v_exp_f32_e32 v140, v140\n" \
"v_mfma_f32_16x16x32_bf16 v[112:115], a[112:115], v[204:207], v[112:115]\n" \
"buffer_load_dwordx4 v224, %[kvs], s68 offen offset:2048 sc1 lds\n" \
"v_mfma_f32_16x16x32_bf16 v[116:119], a[116:119], v[204:207], v[116:119]\n" \
"v_exp_f32_e32 v141, v141\n" \
"v_mfma_f32_16x16x32_bf16 v[84:87], a[116:119], v[200:203], v[84:87]\n" \
"ds_read_b128 a[112:115], v234 offset:45056\n" \
"buffer_load_dwordx4 v[44:47], v249, %[wsr], s60 offen offset:2048 sc1\n" \
"v_cvt_pk_bf16_f32 v198, v140, v141\n" \
"v_mfma_f32_16x16x32_bf16 v[88:91], a[120:123], v[200:203], v[88:91]\n" \
"v_exp_f32_e32 v142, v142\n" \
"v_mfma_f32_16x16x32_bf16 v[120:123], a[120:123], v[204:207], v[120:123]\n" \
"ds_read_b128 a[116:119], v234 offset:46080\n" \
"ds_read_b128 a[120:123], v234 offset:47104\n" \
"v_mfma_f32_16x16x32_bf16 v[124:127], a[124:127], v[204:207], v[124:127]\n" \
"v_exp_f32_e32 v143, v143\n" \
"v_mfma_f32_16x16x32_bf16 v[92:95], a[124:127], v[200:203], v[92:95]\n" \
"ds_read_b128 a[124:127], v234 offset:48128\n" \
"v_cvt_pk_bf16_f32 v199, v142, v143\n" \
"v_smfmac_f32_16x16x64_bf16 a[248:251], a[236:239], v[200:207], v248\n" \
"buffer_load_dwordx4 v224, %[kvs], s68 offen offset:3072 sc1 lds\n" \
"v_xor_b32_e32 v234, 0x10000, v234\n" \
"v_xor_b32_e32 v235, 0x10000, v235\n" \
"v_mfma_f32_16x16x32_bf16 v[128:131], a[64:67], a[32:35], v[176:179]\n" \
"s_add_u32 s68, s68, 0x4000\n" \
"v_exp_f32_e32 v144, v144\n" \
"v_mfma_f32_16x16x32_bf16 v[136:139], a[64:67], a[48:51], v[184:187]\n" \
"ds_read_b128 a[64:67], v234 offset:16384\n" \
"buffer_load_dwordx4 v[48:51], v249, %[wsr], s60 offen offset:3072 sc1\n" \
"v_lshl_or_b32 v251, v240, 12, v251\n" \
"v_mfma_f32_16x16x32_bf16 v[140:143], a[68:71], a[48:51], v[188:191]\n" \
"s_waitcnt vmcnt(25)\n" \
"v_exp_f32_e32 v145, v145\n" \
"v_mfma_f32_16x16x32_bf16 v[132:135], a[68:71], a[32:35], v[180:183]\n" \
"ds_read_b128 a[68:71], v234 offset:17408\n" \
"v_cvt_pk_bf16_f32 v200, v144, v145\n" \
"v_mfma_f32_16x16x32_bf16 v[128:131], a[72:75], a[36:39], v[128:131]\n" \
"v_exp_f32_e32 v146, v146\n" \
"v_mfma_f32_16x16x32_bf16 v[136:139], a[72:75], a[52:55], v[136:139]\n" \
"ds_read_b128 a[72:75], v234 offset:18432\n" \
"buffer_atomic_add v252, v251, %[wsr], s61 offen offset:768 sc1\n" \
"v_mfma_f32_16x16x32_bf16 v[140:143], a[76:79], a[52:55], v[140:143]\n" \
"s_mov_b32 m0, s72\n" \
"v_exp_f32_e32 v147, v147\n" \
"v_mfma_f32_16x16x32_bf16 v[132:135], a[76:79], a[36:39], v[132:135]\n" \
"ds_read_b128 a[76:79], v234 offset:19456\n" \
"v_cvt_pk_bf16_f32 v201, v146, v147\n" \
"v_mfma_f32_16x16x32_bf16 v[128:131], a[80:83], a[40:43], v[128:131]\n" \
"s_waitcnt lgkmcnt(4)\n" \
"s_barrier\n" \
"v_exp_f32_e32 v148, v148\n" \
"v_mfma_f32_16x16x32_bf16 v[136:139], a[80:83], a[56:59], v[136:139]\n" \
"buffer_load_dwordx4 v224, %[kvs], s68 offen offset:0 sc1 lds\n" \
"v_mfma_f32_16x16x32_bf16 v[140:143], a[84:87], a[56:59], v[140:143]\n" \
"s_add_u32 s60, s64, 40960\n" \
"v_exp_f32_e32 v149, v149\n" \
"v_mfma_f32_16x16x32_bf16 v[132:135], a[84:87], a[40:43], v[132:135]\n" \
"ds_read_b128 a[80:83], v234 offset:20480\n" \
"buffer_load_dwordx4 v[52:55], v241, %[wsr], s60 offen offset:0 sc1\n" \
"v_cvt_pk_bf16_f32 v202, v148, v149\n" \
"v_mfma_f32_16x16x32_bf16 v[128:131], a[88:91], a[44:47], v[128:131]\n" \
"s_xor_b32 s71, s71, 0x10000\n" \
"v_exp_f32_e32 v150, v150\n" \
"v_mfma_f32_16x16x32_bf16 v[136:139], a[88:91], a[60:63], v[136:139]\n" \
"ds_read_b128 a[84:87], v234 offset:21504\n" \
"ds_read_b128 a[88:91], v234 offset:22528\n" \
"v_mfma_f32_16x16x32_bf16 v[140:143], a[92:95], a[60:63], v[140:143]\n" \
"buffer_load_dwordx4 v[56:59], v241, %[wsr], s60 offen offset:1024 sc1\n" \
"v_exp_f32_e32 v151, v151\n" \
"v_mfma_f32_16x16x32_bf16 v[132:135], a[92:95], a[44:47], v[132:135]\n" \
"ds_read_b128 a[92:95], v234 offset:23552\n" \
"v_cvt_pk_bf16_f32 v203, v150, v151\n" \
"v_mfma_f32_16x16x32_bf16 v[64:67], a[96:99], v[192:195], v[64:67]\n" \
"v_exp_f32_e32 v152, v152\n" \
"v_mfma_f32_16x16x32_bf16 v[96:99], a[96:99], v[196:199], v[96:99]\n" \
"buffer_load_dwordx4 v224, %[kvs], s68 offen offset:1024 sc1 lds\n" \
"v_mfma_f32_16x16x32_bf16 v[100:103], a[100:103], v[196:199], v[100:103]\n" \
"v_exp_f32_e32 v153, v153\n" \
"v_mfma_f32_16x16x32_bf16 v[68:71], a[100:103], v[192:195], v[68:71]\n" \
"ds_read_b128 a[96:99], v235 offset:57344\n" \
"v_cvt_pk_bf16_f32 v204, v152, v153\n" \
"v_mfma_f32_16x16x32_bf16 v[72:75], a[104:107], v[192:195], v[72:75]\n" \
"v_exp_f32_e32 v154, v154\n" \
"v_mfma_f32_16x16x32_bf16 v[104:107], a[104:107], v[196:199], v[104:107]\n" \
"ds_read_b128 a[100:103], v235 offset:58368\n" \
"ds_read_b128 a[104:107], v235 offset:59392\n" \
"buffer_load_dwordx4 v[60:63], v241, %[wsr], s60 offen offset:2048 sc1\n" \
"v_mfma_f32_16x16x32_bf16 v[108:111], a[108:111], v[196:199], v[108:111]\n" \
"v_exp_f32_e32 v155, v155\n" \
"v_mfma_f32_16x16x32_bf16 v[76:79], a[108:111], v[192:195], v[76:79]\n" \
"ds_read_b128 a[108:111], v235 offset:60416\n" \
"v_cvt_pk_bf16_f32 v205, v154, v155\n" \
"v_mfma_f32_16x16x32_bf16 v[80:83], a[112:115], v[192:195], v[80:83]\n" \
"s_waitcnt lgkmcnt(4)\n" \
"v_exp_f32_e32 v156, v156\n" \
"v_mfma_f32_16x16x32_bf16 v[112:115], a[112:115], v[196:199], v[112:115]\n" \
"buffer_load_dwordx4 v224, %[kvs], s68 offen offset:2048 sc1 lds\n" \
"v_mfma_f32_16x16x32_bf16 v[116:119], a[116:119], v[196:199], v[116:119]\n" \
"v_exp_f32_e32 v157, v157\n" \
"v_mfma_f32_16x16x32_bf16 v[84:87], a[116:119], v[192:195], v[84:87]\n" \
"ds_read_b128 a[112:115], v235 offset:61440\n" \
"v_cvt_pk_bf16_f32 v206, v156, v157\n" \
"v_mfma_f32_16x16x32_bf16 v[88:91], a[120:123], v[192:195], v[88:91]\n" \
"v_exp_f32_e32 v158, v158\n" \
"v_mfma_f32_16x16x32_bf16 v[120:123], a[120:123], v[196:199], v[120:123]\n" \
"ds_read_b128 a[116:119], v235 offset:62464\n" \
"ds_read_b128 a[120:123], v235 offset:63488\n" \
"v_mfma_f32_16x16x32_bf16 v[124:127], a[124:127], v[196:199], v[124:127]\n" \
"v_exp_f32_e32 v159, v159\n" \
"v_mfma_f32_16x16x32_bf16 v[92:95], a[124:127], v[192:195], v[92:95]\n" \
"ds_read_b128 a[124:127], v235 offset:64512\n" \
"v_cvt_pk_bf16_f32 v207, v158, v159\n" \
"v_smfmac_f32_16x16x64_bf16 a[248:251], a[236:239], v[192:199], v248\n" \
"buffer_load_dwordx4 v224, %[kvs], s68 offen offset:3072 sc1 lds\n" \
"v_mfma_f32_16x16x32_bf16 v[144:147], a[64:67], a[32:35], v[176:179]\n" \
"s_add_u32 s68, s68, 0x4000\n" \
"v_exp_f32_e32 v128, v128\n" \
"v_mfma_f32_16x16x32_bf16 v[152:155], a[64:67], a[48:51], v[184:187]\n" \
"ds_read_b128 a[64:67], v234 offset:32768\n" \
"buffer_load_dwordx4 a[0:3], v241, %[wsr], s60 offen offset:3072 sc1\n" \
"v_mfma_f32_16x16x32_bf16 v[156:159], a[68:71], a[48:51], v[188:191]\n" \
"v_exp_f32_e32 v129, v129\n" \
"v_mfma_f32_16x16x32_bf16 v[148:151], a[68:71], a[32:35], v[180:183]\n" \
"ds_read_b128 a[68:71], v234 offset:33792\n" \
"v_cvt_pk_bf16_f32 v192, v128, v129\n" \
"v_mfma_f32_16x16x32_bf16 v[144:147], a[72:75], a[36:39], v[144:147]\n" \
"v_exp_f32_e32 v130, v130\n" \
"v_mfma_f32_16x16x32_bf16 v[152:155], a[72:75], a[52:55], v[152:155]\n" \
"ds_read_b128 a[72:75], v234 offset:34816\n" \
"buffer_load_dwordx4 a[4:7], v249, %[wsr], s60 offen offset:0 sc1\n" \
"v_mfma_f32_16x16x32_bf16 v[156:159], a[76:79], a[52:55], v[156:159]\n" \
"s_mov_b32 m0, s69\n" \
"v_exp_f32_e32 v131, v131\n" \
"v_mfma_f32_16x16x32_bf16 v[148:151], a[76:79], a[36:39], v[148:151]\n" \
"ds_read_b128 a[76:79], v234 offset:35840\n" \
"v_cvt_pk_bf16_f32 v193, v130, v131\n" \
"v_mfma_f32_16x16x32_bf16 v[144:147], a[80:83], a[40:43], v[144:147]\n" \
"s_waitcnt lgkmcnt(4)\n" \
"v_exp_f32_e32 v132, v132\n" \
"v_mfma_f32_16x16x32_bf16 v[152:155], a[80:83], a[56:59], v[152:155]\n" \
"buffer_load_dwordx4 v224, %[kvs], s68 offen offset:0 sc1 lds\n" \
"v_mfma_f32_16x16x32_bf16 v[156:159], a[84:87], a[56:59], v[156:159]\n" \
"v_exp_f32_e32 v133, v133\n" \
"v_mfma_f32_16x16x32_bf16 v[148:151], a[84:87], a[40:43], v[148:151]\n" \
"ds_read_b128 a[80:83], v234 offset:36864\n" \
"buffer_load_dwordx4 a[8:11], v249, %[wsr], s60 offen offset:1024 sc1\n" \
"v_cvt_pk_bf16_f32 v194, v132, v133\n" \
"v_mfma_f32_16x16x32_bf16 v[144:147], a[88:91], a[44:47], v[144:147]\n" \
"s_xor_b32 s72, s72, 0x10000\n" \
"v_exp_f32_e32 v134, v134\n" \
"v_mfma_f32_16x16x32_bf16 v[152:155], a[88:91], a[60:63], v[152:155]\n" \
"ds_read_b128 a[84:87], v234 offset:37888\n" \
"ds_read_b128 a[88:91], v234 offset:38912\n" \
"v_mfma_f32_16x16x32_bf16 v[156:159], a[92:95], a[60:63], v[156:159]\n" \
"v_exp_f32_e32 v135, v135\n" \
"v_mfma_f32_16x16x32_bf16 v[148:151], a[92:95], a[44:47], v[148:151]\n" \
"ds_read_b128 a[92:95], v234 offset:39936\n" \
"v_cvt_pk_bf16_f32 v195, v134, v135\n" \
"v_mfma_f32_16x16x32_bf16 v[64:67], a[96:99], v[200:203], v[64:67]\n" \
"v_exp_f32_e32 v136, v136\n" \
"v_mfma_f32_16x16x32_bf16 v[96:99], a[96:99], v[204:207], v[96:99]\n" \
"buffer_load_dwordx4 v224, %[kvs], s68 offen offset:1024 sc1 lds\n" \
"v_mfma_f32_16x16x32_bf16 v[100:103], a[100:103], v[204:207], v[100:103]\n" \
"v_exp_f32_e32 v137, v137\n" \
"v_mfma_f32_16x16x32_bf16 v[68:71], a[100:103], v[200:203], v[68:71]\n" \
"ds_read_b128 a[96:99], v234 offset:8192\n" \
"buffer_load_dwordx4 a[12:15], v249, %[wsr], s60 offen offset:2048 sc1\n" \
"v_cvt_pk_bf16_f32 v196, v136, v137\n" \
"v_mfma_f32_16x16x32_bf16 v[72:75], a[104:107], v[200:203], v[72:75]\n" \
"v_exp_f32_e32 v138, v138\n" \
"v_mfma_f32_16x16x32_bf16 v[104:107], a[104:107], v[204:207], v[104:107]\n" \
"ds_read_b128 a[100:103], v234 offset:9216\n" \
"ds_read_b128 a[104:107], v234 offset:10240\n" \
"v_mfma_f32_16x16x32_bf16 v[108:111], a[108:111], v[204:207], v[108:111]\n" \
"v_exp_f32_e32 v139, v139\n" \
"v_mfma_f32_16x16x32_bf16 v[76:79], a[108:111], v[200:203], v[76:79]\n" \
"ds_read_b128 a[108:111], v234 offset:11264\n" \
"v_cvt_pk_bf16_f32 v197, v138, v139\n" \
"v_mfma_f32_16x16x32_bf16 v[80:83], a[112:115], v[200:203], v[80:83]\n" \
"s_waitcnt lgkmcnt(4)\n" \
"v_exp_f32_e32 v140, v140\n" \
"v_mfma_f32_16x16x32_bf16 v[112:115], a[112:115], v[204:207], v[112:115]\n" \
"buffer_load_dwordx4 v224, %[kvs], s68 offen offset:2048 sc1 lds\n" \
"v_mfma_f32_16x16x32_bf16 v[116:119], a[116:119], v[204:207], v[116:119]\n" \
"v_exp_f32_e32 v141, v141\n" \
"v_mfma_f32_16x16x32_bf16 v[84:87], a[116:119], v[200:203], v[84:87]\n" \
"ds_read_b128 a[112:115], v234 offset:12288\n" \
"v_cvt_pk_bf16_f32 v198, v140, v141\n" \
"v_mfma_f32_16x16x32_bf16 v[88:91], a[120:123], v[200:203], v[88:91]\n" \
"v_exp_f32_e32 v142, v142\n" \
"v_mfma_f32_16x16x32_bf16 v[120:123], a[120:123], v[204:207], v[120:123]\n" \
"ds_read_b128 a[116:119], v234 offset:13312\n" \
"ds_read_b128 a[120:123], v234 offset:14336\n" \
"v_mfma_f32_16x16x32_bf16 v[124:127], a[124:127], v[204:207], v[124:127]\n" \
"v_exp_f32_e32 v143, v143\n" \
"v_mfma_f32_16x16x32_bf16 v[92:95], a[124:127], v[200:203], v[92:95]\n" \
"ds_read_b128 a[124:127], v234 offset:15360\n" \
"v_cvt_pk_bf16_f32 v199, v142, v143\n" \
"v_smfmac_f32_16x16x64_bf16 a[248:251], a[236:239], v[200:207], v248\n" \
"buffer_load_dwordx4 v224, %[kvs], s68 offen offset:3072 sc1 lds\n" \
"v_mfma_f32_16x16x32_bf16 v[128:131], a[64:67], a[32:35], v[176:179]\n" \
"s_add_u32 s68, s68, 0x4000\n" \
"v_exp_f32_e32 v144, v144\n" \
"v_mfma_f32_16x16x32_bf16 v[136:139], a[64:67], a[48:51], v[184:187]\n" \
"ds_read_b128 a[64:67], v234 offset:49152\n" \
"buffer_load_dwordx4 a[16:19], v249, %[wsr], s60 offen offset:3072 sc1\n" \
"v_mfma_f32_16x16x32_bf16 v[140:143], a[68:71], a[48:51], v[188:191]\n" \
"v_exp_f32_e32 v145, v145\n" \
"v_mfma_f32_16x16x32_bf16 v[132:135], a[68:71], a[32:35], v[180:183]\n" \
"s_add_u32 s60, s64, 49152\n" \
"ds_read_b128 a[68:71], v234 offset:50176\n" \
"v_cvt_pk_bf16_f32 v200, v144, v145\n" \
"v_mfma_f32_16x16x32_bf16 v[128:131], a[72:75], a[36:39], v[128:131]\n" \
"v_exp_f32_e32 v146, v146\n" \
"v_mfma_f32_16x16x32_bf16 v[136:139], a[72:75], a[52:55], v[136:139]\n" \
"ds_read_b128 a[72:75], v234 offset:51200\n" \
"buffer_load_dwordx4 a[20:23], v241, %[wsr], s60 offen offset:0 sc1\n" \
"v_mfma_f32_16x16x32_bf16 v[140:143], a[76:79], a[52:55], v[140:143]\n" \
"s_mov_b32 m0, s70\n" \
"v_exp_f32_e32 v147, v147\n" \
"v_mfma_f32_16x16x32_bf16 v[132:135], a[76:79], a[36:39], v[132:135]\n" \
"s_waitcnt vmcnt(28)\n" \
"ds_read_b128 a[76:79], v234 offset:52224\n" \
"v_cvt_pk_bf16_f32 v201, v146, v147\n" \
"v_mfma_f32_16x16x32_bf16 v[128:131], a[80:83], a[40:43], v[128:131]\n" \
"s_waitcnt lgkmcnt(4)\n" \
"s_barrier\n" \
"v_exp_f32_e32 v148, v148\n" \
"v_mfma_f32_16x16x32_bf16 v[136:139], a[80:83], a[56:59], v[136:139]\n" \
"buffer_load_dwordx4 v224, %[kvs], s68 offen offset:0 sc1 lds\n" \
"v_mfma_f32_16x16x32_bf16 v[140:143], a[84:87], a[56:59], v[140:143]\n" \
"v_exp_f32_e32 v149, v149\n" \
"v_mfma_f32_16x16x32_bf16 v[132:135], a[84:87], a[40:43], v[132:135]\n" \
"ds_read_b128 a[80:83], v234 offset:53248\n" \
"buffer_load_dwordx4 a[24:27], v241, %[wsr], s60 offen offset:1024 sc1\n" \
"v_cvt_pk_bf16_f32 v202, v148, v149\n" \
"v_mfma_f32_16x16x32_bf16 v[128:131], a[88:91], a[44:47], v[128:131]\n" \
"s_xor_b32 s69, s69, 0x10000\n" \
"v_exp_f32_e32 v150, v150\n" \
"v_mfma_f32_16x16x32_bf16 v[136:139], a[88:91], a[60:63], v[136:139]\n" \
"ds_read_b128 a[84:87], v234 offset:54272\n" \
"ds_read_b128 a[88:91], v234 offset:55296\n" \
"v_mfma_f32_16x16x32_bf16 v[140:143], a[92:95], a[60:63], v[140:143]\n" \
"v_exp_f32_e32 v151, v151\n" \
"v_mfma_f32_16x16x32_bf16 v[132:135], a[92:95], a[44:47], v[132:135]\n" \
"ds_read_b128 a[92:95], v234 offset:56320\n" \
"buffer_load_dwordx4 a[28:31], v241, %[wsr], s60 offen offset:2048 sc1\n" \
"v_cvt_pk_bf16_f32 v203, v150, v151\n" \
"v_mfma_f32_16x16x32_bf16 v[64:67], a[96:99], v[192:195], v[64:67]\n" \
"v_exp_f32_e32 v152, v152\n" \
"v_mfma_f32_16x16x32_bf16 v[96:99], a[96:99], v[196:199], v[96:99]\n" \
"buffer_load_dwordx4 v224, %[kvs], s68 offen offset:1024 sc1 lds\n" \
"v_mfma_f32_16x16x32_bf16 v[100:103], a[100:103], v[196:199], v[100:103]\n" \
"v_exp_f32_e32 v153, v153\n" \
"v_mfma_f32_16x16x32_bf16 v[68:71], a[100:103], v[192:195], v[68:71]\n" \
"ds_read_b128 a[96:99], v234 offset:24576\n" \
"v_cvt_pk_bf16_f32 v204, v152, v153\n" \
"v_mfma_f32_16x16x32_bf16 v[72:75], a[104:107], v[192:195], v[72:75]\n" \
"v_exp_f32_e32 v154, v154\n" \
"v_mfma_f32_16x16x32_bf16 v[104:107], a[104:107], v[196:199], v[104:107]\n" \
"ds_read_b128 a[100:103], v234 offset:25600\n" \
"ds_read_b128 a[104:107], v234 offset:26624\n" \
"v_mfma_f32_16x16x32_bf16 v[108:111], a[108:111], v[196:199], v[108:111]\n" \
"v_exp_f32_e32 v155, v155\n" \
"v_mfma_f32_16x16x32_bf16 v[76:79], a[108:111], v[192:195], v[76:79]\n" \
"ds_read_b128 a[108:111], v234 offset:27648\n" \
"buffer_load_dwordx4 v[160:163], v241, %[wsr], s60 offen offset:3072 sc1\n" \
"v_cvt_pk_bf16_f32 v205, v154, v155\n" \
"v_mfma_f32_16x16x32_bf16 v[80:83], a[112:115], v[192:195], v[80:83]\n" \
"s_waitcnt lgkmcnt(4)\n" \
"v_exp_f32_e32 v156, v156\n" \
"v_mfma_f32_16x16x32_bf16 v[112:115], a[112:115], v[196:199], v[112:115]\n" \
"buffer_load_dwordx4 v224, %[kvs], s68 offen offset:2048 sc1 lds\n" \
"v_mfma_f32_16x16x32_bf16 v[116:119], a[116:119], v[196:199], v[116:119]\n" \
"v_exp_f32_e32 v157, v157\n" \
"v_mfma_f32_16x16x32_bf16 v[84:87], a[116:119], v[192:195], v[84:87]\n" \
"ds_read_b128 a[112:115], v234 offset:28672\n" \
"v_cvt_pk_bf16_f32 v206, v156, v157\n" \
"v_mfma_f32_16x16x32_bf16 v[88:91], a[120:123], v[192:195], v[88:91]\n" \
"v_exp_f32_e32 v158, v158\n" \
"v_mfma_f32_16x16x32_bf16 v[120:123], a[120:123], v[196:199], v[120:123]\n" \
"ds_read_b128 a[116:119], v234 offset:29696\n" \
"ds_read_b128 a[120:123], v234 offset:30720\n" \
"v_mfma_f32_16x16x32_bf16 v[124:127], a[124:127], v[196:199], v[124:127]\n" \
"v_exp_f32_e32 v159, v159\n" \
"v_mfma_f32_16x16x32_bf16 v[92:95], a[124:127], v[192:195], v[92:95]\n" \
"ds_read_b128 a[124:127], v234 offset:31744\n" \
"v_cvt_pk_bf16_f32 v207, v158, v159\n" \
"v_smfmac_f32_16x16x64_bf16 a[248:251], a[236:239], v[192:199], v248\n" \
"buffer_load_dwordx4 v224, %[kvs], s68 offen offset:3072 sc1 lds\n" \
"v_mfma_f32_16x16x32_bf16 v[144:147], a[64:67], a[32:35], v[176:179]\n" \
"s_add_u32 s68, s68, 0x4000\n" \
"v_exp_f32_e32 v128, v128\n" \
"v_mfma_f32_16x16x32_bf16 v[152:155], a[64:67], a[48:51], v[184:187]\n" \
"ds_read_b128 a[64:67], v235 offset:0\n" \
"buffer_load_dwordx4 v[164:167], v249, %[wsr], s60 offen offset:0 sc1\n" \
"v_mfma_f32_16x16x32_bf16 v[156:159], a[68:71], a[48:51], v[188:191]\n" \
"v_exp_f32_e32 v129, v129\n" \
"v_mfma_f32_16x16x32_bf16 v[148:151], a[68:71], a[32:35], v[180:183]\n" \
"ds_read_b128 a[68:71], v235 offset:1024\n" \
"v_cvt_pk_bf16_f32 v192, v128, v129\n" \
"v_mfma_f32_16x16x32_bf16 v[144:147], a[72:75], a[36:39], v[144:147]\n" \
"v_exp_f32_e32 v130, v130\n" \
"v_mfma_f32_16x16x32_bf16 v[152:155], a[72:75], a[52:55], v[152:155]\n" \
"ds_read_b128 a[72:75], v235 offset:2048\n" \
"buffer_load_dwordx4 v[168:171], v249, %[wsr], s60 offen offset:1024 sc1\n" \
"v_mfma_f32_16x16x32_bf16 v[156:159], a[76:79], a[52:55], v[156:159]\n" \
"s_mov_b32 m0, s71\n" \
"v_exp_f32_e32 v131, v131\n" \
"v_mfma_f32_16x16x32_bf16 v[148:151], a[76:79], a[36:39], v[148:151]\n" \
"ds_read_b128 a[76:79], v235 offset:3072\n" \
"v_cvt_pk_bf16_f32 v193, v130, v131\n" \
"v_mfma_f32_16x16x32_bf16 v[144:147], a[80:83], a[40:43], v[144:147]\n" \
"s_waitcnt lgkmcnt(4)\n" \
"v_exp_f32_e32 v132, v132\n" \
"v_mfma_f32_16x16x32_bf16 v[152:155], a[80:83], a[56:59], v[152:155]\n" \
"buffer_load_dwordx4 v224, %[kvs], s68 offen offset:0 sc1 lds\n" \
"v_mfma_f32_16x16x32_bf16 v[156:159], a[84:87], a[56:59], v[156:159]\n" \
"v_exp_f32_e32 v133, v133\n" \
"v_mfma_f32_16x16x32_bf16 v[148:151], a[84:87], a[40:43], v[148:151]\n" \
"ds_read_b128 a[80:83], v235 offset:4096\n" \
"v_cvt_pk_bf16_f32 v194, v132, v133\n" \
"v_mfma_f32_16x16x32_bf16 v[144:147], a[88:91], a[44:47], v[144:147]\n" \
"s_xor_b32 s70, s70, 0x10000\n" \
"v_exp_f32_e32 v134, v134\n" \
"v_mfma_f32_16x16x32_bf16 v[152:155], a[88:91], a[60:63], v[152:155]\n" \
"ds_read_b128 a[84:87], v235 offset:5120\n" \
"ds_read_b128 a[88:91], v235 offset:6144\n" \
"buffer_load_dwordx4 v[172:175], v249, %[wsr], s60 offen offset:2048 sc1\n" \
"v_mfma_f32_16x16x32_bf16 v[156:159], a[92:95], a[60:63], v[156:159]\n" \
"v_exp_f32_e32 v135, v135\n" \
"v_mfma_f32_16x16x32_bf16 v[148:151], a[92:95], a[44:47], v[148:151]\n" \
"ds_read_b128 a[92:95], v235 offset:7168\n" \
"v_cvt_pk_bf16_f32 v195, v134, v135\n" \
"v_mfma_f32_16x16x32_bf16 v[64:67], a[96:99], v[200:203], v[64:67]\n" \
"v_exp_f32_e32 v136, v136\n" \
"v_mfma_f32_16x16x32_bf16 v[96:99], a[96:99], v[204:207], v[96:99]\n" \
"buffer_load_dwordx4 v224, %[kvs], s68 offen offset:1024 sc1 lds\n" \
"v_mfma_f32_16x16x32_bf16 v[100:103], a[100:103], v[204:207], v[100:103]\n" \
"v_exp_f32_e32 v137, v137\n" \
"v_mfma_f32_16x16x32_bf16 v[68:71], a[100:103], v[200:203], v[68:71]\n" \
"ds_read_b128 a[96:99], v234 offset:40960\n" \
"v_cvt_pk_bf16_f32 v196, v136, v137\n" \
"v_mfma_f32_16x16x32_bf16 v[72:75], a[104:107], v[200:203], v[72:75]\n" \
"buffer_load_dwordx4 a[240:243], v249, %[wsr], s60 offen offset:3072 sc1\n" \
"v_exp_f32_e32 v138, v138\n" \
"v_mfma_f32_16x16x32_bf16 v[104:107], a[104:107], v[204:207], v[104:107]\n" \
"ds_read_b128 a[100:103], v234 offset:41984\n" \
"ds_read_b128 a[104:107], v234 offset:43008\n" \
"v_mfma_f32_16x16x32_bf16 v[108:111], a[108:111], v[204:207], v[108:111]\n" \
"v_exp_f32_e32 v139, v139\n" \
"v_mfma_f32_16x16x32_bf16 v[76:79], a[108:111], v[200:203], v[76:79]\n" \
"ds_read_b128 a[108:111], v234 offset:44032\n" \
"v_cvt_pk_bf16_f32 v197, v138, v139\n" \
"v_mfma_f32_16x16x32_bf16 v[80:83], a[112:115], v[200:203], v[80:83]\n" \
"s_waitcnt lgkmcnt(4)\n" \
"v_exp_f32_e32 v140, v140\n" \
"v_mfma_f32_16x16x32_bf16 v[112:115], a[112:115], v[204:207], v[112:115]\n" \
"buffer_load_dwordx4 v224, %[kvs], s68 offen offset:2048 sc1 lds\n" \
"v_mfma_f32_16x16x32_bf16 v[116:119], a[116:119], v[204:207], v[116:119]\n" \
"v_exp_f32_e32 v141, v141\n" \
"v_mfma_f32_16x16x32_bf16 v[84:87], a[116:119], v[200:203], v[84:87]\n" \
"ds_read_b128 a[112:115], v234 offset:45056\n" \
"v_cvt_pk_bf16_f32 v198, v140, v141\n" \
"v_mfma_f32_16x16x32_bf16 v[88:91], a[120:123], v[200:203], v[88:91]\n" \
"v_exp_f32_e32 v142, v142\n" \
"v_mfma_f32_16x16x32_bf16 v[120:123], a[120:123], v[204:207], v[120:123]\n" \
"ds_read_b128 a[116:119], v234 offset:46080\n" \
"ds_read_b128 a[120:123], v234 offset:47104\n" \
"v_mfma_f32_16x16x32_bf16 v[124:127], a[124:127], v[204:207], v[124:127]\n" \
"v_exp_f32_e32 v143, v143\n" \
"v_mfma_f32_16x16x32_bf16 v[92:95], a[124:127], v[200:203], v[92:95]\n" \
"ds_read_b128 a[124:127], v234 offset:48128\n" \
"v_cvt_pk_bf16_f32 v199, v142, v143\n" \
"v_smfmac_f32_16x16x64_bf16 a[248:251], a[236:239], v[200:207], v248\n" \
"buffer_load_dwordx4 v224, %[kvs], s68 offen offset:3072 sc1 lds\n" \
"v_xor_b32_e32 v234, 0x10000, v234\n" \
"v_xor_b32_e32 v235, 0x10000, v235\n" \
"s_sub_u32 s73, s73, 1\n" \
"s_cbranch_scc1 .LATLls\n" \
".p2align 6\n" \
".LATLp2:\n" \
"v_mfma_f32_16x16x32_bf16 v[128:131], a[64:67], a[32:35], v[176:179]\n" \
"s_add_u32 s68, s68, 0x4000\n" \
"v_exp_f32_e32 v144, v144\n" \
"v_mfma_f32_16x16x32_bf16 v[136:139], a[64:67], a[48:51], v[184:187]\n" \
"ds_read_b128 a[64:67], v234 offset:16384\n" \
"v_mfma_f32_16x16x32_bf16 v[140:143], a[68:71], a[48:51], v[188:191]\n" \
"v_exp_f32_e32 v145, v145\n" \
"v_mfma_f32_16x16x32_bf16 v[132:135], a[68:71], a[32:35], v[180:183]\n" \
"ds_read_b128 a[68:71], v234 offset:17408\n" \
"v_cvt_pk_bf16_f32 v200, v144, v145\n" \
"v_mfma_f32_16x16x32_bf16 v[128:131], a[72:75], a[36:39], v[128:131]\n" \
"v_exp_f32_e32 v146, v146\n" \
"v_mfma_f32_16x16x32_bf16 v[136:139], a[72:75], a[52:55], v[136:139]\n" \
"ds_read_b128 a[72:75], v234 offset:18432\n" \
"v_mfma_f32_16x16x32_bf16 v[140:143], a[76:79], a[52:55], v[140:143]\n" \
"s_mov_b32 m0, s72\n" \
"v_exp_f32_e32 v147, v147\n" \
"v_mfma_f32_16x16x32_bf16 v[132:135], a[76:79], a[36:39], v[132:135]\n" \
"ds_read_b128 a[76:79], v234 offset:19456\n" \
"v_cvt_pk_bf16_f32 v201, v146, v147\n" \
"v_mfma_f32_16x16x32_bf16 v[128:131], a[80:83], a[40:43], v[128:131]\n" \
"s_waitcnt vmcnt(13) lgkmcnt(4)\n" \
"s_barrier\n" \
"v_exp_f32_e32 v148, v148\n" \
"v_mfma_f32_16x16x32_bf16 v[136:139], a[80:83], a[56:59], v[136:139]\n" \
"buffer_load_dwordx4 v224, %[kvs], s68 offen offset:0 sc1 lds\n" \
"v_mfma_f32_16x16x32_bf16 v[140:143], a[84:87], a[56:59], v[140:143]\n" \
"v_exp_f32_e32 v149, v149\n" \
"v_mfma_f32_16x16x32_bf16 v[132:135], a[84:87], a[40:43], v[132:135]\n" \
"ds_read_b128 a[80:83], v234 offset:20480\n" \
"v_cvt_pk_bf16_f32 v202, v148, v149\n" \
"v_mfma_f32_16x16x32_bf16 v[128:131], a[88:91], a[44:47], v[128:131]\n" \
"s_xor_b32 s71, s71, 0x10000\n" \
"v_exp_f32_e32 v150, v150\n" \
"v_mfma_f32_16x16x32_bf16 v[136:139], a[88:91], a[60:63], v[136:139]\n" \
"ds_read_b128 a[84:87], v234 offset:21504\n" \
"ds_read_b128 a[88:91], v234 offset:22528\n" \
"v_mfma_f32_16x16x32_bf16 v[140:143], a[92:95], a[60:63], v[140:143]\n" \
"v_exp_f32_e32 v151, v151\n" \
"v_mfma_f32_16x16x32_bf16 v[132:135], a[92:95], a[44:47], v[132:135]\n" \
"ds_read_b128 a[92:95], v234 offset:23552\n" \
"v_cvt_pk_bf16_f32 v203, v150, v151\n" \
"v_mfma_f32_16x16x32_bf16 v[64:67], a[96:99], v[192:195], v[64:67]\n" \
"v_exp_f32_e32 v152, v152\n" \
"v_mfma_f32_16x16x32_bf16 v[96:99], a[96:99], v[196:199], v[96:99]\n" \
"buffer_load_dwordx4 v224, %[kvs], s68 offen offset:1024 sc1 lds\n" \
"v_mfma_f32_16x16x32_bf16 v[100:103], a[100:103], v[196:199], v[100:103]\n" \
"v_exp_f32_e32 v153, v153\n" \
"v_mfma_f32_16x16x32_bf16 v[68:71], a[100:103], v[192:195], v[68:71]\n" \
"ds_read_b128 a[96:99], v235 offset:57344\n" \
"v_cvt_pk_bf16_f32 v204, v152, v153\n" \
"v_mfma_f32_16x16x32_bf16 v[72:75], a[104:107], v[192:195], v[72:75]\n" \
"v_exp_f32_e32 v154, v154\n" \
"v_mfma_f32_16x16x32_bf16 v[104:107], a[104:107], v[196:199], v[104:107]\n" \
"ds_read_b128 a[100:103], v235 offset:58368\n" \
"ds_read_b128 a[104:107], v235 offset:59392\n" \
"v_mfma_f32_16x16x32_bf16 v[108:111], a[108:111], v[196:199], v[108:111]\n" \
"v_exp_f32_e32 v155, v155\n" \
"v_mfma_f32_16x16x32_bf16 v[76:79], a[108:111], v[192:195], v[76:79]\n" \
"ds_read_b128 a[108:111], v235 offset:60416\n" \
"v_cvt_pk_bf16_f32 v205, v154, v155\n" \
"v_mfma_f32_16x16x32_bf16 v[80:83], a[112:115], v[192:195], v[80:83]\n" \
"s_waitcnt lgkmcnt(4)\n" \
"v_exp_f32_e32 v156, v156\n" \
"v_mfma_f32_16x16x32_bf16 v[112:115], a[112:115], v[196:199], v[112:115]\n" \
"buffer_load_dwordx4 v224, %[kvs], s68 offen offset:2048 sc1 lds\n" \
"v_mfma_f32_16x16x32_bf16 v[116:119], a[116:119], v[196:199], v[116:119]\n" \
"v_exp_f32_e32 v157, v157\n" \
"v_mfma_f32_16x16x32_bf16 v[84:87], a[116:119], v[192:195], v[84:87]\n" \
"ds_read_b128 a[112:115], v235 offset:61440\n" \
"v_cvt_pk_bf16_f32 v206, v156, v157\n" \
"v_mfma_f32_16x16x32_bf16 v[88:91], a[120:123], v[192:195], v[88:91]\n" \
"v_exp_f32_e32 v158, v158\n" \
"v_mfma_f32_16x16x32_bf16 v[120:123], a[120:123], v[196:199], v[120:123]\n" \
"ds_read_b128 a[116:119], v235 offset:62464\n" \
"ds_read_b128 a[120:123], v235 offset:63488\n" \
"v_mfma_f32_16x16x32_bf16 v[124:127], a[124:127], v[196:199], v[124:127]\n" \
"v_exp_f32_e32 v159, v159\n" \
"v_mfma_f32_16x16x32_bf16 v[92:95], a[124:127], v[192:195], v[92:95]\n" \
"ds_read_b128 a[124:127], v235 offset:64512\n" \
"v_cvt_pk_bf16_f32 v207, v158, v159\n" \
"v_smfmac_f32_16x16x64_bf16 a[248:251], a[236:239], v[192:199], v248\n" \
"buffer_load_dwordx4 v224, %[kvs], s68 offen offset:3072 sc1 lds\n" \
"v_mfma_f32_16x16x32_bf16 v[144:147], a[64:67], a[32:35], v[176:179]\n" \
"s_add_u32 s68, s68, 0x4000\n" \
"v_exp_f32_e32 v128, v128\n" \
"v_mfma_f32_16x16x32_bf16 v[152:155], a[64:67], a[48:51], v[184:187]\n" \
"s_and_b32 s83, s76, 114688\n" \
"ds_read_b128 a[64:67], v234 offset:32768\n" \
"v_mfma_f32_16x16x32_bf16 v[156:159], a[68:71], a[48:51], v[188:191]\n" \
"v_exp_f32_e32 v129, v129\n" \
"v_mfma_f32_16x16x32_bf16 v[148:151], a[68:71], a[32:35], v[180:183]\n" \
"ds_read_b128 a[68:71], v234 offset:33792\n" \
"v_cvt_pk_bf16_f32 v192, v128, v129\n" \
"v_mfma_f32_16x16x32_bf16 v[144:147], a[72:75], a[36:39], v[144:147]\n" \
"s_xor_b32 s83, %[po], s83\n" \
"v_exp_f32_e32 v130, v130\n" \
"v_mfma_f32_16x16x32_bf16 v[152:155], a[72:75], a[52:55], v[152:155]\n" \
"ds_read_b128 a[72:75], v234 offset:34816\n" \
"buffer_store_dwordx4 a[252:255], v241, %[wsr], s83 offen sc1\n" \
"v_mfma_f32_16x16x32_bf16 v[156:159], a[76:79], a[52:55], v[156:159]\n" \
"s_mov_b32 m0, s69\n" \
"v_exp_f32_e32 v131, v131\n" \
"v_mfma_f32_16x16x32_bf16 v[148:151], a[76:79], a[36:39], v[148:151]\n" \
"ds_read_b128 a[76:79], v234 offset:35840\n" \
"v_cvt_pk_bf16_f32 v193, v130, v131\n" \
"v_mfma_f32_16x16x32_bf16 v[144:147], a[80:83], a[40:43], v[144:147]\n" \
"s_waitcnt lgkmcnt(4)\n" \
"v_exp_f32_e32 v132, v132\n" \
"v_mfma_f32_16x16x32_bf16 v[152:155], a[80:83], a[56:59], v[152:155]\n" \
"buffer_load_dwordx4 v224, %[kvs], s68 offen offset:0 sc1 lds\n" \
"v_mfma_f32_16x16x32_bf16 v[156:159], a[84:87], a[56:59], v[156:159]\n" \
"s_lshr_b32 s76, s76, 3\n" \
"v_exp_f32_e32 v133, v133\n" \
"v_mfma_f32_16x16x32_bf16 v[148:151], a[84:87], a[40:43], v[148:151]\n" \
"ds_read_b128 a[80:83], v234 offset:36864\n" \
"v_cvt_pk_bf16_f32 v194, v132, v133\n" \
"v_mfma_f32_16x16x32_bf16 v[144:147], a[88:91], a[44:47], v[144:147]\n" \
"s_xor_b32 s72, s72, 0x10000\n" \
"v_exp_f32_e32 v134, v134\n" \
"v_mfma_f32_16x16x32_bf16 v[152:155], a[88:91], a[60:63], v[152:155]\n" \
"ds_read_b128 a[84:87], v234 offset:37888\n" \
"ds_read_b128 a[88:91], v234 offset:38912\n" \
"v_mfma_f32_16x16x32_bf16 v[156:159], a[92:95], a[60:63], v[156:159]\n" \
"v_exp_f32_e32 v135, v135\n" \
"v_mfma_f32_16x16x32_bf16 v[148:151], a[92:95], a[44:47], v[148:151]\n" \
"ds_read_b128 a[92:95], v234 offset:39936\n" \
"v_cvt_pk_bf16_f32 v195, v134, v135\n" \
"v_mfma_f32_16x16x32_bf16 v[64:67], a[96:99], v[200:203], v[64:67]\n" \
"v_exp_f32_e32 v136, v136\n" \
"v_mfma_f32_16x16x32_bf16 v[96:99], a[96:99], v[204:207], v[96:99]\n" \
"buffer_load_dwordx4 v224, %[kvs], s68 offen offset:1024 sc1 lds\n" \
"v_mfma_f32_16x16x32_bf16 v[100:103], a[100:103], v[204:207], v[100:103]\n" \
"v_exp_f32_e32 v137, v137\n" \
"v_mfma_f32_16x16x32_bf16 v[68:71], a[100:103], v[200:203], v[68:71]\n" \
"ds_read_b128 a[96:99], v234 offset:8192\n" \
"v_cvt_pk_bf16_f32 v196, v136, v137\n" \
"v_mfma_f32_16x16x32_bf16 v[72:75], a[104:107], v[200:203], v[72:75]\n" \
"v_exp_f32_e32 v138, v138\n" \
"v_mfma_f32_16x16x32_bf16 v[104:107], a[104:107], v[204:207], v[104:107]\n" \
"ds_read_b128 a[100:103], v234 offset:9216\n" \
"ds_read_b128 a[104:107], v234 offset:10240\n" \
"v_mfma_f32_16x16x32_bf16 v[108:111], a[108:111], v[204:207], v[108:111]\n" \
"v_exp_f32_e32 v139, v139\n" \
"v_mfma_f32_16x16x32_bf16 v[76:79], a[108:111], v[200:203], v[76:79]\n" \
"ds_read_b128 a[108:111], v234 offset:11264\n" \
"v_cvt_pk_bf16_f32 v197, v138, v139\n" \
"v_mfma_f32_16x16x32_bf16 v[80:83], a[112:115], v[200:203], v[80:83]\n" \
"s_waitcnt lgkmcnt(4)\n" \
"v_exp_f32_e32 v140, v140\n" \
"v_mfma_f32_16x16x32_bf16 v[112:115], a[112:115], v[204:207], v[112:115]\n" \
"buffer_load_dwordx4 v224, %[kvs], s68 offen offset:2048 sc1 lds\n" \
"v_mfma_f32_16x16x32_bf16 v[116:119], a[116:119], v[204:207], v[116:119]\n" \
"v_exp_f32_e32 v141, v141\n" \
"v_mfma_f32_16x16x32_bf16 v[84:87], a[116:119], v[200:203], v[84:87]\n" \
"ds_read_b128 a[112:115], v234 offset:12288\n" \
"v_cvt_pk_bf16_f32 v198, v140, v141\n" \
"v_mfma_f32_16x16x32_bf16 v[88:91], a[120:123], v[200:203], v[88:91]\n" \
"v_exp_f32_e32 v142, v142\n" \
"v_mfma_f32_16x16x32_bf16 v[120:123], a[120:123], v[204:207], v[120:123]\n" \
"ds_read_b128 a[116:119], v234 offset:13312\n" \
"ds_read_b128 a[120:123], v234 offset:14336\n" \
"v_mfma_f32_16x16x32_bf16 v[124:127], a[124:127], v[204:207], v[124:127]\n" \
"v_exp_f32_e32 v143, v143\n" \
"v_mfma_f32_16x16x32_bf16 v[92:95], a[124:127], v[200:203], v[92:95]\n" \
"ds_read_b128 a[124:127], v234 offset:15360\n" \
"v_cvt_pk_bf16_f32 v199, v142, v143\n" \
"v_smfmac_f32_16x16x64_bf16 a[248:251], a[236:239], v[200:207], v248\n" \
"buffer_load_dwordx4 v224, %[kvs], s68 offen offset:3072 sc1 lds\n" \
"v_mfma_f32_16x16x32_bf16 v[128:131], a[64:67], a[32:35], v[176:179]\n" \
"s_add_u32 s68, s68, 0x4000\n" \
"v_exp_f32_e32 v144, v144\n" \
"v_mfma_f32_16x16x32_bf16 v[136:139], a[64:67], a[48:51], v[184:187]\n" \
"ds_read_b128 a[64:67], v234 offset:49152\n" \
"v_mfma_f32_16x16x32_bf16 v[140:143], a[68:71], a[48:51], v[188:191]\n" \
"v_exp_f32_e32 v145, v145\n" \
"v_mfma_f32_16x16x32_bf16 v[132:135], a[68:71], a[32:35], v[180:183]\n" \
"ds_read_b128 a[68:71], v234 offset:50176\n" \
"v_cvt_pk_bf16_f32 v200, v144, v145\n" \
"v_mfma_f32_16x16x32_bf16 v[128:131], a[72:75], a[36:39], v[128:131]\n" \
"v_exp_f32_e32 v146, v146\n" \
"v_mfma_f32_16x16x32_bf16 v[136:139], a[72:75], a[52:55], v[136:139]\n" \
"ds_read_b128 a[72:75], v234 offset:51200\n" \
"v_mfma_f32_16x16x32_bf16 v[140:143], a[76:79], a[52:55], v[140:143]\n" \
"s_mov_b32 m0, s70\n" \
"v_exp_f32_e32 v147, v147\n" \
"v_mfma_f32_16x16x32_bf16 v[132:135], a[76:79], a[36:39], v[132:135]\n" \
"ds_read_b128 a[76:79], v234 offset:52224\n" \
"v_cvt_pk_bf16_f32 v201, v146, v147\n" \
"v_mfma_f32_16x16x32_bf16 v[128:131], a[80:83], a[40:43], v[128:131]\n" \
"s_waitcnt vmcnt(13) lgkmcnt(4)\n" \
"s_barrier\n" \
"v_exp_f32_e32 v148, v148\n" \
"v_mfma_f32_16x16x32_bf16 v[136:139], a[80:83], a[56:59], v[136:139]\n" \
"buffer_load_dwordx4 v224, %[kvs], s68 offen offset:0 sc1 lds\n" \
"v_mfma_f32_16x16x32_bf16 v[140:143], a[84:87], a[56:59], v[140:143]\n" \
"v_exp_f32_e32 v149, v149\n" \
"v_mfma_f32_16x16x32_bf16 v[132:135], a[84:87], a[40:43], v[132:135]\n" \
"ds_read_b128 a[80:83], v234 offset:53248\n" \
"v_cvt_pk_bf16_f32 v202, v148, v149\n" \
"v_mfma_f32_16x16x32_bf16 v[128:131], a[88:91], a[44:47], v[128:131]\n" \
"s_xor_b32 s69, s69, 0x10000\n" \
"v_exp_f32_e32 v150, v150\n" \
"v_mfma_f32_16x16x32_bf16 v[136:139], a[88:91], a[60:63], v[136:139]\n" \
"ds_read_b128 a[84:87], v234 offset:54272\n" \
"ds_read_b128 a[88:91], v234 offset:55296\n" \
"v_mfma_f32_16x16x32_bf16 v[140:143], a[92:95], a[60:63], v[140:143]\n" \
"v_exp_f32_e32 v151, v151\n" \
"v_mfma_f32_16x16x32_bf16 v[132:135], a[92:95], a[44:47], v[132:135]\n" \
"ds_read_b128 a[92:95], v234 offset:56320\n" \
"v_cvt_pk_bf16_f32 v203, v150, v151\n" \
"v_mfma_f32_16x16x32_bf16 v[64:67], a[96:99], v[192:195], v[64:67]\n" \
"v_exp_f32_e32 v152, v152\n" \
"v_mfma_f32_16x16x32_bf16 v[96:99], a[96:99], v[196:199], v[96:99]\n" \
"buffer_load_dwordx4 v224, %[kvs], s68 offen offset:1024 sc1 lds\n" \
"v_mfma_f32_16x16x32_bf16 v[100:103], a[100:103], v[196:199], v[100:103]\n" \
"v_exp_f32_e32 v153, v153\n" \
"v_mfma_f32_16x16x32_bf16 v[68:71], a[100:103], v[192:195], v[68:71]\n" \
"ds_read_b128 a[96:99], v234 offset:24576\n" \
"v_cvt_pk_bf16_f32 v204, v152, v153\n" \
"v_mfma_f32_16x16x32_bf16 v[72:75], a[104:107], v[192:195], v[72:75]\n" \
"v_exp_f32_e32 v154, v154\n" \
"v_mfma_f32_16x16x32_bf16 v[104:107], a[104:107], v[196:199], v[104:107]\n" \
"ds_read_b128 a[100:103], v234 offset:25600\n" \
"ds_read_b128 a[104:107], v234 offset:26624\n" \
"v_mfma_f32_16x16x32_bf16 v[108:111], a[108:111], v[196:199], v[108:111]\n" \
"v_exp_f32_e32 v155, v155\n" \
"v_mfma_f32_16x16x32_bf16 v[76:79], a[108:111], v[192:195], v[76:79]\n" \
"ds_read_b128 a[108:111], v234 offset:27648\n" \
"v_cvt_pk_bf16_f32 v205, v154, v155\n" \
"v_mfma_f32_16x16x32_bf16 v[80:83], a[112:115], v[192:195], v[80:83]\n" \
"s_waitcnt lgkmcnt(4)\n" \
"v_exp_f32_e32 v156, v156\n" \
"v_mfma_f32_16x16x32_bf16 v[112:115], a[112:115], v[196:199], v[112:115]\n" \
"buffer_load_dwordx4 v224, %[kvs], s68 offen offset:2048 sc1 lds\n" \
"v_mfma_f32_16x16x32_bf16 v[116:119], a[116:119], v[196:199], v[116:119]\n" \
"v_exp_f32_e32 v157, v157\n" \
"v_mfma_f32_16x16x32_bf16 v[84:87], a[116:119], v[192:195], v[84:87]\n" \
"ds_read_b128 a[112:115], v234 offset:28672\n" \
"v_cvt_pk_bf16_f32 v206, v156, v157\n" \
"v_mfma_f32_16x16x32_bf16 v[88:91], a[120:123], v[192:195], v[88:91]\n" \
"v_exp_f32_e32 v158, v158\n" \
"v_mfma_f32_16x16x32_bf16 v[120:123], a[120:123], v[196:199], v[120:123]\n" \
"ds_read_b128 a[116:119], v234 offset:29696\n" \
"ds_read_b128 a[120:123], v234 offset:30720\n" \
"v_mfma_f32_16x16x32_bf16 v[124:127], a[124:127], v[196:199], v[124:127]\n" \
"v_exp_f32_e32 v159, v159\n" \
"v_mfma_f32_16x16x32_bf16 v[92:95], a[124:127], v[192:195], v[92:95]\n" \
"ds_read_b128 a[124:127], v234 offset:31744\n" \
"v_cvt_pk_bf16_f32 v207, v158, v159\n" \
"v_smfmac_f32_16x16x64_bf16 a[248:251], a[236:239], v[192:199], v248\n" \
"buffer_load_dwordx4 v224, %[kvs], s68 offen offset:3072 sc1 lds\n" \
"v_mfma_f32_16x16x32_bf16 v[144:147], a[64:67], a[32:35], v[176:179]\n" \
"s_add_u32 s68, s68, 0x4000\n" \
"v_exp_f32_e32 v128, v128\n" \
"v_mfma_f32_16x16x32_bf16 v[152:155], a[64:67], a[48:51], v[184:187]\n" \
"ds_read_b128 a[64:67], v235 offset:0\n" \
"v_mfma_f32_16x16x32_bf16 v[156:159], a[68:71], a[48:51], v[188:191]\n" \
"v_exp_f32_e32 v129, v129\n" \
"v_mfma_f32_16x16x32_bf16 v[148:151], a[68:71], a[32:35], v[180:183]\n" \
"ds_read_b128 a[68:71], v235 offset:1024\n" \
"v_cvt_pk_bf16_f32 v192, v128, v129\n" \
"v_mfma_f32_16x16x32_bf16 v[144:147], a[72:75], a[36:39], v[144:147]\n" \
"v_exp_f32_e32 v130, v130\n" \
"v_mfma_f32_16x16x32_bf16 v[152:155], a[72:75], a[52:55], v[152:155]\n" \
"ds_read_b128 a[72:75], v235 offset:2048\n" \
"v_mfma_f32_16x16x32_bf16 v[156:159], a[76:79], a[52:55], v[156:159]\n" \
"s_mov_b32 m0, s71\n" \
"v_exp_f32_e32 v131, v131\n" \
"v_mfma_f32_16x16x32_bf16 v[148:151], a[76:79], a[36:39], v[148:151]\n" \
"ds_read_b128 a[76:79], v235 offset:3072\n" \
"v_cvt_pk_bf16_f32 v193, v130, v131\n" \
"v_mfma_f32_16x16x32_bf16 v[144:147], a[80:83], a[40:43], v[144:147]\n" \
"s_waitcnt lgkmcnt(4)\n" \
"v_exp_f32_e32 v132, v132\n" \
"v_mfma_f32_16x16x32_bf16 v[152:155], a[80:83], a[56:59], v[152:155]\n" \
"buffer_load_dwordx4 v224, %[kvs], s68 offen offset:0 sc1 lds\n" \
"v_mfma_f32_16x16x32_bf16 v[156:159], a[84:87], a[56:59], v[156:159]\n" \
"v_exp_f32_e32 v133, v133\n" \
"v_mfma_f32_16x16x32_bf16 v[148:151], a[84:87], a[40:43], v[148:151]\n" \
"ds_read_b128 a[80:83], v235 offset:4096\n" \
"v_cvt_pk_bf16_f32 v194, v132, v133\n" \
"v_mfma_f32_16x16x32_bf16 v[144:147], a[88:91], a[44:47], v[144:147]\n" \
"s_xor_b32 s70, s70, 0x10000\n" \
"v_exp_f32_e32 v134, v134\n" \
"v_mfma_f32_16x16x32_bf16 v[152:155], a[88:91], a[60:63], v[152:155]\n" \
"ds_read_b128 a[84:87], v235 offset:5120\n" \
"ds_read_b128 a[88:91], v235 offset:6144\n" \
"v_mfma_f32_16x16x32_bf16 v[156:159], a[92:95], a[60:63], v[156:159]\n" \
"v_exp_f32_e32 v135, v135\n" \
"v_mfma_f32_16x16x32_bf16 v[148:151], a[92:95], a[44:47], v[148:151]\n" \
"ds_read_b128 a[92:95], v235 offset:7168\n" \
"v_cvt_pk_bf16_f32 v195, v134, v135\n" \
"v_mfma_f32_16x16x32_bf16 v[64:67], a[96:99], v[200:203], v[64:67]\n" \
"v_exp_f32_e32 v136, v136\n" \
"v_mfma_f32_16x16x32_bf16 v[96:99], a[96:99], v[204:207], v[96:99]\n" \
"buffer_load_dwordx4 v224, %[kvs], s68 offen offset:1024 sc1 lds\n" \
"v_mfma_f32_16x16x32_bf16 v[100:103], a[100:103], v[204:207], v[100:103]\n" \
"v_exp_f32_e32 v137, v137\n" \
"v_mfma_f32_16x16x32_bf16 v[68:71], a[100:103], v[200:203], v[68:71]\n" \
"ds_read_b128 a[96:99], v234 offset:40960\n" \
"v_cvt_pk_bf16_f32 v196, v136, v137\n" \
"v_mfma_f32_16x16x32_bf16 v[72:75], a[104:107], v[200:203], v[72:75]\n" \
"v_exp_f32_e32 v138, v138\n" \
"v_mfma_f32_16x16x32_bf16 v[104:107], a[104:107], v[204:207], v[104:107]\n" \
"ds_read_b128 a[100:103], v234 offset:41984\n" \
"ds_read_b128 a[104:107], v234 offset:43008\n" \
"v_mfma_f32_16x16x32_bf16 v[108:111], a[108:111], v[204:207], v[108:111]\n" \
"v_exp_f32_e32 v139, v139\n" \
"v_mfma_f32_16x16x32_bf16 v[76:79], a[108:111], v[200:203], v[76:79]\n" \
"ds_read_b128 a[108:111], v234 offset:44032\n" \
"v_cvt_pk_bf16_f32 v197, v138, v139\n" \
"v_mfma_f32_16x16x32_bf16 v[80:83], a[112:115], v[200:203], v[80:83]\n" \
"s_waitcnt lgkmcnt(4)\n" \
"v_exp_f32_e32 v140, v140\n" \
"v_mfma_f32_16x16x32_bf16 v[112:115], a[112:115], v[204:207], v[112:115]\n" \
"buffer_load_dwordx4 v224, %[kvs], s68 offen offset:2048 sc1 lds\n" \
"v_mfma_f32_16x16x32_bf16 v[116:119], a[116:119], v[204:207], v[116:119]\n" \
"v_exp_f32_e32 v141, v141\n" \
"v_mfma_f32_16x16x32_bf16 v[84:87], a[116:119], v[200:203], v[84:87]\n" \
"ds_read_b128 a[112:115], v234 offset:45056\n" \
"v_cvt_pk_bf16_f32 v198, v140, v141\n" \
"v_mfma_f32_16x16x32_bf16 v[88:91], a[120:123], v[200:203], v[88:91]\n" \
"s_sub_u32 s73, s73, 1\n" \
"v_exp_f32_e32 v142, v142\n" \
"v_mfma_f32_16x16x32_bf16 v[120:123], a[120:123], v[204:207], v[120:123]\n" \
"ds_read_b128 a[116:119], v234 offset:46080\n" \
"ds_read_b128 a[120:123], v234 offset:47104\n" \
"v_mfma_f32_16x16x32_bf16 v[124:127], a[124:127], v[204:207], v[124:127]\n" \
"v_exp_f32_e32 v143, v143\n" \
"v_mfma_f32_16x16x32_bf16 v[92:95], a[124:127], v[200:203], v[92:95]\n" \
"ds_read_b128 a[124:127], v234 offset:48128\n" \
"v_cvt_pk_bf16_f32 v199, v142, v143\n" \
"v_smfmac_f32_16x16x64_bf16 a[248:251], a[236:239], v[200:207], v248\n" \
"buffer_load_dwordx4 v224, %[kvs], s68 offen offset:3072 sc1 lds\n" \
"v_xor_b32_e32 v234, 0x10000, v234\n" \
"v_xor_b32_e32 v235, 0x10000, v235\n" \
"s_cbranch_scc0 .LATLp2\n" \
".LATLls:\n" \
"s_cmp_eq_u32 %[w4k], 0\n" \
"s_cbranch_scc1 .LATLz0\n" \
"s_waitcnt lgkmcnt(8)\n" \
"v_mfma_f32_16x16x32_bf16 v[128:131], a[64:67], a[32:35], v[176:179]\n" \
"v_exp_f32_e32 v144, v144\n" \
"v_mfma_f32_16x16x32_bf16 v[136:139], a[64:67], a[48:51], v[184:187]\n" \
"ds_read_b128 a[64:67], v234 offset:16384\n" \
"v_mfma_f32_16x16x32_bf16 v[140:143], a[68:71], a[48:51], v[188:191]\n" \
"v_exp_f32_e32 v145, v145\n" \
"v_mfma_f32_16x16x32_bf16 v[132:135], a[68:71], a[32:35], v[180:183]\n" \
"ds_read_b128 a[68:71], v234 offset:17408\n" \
"v_cvt_pk_bf16_f32 v200, v144, v145\n" \
"v_mfma_f32_16x16x32_bf16 v[128:131], a[72:75], a[36:39], v[128:131]\n" \
"v_exp_f32_e32 v146, v146\n" \
"v_mfma_f32_16x16x32_bf16 v[136:139], a[72:75], a[52:55], v[136:139]\n" \
"ds_read_b128 a[72:75], v234 offset:18432\n" \
"v_mfma_f32_16x16x32_bf16 v[140:143], a[76:79], a[52:55], v[140:143]\n" \
"v_exp_f32_e32 v147, v147\n" \
"v_mfma_f32_16x16x32_bf16 v[132:135], a[76:79], a[36:39], v[132:135]\n" \
"ds_read_b128 a[76:79], v234 offset:19456\n" \
"v_cvt_pk_bf16_f32 v201, v146, v147\n" \
"v_mfma_f32_16x16x32_bf16 v[128:131], a[80:83], a[40:43], v[128:131]\n" \
"v_exp_f32_e32 v148, v148\n" \
"v_mfma_f32_16x16x32_bf16 v[136:139], a[80:83], a[56:59], v[136:139]\n" \
"ds_read_b128 a[80:83], v234 offset:20480\n" \
"v_mfma_f32_16x16x32_bf16 v[140:143], a[84:87], a[56:59], v[140:143]\n" \
"v_exp_f32_e32 v149, v149\n" \
"v_mfma_f32_16x16x32_bf16 v[132:135], a[84:87], a[40:43], v[132:135]\n" \
"ds_read_b128 a[84:87], v234 offset:21504\n" \
"v_cvt_pk_bf16_f32 v202, v148, v149\n" \
"v_mfma_f32_16x16x32_bf16 v[128:131], a[88:91], a[44:47], v[128:131]\n" \
"v_exp_f32_e32 v150, v150\n" \
"v_mfma_f32_16x16x32_bf16 v[136:139], a[88:91], a[60:63], v[136:139]\n" \
"ds_read_b128 a[88:91], v234 offset:22528\n" \
"v_mfma_f32_16x16x32_bf16 v[140:143], a[92:95], a[60:63], v[140:143]\n" \
"v_exp_f32_e32 v151, v151\n" \
"v_mfma_f32_16x16x32_bf16 v[132:135], a[92:95], a[44:47], v[132:135]\n" \
"ds_read_b128 a[92:95], v234 offset:23552\n" \
"s_waitcnt vmcnt(0) lgkmcnt(8)\n" \
"s_barrier\n" \
"v_mfma_f32_16x16x32_bf16 v[64:67], a[96:99], v[192:195], v[64:67]\n" \
"v_exp_f32_e32 v152, v152\n" \
"v_mfma_f32_16x16x32_bf16 v[96:99], a[96:99], v[196:199], v[96:99]\n" \
"ds_read_b128 a[96:99], v235 offset:57344\n" \
"v_cvt_pk_bf16_f32 v203, v150, v151\n" \
"v_mfma_f32_16x16x32_bf16 v[100:103], a[100:103], v[196:199], v[100:103]\n" \
"v_exp_f32_e32 v153, v153\n" \
"v_mfma_f32_16x16x32_bf16 v[68:71], a[100:103], v[192:195], v[68:71]\n" \
"ds_read_b128 a[100:103], v235 offset:58368\n" \
"v_cvt_pk_bf16_f32 v204, v152, v153\n" \
"v_mfma_f32_16x16x32_bf16 v[72:75], a[104:107], v[192:195], v[72:75]\n" \
"v_exp_f32_e32 v154, v154\n" \
"v_mfma_f32_16x16x32_bf16 v[104:107], a[104:107], v[196:199], v[104:107]\n" \
"ds_read_b128 a[104:107], v235 offset:59392\n" \
"buffer_atomic_add v252, v253, %[wsr], s79 offen offset:512 sc1\n" \
"v_mfma_f32_16x16x32_bf16 v[108:111], a[108:111], v[196:199], v[108:111]\n" \
"v_exp_f32_e32 v155, v155\n" \
"v_mfma_f32_16x16x32_bf16 v[76:79], a[108:111], v[192:195], v[76:79]\n" \
"ds_read_b128 a[108:111], v235 offset:60416\n" \
"v_cvt_pk_bf16_f32 v205, v154, v155\n" \
"v_mfma_f32_16x16x32_bf16 v[80:83], a[112:115], v[192:195], v[80:83]\n" \
"v_exp_f32_e32 v156, v156\n" \
"v_mfma_f32_16x16x32_bf16 v[112:115], a[112:115], v[196:199], v[112:115]\n" \
"ds_read_b128 a[112:115], v235 offset:61440\n" \
"v_mfma_f32_16x16x32_bf16 v[116:119], a[116:119], v[196:199], v[116:119]\n" \
"v_exp_f32_e32 v157, v157\n" \
"v_mfma_f32_16x16x32_bf16 v[84:87], a[116:119], v[192:195], v[84:87]\n" \
"ds_read_b128 a[116:119], v235 offset:62464\n" \
"v_cvt_pk_bf16_f32 v206, v156, v157\n" \
"v_mfma_f32_16x16x32_bf16 v[88:91], a[120:123], v[192:195], v[88:91]\n" \
"v_exp_f32_e32 v158, v158\n" \
"v_mfma_f32_16x16x32_bf16 v[120:123], a[120:123], v[196:199], v[120:123]\n" \
"ds_read_b128 a[120:123], v235 offset:63488\n" \
"v_mfma_f32_16x16x32_bf16 v[124:127], a[124:127], v[196:199], v[124:127]\n" \
"v_exp_f32_e32 v159, v159\n" \
"v_mfma_f32_16x16x32_bf16 v[92:95], a[124:127], v[192:195], v[92:95]\n" \
"ds_read_b128 a[124:127], v235 offset:64512\n" \
"v_cvt_pk_bf16_f32 v207, v158, v159\n" \
"v_smfmac_f32_16x16x64_bf16 a[248:251], a[236:239], v[192:199], v248\n" \
"s_cmp_eq_i32 %[w4k], 4096\n" \
"s_cbranch_scc1 .LATLmk10\n" \
".LATLmb10:\n" \
"s_waitcnt lgkmcnt(8)\n" \
"v_mfma_f32_16x16x32_bf16 v[144:147], a[64:67], a[32:35], v[176:179]\n" \
"v_exp_f32_e32 v128, v128\n" \
"v_mfma_f32_16x16x32_bf16 v[152:155], a[64:67], a[48:51], v[184:187]\n" \
"ds_read_b128 a[64:67], v234 offset:32768\n" \
"buffer_load_dword v247, v242, %[wsr], s80 offen offset:768 sc1\n" \
"v_mfma_f32_16x16x32_bf16 v[156:159], a[68:71], a[48:51], v[188:191]\n" \
"v_exp_f32_e32 v129, v129\n" \
"v_mfma_f32_16x16x32_bf16 v[148:151], a[68:71], a[32:35], v[180:183]\n" \
"ds_read_b128 a[68:71], v234 offset:33792\n" \
"v_cvt_pk_bf16_f32 v192, v128, v129\n" \
"v_mfma_f32_16x16x32_bf16 v[144:147], a[72:75], a[36:39], v[144:147]\n" \
"v_exp_f32_e32 v130, v130\n" \
"v_mfma_f32_16x16x32_bf16 v[152:155], a[72:75], a[52:55], v[152:155]\n" \
"ds_read_b128 a[72:75], v234 offset:34816\n" \
"v_cmp_le_i32_e64 s[88:89], 0, v225\n" \
"v_mfma_f32_16x16x32_bf16 v[156:159], a[76:79], a[52:55], v[156:159]\n" \
"v_exp_f32_e32 v131, v131\n" \
"v_mfma_f32_16x16x32_bf16 v[148:151], a[76:79], a[36:39], v[148:151]\n" \
"ds_read_b128 a[76:79], v234 offset:35840\n" \
"v_cvt_pk_bf16_f32 v193, v130, v131\n" \
"v_mfma_f32_16x16x32_bf16 v[144:147], a[80:83], a[40:43], v[144:147]\n" \
"v_exp_f32_e32 v132, v132\n" \
"v_mfma_f32_16x16x32_bf16 v[152:155], a[80:83], a[56:59], v[152:155]\n" \
"ds_read_b128 a[80:83], v234 offset:36864\n" \
"v_cmp_le_i32_e64 s[90:91], 1, v225\n" \
"v_mfma_f32_16x16x32_bf16 v[156:159], a[84:87], a[56:59], v[156:159]\n" \
"v_exp_f32_e32 v133, v133\n" \
"v_mfma_f32_16x16x32_bf16 v[148:151], a[84:87], a[40:43], v[148:151]\n" \
"ds_read_b128 a[84:87], v234 offset:37888\n" \
"v_cvt_pk_bf16_f32 v194, v132, v133\n" \
"v_mfma_f32_16x16x32_bf16 v[144:147], a[88:91], a[44:47], v[144:147]\n" \
"v_exp_f32_e32 v134, v134\n" \
"v_mfma_f32_16x16x32_bf16 v[152:155], a[88:91], a[60:63], v[152:155]\n" \
"ds_read_b128 a[88:91], v234 offset:38912\n" \
"v_cmp_le_i32_e64 s[92:93], 2, v225\n" \
"v_mfma_f32_16x16x32_bf16 v[156:159], a[92:95], a[60:63], v[156:159]\n" \
"v_exp_f32_e32 v135, v135\n" \
"v_mfma_f32_16x16x32_bf16 v[148:151], a[92:95], a[44:47], v[148:151]\n" \
"ds_read_b128 a[92:95], v234 offset:39936\n" \
"s_waitcnt lgkmcnt(8)\n" \
"v_mfma_f32_16x16x32_bf16 v[64:67], a[96:99], v[200:203], v[64:67]\n" \
"v_exp_f32_e32 v136, v136\n" \
"v_mfma_f32_16x16x32_bf16 v[96:99], a[96:99], v[204:207], v[96:99]\n" \
"ds_read_b128 a[96:99], v234 offset:8192\n" \
"v_cvt_pk_bf16_f32 v195, v134, v135\n" \
"v_mfma_f32_16x16x32_bf16 v[100:103], a[100:103], v[204:207], v[100:103]\n" \
"v_exp_f32_e32 v137, v137\n" \
"v_mfma_f32_16x16x32_bf16 v[68:71], a[100:103], v[200:203], v[68:71]\n" \
"ds_read_b128 a[100:103], v234 offset:9216\n" \
"v_cvt_pk_bf16_f32 v196, v136, v137\n" \
"v_mfma_f32_16x16x32_bf16 v[72:75], a[104:107], v[200:203], v[72:75]\n" \
"v_exp_f32_e32 v138, v138\n" \
"v_mfma_f32_16x16x32_bf16 v[104:107], a[104:107], v[204:207], v[104:107]\n" \
"ds_read_b128 a[104:107], v234 offset:10240\n" \
"v_cmp_le_i32_e64 s[94:95], 3, v225\n" \
"v_mfma_f32_16x16x32_bf16 v[108:111], a[108:111], v[204:207], v[108:111]\n" \
"v_exp_f32_e32 v139, v139\n" \
"v_mfma_f32_16x16x32_bf16 v[76:79], a[108:111], v[200:203], v[76:79]\n" \
"ds_read_b128 a[108:111], v234 offset:11264\n" \
"v_cvt_pk_bf16_f32 v197, v138, v139\n" \
"v_mfma_f32_16x16x32_bf16 v[80:83], a[112:115], v[200:203], v[80:83]\n" \
"v_exp_f32_e32 v140, v140\n" \
"v_mfma_f32_16x16x32_bf16 v[112:115], a[112:115], v[204:207], v[112:115]\n" \
"ds_read_b128 a[112:115], v234 offset:12288\n" \
"v_mfma_f32_16x16x32_bf16 v[116:119], a[116:119], v[204:207], v[116:119]\n" \
"v_exp_f32_e32 v141, v141\n" \
"v_mfma_f32_16x16x32_bf16 v[84:87], a[116:119], v[200:203], v[84:87]\n" \
"ds_read_b128 a[116:119], v234 offset:13312\n" \
"v_cvt_pk_bf16_f32 v198, v140, v141\n" \
"v_mfma_f32_16x16x32_bf16 v[88:91], a[120:123], v[200:203], v[88:91]\n" \
"v_exp_f32_e32 v142, v142\n" \
"v_mfma_f32_16x16x32_bf16 v[120:123], a[120:123], v[204:207], v[120:123]\n" \
"ds_read_b128 a[120:123], v234 offset:14336\n" \
"v_mfma_f32_16x16x32_bf16 v[124:127], a[124:127], v[204:207], v[124:127]\n" \
"v_exp_f32_e32 v143, v143\n" \
"v_mfma_f32_16x16x32_bf16 v[92:95], a[124:127], v[200:203], v[92:95]\n" \
"ds_read_b128 a[124:127], v234 offset:15360\n" \
"v_cvt_pk_bf16_f32 v199, v142, v143\n" \
"v_smfmac_f32_16x16x64_bf16 a[248:251], a[236:239], v[200:207], v248\n" \
"s_cmp_eq_u32 %[w4k], 4096\n" \
"s_cbranch_scc1 .LATLdw1\n" \
"s_cmp_eq_i32 %[w4k], 8192\n" \
"s_cbranch_scc1 .LATLmk11\n" \
".LATLmb11:\n" \
"s_waitcnt lgkmcnt(8)\n" \
"v_mfma_f32_16x16x32_bf16 v[128:131], a[64:67], a[32:35], v[176:179]\n" \
"v_exp_f32_e32 v144, v144\n" \
"v_mfma_f32_16x16x32_bf16 v[136:139], a[64:67], a[48:51], v[184:187]\n" \
"ds_read_b128 a[64:67], v234 offset:49152\n" \
"v_mfma_f32_16x16x32_bf16 v[140:143], a[68:71], a[48:51], v[188:191]\n" \
"v_exp_f32_e32 v145, v145\n" \
"v_mfma_f32_16x16x32_bf16 v[132:135], a[68:71], a[32:35], v[180:183]\n" \
"ds_read_b128 a[68:71], v234 offset:50176\n" \
"v_cvt_pk_bf16_f32 v200, v144, v145\n" \
"v_mfma_f32_16x16x32_bf16 v[128:131], a[72:75], a[36:39], v[128:131]\n" \
"v_exp_f32_e32 v146, v146\n" \
"v_mfma_f32_16x16x32_bf16 v[136:139], a[72:75], a[52:55], v[136:139]\n" \
"ds_read_b128 a[72:75], v234 offset:51200\n" \
"v_cndmask_b32_e64 v176, v252, v176, s[88:89]\n" \
"v_cndmask_b32_e64 v190, v252, v190, s[92:93]\n" \
"v_mfma_f32_16x16x32_bf16 v[140:143], a[76:79], a[52:55], v[140:143]\n" \
"v_exp_f32_e32 v147, v147\n" \
"v_cndmask_b32_e64 v191, v252, v191, s[94:95]\n" \
"v_mfma_f32_16x16x32_bf16 v[132:135], a[76:79], a[36:39], v[132:135]\n" \
"ds_read_b128 a[76:79], v234 offset:52224\n" \
"v_cvt_pk_bf16_f32 v201, v146, v147\n" \
"v_mfma_f32_16x16x32_bf16 v[128:131], a[80:83], a[40:43], v[128:131]\n" \
"v_exp_f32_e32 v148, v148\n" \
"v_mfma_f32_16x16x32_bf16 v[136:139], a[80:83], a[56:59], v[136:139]\n" \
"ds_read_b128 a[80:83], v234 offset:53248\n" \
"v_cndmask_b32_e64 v177, v252, v177, s[90:91]\n" \
"v_mfma_f32_16x16x32_bf16 v[140:143], a[84:87], a[56:59], v[140:143]\n" \
"v_exp_f32_e32 v149, v149\n" \
"v_mfma_f32_16x16x32_bf16 v[132:135], a[84:87], a[40:43], v[132:135]\n" \
"ds_read_b128 a[84:87], v234 offset:54272\n" \
"v_cvt_pk_bf16_f32 v202, v148, v149\n" \
"v_mfma_f32_16x16x32_bf16 v[128:131], a[88:91], a[44:47], v[128:131]\n" \
"v_exp_f32_e32 v150, v150\n" \
"v_mfma_f32_16x16x32_bf16 v[136:139], a[88:91], a[60:63], v[136:139]\n" \
"ds_read_b128 a[88:91], v234 offset:55296\n" \
"v_cndmask_b32_e64 v178, v252, v178, s[92:93]\n" \
"v_mfma_f32_16x16x32_bf16 v[140:143], a[92:95], a[60:63], v[140:143]\n" \
"v_exp_f32_e32 v151, v151\n" \
"v_mfma_f32_16x16x32_bf16 v[132:135], a[92:95], a[44:47], v[132:135]\n" \
"ds_read_b128 a[92:95], v234 offset:56320\n" \
"s_waitcnt lgkmcnt(8)\n" \
"v_mfma_f32_16x16x32_bf16 v[64:67], a[96:99], v[192:195], v[64:67]\n" \
"v_exp_f32_e32 v152, v152\n" \
"v_mfma_f32_16x16x32_bf16 v[96:99], a[96:99], v[196:199], v[96:99]\n" \
"ds_read_b128 a[96:99], v234 offset:24576\n" \
"v_cvt_pk_bf16_f32 v203, v150, v151\n" \
"v_mfma_f32_16x16x32_bf16 v[100:103], a[100:103], v[196:199], v[100:103]\n" \
"v_exp_f32_e32 v153, v153\n" \
"v_mfma_f32_16x16x32_bf16 v[68:71], a[100:103], v[192:195], v[68:71]\n" \
"ds_read_b128 a[100:103], v234 offset:25600\n" \
"v_cvt_pk_bf16_f32 v204, v152, v153\n" \
"v_mfma_f32_16x16x32_bf16 v[72:75], a[104:107], v[192:195], v[72:75]\n" \
"v_exp_f32_e32 v154, v154\n" \
"v_mfma_f32_16x16x32_bf16 v[104:107], a[104:107], v[196:199], v[104:107]\n" \
"ds_read_b128 a[104:107], v234 offset:26624\n" \
"v_cndmask_b32_e64 v179, v252, v179, s[94:95]\n" \
"v_mfma_f32_16x16x32_bf16 v[108:111], a[108:111], v[196:199], v[108:111]\n" \
"v_exp_f32_e32 v155, v155\n" \
"v_mfma_f32_16x16x32_bf16 v[76:79], a[108:111], v[192:195], v[76:79]\n" \
"ds_read_b128 a[108:111], v234 offset:27648\n" \
"v_cvt_pk_bf16_f32 v205, v154, v155\n" \
"v_mfma_f32_16x16x32_bf16 v[80:83], a[112:115], v[192:195], v[80:83]\n" \
"v_exp_f32_e32 v156, v156\n" \
"v_mfma_f32_16x16x32_bf16 v[112:115], a[112:115], v[196:199], v[112:115]\n" \
"ds_read_b128 a[112:115], v234 offset:28672\n" \
"v_cndmask_b32_e64 v188, v252, v188, s[88:89]\n" \
"v_mfma_f32_16x16x32_bf16 v[116:119], a[116:119], v[196:199], v[116:119]\n" \
"v_exp_f32_e32 v157, v157\n" \
"v_mfma_f32_16x16x32_bf16 v[84:87], a[116:119], v[192:195], v[84:87]\n" \
"ds_read_b128 a[116:119], v234 offset:29696\n" \
"v_cvt_pk_bf16_f32 v206, v156, v157\n" \
"v_mfma_f32_16x16x32_bf16 v[88:91], a[120:123], v[192:195], v[88:91]\n" \
"v_exp_f32_e32 v158, v158\n" \
"v_mfma_f32_16x16x32_bf16 v[120:123], a[120:123], v[196:199], v[120:123]\n" \
"ds_read_b128 a[120:123], v234 offset:30720\n" \
"v_cndmask_b32_e64 v189, v252, v189, s[90:91]\n" \
"v_mfma_f32_16x16x32_bf16 v[124:127], a[124:127], v[196:199], v[124:127]\n" \
"v_exp_f32_e32 v159, v159\n" \
"v_mfma_f32_16x16x32_bf16 v[92:95], a[124:127], v[192:195], v[92:95]\n" \
"ds_read_b128 a[124:127], v234 offset:31744\n" \
"v_cvt_pk_bf16_f32 v207, v158, v159\n" \
"v_smfmac_f32_16x16x64_bf16 a[248:251], a[236:239], v[192:199], v248\n" \
"s_cmp_eq_u32 %[w4k], 8192\n" \
"s_cbranch_scc1 .LATLdw2\n" \
"s_waitcnt lgkmcnt(8)\n" \
"v_mfma_f32_16x16x32_bf16 v[144:147], a[64:67], a[32:35], v[176:179]\n" \
"v_exp_f32_e32 v128, v128\n" \
"v_mfma_f32_16x16x32_bf16 v[152:155], a[64:67], a[48:51], v[184:187]\n" \
"v_exp_f32_e32 v129, v129\n" \
"v_mfma_f32_16x16x32_bf16 v[156:159], a[68:71], a[48:51], v[188:191]\n" \
"v_exp_f32_e32 v130, v130\n" \
"v_mfma_f32_16x16x32_bf16 v[144:147], a[72:75], a[36:39], v[144:147]\n" \
"v_cvt_pk_bf16_f32 v192, v128, v129\n" \
"v_mfma_f32_16x16x32_bf16 v[152:155], a[72:75], a[52:55], v[152:155]\n" \
"v_exp_f32_e32 v131, v131\n" \
"v_mfma_f32_16x16x32_bf16 v[156:159], a[76:79], a[52:55], v[156:159]\n" \
"v_exp_f32_e32 v132, v132\n" \
"v_mfma_f32_16x16x32_bf16 v[144:147], a[80:83], a[40:43], v[144:147]\n" \
"v_cvt_pk_bf16_f32 v193, v130, v131\n" \
"v_mfma_f32_16x16x32_bf16 v[152:155], a[80:83], a[56:59], v[152:155]\n" \
"v_exp_f32_e32 v133, v133\n" \
"v_mfma_f32_16x16x32_bf16 v[156:159], a[84:87], a[56:59], v[156:159]\n" \
"v_exp_f32_e32 v134, v134\n" \
"v_mfma_f32_16x16x32_bf16 v[144:147], a[88:91], a[44:47], v[144:147]\n" \
"v_cvt_pk_bf16_f32 v194, v132, v133\n" \
"v_mfma_f32_16x16x32_bf16 v[152:155], a[88:91], a[60:63], v[152:155]\n" \
"v_exp_f32_e32 v135, v135\n" \
"v_mfma_f32_16x16x32_bf16 v[156:159], a[92:95], a[60:63], v[156:159]\n" \
"s_waitcnt vmcnt(0) lgkmcnt(0)\n" \
"v_exp_f32_e32 v136, v136\n" \
"v_mfma_f32_16x16x32_bf16 v[64:67], a[96:99], v[200:203], v[64:67]\n" \
"v_cvt_pk_bf16_f32 v195, v134, v135\n" \
"v_mfma_f32_16x16x32_bf16 v[96:99], a[96:99], v[204:207], v[96:99]\n" \
"ds_read_b128 a[96:99], v234 offset:40960\n" \
"v_mfma_f32_16x16x32_bf16 v[100:103], a[100:103], v[204:207], v[100:103]\n" \
"v_exp_f32_e32 v137, v137\n" \
"v_mfma_f32_16x16x32_bf16 v[68:71], a[100:103], v[200:203], v[68:71]\n" \
"ds_read_b128 a[100:103], v234 offset:41984\n" \
"v_mfma_f32_16x16x32_bf16 v[72:75], a[104:107], v[200:203], v[72:75]\n" \
"v_exp_f32_e32 v138, v138\n" \
"v_mfma_f32_16x16x32_bf16 v[104:107], a[104:107], v[204:207], v[104:107]\n" \
"ds_read_b128 a[104:107], v234 offset:43008\n" \
"v_cvt_pk_bf16_f32 v196, v136, v137\n" \
"v_mfma_f32_16x16x32_bf16 v[108:111], a[108:111], v[204:207], v[108:111]\n" \
"v_exp_f32_e32 v139, v139\n" \
"v_mfma_f32_16x16x32_bf16 v[76:79], a[108:111], v[200:203], v[76:79]\n" \
"ds_read_b128 a[108:111], v234 offset:44032\n" \
"v_mfma_f32_16x16x32_bf16 v[80:83], a[112:115], v[200:203], v[80:83]\n" \
"v_exp_f32_e32 v140, v140\n" \
"v_mfma_f32_16x16x32_bf16 v[112:115], a[112:115], v[204:207], v[112:115]\n" \
"ds_read_b128 a[112:115], v234 offset:45056\n" \
"v_cvt_pk_bf16_f32 v197, v138, v139\n" \
"v_mfma_f32_16x16x32_bf16 v[116:119], a[116:119], v[204:207], v[116:119]\n" \
"v_exp_f32_e32 v141, v141\n" \
"v_mfma_f32_16x16x32_bf16 v[84:87], a[116:119], v[200:203], v[84:87]\n" \
"ds_read_b128 a[116:119], v234 offset:46080\n" \
"v_mfma_f32_16x16x32_bf16 v[88:91], a[120:123], v[200:203], v[88:91]\n" \
"v_exp_f32_e32 v142, v142\n" \
"v_mfma_f32_16x16x32_bf16 v[120:123], a[120:123], v[204:207], v[120:123]\n" \
"ds_read_b128 a[120:123], v234 offset:47104\n" \
"v_cvt_pk_bf16_f32 v198, v140, v141\n" \
"v_mfma_f32_16x16x32_bf16 v[124:127], a[124:127], v[204:207], v[124:127]\n" \
"v_exp_f32_e32 v143, v143\n" \
"v_mfma_f32_16x16x32_bf16 v[92:95], a[124:127], v[200:203], v[92:95]\n" \
"ds_read_b128 a[124:127], v234 offset:48128\n" \
"v_cvt_pk_bf16_f32 v199, v142, v143\n" \
"v_smfmac_f32_16x16x64_bf16 a[248:251], a[236:239], v[200:207], v248\n" \
".LATLdr3:\n" \
"s_waitcnt lgkmcnt(4)\n" \
"v_mfma_f32_16x16x32_bf16 v[64:67], a[96:99], v[192:195], v[64:67]\n" \
"v_mov_b32_e32 v178, 0\n" \
"v_mov_b32_e32 v179, 0\n" \
"v_exp_f32_e32 v144, v144\n" \
"v_mfma_f32_16x16x32_bf16 v[96:99], a[96:99], v[196:199], v[96:99]\n" \
"ds_read_b128 a[96:99], v234 offset:57344\n" \
"v_exp_f32_e32 v145, v145\n" \
"v_mfma_f32_16x16x32_bf16 v[100:103], a[100:103], v[196:199], v[100:103]\n" \
"s_xor_b32 s70, s77, 4096\n" \
"v_exp_f32_e32 v146, v146\n" \
"v_mfma_f32_16x16x32_bf16 v[68:71], a[100:103], v[192:195], v[68:71]\n" \
"s_xor_b32 s71, s77, 262144\n" \
"ds_read_b128 a[100:103], v234 offset:58368\n" \
"v_cvt_pk_bf16_f32 v176, v144, v145\n" \
"v_mfma_f32_16x16x32_bf16 v[72:75], a[104:107], v[192:195], v[72:75]\n" \
"s_xor_b32 s72, s77, 266240\n" \
"v_exp_f32_e32 v147, v147\n" \
"v_mfma_f32_16x16x32_bf16 v[104:107], a[104:107], v[196:199], v[104:107]\n" \
"ds_read_b128 a[104:107], v234 offset:59392\n" \
"v_cvt_pk_bf16_f32 v177, v146, v147\n" \
"v_mfma_f32_16x16x32_bf16 v[108:111], a[108:111], v[196:199], v[108:111]\n" \
"v_exp_f32_e32 v152, v152\n" \
"v_mfma_f32_16x16x32_bf16 v[76:79], a[108:111], v[192:195], v[76:79]\n" \
"s_waitcnt lgkmcnt(3)\n" \
"ds_read_b128 a[108:111], v234 offset:60416\n" \
"v_exp_f32_e32 v153, v153\n" \
"v_mfma_f32_16x16x32_bf16 v[80:83], a[112:115], v[192:195], v[80:83]\n" \
"v_exp_f32_e32 v154, v154\n" \
"v_cvt_pk_bf16_f32 v180, v152, v153\n" \
"v_mfma_f32_16x16x32_bf16 v[112:115], a[112:115], v[196:199], v[112:115]\n" \
"ds_read_b128 a[112:115], v234 offset:61440\n" \
"v_exp_f32_e32 v155, v155\n" \
"v_mfma_f32_16x16x32_bf16 v[116:119], a[116:119], v[196:199], v[116:119]\n" \
"v_exp_f32_e32 v156, v156\n" \
"v_cvt_pk_bf16_f32 v181, v154, v155\n" \
"v_mfma_f32_16x16x32_bf16 v[84:87], a[116:119], v[192:195], v[84:87]\n" \
"ds_read_b128 a[116:119], v234 offset:62464\n" \
"v_exp_f32_e32 v157, v157\n" \
"v_mfma_f32_16x16x32_bf16 v[88:91], a[120:123], v[192:195], v[88:91]\n" \
"v_exp_f32_e32 v158, v158\n" \
"v_mfma_f32_16x16x32_bf16 v[120:123], a[120:123], v[196:199], v[120:123]\n" \
"ds_read_b128 a[120:123], v234 offset:63488\n" \
"v_cvt_pk_bf16_f32 v182, v156, v157\n" \
"v_cmp_eq_u32_e32 vcc, v246, v247\n" \
"v_mfma_f32_16x16x32_bf16 v[124:127], a[124:127], v[196:199], v[124:127]\n" \
"v_exp_f32_e32 v159, v159\n" \
"v_mfma_f32_16x16x32_bf16 v[92:95], a[124:127], v[192:195], v[92:95]\n" \
"ds_read_b128 a[124:127], v234 offset:64512\n" \
"v_cvt_pk_bf16_f32 v183, v158, v159\n" \
"v_smfmac_f32_16x16x64_bf16 a[248:251], a[236:239], v[192:199], v248\n" \
"s_and_b32 s73, vcc_lo, 0xffff\n" \
".LATLfin:\n" \
"s_nop 1\n" \
"v_smfmac_f32_16x16x64_bf16 a[248:251], a[236:239], v[176:183], v248\n" \
"s_waitcnt lgkmcnt(4)\n" \
"v_mfma_f32_16x16x32_bf16 v[96:99], a[96:99], v[180:183], v[96:99]\n" \
"s_mul_i32 s61, %[db], 0x2020\n" \
"v_add_u32_e32 v224, 131072, v241\n" \
"s_and_b32 s61, s61, 0x3c020\n" \
"v_mfma_f32_16x16x32_bf16 v[100:103], a[100:103], v[180:183], v[100:103]\n" \
"s_mul_i32 s62, %[h], 0x84\n" \
"s_and_b32 s62, s62, 0xc1c\n" \
"s_or_b32 s61, s61, s62\n" \
"v_mfma_f32_16x16x32_bf16 v[104:107], a[104:107], v[180:183], v[104:107]\n" \
"v_and_b32_e32 v251, 60, v240\n" \
"v_lshlrev_b32_e32 v251, 25, v251\n" \
"v_mfma_f32_16x16x32_bf16 v[108:111], a[108:111], v[180:183], v[108:111]\n" \
"s_waitcnt lgkmcnt(0)\n" \
"v_lshl_or_b32 v251, v240, 12, v251\n" \
"v_mfma_f32_16x16x32_bf16 v[112:115], a[112:115], v[180:183], v[112:115]\n" \
"v_accvgpr_read_b32 v128, a248\n" \
"v_accvgpr_read_b32 v129, a249\n" \
"v_mfma_f32_16x16x32_bf16 v[116:119], a[116:119], v[180:183], v[116:119]\n" \
"v_accvgpr_read_b32 v130, a250\n" \
"v_accvgpr_read_b32 v131, a251\n" \
"v_mfma_f32_16x16x32_bf16 v[120:123], a[120:123], v[180:183], v[120:123]\n" \
"v_add_f32_e32 v132, v128, v129\n" \
"v_add_f32_e32 v133, v130, v131\n" \
"v_mfma_f32_16x16x32_bf16 v[124:127], a[124:127], v[180:183], v[124:127]\n" \
"v_rcp_f32_e32 v128, v132\n" \
"v_mfma_f32_16x16x32_bf16 v[64:67], a[96:99], v[176:179], v[64:67]\n" \
"v_rcp_f32_e32 v130, v133\n" \
"v_mfma_f32_16x16x32_bf16 v[68:71], a[100:103], v[176:179], v[68:71]\n" \
"v_max_f32_e32 v134, v132, v133\n" \
"v_cmp_nge_f32_e32 vcc, s75, v134\n" \
"v_mfma_f32_16x16x32_bf16 v[72:75], a[104:107], v[176:179], v[72:75]\n" \
"s_or_b64 s[86:87], s[86:87], vcc\n" \
"v_cmp_u_f32_e32 vcc, v132, v133\n" \
"s_or_b64 s[86:87], s[86:87], vcc\n" \
"v_mfma_f32_16x16x32_bf16 v[76:79], a[108:111], v[176:179], v[76:79]\n" \
"v_mov_b32_e32 v134, 0\n" \
"v_mov_b32_e32 v129, v128\n" \
"v_mfma_f32_16x16x32_bf16 v[80:83], a[112:115], v[176:179], v[80:83]\n" \
"v_mov_b32_e32 v131, v130\n" \
"v_mfma_f32_16x16x32_bf16 v[84:87], a[116:119], v[176:179], v[84:87]\n" \
"v_mfma_f32_16x16x32_bf16 v[88:91], a[120:123], v[176:179], v[88:91]\n" \
"v_mfma_f32_16x16x32_bf16 v[92:95], a[124:127], v[176:179], v[92:95]\n" \
"v_pk_mul_f32 v[96:97], v[96:97], v[130:131]\n" \
"v_pk_mul_f32 v[98:99], v[98:99], v[130:131]\n" \
"v_pk_mul_f32 v[100:101], v[100:101], v[130:131]\n" \
"v_pk_mul_f32 v[102:103], v[102:103], v[130:131]\n" \
"v_cvt_pk_bf16_f32 v96, v96, v97\n" \
"v_cvt_pk_bf16_f32 v97, v98, v99\n" \
"v_cvt_pk_bf16_f32 v98, v100, v101\n" \
"v_cvt_pk_bf16_f32 v99, v102, v103\n" \
"buffer_store_dwordx4 v[96:99], v249, %[wsr], s66 offen offset:0 sc1\n" \
"v_pk_maximum3_f16 v134, v134, v96, v97\n" \
"v_pk_maximum3_f16 v134, v134, v98, v99\n" \
"v_pk_mul_f32 v[104:105], v[104:105], v[130:131]\n" \
"v_pk_mul_f32 v[106:107], v[106:107], v[130:131]\n" \
"v_pk_mul_f32 v[108:109], v[108:109], v[130:131]\n" \
"v_pk_mul_f32 v[110:111], v[110:111], v[130:131]\n" \
"v_cvt_pk_bf16_f32 v100, v104, v105\n" \
"v_cvt_pk_bf16_f32 v101, v106, v107\n" \
"v_cvt_pk_bf16_f32 v102, v108, v109\n" \
"v_cvt_pk_bf16_f32 v103, v110, v111\n" \
"buffer_store_dwordx4 v[100:103], v249, %[wsr], s66 offen offset:1024 sc1\n" \
"v_pk_maximum3_f16 v134, v134, v100, v101\n" \
"v_pk_maximum3_f16 v134, v134, v102, v103\n" \
"v_pk_mul_f32 v[112:113], v[112:113], v[130:131]\n" \
"v_pk_mul_f32 v[114:115], v[114:115], v[130:131]\n" \
"v_pk_mul_f32 v[116:117], v[116:117], v[130:131]\n" \
"v_pk_mul_f32 v[118:119], v[118:119], v[130:131]\n" \
"v_cvt_pk_bf16_f32 v104, v112, v113\n" \
"v_cvt_pk_bf16_f32 v105, v114, v115\n" \
"v_cvt_pk_bf16_f32 v106, v116, v117\n" \
"v_cvt_pk_bf16_f32 v107, v118, v119\n" \
"buffer_store_dwordx4 v[104:107], v249, %[wsr], s66 offen offset:2048 sc1\n" \
"v_pk_maximum3_f16 v134, v134, v104, v105\n" \
"v_pk_maximum3_f16 v134, v134, v106, v107\n" \
"v_pk_mul_f32 v[120:121], v[120:121], v[130:131]\n" \
"v_pk_mul_f32 v[122:123], v[122:123], v[130:131]\n" \
"v_pk_mul_f32 v[124:125], v[124:125], v[130:131]\n" \
"v_pk_mul_f32 v[126:127], v[126:127], v[130:131]\n" \
"v_cvt_pk_bf16_f32 v108, v120, v121\n" \
"v_cvt_pk_bf16_f32 v109, v122, v123\n" \
"v_cvt_pk_bf16_f32 v110, v124, v125\n" \
"v_cvt_pk_bf16_f32 v111, v126, v127\n" \
"buffer_store_dwordx4 v[108:111], v249, %[wsr], s66 offen offset:3072 sc1\n" \
"v_pk_maximum3_f16 v134, v134, v108, v109\n" \
"v_pk_maximum3_f16 v134, v134, v110, v111\n" \
"v_pk_mul_f32 v[64:65], v[64:65], v[128:129]\n" \
"v_pk_mul_f32 v[66:67], v[66:67], v[128:129]\n" \
"v_pk_mul_f32 v[68:69], v[68:69], v[128:129]\n" \
"v_pk_mul_f32 v[70:71], v[70:71], v[128:129]\n" \
"v_cvt_pk_bf16_f32 v112, v64, v65\n" \
"v_cvt_pk_bf16_f32 v113, v66, v67\n" \
"v_cvt_pk_bf16_f32 v114, v68, v69\n" \
"v_cvt_pk_bf16_f32 v115, v70, v71\n" \
"buffer_store_dwordx4 v[112:115], v241, %[wsr], s66 offen offset:0 sc1\n" \
"v_pk_maximum3_f16 v134, v134, v112, v113\n" \
"v_pk_maximum3_f16 v134, v134, v114, v115\n" \
"v_pk_mul_f32 v[72:73], v[72:73], v[128:129]\n" \
"v_pk_mul_f32 v[74:75], v[74:75], v[128:129]\n" \
"v_pk_mul_f32 v[76:77], v[76:77], v[128:129]\n" \
"v_pk_mul_f32 v[78:79], v[78:79], v[128:129]\n" \
"v_cvt_pk_bf16_f32 v116, v72, v73\n" \
"v_cvt_pk_bf16_f32 v117, v74, v75\n" \
"v_cvt_pk_bf16_f32 v118, v76, v77\n" \
"v_cvt_pk_bf16_f32 v119, v78, v79\n" \
"buffer_store_dwordx4 v[116:119], v241, %[wsr], s66 offen offset:1024 sc1\n" \
"v_pk_maximum3_f16 v134, v134, v116, v117\n" \
"v_pk_maximum3_f16 v134, v134, v118, v119\n" \
"v_pk_mul_f32 v[80:81], v[80:81], v[128:129]\n" \
"v_pk_mul_f32 v[82:83], v[82:83], v[128:129]\n" \
"v_pk_mul_f32 v[84:85], v[84:85], v[128:129]\n" \
"v_pk_mul_f32 v[86:87], v[86:87], v[128:129]\n" \
"v_cvt_pk_bf16_f32 v120, v80, v81\n" \
"v_cvt_pk_bf16_f32 v121, v82, v83\n" \
"v_cvt_pk_bf16_f32 v122, v84, v85\n" \
"v_cvt_pk_bf16_f32 v123, v86, v87\n" \
"buffer_store_dwordx4 v[120:123], v241, %[wsr], s66 offen offset:2048 sc1\n" \
"v_pk_maximum3_f16 v134, v134, v120, v121\n" \
"v_pk_maximum3_f16 v134, v134, v122, v123\n" \
"v_pk_mul_f32 v[88:89], v[88:89], v[128:129]\n" \
"v_pk_mul_f32 v[90:91], v[90:91], v[128:129]\n" \
"v_pk_mul_f32 v[92:93], v[92:93], v[128:129]\n" \
"v_pk_mul_f32 v[94:95], v[94:95], v[128:129]\n" \
"v_cvt_pk_bf16_f32 v124, v88, v89\n" \
"v_cvt_pk_bf16_f32 v125, v90, v91\n" \
"v_cvt_pk_bf16_f32 v126, v92, v93\n" \
"v_cvt_pk_bf16_f32 v127, v94, v95\n" \
"buffer_store_dwordx4 v[124:127], v241, %[wsr], s66 offen offset:3072 sc1\n" \
"v_pk_maximum3_f16 v134, v134, v124, v125\n" \
"v_pk_maximum3_f16 v134, v134, v126, v127\n" \
"v_cmp_u_f16_sdwa vcc, v134, v134 src0_sel:WORD_0 src1_sel:WORD_1\n" \
"s_or_b64 s[86:87], s[86:87], vcc\n" \
"s_cmp_lg_u64 s[86:87], 0\n" \
"s_cbranch_scc1 .LATLslowB\n" \
".LATLbackB:\n" \
"s_cmp_lg_u32 s73, 0\n" \
"s_cbranch_scc1 .LATLlt\n" \
"s_cmp_lt_u32 %[w4k], 0\n" \
"s_cbranch_scc1 .LATLpp\n" \
"s_waitcnt vmcnt(16)\n" \
".LATLppb:\n" \
"s_barrier\n" \
"ds_read_b128 a[64:67], v224 offset:0\n" \
"ds_read_b128 a[68:71], v224 offset:1024\n" \
"ds_read_b128 a[72:75], v224 offset:2048\n" \
"ds_read_b128 a[76:79], v224 offset:3072\n" \
"ds_read_b128 a[80:83], v224 offset:4096\n" \
"ds_read_b128 a[84:87], v224 offset:5120\n" \
"ds_read_b128 a[88:91], v224 offset:6144\n" \
"ds_read_b128 a[92:95], v224 offset:7168\n" \
"ds_read_b128 a[96:99], v224 offset:8192\n" \
"ds_read_b128 a[100:103], v224 offset:9216\n" \
"s_waitcnt lgkmcnt(8)\n" \
"v_mfma_f32_16x16x32_bf16 v[64:67], a[128:131], a[64:67], 0\n" \
"v_mfma_f32_16x16x32_bf16 v[68:71], a[132:135], a[64:67], 0\n" \
"ds_read_b128 a[104:107], v224 offset:10240\n" \
"v_mfma_f32_16x16x32_bf16 v[64:67], a[136:139], a[68:71], v[64:67]\n" \
"v_mfma_f32_16x16x32_bf16 v[68:71], a[140:143], a[68:71], v[68:71]\n" \
"s_waitcnt lgkmcnt(7)\n" \
"ds_read_b128 a[108:111], v224 offset:11264\n" \
"v_mfma_f32_16x16x32_bf16 v[64:67], a[144:147], a[72:75], v[64:67]\n" \
"s_add_u32 m0, s82, 0x8000\n" \
"v_mfma_f32_16x16x32_bf16 v[68:71], a[148:151], a[72:75], v[68:71]\n" \
"s_add_u32 s60, s72, 0x0\n" \
"ds_read_b128 a[112:115], v224 offset:12288\n" \
"v_mfma_f32_16x16x32_bf16 v[64:67], a[152:155], a[76:79], v[64:67]\n" \
"buffer_load_dwordx4 v241, %[wsr], s60 offen offset:0 sc1 lds\n" \
"v_mfma_f32_16x16x32_bf16 v[68:71], a[156:159], a[76:79], v[68:71]\n" \
"s_waitcnt lgkmcnt(7)\n" \
"ds_read_b128 a[116:119], v224 offset:13312\n" \
"v_mfma_f32_16x16x32_bf16 v[64:67], a[160:163], a[80:83], v[64:67]\n" \
"v_mfma_f32_16x16x32_bf16 v[68:71], a[164:167], a[80:83], v[68:71]\n" \
"ds_read_b128 a[120:123], v224 offset:14336\n" \
"v_mfma_f32_16x16x32_bf16 v[64:67], a[168:171], a[84:87], v[64:67]\n" \
"buffer_load_dwordx4 v241, %[wsr], s60 offen offset:1024 sc1 lds\n" \
"v_mfma_f32_16x16x32_bf16 v[68:71], a[172:175], a[84:87], v[68:71]\n" \
"s_waitcnt lgkmcnt(7)\n" \
"ds_read_b128 a[124:127], v224 offset:15360\n" \
"v_mfma_f32_16x16x32_bf16 v[64:67], a[176:179], a[88:91], v[64:67]\n" \
"v_mfma_f32_16x16x32_bf16 v[68:71], a[180:183], a[88:91], v[68:71]\n" \
"ds_read_b128 a[64:67], v224 offset:16384\n" \
"v_mfma_f32_16x16x32_bf16 v[64:67], a[184:187], a[92:95], v[64:67]\n" \
"buffer_load_dwordx4 v241, %[wsr], s60 offen offset:2048 sc1 lds\n" \
"v_mfma_f32_16x16x32_bf16 v[68:71], a[188:191], a[92:95], v[68:71]\n" \
"s_waitcnt lgkmcnt(7)\n" \
"ds_read_b128 a[68:71], v224 offset:17408\n" \
"v_mfma_f32_16x16x32_bf16 v[64:67], a[192:195], a[96:99], v[64:67]\n" \
"v_mfma_f32_16x16x32_bf16 v[68:71], a[196:199], a[96:99], v[68:71]\n" \
"ds_read_b128 a[72:75], v224 offset:18432\n" \
"v_mfma_f32_16x16x32_bf16 v[64:67], a[200:203], a[100:103], v[64:67]\n" \
"buffer_load_dwordx4 v241, %[wsr], s60 offen offset:3072 sc1 lds\n" \
"v_mfma_f32_16x16x32_bf16 v[68:71], a[204:207], a[100:103], v[68:71]\n" \
"s_waitcnt lgkmcnt(7)\n" \
"ds_read_b128 a[76:79], v224 offset:19456\n" \
"v_mfma_f32_16x16x32_bf16 v[64:67], a[208:211], a[104:107], v[64:67]\n" \
"s_add_u32 m0, s82, 0xc000\n" \
"v_mfma_f32_16x16x32_bf16 v[68:71], a[212:215], a[104:107], v[68:71]\n" \
"s_add_u32 s60, s72, 0x8000\n" \
"ds_read_b128 a[80:83], v224 offset:20480\n" \
"v_mfma_f32_16x16x32_bf16 v[64:67], a[216:219], a[108:111], v[64:67]\n" \
"buffer_load_dwordx4 v241, %[wsr], s60 offen offset:0 sc1 lds\n" \
"v_mfma_f32_16x16x32_bf16 v[68:71], a[220:223], a[108:111], v[68:71]\n" \
"s_waitcnt lgkmcnt(7)\n" \
"ds_read_b128 a[84:87], v224 offset:21504\n" \
"v_mfma_f32_16x16x32_bf16 v[64:67], a[224:227], a[112:115], v[64:67]\n" \
"s_waitcnt vmcnt(13)\n" \
"v_mfma_f32_16x16x32_bf16 v[68:71], a[228:231], a[112:115], v[68:71]\n" \
"ds_read_b128 a[88:91], v224 offset:22528\n" \
"v_mfma_f32_16x16x32_bf16 v[64:67], a[232:235], a[116:119], v[64:67]\n" \
"buffer_load_dwordx4 v241, %[wsr], s60 offen offset:1024 sc1 lds\n" \
"v_mfma_f32_16x16x32_bf16 v[68:71], v[0:3], a[116:119], v[68:71]\n" \
"s_waitcnt lgkmcnt(7)\n" \
"ds_read_b128 a[92:95], v224 offset:23552\n" \
"v_mfma_f32_16x16x32_bf16 v[64:67], v[4:7], a[120:123], v[64:67]\n" \
"v_mfma_f32_16x16x32_bf16 v[68:71], v[8:11], a[120:123], v[68:71]\n" \
"ds_read_b128 a[96:99], v224 offset:24576\n" \
"v_mfma_f32_16x16x32_bf16 v[64:67], v[12:15], a[124:127], v[64:67]\n" \
"buffer_load_dwordx4 v241, %[wsr], s60 offen offset:2048 sc1 lds\n" \
"v_mfma_f32_16x16x32_bf16 v[68:71], v[16:19], a[124:127], v[68:71]\n" \
"s_waitcnt lgkmcnt(7)\n" \
"ds_read_b128 a[100:103], v224 offset:25600\n" \
"s_waitcnt vmcnt(7)\n" \
"v_mfma_f32_16x16x32_bf16 v[64:67], v[20:23], a[64:67], v[64:67]\n" \
"buffer_atomic_add v252, v251, %[wsr], s61 offen offset:896 sc1\n" \
"v_mfma_f32_16x16x32_bf16 v[68:71], v[24:27], a[64:67], v[68:71]\n" \
"ds_read_b128 a[104:107], v224 offset:26624\n" \
"v_mfma_f32_16x16x32_bf16 v[64:67], v[28:31], a[68:71], v[64:67]\n" \
"buffer_load_dwordx4 v241, %[wsr], s60 offen offset:3072 sc1 lds\n" \
"v_mfma_f32_16x16x32_bf16 v[68:71], v[32:35], a[68:71], v[68:71]\n" \
"s_waitcnt lgkmcnt(7)\n" \
"ds_read_b128 a[108:111], v224 offset:27648\n" \
"v_mfma_f32_16x16x32_bf16 v[64:67], v[36:39], a[72:75], v[64:67]\n" \
"s_add_u32 m0, s67, 0x8000\n" \
"s_and_b32 s73, %[pair], 31\n" \
"s_lshl_b32 s73, s73, 16\n" \
"v_mfma_f32_16x16x32_bf16 v[68:71], v[40:43], a[72:75], v[68:71]\n" \
"s_add_u32 s60, s77, 0x0\n" \
"ds_read_b128 a[112:115], v224 offset:28672\n" \
"s_lshl2_add_u32 s73, %[w4k], s73\n" \
"v_mfma_f32_16x16x32_bf16 v[64:67], v[44:47], a[76:79], v[64:67]\n" \
"buffer_load_dwordx4 v241, %[wsr], s60 offen offset:0 sc1 lds\n" \
"v_mfma_f32_16x16x32_bf16 v[68:71], v[48:51], a[76:79], v[68:71]\n" \
"s_waitcnt lgkmcnt(7)\n" \
"ds_read_b128 a[116:119], v224 offset:29696\n" \
"v_lshrrev_b32_e32 v201, 2, v240\n" \
"v_mfma_f32_16x16x32_bf16 v[64:67], v[52:55], a[80:83], v[64:67]\n" \
"v_and_b32_e32 v203, 3, v240\n" \
"v_lshlrev_b32_e32 v200, 13, v201\n" \
"v_mfma_f32_16x16x32_bf16 v[68:71], v[56:59], a[80:83], v[68:71]\n" \
"ds_read_b128 a[120:123], v224 offset:30720\n" \
"v_lshl_add_u32 v200, v203, 4, v200\n" \
"v_mfma_f32_16x16x32_bf16 v[64:67], v[60:63], a[84:87], v[64:67]\n" \
"buffer_load_dwordx4 v241, %[wsr], s60 offen offset:1024 sc1 lds\n" \
"v_mfma_f32_16x16x32_bf16 v[68:71], a[0:3], a[84:87], v[68:71]\n" \
"s_waitcnt lgkmcnt(7)\n" \
"ds_read_b128 a[124:127], v224 offset:31744\n" \
"v_lshlrev_b32_e32 v202, 9, v203\n" \
"v_mfma_f32_16x16x32_bf16 v[64:67], a[4:7], a[88:91], v[64:67]\n" \
"v_lshl_add_u32 v202, v201, 2, v202\n" \
"v_add_u32_e32 v202, s67, v202\n" \
"v_mfma_f32_16x16x32_bf16 v[68:71], a[8:11], a[88:91], v[68:71]\n" \
"s_andn2_b32 s63, %[pair], 31\n" \
"v_add_u32_e32 v201, s67, v241\n" \
"s_lshl_b32 s63, s63, 13\n" \
"v_mfma_f32_16x16x32_bf16 v[64:67], a[12:15], a[92:95], v[64:67]\n" \
"buffer_load_dwordx4 v241, %[wsr], s60 offen offset:2048 sc1 lds\n" \
"v_mfma_f32_16x16x32_bf16 v[68:71], a[16:19], a[92:95], v[68:71]\n" \
"s_waitcnt lgkmcnt(6)\n" \
"s_lshr_b32 s68, s73, 8\n" \
"s_add_u32 s63, s63, s68\n" \
"v_mfma_f32_16x16x32_bf16 v[64:67], a[20:23], a[96:99], v[64:67]\n" \
"s_add_u32 s63, s63, 2097152\n" \
"s_add_u32 s79, s63, 0x20000\n" \
"s_lshr_b32 s68, %[pair], 5\n" \
"v_mfma_f32_16x16x32_bf16 v[68:71], a[24:27], a[96:99], v[68:71]\n" \
"s_lshl_b32 s68, s68, 7\n" \
"s_lshl1_add_u32 s73, s73, s68\n" \
"v_lshrrev_b32_e32 v204, 3, v240\n" \
"v_mfma_f32_16x16x32_bf16 v[64:67], a[28:31], a[100:103], v[64:67]\n" \
"buffer_load_dwordx4 v241, %[wsr], s60 offen offset:3072 sc1 lds\n" \
"v_mfma_f32_16x16x32_bf16 v[68:71], v[160:163], a[100:103], v[68:71]\n" \
"s_waitcnt lgkmcnt(4)\n" \
"v_and_b32_e32 v205, 7, v240\n" \
"v_lshlrev_b32_e32 v204, 11, v204\n" \
"v_mfma_f32_16x16x32_bf16 v[64:67], v[164:167], a[104:107], v[64:67]\n" \
"s_waitcnt lgkmcnt(0)\n" \
"s_barrier\n" \
"v_mfma_f32_16x16x32_bf16 v[68:71], v[168:171], a[104:107], v[68:71]\n" \
"ds_read_b128 a[64:67], v235 offset:0\n" \
"v_lshl_add_u32 v204, v205, 4, v204\n" \
"v_mfma_f32_16x16x32_bf16 v[64:67], v[172:175], a[108:111], v[64:67]\n" \
"ds_read_b128 a[68:71], v235 offset:1024\n" \
"v_add_u32_e32 v204, s73, v204\n" \
"v_mfma_f32_16x16x32_bf16 v[68:71], a[240:243], a[108:111], v[68:71]\n" \
"ds_read_b128 a[72:75], v235 offset:2048\n" \
"v_add_u32_e32 v205, 0x4000, v204\n" \
"v_mfma_f32_16x16x32_bf16 v[64:67], v[226:229], a[112:115], v[64:67]\n" \
"ds_read_b128 a[76:79], v235 offset:3072\n" \
"v_and_b32_e32 v225, 15, v240\n" \
"v_mfma_f32_16x16x32_bf16 v[68:71], v[230:233], a[112:115], v[68:71]\n" \
"ds_read_b128 a[80:83], v235 offset:4096\n" \
"v_lshlrev_b32_e32 v225, 11, v225\n" \
"v_mfma_f32_16x16x32_bf16 v[64:67], v[236:239], a[116:119], v[64:67]\n" \
"ds_read_b128 a[84:87], v235 offset:5120\n" \
"v_and_b32_e32 v206, 16, v240\n" \
"v_mfma_f32_16x16x32_bf16 v[68:71], a[244:247], a[116:119], v[68:71]\n" \
"ds_read_b128 a[88:91], v235 offset:6144\n" \
"v_lshl_add_u32 v225, v206, 1, v225\n" \
"v_mfma_f32_16x16x32_bf16 v[64:67], v[208:211], a[120:123], v[64:67]\n" \
"ds_read_b128 a[92:95], v235 offset:7168\n" \
"s_add_u32 m0, s67, 0xc000\n" \
"v_mfma_f32_16x16x32_bf16 v[68:71], v[212:215], a[120:123], v[68:71]\n" \
"s_add_u32 s60, s77, 0x8000\n" \
"ds_read_b128 a[96:99], v235 offset:8192\n" \
"v_lshrrev_b32_e32 v206, 5, v240\n" \
"v_mfma_f32_16x16x32_bf16 v[64:67], v[216:219], a[124:127], v[64:67]\n" \
"buffer_load_dwordx4 v241, %[wsr], s60 offen offset:0 sc1 lds\n" \
"v_mfma_f32_16x16x32_bf16 v[68:71], v[220:223], a[124:127], v[68:71]\n" \
"s_waitcnt lgkmcnt(7)\n" \
"ds_read_b128 a[100:103], v235 offset:9216\n" \
"v_lshl_add_u32 v225, v206, 4, v225\n" \
"v_mfma_f32_16x16x32_bf16 v[72:75], a[128:131], a[64:67], 0\n" \
"s_andn2_b32 s73, %[h], 1\n" \
"s_lshl_b32 s73, s73, 16\n" \
"s_and_b32 s68, %[pair], 7\n" \
"v_mfma_f32_16x16x32_bf16 v[76:79], a[132:135], a[64:67], 0\n" \
"s_mul_i32 s68, s68, 0x8080\n" \
"ds_read_b128 a[104:107], v235 offset:10240\n" \
"s_and_b32 s68, s68, 0x18200\n" \
"v_mfma_f32_16x16x32_bf16 v[72:75], a[136:139], a[68:71], v[72:75]\n" \
"buffer_load_dwordx4 v241, %[wsr], s60 offen offset:1024 sc1 lds\n" \
"v_mfma_f32_16x16x32_bf16 v[76:79], a[140:143], a[68:71], v[76:79]\n" \
"s_waitcnt lgkmcnt(7)\n" \
"ds_read_b128 a[108:111], v235 offset:11264\n" \
"s_or_b32 s73, s73, s68\n" \
"v_mfma_f32_16x16x32_bf16 v[72:75], a[144:147], a[72:75], v[72:75]\n" \
"s_and_b32 s68, %[h], 1\n" \
"s_lshl_b32 s68, s68, 8\n" \
"s_or_b32 s73, s73, s68\n" \
"v_mfma_f32_16x16x32_bf16 v[76:79], a[148:151], a[72:75], v[76:79]\n" \
"s_lshr_b32 s68, %[w4k], 6\n" \
"ds_read_b128 a[112:115], v235 offset:12288\n" \
"v_cvt_pk_bf16_f32 v64, v64, v65\n" \
"v_mfma_f32_16x16x32_bf16 v[72:75], a[152:155], a[76:79], v[72:75]\n" \
"buffer_load_dwordx4 v241, %[wsr], s60 offen offset:2048 sc1 lds\n" \
"v_mfma_f32_16x16x32_bf16 v[76:79], a[156:159], a[76:79], v[76:79]\n" \
"s_waitcnt lgkmcnt(7)\n" \
"ds_read_b128 a[116:119], v235 offset:13312\n" \
"v_cvt_pk_bf16_f32 v65, v66, v67\n" \
"v_mfma_f32_16x16x32_bf16 v[72:75], a[160:163], a[80:83], v[72:75]\n" \
"s_or_b32 s73, s73, s68\n" \
"v_cvt_pk_bf16_f32 v66, v68, v69\n" \
"v_cvt_pk_bf16_f32 v67, v70, v71\n" \
"v_mfma_f32_16x16x32_bf16 v[76:79], a[164:167], a[80:83], v[76:79]\n" \
"ds_read_b128 a[120:123], v235 offset:14336\n" \
"v_add_u32_e32 v225, s73, v225\n" \
"v_mfma_f32_16x16x32_bf16 v[72:75], a[168:171], a[84:87], v[72:75]\n" \
"buffer_load_dwordx4 v241, %[wsr], s60 offen offset:3072 sc1 lds\n" \
"v_mfma_f32_16x16x32_bf16 v[76:79], a[172:175], a[84:87], v[76:79]\n" \
"s_xor_b32 s83, %[po], 16384\n" \
"s_waitcnt lgkmcnt(7)\n" \
"ds_read_b128 a[124:127], v235 offset:15360\n" \
"v_mfma_f32_16x16x32_bf16 v[72:75], a[176:179], a[88:91], v[72:75]\n" \
"buffer_store_dwordx4 v[64:67], v241, %[wsr], s83 offen offset:0 sc1\n" \
"v_add_u32_e32 v206, 0x200000, v225\n" \
"v_mfma_f32_16x16x32_bf16 v[76:79], a[180:183], a[88:91], v[76:79]\n" \
"ds_read_b128 a[64:67], v235 offset:16384\n" \
"v_mfma_f32_16x16x32_bf16 v[72:75], a[184:187], a[92:95], v[72:75]\n" \
"v_mfma_f32_16x16x32_bf16 v[76:79], a[188:191], a[92:95], v[76:79]\n" \
"s_waitcnt lgkmcnt(7)\n" \
"ds_read_b128 a[68:71], v235 offset:17408\n" \
"v_mfma_f32_16x16x32_bf16 v[72:75], a[192:195], a[96:99], v[72:75]\n" \
"v_mfma_f32_16x16x32_bf16 v[76:79], a[196:199], a[96:99], v[76:79]\n" \
"ds_read_b128 a[72:75], v235 offset:18432\n" \
"v_mfma_f32_16x16x32_bf16 v[72:75], a[200:203], a[100:103], v[72:75]\n" \
"v_mfma_f32_16x16x32_bf16 v[76:79], a[204:207], a[100:103], v[76:79]\n" \
"s_waitcnt lgkmcnt(7)\n" \
"ds_read_b128 a[76:79], v235 offset:19456\n" \
"v_mfma_f32_16x16x32_bf16 v[72:75], a[208:211], a[104:107], v[72:75]\n" \
"v_mfma_f32_16x16x32_bf16 v[76:79], a[212:215], a[104:107], v[76:79]\n" \
"ds_read_b128 a[80:83], v235 offset:20480\n" \
"v_mfma_f32_16x16x32_bf16 v[72:75], a[216:219], a[108:111], v[72:75]\n" \
"v_mfma_f32_16x16x32_bf16 v[76:79], a[220:223], a[108:111], v[76:79]\n" \
"s_waitcnt lgkmcnt(7)\n" \
"ds_read_b128 a[84:87], v235 offset:21504\n" \
"v_mfma_f32_16x16x32_bf16 v[72:75], a[224:227], a[112:115], v[72:75]\n" \
"v_mfma_f32_16x16x32_bf16 v[76:79], a[228:231], a[112:115], v[76:79]\n" \
"ds_read_b128 a[88:91], v235 offset:22528\n" \
"v_mfma_f32_16x16x32_bf16 v[72:75], a[232:235], a[116:119], v[72:75]\n" \
"v_mfma_f32_16x16x32_bf16 v[76:79], v[0:3], a[116:119], v[76:79]\n" \
"s_waitcnt lgkmcnt(7)\n" \
"ds_read_b128 a[92:95], v235 offset:23552\n" \
"v_mfma_f32_16x16x32_bf16 v[72:75], v[4:7], a[120:123], v[72:75]\n" \
"v_mfma_f32_16x16x32_bf16 v[76:79], v[8:11], a[120:123], v[76:79]\n" \
"ds_read_b128 a[96:99], v235 offset:24576\n" \
"v_mfma_f32_16x16x32_bf16 v[72:75], v[12:15], a[124:127], v[72:75]\n" \
"v_mfma_f32_16x16x32_bf16 v[76:79], v[16:19], a[124:127], v[76:79]\n" \
"s_waitcnt lgkmcnt(7)\n" \
"ds_read_b128 a[100:103], v235 offset:25600\n" \
"v_mfma_f32_16x16x32_bf16 v[72:75], v[20:23], a[64:67], v[72:75]\n" \
"v_mfma_f32_16x16x32_bf16 v[76:79], v[24:27], a[64:67], v[76:79]\n" \
"ds_read_b128 a[104:107], v235 offset:26624\n" \
"v_mfma_f32_16x16x32_bf16 v[72:75], v[28:31], a[68:71], v[72:75]\n" \
"v_mfma_f32_16x16x32_bf16 v[76:79], v[32:35], a[68:71], v[76:79]\n" \
"s_waitcnt lgkmcnt(7)\n" \
"ds_read_b128 a[108:111], v235 offset:27648\n" \
"v_mfma_f32_16x16x32_bf16 v[72:75], v[36:39], a[72:75], v[72:75]\n" \
"v_mfma_f32_16x16x32_bf16 v[76:79], v[40:43], a[72:75], v[76:79]\n" \
"ds_read_b128 a[112:115], v235 offset:28672\n" \
"v_mfma_f32_16x16x32_bf16 v[72:75], v[44:47], a[76:79], v[72:75]\n" \
"v_mfma_f32_16x16x32_bf16 v[76:79], v[48:51], a[76:79], v[76:79]\n" \
"s_waitcnt lgkmcnt(7)\n" \
"ds_read_b128 a[116:119], v235 offset:29696\n" \
"v_mfma_f32_16x16x32_bf16 v[72:75], v[52:55], a[80:83], v[72:75]\n" \
"v_mfma_f32_16x16x32_bf16 v[76:79], v[56:59], a[80:83], v[76:79]\n" \
"ds_read_b128 a[120:123], v235 offset:30720\n" \
"v_mfma_f32_16x16x32_bf16 v[72:75], v[60:63], a[84:87], v[72:75]\n" \
"v_mfma_f32_16x16x32_bf16 v[76:79], a[0:3], a[84:87], v[76:79]\n" \
"s_waitcnt lgkmcnt(7)\n" \
"ds_read_b128 a[124:127], v235 offset:31744\n" \
"v_mfma_f32_16x16x32_bf16 v[72:75], a[4:7], a[88:91], v[72:75]\n" \
"v_mfma_f32_16x16x32_bf16 v[76:79], a[8:11], a[88:91], v[76:79]\n" \
"v_mfma_f32_16x16x32_bf16 v[72:75], a[12:15], a[92:95], v[72:75]\n" \
"v_mfma_f32_16x16x32_bf16 v[76:79], a[16:19], a[92:95], v[76:79]\n" \
"s_waitcnt lgkmcnt(6)\n" \
"v_mfma_f32_16x16x32_bf16 v[72:75], a[20:23], a[96:99], v[72:75]\n" \
"v_mfma_f32_16x16x32_bf16 v[76:79], a[24:27], a[96:99], v[76:79]\n" \
"v_mfma_f32_16x16x32_bf16 v[72:75], a[28:31], a[100:103], v[72:75]\n" \
"v_mfma_f32_16x16x32_bf16 v[76:79], v[160:163], a[100:103], v[76:79]\n" \
"s_waitcnt lgkmcnt(4)\n" \
"v_mfma_f32_16x16x32_bf16 v[72:75], v[164:167], a[104:107], v[72:75]\n" \
"s_waitcnt vmcnt(1) lgkmcnt(0)\n" \
"s_barrier\n" \
"v_mfma_f32_16x16x32_bf16 v[76:79], v[168:171], a[104:107], v[76:79]\n" \
"ds_read_b128 a[64:67], v235 offset:32768\n" \
"v_mfma_f32_16x16x32_bf16 v[72:75], v[172:175], a[108:111], v[72:75]\n" \
"ds_read_b128 a[68:71], v235 offset:33792\n" \
"v_mfma_f32_16x16x32_bf16 v[76:79], a[240:243], a[108:111], v[76:79]\n" \
"ds_read_b128 a[72:75], v235 offset:34816\n" \
"v_mfma_f32_16x16x32_bf16 v[72:75], v[226:229], a[112:115], v[72:75]\n" \
"ds_read_b128 a[76:79], v235 offset:35840\n" \
"v_mfma_f32_16x16x32_bf16 v[76:79], v[230:233], a[112:115], v[76:79]\n" \
"ds_read_b128 a[80:83], v235 offset:36864\n" \
"v_mfma_f32_16x16x32_bf16 v[72:75], v[236:239], a[116:119], v[72:75]\n" \
"ds_read_b128 a[84:87], v235 offset:37888\n" \
"v_mfma_f32_16x16x32_bf16 v[76:79], a[244:247], a[116:119], v[76:79]\n" \
"ds_read_b128 a[88:91], v235 offset:38912\n" \
"v_mfma_f32_16x16x32_bf16 v[72:75], v[208:211], a[120:123], v[72:75]\n" \
"ds_read_b128 a[92:95], v235 offset:39936\n" \
"v_mfma_f32_16x16x32_bf16 v[76:79], v[212:215], a[120:123], v[76:79]\n" \
"ds_read_b128 a[96:99], v235 offset:40960\n" \
"v_mfma_f32_16x16x32_bf16 v[72:75], v[216:219], a[124:127], v[72:75]\n" \
"buffer_load_dword v247, v242, %[wsr], s80 offen offset:768 sc1\n" \
"v_mfma_f32_16x16x32_bf16 v[76:79], v[220:223], a[124:127], v[76:79]\n" \
"s_waitcnt lgkmcnt(7)\n" \
"ds_read_b128 a[100:103], v235 offset:41984\n" \
"v_mfma_f32_16x16x32_bf16 v[80:83], a[128:131], a[64:67], 0\n" \
"v_mfma_f32_16x16x32_bf16 v[84:87], a[132:135], a[64:67], 0\n" \
"ds_read_b128 a[104:107], v235 offset:43008\n" \
"v_mfma_f32_16x16x32_bf16 v[80:83], a[136:139], a[68:71], v[80:83]\n" \
"v_mfma_f32_16x16x32_bf16 v[84:87], a[140:143], a[68:71], v[84:87]\n" \
"s_waitcnt lgkmcnt(7)\n" \
"ds_read_b128 a[108:111], v235 offset:44032\n" \
"v_mfma_f32_16x16x32_bf16 v[80:83], a[144:147], a[72:75], v[80:83]\n" \
"v_mfma_f32_16x16x32_bf16 v[84:87], a[148:151], a[72:75], v[84:87]\n" \
"ds_read_b128 a[112:115], v235 offset:45056\n" \
"v_cvt_pk_bf16_f32 v72, v72, v73\n" \
"v_mfma_f32_16x16x32_bf16 v[80:83], a[152:155], a[76:79], v[80:83]\n" \
"v_cvt_pk_bf16_f32 v73, v74, v75\n" \
"v_cvt_pk_bf16_f32 v74, v76, v77\n" \
"v_mfma_f32_16x16x32_bf16 v[84:87], a[156:159], a[76:79], v[84:87]\n" \
"s_waitcnt lgkmcnt(7)\n" \
"ds_read_b128 a[116:119], v235 offset:46080\n" \
"v_cvt_pk_bf16_f32 v75, v78, v79\n" \
"v_mfma_f32_16x16x32_bf16 v[80:83], a[160:163], a[80:83], v[80:83]\n" \
"v_mfma_f32_16x16x32_bf16 v[84:87], a[164:167], a[80:83], v[84:87]\n" \
"ds_read_b128 a[120:123], v235 offset:47104\n" \
"v_mfma_f32_16x16x32_bf16 v[80:83], a[168:171], a[84:87], v[80:83]\n" \
"v_mfma_f32_16x16x32_bf16 v[84:87], a[172:175], a[84:87], v[84:87]\n" \
"s_xor_b32 s83, %[po], 32768\n" \
"s_waitcnt lgkmcnt(7)\n" \
"ds_read_b128 a[124:127], v235 offset:48128\n" \
"v_mfma_f32_16x16x32_bf16 v[80:83], a[176:179], a[88:91], v[80:83]\n" \
"buffer_store_dwordx4 v[72:75], v241, %[wsr], s83 offen offset:0 sc1\n" \
"v_mfma_f32_16x16x32_bf16 v[84:87], a[180:183], a[88:91], v[84:87]\n" \
"ds_read_b128 a[64:67], v235 offset:49152\n" \
"v_mfma_f32_16x16x32_bf16 v[80:83], a[184:187], a[92:95], v[80:83]\n" \
"v_mfma_f32_16x16x32_bf16 v[84:87], a[188:191], a[92:95], v[84:87]\n" \
"s_waitcnt lgkmcnt(7)\n" \
"ds_read_b128 a[68:71], v235 offset:50176\n" \
"v_mfma_f32_16x16x32_bf16 v[80:83], a[192:195], a[96:99], v[80:83]\n" \
"v_mfma_f32_16x16x32_bf16 v[84:87], a[196:199], a[96:99], v[84:87]\n" \
"ds_read_b128 a[72:75], v235 offset:51200\n" \
"v_mfma_f32_16x16x32_bf16 v[80:83], a[200:203], a[100:103], v[80:83]\n" \
"v_mfma_f32_16x16x32_bf16 v[84:87], a[204:207], a[100:103], v[84:87]\n" \
"s_waitcnt lgkmcnt(7)\n" \
"ds_read_b128 a[76:79], v235 offset:52224\n" \
"v_mfma_f32_16x16x32_bf16 v[80:83], a[208:211], a[104:107], v[80:83]\n" \
"v_mfma_f32_16x16x32_bf16 v[84:87], a[212:215], a[104:107], v[84:87]\n" \
"ds_read_b128 a[80:83], v235 offset:53248\n" \
"v_mfma_f32_16x16x32_bf16 v[80:83], a[216:219], a[108:111], v[80:83]\n" \
"v_mfma_f32_16x16x32_bf16 v[84:87], a[220:223], a[108:111], v[84:87]\n" \
"s_waitcnt lgkmcnt(7)\n" \
"ds_read_b128 a[84:87], v235 offset:54272\n" \
"v_mfma_f32_16x16x32_bf16 v[80:83], a[224:227], a[112:115], v[80:83]\n" \
"v_mfma_f32_16x16x32_bf16 v[84:87], a[228:231], a[112:115], v[84:87]\n" \
"ds_read_b128 a[88:91], v235 offset:55296\n" \
"v_mfma_f32_16x16x32_bf16 v[80:83], a[232:235], a[116:119], v[80:83]\n" \
"v_mfma_f32_16x16x32_bf16 v[84:87], v[0:3], a[116:119], v[84:87]\n" \
"s_waitcnt lgkmcnt(7)\n" \
"ds_read_b128 a[92:95], v235 offset:56320\n" \
"v_mfma_f32_16x16x32_bf16 v[80:83], v[4:7], a[120:123], v[80:83]\n" \
"v_mfma_f32_16x16x32_bf16 v[84:87], v[8:11], a[120:123], v[84:87]\n" \
"ds_read_b128 a[96:99], v235 offset:57344\n" \
"v_mfma_f32_16x16x32_bf16 v[80:83], v[12:15], a[124:127], v[80:83]\n" \
"v_mfma_f32_16x16x32_bf16 v[84:87], v[16:19], a[124:127], v[84:87]\n" \
"s_waitcnt lgkmcnt(7)\n" \
"ds_read_b128 a[100:103], v235 offset:58368\n" \
"v_mfma_f32_16x16x32_bf16 v[80:83], v[20:23], a[64:67], v[80:83]\n" \
"v_mfma_f32_16x16x32_bf16 v[84:87], v[24:27], a[64:67], v[84:87]\n" \
"ds_read_b128 a[104:107], v235 offset:59392\n" \
"v_mfma_f32_16x16x32_bf16 v[80:83], v[28:31], a[68:71], v[80:83]\n" \
"v_mfma_f32_16x16x32_bf16 v[84:87], v[32:35], a[68:71], v[84:87]\n" \
"s_waitcnt lgkmcnt(7)\n" \
"ds_read_b128 a[108:111], v235 offset:60416\n" \
"v_mfma_f32_16x16x32_bf16 v[80:83], v[36:39], a[72:75], v[80:83]\n" \
"s_waitcnt vmcnt(1)\n" \
"v_cmp_eq_u32_e32 vcc, v246, v247\n" \
"v_mfma_f32_16x16x32_bf16 v[84:87], v[40:43], a[72:75], v[84:87]\n" \
"ds_read_b128 a[112:115], v235 offset:61440\n" \
"v_mfma_f32_16x16x32_bf16 v[80:83], v[44:47], a[76:79], v[80:83]\n" \
"s_and_b32 s88, vcc_hi, s81\n" \
"s_cbranch_scc1 .LATLsb\n" \
".LATLsbb:\n" \
"v_mfma_f32_16x16x32_bf16 v[84:87], v[48:51], a[76:79], v[84:87]\n" \
"s_waitcnt lgkmcnt(7)\n" \
"ds_read_b128 a[116:119], v235 offset:62464\n" \
"v_mfma_f32_16x16x32_bf16 v[80:83], v[52:55], a[80:83], v[80:83]\n" \
"s_add_u32 m0, s67, 0x0\n" \
"v_mfma_f32_16x16x32_bf16 v[84:87], v[56:59], a[80:83], v[84:87]\n" \
"s_add_u32 s60, s70, 0x800000\n" \
"ds_read_b128 a[120:123], v235 offset:63488\n" \
"v_mfma_f32_16x16x32_bf16 v[80:83], v[60:63], a[84:87], v[80:83]\n" \
"buffer_load_dwordx4 v241, %[wsr], s60 offen offset:0 sc1 lds\n" \
"v_mfma_f32_16x16x32_bf16 v[84:87], a[0:3], a[84:87], v[84:87]\n" \
"s_waitcnt lgkmcnt(7)\n" \
"ds_read_b128 a[124:127], v235 offset:64512\n" \
"v_mfma_f32_16x16x32_bf16 v[80:83], a[4:7], a[88:91], v[80:83]\n" \
"v_mfma_f32_16x16x32_bf16 v[84:87], a[8:11], a[88:91], v[84:87]\n" \
"ds_read_b128 a[64:67], v234 offset:32768\n" \
"v_mfma_f32_16x16x32_bf16 v[80:83], a[12:15], a[92:95], v[80:83]\n" \
"buffer_load_dwordx4 v241, %[wsr], s60 offen offset:1024 sc1 lds\n" \
"v_mfma_f32_16x16x32_bf16 v[84:87], a[16:19], a[92:95], v[84:87]\n" \
"s_waitcnt lgkmcnt(7)\n" \
"ds_read_b128 a[68:71], v234 offset:33792\n" \
"v_mfma_f32_16x16x32_bf16 v[80:83], a[20:23], a[96:99], v[80:83]\n" \
"v_mfma_f32_16x16x32_bf16 v[84:87], a[24:27], a[96:99], v[84:87]\n" \
"ds_read_b128 a[72:75], v234 offset:34816\n" \
"v_mfma_f32_16x16x32_bf16 v[80:83], a[28:31], a[100:103], v[80:83]\n" \
"buffer_load_dwordx4 v241, %[wsr], s60 offen offset:2048 sc1 lds\n" \
"v_mfma_f32_16x16x32_bf16 v[84:87], v[160:163], a[100:103], v[84:87]\n" \
"s_waitcnt lgkmcnt(7)\n" \
"ds_read_b128 a[76:79], v234 offset:35840\n" \
"v_mfma_f32_16x16x32_bf16 v[80:83], v[164:167], a[104:107], v[80:83]\n" \
"v_mfma_f32_16x16x32_bf16 v[84:87], v[168:171], a[104:107], v[84:87]\n" \
"ds_read_b128 a[80:83], v234 offset:36864\n" \
"v_mfma_f32_16x16x32_bf16 v[80:83], v[172:175], a[108:111], v[80:83]\n" \
"buffer_load_dwordx4 v241, %[wsr], s60 offen offset:3072 sc1 lds\n" \
"v_mfma_f32_16x16x32_bf16 v[84:87], a[240:243], a[108:111], v[84:87]\n" \
"s_waitcnt lgkmcnt(7)\n" \
"ds_read_b128 a[84:87], v234 offset:37888\n" \
"v_mfma_f32_16x16x32_bf16 v[80:83], v[226:229], a[112:115], v[80:83]\n" \
"s_add_u32 m0, s67, 0x4000\n" \
"v_mfma_f32_16x16x32_bf16 v[84:87], v[230:233], a[112:115], v[84:87]\n" \
"s_add_u32 s60, s70, 0x808000\n" \
"ds_read_b128 a[88:91], v234 offset:38912\n" \
"v_mfma_f32_16x16x32_bf16 v[80:83], v[236:239], a[116:119], v[80:83]\n" \
"buffer_load_dwordx4 v241, %[wsr], s60 offen offset:0 sc1 lds\n" \
"v_mfma_f32_16x16x32_bf16 v[84:87], a[244:247], a[116:119], v[84:87]\n" \
"s_waitcnt lgkmcnt(7)\n" \
"ds_read_b128 a[92:95], v234 offset:39936\n" \
"v_mfma_f32_16x16x32_bf16 v[80:83], v[208:211], a[120:123], v[80:83]\n" \
"v_mfma_f32_16x16x32_bf16 v[84:87], v[212:215], a[120:123], v[84:87]\n" \
"ds_read_b128 a[96:99], v234 offset:40960\n" \
"v_mfma_f32_16x16x32_bf16 v[80:83], v[216:219], a[124:127], v[80:83]\n" \
"buffer_load_dwordx4 v241, %[wsr], s60 offen offset:1024 sc1 lds\n" \
"v_mfma_f32_16x16x32_bf16 v[84:87], v[220:223], a[124:127], v[84:87]\n" \
"s_waitcnt lgkmcnt(7)\n" \
"ds_read_b128 a[100:103], v234 offset:41984\n" \
"v_mfma_f32_16x16x32_bf16 v[112:115], a[128:131], a[64:67], 0\n" \
"v_mfma_f32_16x16x32_bf16 v[116:119], a[132:135], a[64:67], 0\n" \
"ds_read_b128 a[104:107], v234 offset:43008\n" \
"v_mfma_f32_16x16x32_bf16 v[112:115], a[136:139], a[68:71], v[112:115]\n" \
"buffer_load_dwordx4 v241, %[wsr], s60 offen offset:2048 sc1 lds\n" \
"v_mfma_f32_16x16x32_bf16 v[116:119], a[140:143], a[68:71], v[116:119]\n" \
"s_waitcnt lgkmcnt(7)\n" \
"ds_read_b128 a[108:111], v234 offset:44032\n" \
"v_mfma_f32_16x16x32_bf16 v[112:115], a[144:147], a[72:75], v[112:115]\n" \
"v_mfma_f32_16x16x32_bf16 v[116:119], a[148:151], a[72:75], v[116:119]\n" \
"ds_read_b128 a[112:115], v234 offset:45056\n" \
"v_cvt_pk_bf16_f32 v80, v80, v81\n" \
"v_mfma_f32_16x16x32_bf16 v[112:115], a[152:155], a[76:79], v[112:115]\n" \
"buffer_load_dwordx4 v241, %[wsr], s60 offen offset:3072 sc1 lds\n" \
"v_mfma_f32_16x16x32_bf16 v[116:119], a[156:159], a[76:79], v[116:119]\n" \
"s_waitcnt lgkmcnt(7)\n" \
"ds_read_b128 a[116:119], v234 offset:46080\n" \
"v_cvt_pk_bf16_f32 v81, v82, v83\n" \
"v_mfma_f32_16x16x32_bf16 v[112:115], a[160:163], a[80:83], v[112:115]\n" \
"s_add_u32 m0, %[w4k], 0x20000\n" \
"v_cvt_pk_bf16_f32 v82, v84, v85\n" \
"v_cvt_pk_bf16_f32 v83, v86, v87\n" \
"v_mfma_f32_16x16x32_bf16 v[116:119], a[164:167], a[80:83], v[116:119]\n" \
"s_add_u32 s60, s71, 0x800000\n" \
"ds_read_b128 a[120:123], v234 offset:47104\n" \
"v_mfma_f32_16x16x32_bf16 v[112:115], a[168:171], a[84:87], v[112:115]\n" \
"buffer_load_dwordx4 v241, %[wsr], s60 offen offset:0 sc1 lds\n" \
"v_mfma_f32_16x16x32_bf16 v[116:119], a[172:175], a[84:87], v[116:119]\n" \
"s_xor_b32 s83, %[po], 49152\n" \
"s_waitcnt lgkmcnt(7)\n" \
"ds_read_b128 a[124:127], v234 offset:48128\n" \
"v_mfma_f32_16x16x32_bf16 v[112:115], a[176:179], a[88:91], v[112:115]\n" \
"buffer_store_dwordx4 v[80:83], v241, %[wsr], s83 offen offset:0 sc1\n" \
"v_mfma_f32_16x16x32_bf16 v[116:119], a[180:183], a[88:91], v[116:119]\n" \
"ds_read_b128 a[64:67], v234 offset:49152\n" \
"v_mfma_f32_16x16x32_bf16 v[112:115], a[184:187], a[92:95], v[112:115]\n" \
"buffer_load_dwordx4 v241, %[wsr], s60 offen offset:1024 sc1 lds\n" \
"v_mfma_f32_16x16x32_bf16 v[116:119], a[188:191], a[92:95], v[116:119]\n" \
"s_waitcnt lgkmcnt(7)\n" \
"ds_read_b128 a[68:71], v234 offset:50176\n" \
"v_mfma_f32_16x16x32_bf16 v[112:115], a[192:195], a[96:99], v[112:115]\n" \
"v_mfma_f32_16x16x32_bf16 v[116:119], a[196:199], a[96:99], v[116:119]\n" \
"ds_read_b128 a[72:75], v234 offset:51200\n" \
"v_mfma_f32_16x16x32_bf16 v[112:115], a[200:203], a[100:103], v[112:115]\n" \
"buffer_load_dwordx4 v241, %[wsr], s60 offen offset:2048 sc1 lds\n" \
"v_mfma_f32_16x16x32_bf16 v[116:119], a[204:207], a[100:103], v[116:119]\n" \
"s_waitcnt lgkmcnt(7)\n" \
"ds_read_b128 a[76:79], v234 offset:52224\n" \
"v_mfma_f32_16x16x32_bf16 v[112:115], a[208:211], a[104:107], v[112:115]\n" \
"v_mfma_f32_16x16x32_bf16 v[116:119], a[212:215], a[104:107], v[116:119]\n" \
"ds_read_b128 a[80:83], v234 offset:53248\n" \
"v_mfma_f32_16x16x32_bf16 v[112:115], a[216:219], a[108:111], v[112:115]\n" \
"buffer_load_dwordx4 v241, %[wsr], s60 offen offset:3072 sc1 lds\n" \
"v_mfma_f32_16x16x32_bf16 v[116:119], a[220:223], a[108:111], v[116:119]\n" \
"s_waitcnt lgkmcnt(7)\n" \
"ds_read_b128 a[84:87], v234 offset:54272\n" \
"v_mfma_f32_16x16x32_bf16 v[112:115], a[224:227], a[112:115], v[112:115]\n" \
"s_add_u32 m0, %[w4k], 0x24000\n" \
"v_mfma_f32_16x16x32_bf16 v[116:119], a[228:231], a[112:115], v[116:119]\n" \
"s_add_u32 s60, s71, 0x808000\n" \
"ds_read_b128 a[88:91], v234 offset:55296\n" \
"v_mfma_f32_16x16x32_bf16 v[112:115], a[232:235], a[116:119], v[112:115]\n" \
"buffer_load_dwordx4 v241, %[wsr], s60 offen offset:0 sc1 lds\n" \
"v_mfma_f32_16x16x32_bf16 v[116:119], v[0:3], a[116:119], v[116:119]\n" \
"s_waitcnt lgkmcnt(7)\n" \
"ds_read_b128 a[92:95], v234 offset:56320\n" \
"v_mfma_f32_16x16x32_bf16 v[112:115], v[4:7], a[120:123], v[112:115]\n" \
"v_mfma_f32_16x16x32_bf16 v[116:119], v[8:11], a[120:123], v[116:119]\n" \
"ds_read_b128 a[96:99], v234 offset:57344\n" \
"v_mfma_f32_16x16x32_bf16 v[112:115], v[12:15], a[124:127], v[112:115]\n" \
"buffer_load_dwordx4 v241, %[wsr], s60 offen offset:1024 sc1 lds\n" \
"v_mfma_f32_16x16x32_bf16 v[116:119], v[16:19], a[124:127], v[116:119]\n" \
"s_waitcnt lgkmcnt(7)\n" \
"ds_read_b128 a[100:103], v234 offset:58368\n" \
"v_mfma_f32_16x16x32_bf16 v[112:115], v[20:23], a[64:67], v[112:115]\n" \
"v_mfma_f32_16x16x32_bf16 v[116:119], v[24:27], a[64:67], v[116:119]\n" \
"ds_read_b128 a[104:107], v234 offset:59392\n" \
"v_mfma_f32_16x16x32_bf16 v[112:115], v[28:31], a[68:71], v[112:115]\n" \
"buffer_load_dwordx4 v241, %[wsr], s60 offen offset:2048 sc1 lds\n" \
"v_mfma_f32_16x16x32_bf16 v[116:119], v[32:35], a[68:71], v[116:119]\n" \
"s_waitcnt lgkmcnt(7)\n" \
"ds_read_b128 a[108:111], v234 offset:60416\n" \
"v_mfma_f32_16x16x32_bf16 v[112:115], v[36:39], a[72:75], v[112:115]\n" \
"v_mfma_f32_16x16x32_bf16 v[116:119], v[40:43], a[72:75], v[116:119]\n" \
"ds_read_b128 a[112:115], v234 offset:61440\n" \
"v_mfma_f32_16x16x32_bf16 v[112:115], v[44:47], a[76:79], v[112:115]\n" \
"buffer_load_dwordx4 v241, %[wsr], s60 offen offset:3072 sc1 lds\n" \
"v_mfma_f32_16x16x32_bf16 v[116:119], v[48:51], a[76:79], v[116:119]\n" \
"s_waitcnt lgkmcnt(7)\n" \
"ds_read_b128 a[116:119], v234 offset:62464\n" \
"v_mfma_f32_16x16x32_bf16 v[112:115], v[52:55], a[80:83], v[112:115]\n" \
"v_mfma_f32_16x16x32_bf16 v[116:119], v[56:59], a[80:83], v[116:119]\n" \
"ds_read_b128 a[120:123], v234 offset:63488\n" \
"v_mfma_f32_16x16x32_bf16 v[112:115], v[60:63], a[84:87], v[112:115]\n" \
"buffer_load_dword v247, v242, %[wsr], s80 offen offset:512 sc1\n" \
"v_mfma_f32_16x16x32_bf16 v[116:119], a[0:3], a[84:87], v[116:119]\n" \
"s_waitcnt lgkmcnt(7)\n" \
"ds_read_b128 a[124:127], v234 offset:64512\n" \
"v_mfma_f32_16x16x32_bf16 v[112:115], a[4:7], a[88:91], v[112:115]\n" \
"s_add_u32 m0, s82, 0x0\n" \
"v_mfma_f32_16x16x32_bf16 v[116:119], a[8:11], a[88:91], v[116:119]\n" \
"s_add_u32 s60, s72, 0x800000\n" \
"v_mfma_f32_16x16x32_bf16 v[112:115], a[12:15], a[92:95], v[112:115]\n" \
"buffer_load_dwordx4 v241, %[wsr], s60 offen offset:0 sc1 lds\n" \
"v_mfma_f32_16x16x32_bf16 v[116:119], a[16:19], a[92:95], v[116:119]\n" \
"s_waitcnt lgkmcnt(6)\n" \
"v_mfma_f32_16x16x32_bf16 v[112:115], a[20:23], a[96:99], v[112:115]\n" \
"v_mfma_f32_16x16x32_bf16 v[116:119], a[24:27], a[96:99], v[116:119]\n" \
"v_mfma_f32_16x16x32_bf16 v[112:115], a[28:31], a[100:103], v[112:115]\n" \
"buffer_load_dwordx4 v241, %[wsr], s60 offen offset:1024 sc1 lds\n" \
"v_mfma_f32_16x16x32_bf16 v[116:119], v[160:163], a[100:103], v[116:119]\n" \
"s_waitcnt lgkmcnt(4)\n" \
"v_mfma_f32_16x16x32_bf16 v[112:115], v[164:167], a[104:107], v[112:115]\n" \
"s_waitcnt vmcnt(12) lgkmcnt(0)\n" \
"s_barrier\n" \
"v_mfma_f32_16x16x32_bf16 v[116:119], v[168:171], a[104:107], v[116:119]\n" \
"ds_read_b128 a[64:67], v234 offset:0\n" \
"v_mfma_f32_16x16x32_bf16 v[112:115], v[172:175], a[108:111], v[112:115]\n" \
"ds_read_b128 a[68:71], v234 offset:1024\n" \
"v_mfma_f32_16x16x32_bf16 v[116:119], a[240:243], a[108:111], v[116:119]\n" \
"ds_read_b128 a[72:75], v234 offset:2048\n" \
"v_mfma_f32_16x16x32_bf16 v[112:115], v[226:229], a[112:115], v[112:115]\n" \
"ds_read_b128 a[76:79], v234 offset:3072\n" \
"v_mfma_f32_16x16x32_bf16 v[116:119], v[230:233], a[112:115], v[116:119]\n" \
"ds_read_b128 a[80:83], v234 offset:4096\n" \
"v_mfma_f32_16x16x32_bf16 v[112:115], v[236:239], a[116:119], v[112:115]\n" \
"ds_read_b128 a[84:87], v234 offset:5120\n" \
"v_mfma_f32_16x16x32_bf16 v[116:119], a[244:247], a[116:119], v[116:119]\n" \
"ds_read_b128 a[88:91], v234 offset:6144\n" \
"v_mfma_f32_16x16x32_bf16 v[112:115], v[208:211], a[120:123], v[112:115]\n" \
"ds_read_b128 a[92:95], v234 offset:7168\n" \
"v_mfma_f32_16x16x32_bf16 v[116:119], v[212:215], a[120:123], v[116:119]\n" \
"ds_read_b128 a[96:99], v234 offset:8192\n" \
"v_mfma_f32_16x16x32_bf16 v[112:115], v[216:219], a[124:127], v[112:115]\n" \
"buffer_load_dwordx4 v241, %[wsr], s60 offen offset:2048 sc1 lds\n" \
"v_mfma_f32_16x16x32_bf16 v[116:119], v[220:223], a[124:127], v[116:119]\n" \
"s_waitcnt lgkmcnt(7)\n" \
"ds_read_b128 a[100:103], v234 offset:9216\n" \
"v_mfma_f32_16x16x32_bf16 v[88:91], a[128:131], a[64:67], 0\n" \
"v_mfma_f32_16x16x32_bf16 v[92:95], a[132:135], a[64:67], 0\n" \
"ds_read_b128 a[104:107], v234 offset:10240\n" \
"v_mfma_f32_16x16x32_bf16 v[88:91], a[136:139], a[68:71], v[88:91]\n" \
"buffer_load_dwordx4 v241, %[wsr], s60 offen offset:3072 sc1 lds\n" \
"v_mfma_f32_16x16x32_bf16 v[92:95], a[140:143], a[68:71], v[92:95]\n" \
"s_waitcnt lgkmcnt(7)\n" \
"ds_read_b128 a[108:111], v234 offset:11264\n" \
"v_mfma_f32_16x16x32_bf16 v[88:91], a[144:147], a[72:75], v[88:91]\n" \
"s_add_u32 m0, s82, 0x4000\n" \
"v_mfma_f32_16x16x32_bf16 v[92:95], a[148:151], a[72:75], v[92:95]\n" \
"s_add_u32 s60, s72, 0x808000\n" \
"ds_read_b128 a[112:115], v234 offset:12288\n" \
"v_mfma_f32_16x16x32_bf16 v[88:91], a[152:155], a[76:79], v[88:91]\n" \
"buffer_load_dwordx4 v241, %[wsr], s60 offen offset:0 sc1 lds\n" \
"v_mfma_f32_16x16x32_bf16 v[92:95], a[156:159], a[76:79], v[92:95]\n" \
"s_waitcnt lgkmcnt(7)\n" \
"ds_read_b128 a[116:119], v234 offset:13312\n" \
"v_mfma_f32_16x16x32_bf16 v[88:91], a[160:163], a[80:83], v[88:91]\n" \
"v_mfma_f32_16x16x32_bf16 v[92:95], a[164:167], a[80:83], v[92:95]\n" \
"ds_read_b128 a[120:123], v234 offset:14336\n" \
"v_mfma_f32_16x16x32_bf16 v[88:91], a[168:171], a[84:87], v[88:91]\n" \
"buffer_load_dwordx4 v241, %[wsr], s60 offen offset:1024 sc1 lds\n" \
"v_mfma_f32_16x16x32_bf16 v[92:95], a[172:175], a[84:87], v[92:95]\n" \
"s_waitcnt lgkmcnt(7)\n" \
"ds_read_b128 a[124:127], v234 offset:15360\n" \
"v_mfma_f32_16x16x32_bf16 v[88:91], a[176:179], a[88:91], v[88:91]\n" \
"v_mfma_f32_16x16x32_bf16 v[92:95], a[180:183], a[88:91], v[92:95]\n" \
"ds_read_b128 a[64:67], v234 offset:16384\n" \
"v_mfma_f32_16x16x32_bf16 v[88:91], a[184:187], a[92:95], v[88:91]\n" \
"buffer_load_dwordx4 v241, %[wsr], s60 offen offset:2048 sc1 lds\n" \
"v_mfma_f32_16x16x32_bf16 v[92:95], a[188:191], a[92:95], v[92:95]\n" \
"s_waitcnt lgkmcnt(7)\n" \
"ds_read_b128 a[68:71], v234 offset:17408\n" \
"v_mfma_f32_16x16x32_bf16 v[88:91], a[192:195], a[96:99], v[88:91]\n" \
"v_mfma_f32_16x16x32_bf16 v[92:95], a[196:199], a[96:99], v[92:95]\n" \
"ds_read_b128 a[72:75], v234 offset:18432\n" \
"v_mfma_f32_16x16x32_bf16 v[88:91], a[200:203], a[100:103], v[88:91]\n" \
"buffer_load_dwordx4 v241, %[wsr], s60 offen offset:3072 sc1 lds\n" \
"v_mfma_f32_16x16x32_bf16 v[92:95], a[204:207], a[100:103], v[92:95]\n" \
"s_waitcnt lgkmcnt(7)\n" \
"ds_read_b128 a[76:79], v234 offset:19456\n" \
"v_mfma_f32_16x16x32_bf16 v[88:91], a[208:211], a[104:107], v[88:91]\n" \
"s_add_u32 m0, s82, 0x8000\n" \
"v_mfma_f32_16x16x32_bf16 v[92:95], a[212:215], a[104:107], v[92:95]\n" \
"s_add_u32 s60, s77, 0x800000\n" \
"ds_read_b128 a[80:83], v234 offset:20480\n" \
"v_mfma_f32_16x16x32_bf16 v[88:91], a[216:219], a[108:111], v[88:91]\n" \
"buffer_load_dwordx4 v241, %[wsr], s60 offen offset:0 sc1 lds\n" \
"v_mfma_f32_16x16x32_bf16 v[92:95], a[220:223], a[108:111], v[92:95]\n" \
"s_waitcnt lgkmcnt(7)\n" \
"ds_read_b128 a[84:87], v234 offset:21504\n" \
"v_mfma_f32_16x16x32_bf16 v[88:91], a[224:227], a[112:115], v[88:91]\n" \
"v_mfma_f32_16x16x32_bf16 v[92:95], a[228:231], a[112:115], v[92:95]\n" \
"ds_read_b128 a[88:91], v234 offset:22528\n" \
"v_mfma_f32_16x16x32_bf16 v[88:91], a[232:235], a[116:119], v[88:91]\n" \
"buffer_load_dwordx4 v241, %[wsr], s60 offen offset:1024 sc1 lds\n" \
"v_mfma_f32_16x16x32_bf16 v[92:95], v[0:3], a[116:119], v[92:95]\n" \
"s_waitcnt lgkmcnt(7)\n" \
"ds_read_b128 a[92:95], v234 offset:23552\n" \
"v_mfma_f32_16x16x32_bf16 v[88:91], v[4:7], a[120:123], v[88:91]\n" \
"v_mfma_f32_16x16x32_bf16 v[92:95], v[8:11], a[120:123], v[92:95]\n" \
"ds_read_b128 a[96:99], v234 offset:24576\n" \
"v_mfma_f32_16x16x32_bf16 v[88:91], v[12:15], a[124:127], v[88:91]\n" \
"buffer_load_dwordx4 v241, %[wsr], s60 offen offset:2048 sc1 lds\n" \
"v_mfma_f32_16x16x32_bf16 v[92:95], v[16:19], a[124:127], v[92:95]\n" \
"s_waitcnt lgkmcnt(7)\n" \
"ds_read_b128 a[100:103], v234 offset:25600\n" \
"v_mfma_f32_16x16x32_bf16 v[88:91], v[20:23], a[64:67], v[88:91]\n" \
"v_mfma_f32_16x16x32_bf16 v[92:95], v[24:27], a[64:67], v[92:95]\n" \
"ds_read_b128 a[104:107], v234 offset:26624\n" \
"v_mfma_f32_16x16x32_bf16 v[88:91], v[28:31], a[68:71], v[88:91]\n" \
"buffer_load_dwordx4 v241, %[wsr], s60 offen offset:3072 sc1 lds\n" \
"v_mfma_f32_16x16x32_bf16 v[92:95], v[32:35], a[68:71], v[92:95]\n" \
"s_waitcnt lgkmcnt(7)\n" \
"ds_read_b128 a[108:111], v234 offset:27648\n" \
"v_mfma_f32_16x16x32_bf16 v[88:91], v[36:39], a[72:75], v[88:91]\n" \
"v_mfma_f32_16x16x32_bf16 v[92:95], v[40:43], a[72:75], v[92:95]\n" \
"ds_read_b128 a[112:115], v234 offset:28672\n" \
"v_mfma_f32_16x16x32_bf16 v[88:91], v[44:47], a[76:79], v[88:91]\n" \
"s_waitcnt vmcnt(12)\n" \
"v_cmp_eq_u32_e32 vcc, v245, v247\n" \
"v_mfma_f32_16x16x32_bf16 v[92:95], v[48:51], a[76:79], v[92:95]\n" \
"s_waitcnt lgkmcnt(7)\n" \
"ds_read_b128 a[116:119], v234 offset:29696\n" \
"v_mfma_f32_16x16x32_bf16 v[88:91], v[52:55], a[80:83], v[88:91]\n" \
"s_and_b32 s88, vcc_lo, -1\n" \
"s_cbranch_scc1 .LATLsy\n" \
".LATLsyb:\n" \
"v_mfma_f32_16x16x32_bf16 v[92:95], v[56:59], a[80:83], v[92:95]\n" \
"ds_read_b128 a[120:123], v234 offset:30720\n" \
"v_mfma_f32_16x16x32_bf16 v[88:91], v[60:63], a[84:87], v[88:91]\n" \
"buffer_load_dwordx4 v[152:155], v200, %[wsr], s63 offen sc1\n" \
"s_add_u32 m0, s82, 0xc000\n" \
"v_mfma_f32_16x16x32_bf16 v[92:95], a[0:3], a[84:87], v[92:95]\n" \
"s_add_u32 s60, s77, 0x808000\n" \
"s_waitcnt lgkmcnt(7)\n" \
"ds_read_b128 a[124:127], v234 offset:31744\n" \
"v_mfma_f32_16x16x32_bf16 v[88:91], a[4:7], a[88:91], v[88:91]\n" \
"buffer_load_dwordx4 v241, %[wsr], s60 offen offset:0 sc1 lds\n" \
"v_mfma_f32_16x16x32_bf16 v[92:95], a[8:11], a[88:91], v[92:95]\n" \
"v_mfma_f32_16x16x32_bf16 v[88:91], a[12:15], a[92:95], v[88:91]\n" \
"buffer_load_dwordx4 v[156:159], v200, %[wsr], s79 offen sc1\n" \
"v_mfma_f32_16x16x32_bf16 v[92:95], a[16:19], a[92:95], v[92:95]\n" \
"s_waitcnt lgkmcnt(6)\n" \
"v_mfma_f32_16x16x32_bf16 v[88:91], a[20:23], a[96:99], v[88:91]\n" \
"buffer_load_dwordx4 v241, %[wsr], s60 offen offset:1024 sc1 lds\n" \
"v_mfma_f32_16x16x32_bf16 v[92:95], a[24:27], a[96:99], v[92:95]\n" \
"v_mfma_f32_16x16x32_bf16 v[88:91], a[28:31], a[100:103], v[88:91]\n" \
"v_mfma_f32_16x16x32_bf16 v[92:95], v[160:163], a[100:103], v[92:95]\n" \
"s_waitcnt lgkmcnt(4)\n" \
"v_mfma_f32_16x16x32_bf16 v[88:91], v[164:167], a[104:107], v[88:91]\n" \
"s_waitcnt lgkmcnt(0)\n" \
"s_barrier\n" \
"v_mfma_f32_16x16x32_bf16 v[92:95], v[168:171], a[104:107], v[92:95]\n" \
"ds_read_b128 a[64:67], v224 offset:0\n" \
"v_mfma_f32_16x16x32_bf16 v[88:91], v[172:175], a[108:111], v[88:91]\n" \
"ds_read_b128 a[68:71], v224 offset:1024\n" \
"v_mfma_f32_16x16x32_bf16 v[92:95], a[240:243], a[108:111], v[92:95]\n" \
"ds_read_b128 a[72:75], v224 offset:2048\n" \
"v_mfma_f32_16x16x32_bf16 v[88:91], v[226:229], a[112:115], v[88:91]\n" \
"ds_read_b128 a[76:79], v224 offset:3072\n" \
"v_mfma_f32_16x16x32_bf16 v[92:95], v[230:233], a[112:115], v[92:95]\n" \
"ds_read_b128 a[80:83], v224 offset:4096\n" \
"v_mfma_f32_16x16x32_bf16 v[88:91], v[236:239], a[116:119], v[88:91]\n" \
"ds_read_b128 a[84:87], v224 offset:5120\n" \
"v_mfma_f32_16x16x32_bf16 v[92:95], a[244:247], a[116:119], v[92:95]\n" \
"ds_read_b128 a[88:91], v224 offset:6144\n" \
"v_mfma_f32_16x16x32_bf16 v[88:91], v[208:211], a[120:123], v[88:91]\n" \
"ds_read_b128 a[92:95], v224 offset:7168\n" \
"v_mfma_f32_16x16x32_bf16 v[92:95], v[212:215], a[120:123], v[92:95]\n" \
"ds_read_b128 a[96:99], v224 offset:8192\n" \
"v_mfma_f32_16x16x32_bf16 v[88:91], v[216:219], a[124:127], v[88:91]\n" \
"buffer_load_dwordx4 v241, %[wsr], s60 offen offset:2048 sc1 lds\n" \
"v_mfma_f32_16x16x32_bf16 v[92:95], v[220:223], a[124:127], v[92:95]\n" \
"s_waitcnt lgkmcnt(7)\n" \
"ds_read_b128 a[100:103], v224 offset:9216\n" \
"v_mfma_f32_16x16x32_bf16 v[96:99], a[128:131], a[64:67], 0\n" \
"v_mfma_f32_16x16x32_bf16 v[100:103], a[132:135], a[64:67], 0\n" \
"ds_read_b128 a[104:107], v224 offset:10240\n" \
"v_mfma_f32_16x16x32_bf16 v[96:99], a[136:139], a[68:71], v[96:99]\n" \
"buffer_load_dwordx4 v241, %[wsr], s60 offen offset:3072 sc1 lds\n" \
"v_mfma_f32_16x16x32_bf16 v[100:103], a[140:143], a[68:71], v[100:103]\n" \
"s_waitcnt lgkmcnt(7)\n" \
"ds_read_b128 a[108:111], v224 offset:11264\n" \
"v_mfma_f32_16x16x32_bf16 v[96:99], a[144:147], a[72:75], v[96:99]\n" \
"v_mfma_f32_16x16x32_bf16 v[100:103], a[148:151], a[72:75], v[100:103]\n" \
"ds_read_b128 a[112:115], v224 offset:12288\n" \
"v_cvt_pk_bf16_f32 v88, v88, v89\n" \
"v_mfma_f32_16x16x32_bf16 v[96:99], a[152:155], a[76:79], v[96:99]\n" \
"v_cvt_pk_bf16_f32 v89, v90, v91\n" \
"v_cvt_pk_bf16_f32 v90, v92, v93\n" \
"v_mfma_f32_16x16x32_bf16 v[100:103], a[156:159], a[76:79], v[100:103]\n" \
"s_waitcnt lgkmcnt(7)\n" \
"ds_read_b128 a[116:119], v224 offset:13312\n" \
"v_cvt_pk_bf16_f32 v91, v94, v95\n" \
"v_mfma_f32_16x16x32_bf16 v[96:99], a[160:163], a[80:83], v[96:99]\n" \
"v_mfma_f32_16x16x32_bf16 v[100:103], a[164:167], a[80:83], v[100:103]\n" \
"ds_read_b128 a[120:123], v224 offset:14336\n" \
"v_mfma_f32_16x16x32_bf16 v[96:99], a[168:171], a[84:87], v[96:99]\n" \
"v_mfma_f32_16x16x32_bf16 v[100:103], a[172:175], a[84:87], v[100:103]\n" \
"s_xor_b32 s83, %[po], 81920\n" \
"s_waitcnt lgkmcnt(7)\n" \
"ds_read_b128 a[124:127], v224 offset:15360\n" \
"v_mfma_f32_16x16x32_bf16 v[96:99], a[176:179], a[88:91], v[96:99]\n" \
"buffer_store_dwordx4 v[88:91], v241, %[wsr], s83 offen offset:0 sc1\n" \
"v_mfma_f32_16x16x32_bf16 v[100:103], a[180:183], a[88:91], v[100:103]\n" \
"ds_read_b128 a[64:67], v224 offset:16384\n" \
"v_mfma_f32_16x16x32_bf16 v[96:99], a[184:187], a[92:95], v[96:99]\n" \
"v_mfma_f32_16x16x32_bf16 v[100:103], a[188:191], a[92:95], v[100:103]\n" \
"s_waitcnt lgkmcnt(7)\n" \
"ds_read_b128 a[68:71], v224 offset:17408\n" \
"v_mfma_f32_16x16x32_bf16 v[96:99], a[192:195], a[96:99], v[96:99]\n" \
"v_mfma_f32_16x16x32_bf16 v[100:103], a[196:199], a[96:99], v[100:103]\n" \
"ds_read_b128 a[72:75], v224 offset:18432\n" \
"v_mfma_f32_16x16x32_bf16 v[96:99], a[200:203], a[100:103], v[96:99]\n" \
"v_mfma_f32_16x16x32_bf16 v[100:103], a[204:207], a[100:103], v[100:103]\n" \
"s_waitcnt lgkmcnt(7)\n" \
"ds_read_b128 a[76:79], v224 offset:19456\n" \
"v_mfma_f32_16x16x32_bf16 v[96:99], a[208:211], a[104:107], v[96:99]\n" \
"v_mfma_f32_16x16x32_bf16 v[100:103], a[212:215], a[104:107], v[100:103]\n" \
"ds_read_b128 a[80:83], v224 offset:20480\n" \
"v_mfma_f32_16x16x32_bf16 v[96:99], a[216:219], a[108:111], v[96:99]\n" \
"v_mfma_f32_16x16x32_bf16 v[100:103], a[220:223], a[108:111], v[100:103]\n" \
"s_waitcnt lgkmcnt(7)\n" \
"ds_read_b128 a[84:87], v224 offset:21504\n" \
"v_mfma_f32_16x16x32_bf16 v[96:99], a[224:227], a[112:115], v[96:99]\n" \
"v_mfma_f32_16x16x32_bf16 v[100:103], a[228:231], a[112:115], v[100:103]\n" \
"ds_read_b128 a[88:91], v224 offset:22528\n" \
"v_mfma_f32_16x16x32_bf16 v[96:99], a[232:235], a[116:119], v[96:99]\n" \
"v_mfma_f32_16x16x32_bf16 v[100:103], v[0:3], a[116:119], v[100:103]\n" \
"s_waitcnt lgkmcnt(7)\n" \
"ds_read_b128 a[92:95], v224 offset:23552\n" \
"v_mfma_f32_16x16x32_bf16 v[96:99], v[4:7], a[120:123], v[96:99]\n" \
"v_mfma_f32_16x16x32_bf16 v[100:103], v[8:11], a[120:123], v[100:103]\n" \
"s_xor_b32 s74, %[po], 1024\n" \
"ds_read_b128 a[96:99], v224 offset:24576\n" \
"v_mfma_f32_16x16x32_bf16 v[96:99], v[12:15], a[124:127], v[96:99]\n" \
"buffer_load_dwordx4 v[128:131], v241, %[wsr], s74 offen offset:0 sc1\n" \
"v_mfma_f32_16x16x32_bf16 v[100:103], v[16:19], a[124:127], v[100:103]\n" \
"s_waitcnt lgkmcnt(7)\n" \
"ds_read_b128 a[100:103], v224 offset:25600\n" \
"v_mfma_f32_16x16x32_bf16 v[96:99], v[20:23], a[64:67], v[96:99]\n" \
"v_mfma_f32_16x16x32_bf16 v[100:103], v[24:27], a[64:67], v[100:103]\n" \
"s_xor_b32 s74, %[po], 2048\n" \
"ds_read_b128 a[104:107], v224 offset:26624\n" \
"v_mfma_f32_16x16x32_bf16 v[96:99], v[28:31], a[68:71], v[96:99]\n" \
"buffer_load_dwordx4 v[136:139], v241, %[wsr], s74 offen offset:0 sc1\n" \
"v_mfma_f32_16x16x32_bf16 v[100:103], v[32:35], a[68:71], v[100:103]\n" \
"s_waitcnt lgkmcnt(7)\n" \
"ds_read_b128 a[108:111], v224 offset:27648\n" \
"v_mfma_f32_16x16x32_bf16 v[96:99], v[36:39], a[72:75], v[96:99]\n" \
"v_mfma_f32_16x16x32_bf16 v[100:103], v[40:43], a[72:75], v[100:103]\n" \
"s_xor_b32 s74, %[po], 3072\n" \
"ds_read_b128 a[112:115], v224 offset:28672\n" \
"v_mfma_f32_16x16x32_bf16 v[96:99], v[44:47], a[76:79], v[96:99]\n" \
"buffer_load_dwordx4 v[144:147], v241, %[wsr], s74 offen offset:0 sc1\n" \
"v_mfma_f32_16x16x32_bf16 v[100:103], v[48:51], a[76:79], v[100:103]\n" \
"s_waitcnt lgkmcnt(7)\n" \
"ds_read_b128 a[116:119], v224 offset:29696\n" \
"v_mfma_f32_16x16x32_bf16 v[96:99], v[52:55], a[80:83], v[96:99]\n" \
"v_mfma_f32_16x16x32_bf16 v[100:103], v[56:59], a[80:83], v[100:103]\n" \
"ds_read_b128 a[120:123], v224 offset:30720\n" \
"v_mfma_f32_16x16x32_bf16 v[96:99], v[60:63], a[84:87], v[96:99]\n" \
"v_mfma_f32_16x16x32_bf16 v[100:103], a[0:3], a[84:87], v[100:103]\n" \
"s_waitcnt lgkmcnt(7)\n" \
"ds_read_b128 a[124:127], v224 offset:31744\n" \
"v_mfma_f32_16x16x32_bf16 v[96:99], a[4:7], a[88:91], v[96:99]\n" \
"v_mfma_f32_16x16x32_bf16 v[100:103], a[8:11], a[88:91], v[100:103]\n" \
"v_mfma_f32_16x16x32_bf16 v[96:99], a[12:15], a[92:95], v[96:99]\n" \
"s_waitcnt vmcnt(7)\n" \
"ds_write_b32 v202, v152 offset:0\n" \
"v_mfma_f32_16x16x32_bf16 v[100:103], a[16:19], a[92:95], v[100:103]\n" \
"s_waitcnt lgkmcnt(7)\n" \
"ds_write_b32 v202, v153 offset:128\n" \
"v_mfma_f32_16x16x32_bf16 v[96:99], a[20:23], a[96:99], v[96:99]\n" \
"ds_write_b32 v202, v154 offset:256\n" \
"v_mfma_f32_16x16x32_bf16 v[100:103], a[24:27], a[96:99], v[100:103]\n" \
"ds_write_b32 v202, v155 offset:384\n" \
"v_mfma_f32_16x16x32_bf16 v[96:99], a[28:31], a[100:103], v[96:99]\n" \
"ds_write_b32 v202, v156 offset:64\n" \
"v_mfma_f32_16x16x32_bf16 v[100:103], v[160:163], a[100:103], v[100:103]\n" \
"s_waitcnt lgkmcnt(9)\n" \
"ds_write_b32 v202, v157 offset:192\n" \
"v_mfma_f32_16x16x32_bf16 v[96:99], v[164:167], a[104:107], v[96:99]\n" \
"s_waitcnt vmcnt(4) lgkmcnt(6)\n" \
"s_barrier\n" \
"v_mfma_f32_16x16x32_bf16 v[100:103], v[168:171], a[104:107], v[100:103]\n" \
"ds_read_b128 a[64:67], v235 offset:0\n" \
"v_mfma_f32_16x16x32_bf16 v[96:99], v[172:175], a[108:111], v[96:99]\n" \
"ds_read_b128 a[68:71], v235 offset:1024\n" \
"v_mfma_f32_16x16x32_bf16 v[100:103], a[240:243], a[108:111], v[100:103]\n" \
"ds_read_b128 a[72:75], v235 offset:2048\n" \
"v_mfma_f32_16x16x32_bf16 v[96:99], v[226:229], a[112:115], v[96:99]\n" \
"ds_read_b128 a[76:79], v235 offset:3072\n" \
"v_mfma_f32_16x16x32_bf16 v[100:103], v[230:233], a[112:115], v[100:103]\n" \
"ds_read_b128 a[80:83], v235 offset:4096\n" \
"v_mfma_f32_16x16x32_bf16 v[96:99], v[236:239], a[116:119], v[96:99]\n" \
"ds_read_b128 a[84:87], v235 offset:5120\n" \
"v_mfma_f32_16x16x32_bf16 v[100:103], a[244:247], a[116:119], v[100:103]\n" \
"ds_read_b128 a[88:91], v235 offset:6144\n" \
"v_mfma_f32_16x16x32_bf16 v[96:99], v[208:211], a[120:123], v[96:99]\n" \
"ds_read_b128 a[92:95], v235 offset:7168\n" \
"v_mfma_f32_16x16x32_bf16 v[100:103], v[212:215], a[120:123], v[100:103]\n" \
"ds_read_b128 a[96:99], v235 offset:8192\n" \
"v_mfma_f32_16x16x32_bf16 v[96:99], v[216:219], a[124:127], v[96:99]\n" \
"ds_write_b32 v202, v158 offset:320\n" \
"v_mfma_f32_16x16x32_bf16 v[100:103], v[220:223], a[124:127], v[100:103]\n" \
"s_waitcnt lgkmcnt(8)\n" \
"ds_read_b128 a[100:103], v235 offset:9216\n" \
"v_mfma_f32_16x16x32_bf16 v[104:107], a[128:131], a[64:67], 0\n" \
"ds_write_b32 v202, v159 offset:448\n" \
"v_mfma_f32_16x16x32_bf16 v[108:111], a[132:135], a[64:67], 0\n" \
"ds_read_b128 a[104:107], v235 offset:10240\n" \
"ds_read_b128 v[152:155], v201 offset:0\n" \
"v_mfma_f32_16x16x32_bf16 v[104:107], a[136:139], a[68:71], v[104:107]\n" \
"ds_read_b128 v[156:159], v201 offset:1024\n" \
"v_mfma_f32_16x16x32_bf16 v[108:111], a[140:143], a[68:71], v[108:111]\n" \
"s_waitcnt lgkmcnt(11)\n" \
"ds_read_b128 a[108:111], v235 offset:11264\n" \
"v_mfma_f32_16x16x32_bf16 v[104:107], a[144:147], a[72:75], v[104:107]\n" \
"v_mfma_f32_16x16x32_bf16 v[108:111], a[148:151], a[72:75], v[108:111]\n" \
"ds_read_b128 a[112:115], v235 offset:12288\n" \
"v_cvt_pk_bf16_f32 v96, v96, v97\n" \
"v_mfma_f32_16x16x32_bf16 v[104:107], a[152:155], a[76:79], v[104:107]\n" \
"v_cvt_pk_bf16_f32 v97, v98, v99\n" \
"v_cvt_pk_bf16_f32 v98, v100, v101\n" \
"v_mfma_f32_16x16x32_bf16 v[108:111], a[156:159], a[76:79], v[108:111]\n" \
"s_waitcnt lgkmcnt(11)\n" \
"ds_read_b128 a[116:119], v235 offset:13312\n" \
"v_cvt_pk_bf16_f32 v99, v102, v103\n" \
"v_mfma_f32_16x16x32_bf16 v[104:107], a[160:163], a[80:83], v[104:107]\n" \
"v_mfma_f32_16x16x32_bf16 v[108:111], a[164:167], a[80:83], v[108:111]\n" \
"ds_read_b128 a[120:123], v235 offset:14336\n" \
"v_mfma_f32_16x16x32_bf16 v[104:107], a[168:171], a[84:87], v[104:107]\n" \
"v_mfma_f32_16x16x32_bf16 v[108:111], a[172:175], a[84:87], v[108:111]\n" \
"s_xor_b32 s83, %[po], 98304\n" \
"s_waitcnt lgkmcnt(11)\n" \
"ds_read_b128 a[124:127], v235 offset:15360\n" \
"v_mfma_f32_16x16x32_bf16 v[104:107], a[176:179], a[88:91], v[104:107]\n" \
"buffer_store_dwordx4 v[96:99], v241, %[wsr], s83 offen offset:0 sc1\n" \
"v_mfma_f32_16x16x32_bf16 v[108:111], a[180:183], a[88:91], v[108:111]\n" \
"ds_read_b128 a[64:67], v235 offset:16384\n" \
"v_mfma_f32_16x16x32_bf16 v[104:107], a[184:187], a[92:95], v[104:107]\n" \
"v_mfma_f32_16x16x32_bf16 v[108:111], a[188:191], a[92:95], v[108:111]\n" \
"s_waitcnt lgkmcnt(10)\n" \
"ds_read_b128 a[68:71], v235 offset:17408\n" \
"s_waitcnt lgkmcnt(7)\n" \
"v_mfma_f32_16x16x32_bf16 v[104:107], a[192:195], a[96:99], v[104:107]\n" \
"global_store_dwordx4 v204, v[152:155], %[out] sc0 sc1\n" \
"v_mfma_f32_16x16x32_bf16 v[108:111], a[196:199], a[96:99], v[108:111]\n" \
"ds_read_b128 a[72:75], v235 offset:18432\n" \
"v_mfma_f32_16x16x32_bf16 v[104:107], a[200:203], a[100:103], v[104:107]\n" \
"v_mfma_f32_16x16x32_bf16 v[108:111], a[204:207], a[100:103], v[108:111]\n" \
"s_waitcnt lgkmcnt(7)\n" \
"ds_read_b128 a[76:79], v235 offset:19456\n" \
"v_mfma_f32_16x16x32_bf16 v[104:107], a[208:211], a[104:107], v[104:107]\n" \
"global_store_dwordx4 v205, v[156:159], %[out] sc0 sc1\n" \
"v_mfma_f32_16x16x32_bf16 v[108:111], a[212:215], a[104:107], v[108:111]\n" \
"ds_read_b128 a[80:83], v235 offset:20480\n" \
"v_mfma_f32_16x16x32_bf16 v[104:107], a[216:219], a[108:111], v[104:107]\n" \
"v_mfma_f32_16x16x32_bf16 v[108:111], a[220:223], a[108:111], v[108:111]\n" \
"s_waitcnt lgkmcnt(7)\n" \
"ds_read_b128 a[84:87], v235 offset:21504\n" \
"v_mfma_f32_16x16x32_bf16 v[104:107], a[224:227], a[112:115], v[104:107]\n" \
"v_mfma_f32_16x16x32_bf16 v[108:111], a[228:231], a[112:115], v[108:111]\n" \
"ds_read_b128 a[88:91], v235 offset:22528\n" \
"v_mfma_f32_16x16x32_bf16 v[104:107], a[232:235], a[116:119], v[104:107]\n" \
"s_waitcnt vmcnt(3)\n" \
"v_max3_u32 v250, v130, v131, v128\n" \
"v_max3_u32 v251, v136, v137, v129\n" \
"v_mfma_f32_16x16x32_bf16 v[108:111], v[0:3], a[116:119], v[108:111]\n" \
"s_waitcnt lgkmcnt(7)\n" \
"ds_read_b128 a[92:95], v235 offset:23552\n" \
"v_max3_u32 v250, v138, v139, v250\n" \
"v_mfma_f32_16x16x32_bf16 v[104:107], v[4:7], a[120:123], v[104:107]\n" \
"v_max3_u32 v251, v144, v145, v251\n" \
"v_max3_u32 v250, v146, v147, v250\n" \
"v_mfma_f32_16x16x32_bf16 v[108:111], v[8:11], a[120:123], v[108:111]\n" \
"ds_read_b128 a[96:99], v235 offset:24576\n" \
"v_max_u32_e32 v250, v250, v251\n" \
"v_mfma_f32_16x16x32_bf16 v[104:107], v[12:15], a[124:127], v[104:107]\n" \
"v_cmp_eq_u32_e32 vcc, -1, v250\n" \
"v_mfma_f32_16x16x32_bf16 v[108:111], v[16:19], a[124:127], v[108:111]\n" \
"s_waitcnt lgkmcnt(7)\n" \
"ds_read_b128 a[100:103], v235 offset:25600\n" \
"v_mfma_f32_16x16x32_bf16 v[104:107], v[20:23], a[64:67], v[104:107]\n" \
"s_cbranch_vccnz .LATLxa\n" \
".LATLxab:\n" \
"v_mfma_f32_16x16x32_bf16 v[108:111], v[24:27], a[64:67], v[108:111]\n" \
"ds_read_b128 a[104:107], v235 offset:26624\n" \
"v_and_b32_e32 v135, 0xffff0000, v131\n" \
"v_mfma_f32_16x16x32_bf16 v[104:107], v[28:31], a[68:71], v[104:107]\n" \
"v_lshlrev_b32_e32 v134, 16, v131\n" \
"v_and_b32_e32 v133, 0xffff0000, v130\n" \
"v_mfma_f32_16x16x32_bf16 v[108:111], v[32:35], a[68:71], v[108:111]\n" \
"s_waitcnt lgkmcnt(7)\n" \
"ds_read_b128 a[108:111], v235 offset:27648\n" \
"v_lshlrev_b32_e32 v132, 16, v130\n" \
"v_mfma_f32_16x16x32_bf16 v[104:107], v[36:39], a[72:75], v[104:107]\n" \
"v_and_b32_e32 v131, 0xffff0000, v129\n" \
"v_lshlrev_b32_e32 v130, 16, v129\n" \
"v_mfma_f32_16x16x32_bf16 v[108:111], v[40:43], a[72:75], v[108:111]\n" \
"ds_read_b128 a[112:115], v235 offset:28672\n" \
"v_and_b32_e32 v129, 0xffff0000, v128\n" \
"v_mfma_f32_16x16x32_bf16 v[104:107], v[44:47], a[76:79], v[104:107]\n" \
"v_lshlrev_b32_e32 v128, 16, v128\n" \
"v_and_b32_e32 v143, 0xffff0000, v139\n" \
"v_mfma_f32_16x16x32_bf16 v[108:111], v[48:51], a[76:79], v[108:111]\n" \
"s_waitcnt lgkmcnt(7)\n" \
"ds_read_b128 a[116:119], v235 offset:29696\n" \
"v_lshlrev_b32_e32 v142, 16, v139\n" \
"v_mfma_f32_16x16x32_bf16 v[104:107], v[52:55], a[80:83], v[104:107]\n" \
"v_and_b32_e32 v141, 0xffff0000, v138\n" \
"v_lshlrev_b32_e32 v140, 16, v138\n" \
"v_mfma_f32_16x16x32_bf16 v[108:111], v[56:59], a[80:83], v[108:111]\n" \
"ds_read_b128 a[120:123], v235 offset:30720\n" \
"v_and_b32_e32 v139, 0xffff0000, v137\n" \
"v_mfma_f32_16x16x32_bf16 v[104:107], v[60:63], a[84:87], v[104:107]\n" \
"v_lshlrev_b32_e32 v138, 16, v137\n" \
"v_and_b32_e32 v137, 0xffff0000, v136\n" \
"v_mfma_f32_16x16x32_bf16 v[108:111], a[0:3], a[84:87], v[108:111]\n" \
"s_waitcnt lgkmcnt(7)\n" \
"ds_read_b128 a[124:127], v235 offset:31744\n" \
"v_lshlrev_b32_e32 v136, 16, v136\n" \
"v_mfma_f32_16x16x32_bf16 v[104:107], a[4:7], a[88:91], v[104:107]\n" \
"v_add_f32_e32 v128, v128, v136\n" \
"v_add_f32_e32 v129, v129, v137\n" \
"v_mfma_f32_16x16x32_bf16 v[108:111], a[8:11], a[88:91], v[108:111]\n" \
"ds_read_b128 a[64:67], v235 offset:32768\n" \
"v_add_f32_e32 v130, v130, v138\n" \
"v_mfma_f32_16x16x32_bf16 v[104:107], a[12:15], a[92:95], v[104:107]\n" \
"v_add_f32_e32 v131, v131, v139\n" \
"v_add_f32_e32 v132, v132, v140\n" \
"v_mfma_f32_16x16x32_bf16 v[108:111], a[16:19], a[92:95], v[108:111]\n" \
"s_waitcnt lgkmcnt(7)\n" \
"ds_read_b128 a[68:71], v235 offset:33792\n" \
"v_add_f32_e32 v133, v133, v141\n" \
"v_mfma_f32_16x16x32_bf16 v[104:107], a[20:23], a[96:99], v[104:107]\n" \
"v_add_f32_e32 v134, v134, v142\n" \
"v_add_f32_e32 v135, v135, v143\n" \
"v_mfma_f32_16x16x32_bf16 v[108:111], a[24:27], a[96:99], v[108:111]\n" \
"ds_read_b128 a[72:75], v235 offset:34816\n" \
"v_and_b32_e32 v151, 0xffff0000, v147\n" \
"v_mfma_f32_16x16x32_bf16 v[104:107], a[28:31], a[100:103], v[104:107]\n" \
"v_lshlrev_b32_e32 v150, 16, v147\n" \
"v_and_b32_e32 v149, 0xffff0000, v146\n" \
"v_mfma_f32_16x16x32_bf16 v[108:111], v[160:163], a[100:103], v[108:111]\n" \
"s_waitcnt lgkmcnt(7)\n" \
"ds_read_b128 a[76:79], v235 offset:35840\n" \
"v_lshlrev_b32_e32 v148, 16, v146\n" \
"v_mfma_f32_16x16x32_bf16 v[104:107], v[164:167], a[104:107], v[104:107]\n" \
"v_and_b32_e32 v147, 0xffff0000, v145\n" \
"v_lshlrev_b32_e32 v146, 16, v145\n" \
"v_mfma_f32_16x16x32_bf16 v[108:111], v[168:171], a[104:107], v[108:111]\n" \
"ds_read_b128 a[80:83], v235 offset:36864\n" \
"v_and_b32_e32 v145, 0xffff0000, v144\n" \
"v_mfma_f32_16x16x32_bf16 v[104:107], v[172:175], a[108:111], v[104:107]\n" \
"v_lshlrev_b32_e32 v144, 16, v144\n" \
"v_add_f32_e32 v128, v128, v144\n" \
"v_mfma_f32_16x16x32_bf16 v[108:111], a[240:243], a[108:111], v[108:111]\n" \
"s_waitcnt lgkmcnt(7)\n" \
"ds_read_b128 a[84:87], v235 offset:37888\n" \
"v_add_f32_e32 v129, v129, v145\n" \
"v_mfma_f32_16x16x32_bf16 v[104:107], v[226:229], a[112:115], v[104:107]\n" \
"v_add_f32_e32 v130, v130, v146\n" \
"v_add_f32_e32 v131, v131, v147\n" \
"v_mfma_f32_16x16x32_bf16 v[108:111], v[230:233], a[112:115], v[108:111]\n" \
"ds_read_b128 a[88:91], v235 offset:38912\n" \
"v_add_f32_e32 v132, v132, v148\n" \
"v_mfma_f32_16x16x32_bf16 v[104:107], v[236:239], a[116:119], v[104:107]\n" \
"v_add_f32_e32 v133, v133, v149\n" \
"v_add_f32_e32 v134, v134, v150\n" \
"v_mfma_f32_16x16x32_bf16 v[108:111], a[244:247], a[116:119], v[108:111]\n" \
"s_xor_b32 s74, %[po], 66560\n" \
"s_waitcnt lgkmcnt(7)\n" \
"ds_read_b128 a[92:95], v235 offset:39936\n" \
"v_mfma_f32_16x16x32_bf16 v[104:107], v[208:211], a[120:123], v[104:107]\n" \
"buffer_load_dwordx4 v[176:179], v241, %[wsr], s74 offen offset:0 sc1\n" \
"v_mfma_f32_16x16x32_bf16 v[108:111], v[212:215], a[120:123], v[108:111]\n" \
"ds_read_b128 a[96:99], v235 offset:40960\n" \
"v_add_f32_e32 v135, v135, v151\n" \
"v_mfma_f32_16x16x32_bf16 v[104:107], v[216:219], a[124:127], v[104:107]\n" \
"v_add_f32_e32 v128, v128, v112\n" \
"v_add_f32_e32 v129, v129, v113\n" \
"v_mfma_f32_16x16x32_bf16 v[108:111], v[220:223], a[124:127], v[108:111]\n" \
"s_waitcnt lgkmcnt(7)\n" \
"ds_read_b128 a[100:103], v235 offset:41984\n" \
"v_add_f32_e32 v130, v130, v114\n" \
"v_mfma_f32_16x16x32_bf16 v[120:123], a[128:131], a[64:67], 0\n" \
"v_add_f32_e32 v131, v131, v115\n" \
"v_add_f32_e32 v132, v132, v116\n" \
"v_mfma_f32_16x16x32_bf16 v[124:127], a[132:135], a[64:67], 0\n" \
"ds_read_b128 a[104:107], v235 offset:43008\n" \
"v_add_f32_e32 v133, v133, v117\n" \
"v_mfma_f32_16x16x32_bf16 v[120:123], a[136:139], a[68:71], v[120:123]\n" \
"v_add_f32_e32 v134, v134, v118\n" \
"v_add_f32_e32 v135, v135, v119\n" \
"v_mfma_f32_16x16x32_bf16 v[124:127], a[140:143], a[68:71], v[124:127]\n" \
"s_waitcnt lgkmcnt(7)\n" \
"ds_read_b128 a[108:111], v235 offset:44032\n" \
"v_cvt_pk_bf16_f32 v128, v128, v129\n" \
"v_mfma_f32_16x16x32_bf16 v[120:123], a[144:147], a[72:75], v[120:123]\n" \
"v_cvt_pk_bf16_f32 v129, v130, v131\n" \
"v_cvt_pk_bf16_f32 v130, v132, v133\n" \
"v_mfma_f32_16x16x32_bf16 v[124:127], a[148:151], a[72:75], v[124:127]\n" \
"ds_read_b128 a[112:115], v235 offset:45056\n" \
"v_cvt_pk_bf16_f32 v104, v104, v105\n" \
"v_mfma_f32_16x16x32_bf16 v[120:123], a[152:155], a[76:79], v[120:123]\n" \
"v_cvt_pk_bf16_f32 v105, v106, v107\n" \
"v_cvt_pk_bf16_f32 v106, v108, v109\n" \
"v_mfma_f32_16x16x32_bf16 v[124:127], a[156:159], a[76:79], v[124:127]\n" \
"s_waitcnt lgkmcnt(7)\n" \
"ds_read_b128 a[116:119], v235 offset:46080\n" \
"v_cvt_pk_bf16_f32 v107, v110, v111\n" \
"v_mfma_f32_16x16x32_bf16 v[120:123], a[160:163], a[80:83], v[120:123]\n" \
"v_cvt_pk_bf16_f32 v131, v134, v135\n" \
"v_mfma_f32_16x16x32_bf16 v[124:127], a[164:167], a[80:83], v[124:127]\n" \
"ds_read_b128 a[120:123], v235 offset:47104\n" \
"v_mfma_f32_16x16x32_bf16 v[120:123], a[168:171], a[84:87], v[120:123]\n" \
"v_permlane16_swap_b32_e32 v128, v130\n" \
"v_mfma_f32_16x16x32_bf16 v[124:127], a[172:175], a[84:87], v[124:127]\n" \
"s_xor_b32 s83, %[po], 114688\n" \
"s_waitcnt lgkmcnt(7)\n" \
"ds_read_b128 a[124:127], v235 offset:48128\n" \
"v_mfma_f32_16x16x32_bf16 v[120:123], a[176:179], a[88:91], v[120:123]\n" \
"buffer_store_dwordx4 v[104:107], v241, %[wsr], s83 offen offset:0 sc1\n" \
"v_mfma_f32_16x16x32_bf16 v[124:127], a[180:183], a[88:91], v[124:127]\n" \
"ds_read_b128 a[64:67], v235 offset:49152\n" \
"v_mfma_f32_16x16x32_bf16 v[120:123], a[184:187], a[92:95], v[120:123]\n" \
"v_permlane16_swap_b32_e32 v129, v131\n" \
"v_mfma_f32_16x16x32_bf16 v[124:127], a[188:191], a[92:95], v[124:127]\n" \
"s_waitcnt lgkmcnt(7)\n" \
"ds_read_b128 a[68:71], v235 offset:50176\n" \
"v_mfma_f32_16x16x32_bf16 v[120:123], a[192:195], a[96:99], v[120:123]\n" \
"global_store_dwordx4 v225, v[128:131], %[out] offset:1024 sc0 sc1\n" \
"v_mfma_f32_16x16x32_bf16 v[124:127], a[196:199], a[96:99], v[124:127]\n" \
"ds_read_b128 a[72:75], v235 offset:51200\n" \
"v_mfma_f32_16x16x32_bf16 v[120:123], a[200:203], a[100:103], v[120:123]\n" \
"v_mfma_f32_16x16x32_bf16 v[124:127], a[204:207], a[100:103], v[124:127]\n" \
"s_waitcnt lgkmcnt(7)\n" \
"ds_read_b128 a[76:79], v235 offset:52224\n" \
"v_mfma_f32_16x16x32_bf16 v[120:123], a[208:211], a[104:107], v[120:123]\n" \
"v_mfma_f32_16x16x32_bf16 v[124:127], a[212:215], a[104:107], v[124:127]\n" \
"ds_read_b128 a[80:83], v235 offset:53248\n" \
"v_mfma_f32_16x16x32_bf16 v[120:123], a[216:219], a[108:111], v[120:123]\n" \
"v_mfma_f32_16x16x32_bf16 v[124:127], a[220:223], a[108:111], v[124:127]\n" \
"s_waitcnt lgkmcnt(7)\n" \
"ds_read_b128 a[84:87], v235 offset:54272\n" \
"v_mfma_f32_16x16x32_bf16 v[120:123], a[224:227], a[112:115], v[120:123]\n" \
"v_mfma_f32_16x16x32_bf16 v[124:127], a[228:231], a[112:115], v[124:127]\n" \
"s_xor_b32 s74, %[po], 67584\n" \
"ds_read_b128 a[88:91], v235 offset:55296\n" \
"v_mfma_f32_16x16x32_bf16 v[120:123], a[232:235], a[116:119], v[120:123]\n" \
"buffer_load_dwordx4 v[184:187], v241, %[wsr], s74 offen offset:0 sc1\n" \
"v_mfma_f32_16x16x32_bf16 v[124:127], v[0:3], a[116:119], v[124:127]\n" \
"s_waitcnt lgkmcnt(7)\n" \
"ds_read_b128 a[92:95], v235 offset:56320\n" \
"v_mfma_f32_16x16x32_bf16 v[120:123], v[4:7], a[120:123], v[120:123]\n" \
"v_mfma_f32_16x16x32_bf16 v[124:127], v[8:11], a[120:123], v[124:127]\n" \
"ds_read_b128 a[96:99], v235 offset:57344\n" \
"v_mfma_f32_16x16x32_bf16 v[120:123], v[12:15], a[124:127], v[120:123]\n" \
"v_mfma_f32_16x16x32_bf16 v[124:127], v[16:19], a[124:127], v[124:127]\n" \
"s_waitcnt lgkmcnt(7)\n" \
"ds_read_b128 a[100:103], v235 offset:58368\n" \
"v_mfma_f32_16x16x32_bf16 v[120:123], v[20:23], a[64:67], v[120:123]\n" \
"v_mfma_f32_16x16x32_bf16 v[124:127], v[24:27], a[64:67], v[124:127]\n" \
"ds_read_b128 a[104:107], v235 offset:59392\n" \
"v_mfma_f32_16x16x32_bf16 v[120:123], v[28:31], a[68:71], v[120:123]\n" \
"v_mfma_f32_16x16x32_bf16 v[124:127], v[32:35], a[68:71], v[124:127]\n" \
"s_waitcnt lgkmcnt(7)\n" \
"ds_read_b128 a[108:111], v235 offset:60416\n" \
"v_mfma_f32_16x16x32_bf16 v[120:123], v[36:39], a[72:75], v[120:123]\n" \
"v_mfma_f32_16x16x32_bf16 v[124:127], v[40:43], a[72:75], v[124:127]\n" \
"ds_read_b128 a[112:115], v235 offset:61440\n" \
"v_mfma_f32_16x16x32_bf16 v[120:123], v[44:47], a[76:79], v[120:123]\n" \
"v_mfma_f32_16x16x32_bf16 v[124:127], v[48:51], a[76:79], v[124:127]\n" \
"s_waitcnt lgkmcnt(7)\n" \
"ds_read_b128 a[116:119], v235 offset:62464\n" \
"v_mfma_f32_16x16x32_bf16 v[120:123], v[52:55], a[80:83], v[120:123]\n" \
"v_mfma_f32_16x16x32_bf16 v[124:127], v[56:59], a[80:83], v[124:127]\n" \
"ds_read_b128 a[120:123], v235 offset:63488\n" \
"v_mfma_f32_16x16x32_bf16 v[120:123], v[60:63], a[84:87], v[120:123]\n" \
"v_mfma_f32_16x16x32_bf16 v[124:127], a[0:3], a[84:87], v[124:127]\n" \
"s_waitcnt lgkmcnt(7)\n" \
"ds_read_b128 a[124:127], v235 offset:64512\n" \
"v_mfma_f32_16x16x32_bf16 v[120:123], a[4:7], a[88:91], v[120:123]\n" \
"v_mfma_f32_16x16x32_bf16 v[124:127], a[8:11], a[88:91], v[124:127]\n" \
"v_mfma_f32_16x16x32_bf16 v[120:123], a[12:15], a[92:95], v[120:123]\n" \
"v_mfma_f32_16x16x32_bf16 v[124:127], a[16:19], a[92:95], v[124:127]\n" \
"s_waitcnt lgkmcnt(6)\n" \
"v_mfma_f32_16x16x32_bf16 v[120:123], a[20:23], a[96:99], v[120:123]\n" \
"v_mfma_f32_16x16x32_bf16 v[124:127], a[24:27], a[96:99], v[124:127]\n" \
"v_mfma_f32_16x16x32_bf16 v[120:123], a[28:31], a[100:103], v[120:123]\n" \
"v_mfma_f32_16x16x32_bf16 v[124:127], v[160:163], a[100:103], v[124:127]\n" \
"s_waitcnt lgkmcnt(4)\n" \
"v_mfma_f32_16x16x32_bf16 v[120:123], v[164:167], a[104:107], v[120:123]\n" \
"v_mfma_f32_16x16x32_bf16 v[124:127], v[168:171], a[104:107], v[124:127]\n" \
"v_mfma_f32_16x16x32_bf16 v[120:123], v[172:175], a[108:111], v[120:123]\n" \
"v_mfma_f32_16x16x32_bf16 v[124:127], a[240:243], a[108:111], v[124:127]\n" \
"s_xor_b32 s74, %[po], 68608\n" \
"s_waitcnt lgkmcnt(2)\n" \
"v_mfma_f32_16x16x32_bf16 v[120:123], v[226:229], a[112:115], v[120:123]\n" \
"buffer_load_dwordx4 v[192:195], v241, %[wsr], s74 offen offset:0 sc1\n" \
"v_mfma_f32_16x16x32_bf16 v[124:127], v[230:233], a[112:115], v[124:127]\n" \
"v_mfma_f32_16x16x32_bf16 v[120:123], v[236:239], a[116:119], v[120:123]\n" \
"v_mfma_f32_16x16x32_bf16 v[124:127], a[244:247], a[116:119], v[124:127]\n" \
"s_waitcnt lgkmcnt(0)\n" \
"v_mfma_f32_16x16x32_bf16 v[120:123], v[208:211], a[120:123], v[120:123]\n" \
"v_mfma_f32_16x16x32_bf16 v[124:127], v[212:215], a[120:123], v[124:127]\n" \
"v_mfma_f32_16x16x32_bf16 v[120:123], v[216:219], a[124:127], v[120:123]\n" \
"v_mfma_f32_16x16x32_bf16 v[124:127], v[220:223], a[124:127], v[124:127]\n" \
"s_waitcnt vmcnt(1)\n" \
"v_max3_u32 v250, v178, v179, v176\n" \
"v_max3_u32 v251, v184, v185, v177\n" \
"v_max3_u32 v250, v186, v187, v250\n" \
"v_max_u32_e32 v250, v250, v251\n" \
"v_cmp_eq_u32_e32 vcc, -1, v250\n" \
"s_nop 3\n" \
"s_cbranch_vccnz .LATLxb\n" \
".LATLxbb:\n" \
"v_and_b32_e32 v183, 0xffff0000, v179\n" \
"v_lshlrev_b32_e32 v182, 16, v179\n" \
"v_and_b32_e32 v181, 0xffff0000, v178\n" \
"v_lshlrev_b32_e32 v180, 16, v178\n" \
"v_and_b32_e32 v179, 0xffff0000, v177\n" \
"v_lshlrev_b32_e32 v178, 16, v177\n" \
"v_and_b32_e32 v177, 0xffff0000, v176\n" \
"v_lshlrev_b32_e32 v176, 16, v176\n" \
"v_and_b32_e32 v191, 0xffff0000, v187\n" \
"v_lshlrev_b32_e32 v190, 16, v187\n" \
"v_and_b32_e32 v189, 0xffff0000, v186\n" \
"v_lshlrev_b32_e32 v188, 16, v186\n" \
"v_and_b32_e32 v187, 0xffff0000, v185\n" \
"v_lshlrev_b32_e32 v186, 16, v185\n" \
"v_and_b32_e32 v185, 0xffff0000, v184\n" \
"v_lshlrev_b32_e32 v184, 16, v184\n" \
"v_add_f32_e32 v176, v176, v184\n" \
"v_add_f32_e32 v177, v177, v185\n" \
"v_add_f32_e32 v178, v178, v186\n" \
"v_add_f32_e32 v179, v179, v187\n" \
"v_add_f32_e32 v180, v180, v188\n" \
"v_add_f32_e32 v181, v181, v189\n" \
"v_add_f32_e32 v182, v182, v190\n" \
"v_add_f32_e32 v183, v183, v191\n" \
"v_add_f32_e32 v176, v176, v120\n" \
"v_add_f32_e32 v177, v177, v121\n" \
"v_add_f32_e32 v178, v178, v122\n" \
"v_add_f32_e32 v179, v179, v123\n" \
"v_add_f32_e32 v180, v180, v124\n" \
"v_add_f32_e32 v181, v181, v125\n" \
"v_add_f32_e32 v182, v182, v126\n" \
"v_add_f32_e32 v183, v183, v127\n" \
"s_waitcnt vmcnt(0)\n" \
"v_max3_u32 v250, v194, v195, v192\n" \
"v_max_u32_e32 v250, v250, v193\n" \
"v_cmp_eq_u32_e32 vcc, -1, v250\n" \
"s_nop 3\n" \
"s_cbranch_vccnz .LATLxc\n" \
".LATLxcb:\n" \
"v_and_b32_e32 v199, 0xffff0000, v195\n" \
"v_lshlrev_b32_e32 v198, 16, v195\n" \
"v_and_b32_e32 v197, 0xffff0000, v194\n" \
"v_lshlrev_b32_e32 v196, 16, v194\n" \
"v_and_b32_e32 v195, 0xffff0000, v193\n" \
"v_lshlrev_b32_e32 v194, 16, v193\n" \
"v_and_b32_e32 v193, 0xffff0000, v192\n" \
"v_lshlrev_b32_e32 v192, 16, v192\n" \
"v_add_f32_e32 v176, v176, v192\n" \
"v_add_f32_e32 v177, v177, v193\n" \
"v_add_f32_e32 v178, v178, v194\n" \
"v_add_f32_e32 v179, v179, v195\n" \
"v_add_f32_e32 v180, v180, v196\n" \
"v_add_f32_e32 v181, v181, v197\n" \
"v_add_f32_e32 v182, v182, v198\n" \
"v_add_f32_e32 v183, v183, v199\n" \
"v_cvt_pk_bf16_f32 v176, v176, v177\n" \
"v_cvt_pk_bf16_f32 v177, v178, v179\n" \
"v_cvt_pk_bf16_f32 v178, v180, v181\n" \
"v_cvt_pk_bf16_f32 v179, v182, v183\n" \
"s_nop 0\n" \
"v_permlane16_swap_b32_e32 v176, v178\n" \
"v_permlane16_swap_b32_e32 v177, v179\n" \
"global_store_dwordx4 v206, v[176:179], %[out] offset:1024 sc0 sc1\n" \
".LATLte:\n" \
"s_endpgm\n" \
".LATLxc:\n" \
"s_mov_b32 s96, 0x2000\n" \
".LATLxc_l:\n" \
"s_xor_b32 s61, %[po], 68608\n" \
"buffer_load_dwordx4 v[192:195], v241, %[wsr], s61 offen offset:0 sc1\n" \
"s_waitcnt vmcnt(0)\n" \
".LATLxc_m:\n" \
"v_max3_u32 v250, v194, v195, v192\n" \
"v_max_u32_e32 v250, v250, v193\n" \
"v_cmp_eq_u32_e32 vcc, -1, v250\n" \
"s_sub_u32 s96, s96, 1\n" \
"s_cbranch_scc1 .LATLxcb\n" \
"s_nop 2\n" \
"s_cbranch_vccnz .LATLxc_l\n" \
"s_branch .LATLxcb\n" \
".LATLxb:\n" \
"s_mov_b32 s96, 0x2000\n" \
".LATLxb_l:\n" \
"s_xor_b32 s61, %[po], 66560\n" \
"buffer_load_dwordx4 v[176:179], v241, %[wsr], s61 offen offset:0 sc1\n" \
"s_xor_b32 s61, %[po], 67584\n" \
"buffer_load_dwordx4 v[184:187], v241, %[wsr], s61 offen offset:0 sc1\n" \
"s_waitcnt vmcnt(0)\n" \
".LATLxb_m:\n" \
"v_max3_u32 v250, v178, v179, v176\n" \
"v_max3_u32 v251, v184, v185, v177\n" \
"v_max3_u32 v250, v186, v187, v250\n" \
"v_max_u32_e32 v250, v250, v251\n" \
"v_cmp_eq_u32_e32 vcc, -1, v250\n" \
"s_sub_u32 s96, s96, 1\n" \
"s_cbranch_scc1 .LATLxbb\n" \
"s_nop 2\n" \
"s_cbranch_vccnz .LATLxb_l\n" \
"s_branch .LATLxbb\n" \
".LATLsu:\n" \
"s_mov_b32 s96, 0x2000\n" \
".LATLsu_l:\n" \
"s_sleep 1\n" \
"buffer_load_dword v247, v242, %[wsr], s80 offen offset:256 sc1\n" \
"s_waitcnt vmcnt(0)\n" \
"v_cmp_eq_u32_e32 vcc, v244, v247\n" \
"s_sub_u32 s96, s96, 1\n" \
"s_cbranch_scc1 .LATLsub\n" \
"s_nop 2\n" \
"s_and_b32 s88, vcc_lo, -1\n" \
"s_cbranch_scc1 .LATLsu_l\n" \
"s_branch .LATLsub\n" \
".LATLsw:\n" \
"s_mov_b32 s96, 0x2000\n" \
".LATLsw_l:\n" \
"s_sleep 1\n" \
"buffer_load_dword v247, v242, %[wsr], s80 offen offset:256 sc1\n" \
"s_waitcnt vmcnt(0)\n" \
"v_cmp_eq_u32_e32 vcc, v244, v247\n" \
"s_sub_u32 s96, s96, 1\n" \
"s_cbranch_scc1 .LATLswb\n" \
"s_nop 2\n" \
"s_and_b32 s88, vcc_hi, -1\n" \
"s_cbranch_scc1 .LATLsw_l\n" \
"s_branch .LATLswb\n" \
".LATLsk:\n" \
"s_mov_b32 s96, 0x2000\n" \
".LATLsk_l:\n" \
"s_sleep 1\n" \
"buffer_load_dword v247, v242, %[wsr], s80 offen offset:0 sc1\n" \
"s_waitcnt vmcnt(0)\n" \
"v_cmp_eq_u32_e32 vcc, v243, v247\n" \
"s_sub_u32 s96, s96, 1\n" \
"s_cbranch_scc1 .LATLskb\n" \
"s_nop 2\n" \
"s_and_b32 s88, vcc_lo, -1\n" \
"s_cbranch_scc1 .LATLsk_l\n" \
"s_branch .LATLskb\n" \
".LATLsv:\n" \
"s_mov_b32 s96, 0x2000\n" \
".LATLsv_l:\n" \
"s_sleep 1\n" \
"buffer_load_dword v247, v242, %[wsr], s80 offen offset:0 sc1\n" \
"s_waitcnt vmcnt(0)\n" \
"v_cmp_eq_u32_e32 vcc, v243, v247\n" \
"s_sub_u32 s96, s96, 1\n" \
"s_cbranch_scc1 .LATLsvb\n" \
"s_nop 2\n" \
"s_and_b32 s88, vcc_hi, -1\n" \
"s_cbranch_scc1 .LATLsv_l\n" \
"s_branch .LATLsvb\n" \
".LATLpz:\n" \
"v_cmp_le_i32_e64 s[88:89], 0, v225\n" \
"v_cmp_le_i32_e64 s[90:91], 1, v225\n" \
"v_cmp_le_i32_e64 s[92:93], 2, v225\n" \
"v_cmp_le_i32_e64 s[94:95], 3, v225\n" \
"v_mov_b32_e32 v132, v252\n" \
"v_mov_b32_e32 v133, v252\n" \
"v_mov_b32_e32 v134, v252\n" \
"v_mov_b32_e32 v135, v252\n" \
"v_cndmask_b32_e64 v128, v252, v128, s[88:89]\n" \
"v_cndmask_b32_e64 v129, v252, v129, s[90:91]\n" \
"v_cndmask_b32_e64 v130, v252, v130, s[92:93]\n" \
"v_cndmask_b32_e64 v131, v252, v131, s[94:95]\n" \
"v_cndmask_b32_e64 v140, v252, v140, s[88:89]\n" \
"v_cndmask_b32_e64 v141, v252, v141, s[90:91]\n" \
"v_cndmask_b32_e64 v142, v252, v142, s[92:93]\n" \
"v_cndmask_b32_e64 v143, v252, v143, s[94:95]\n" \
"s_branch .LATLpzb\n" \
".LATLmk0:\n" \
"s_cmp_eq_i32 s74, -1\n" \
"s_cbranch_scc0 .LATLma0\n" \
"v_cmp_le_i32_e64 s[88:89], 0, v225\n" \
"v_cmp_le_i32_e64 s[90:91], 1, v225\n" \
"v_cmp_le_i32_e64 s[92:93], 2, v225\n" \
"v_cmp_le_i32_e64 s[94:95], 3, v225\n" \
"v_mov_b32_e32 v164, v252\n" \
"v_mov_b32_e32 v165, v252\n" \
"v_mov_b32_e32 v166, v252\n" \
"v_mov_b32_e32 v167, v252\n" \
"v_cndmask_b32_e64 v160, v252, v160, s[88:89]\n" \
"v_cndmask_b32_e64 v161, v252, v161, s[90:91]\n" \
"v_cndmask_b32_e64 v162, v252, v162, s[92:93]\n" \
"v_cndmask_b32_e64 v163, v252, v163, s[94:95]\n" \
"v_cndmask_b32_e64 v172, v252, v172, s[88:89]\n" \
"v_cndmask_b32_e64 v173, v252, v173, s[90:91]\n" \
"v_cndmask_b32_e64 v174, v252, v174, s[92:93]\n" \
"v_cndmask_b32_e64 v175, v252, v175, s[94:95]\n" \
"s_nop 0\n" \
"s_branch .LATLmb0\n" \
".LATLma0:\n" \
"v_mov_b32_e32 v160, v252\n" \
"v_mov_b32_e32 v161, v252\n" \
"v_mov_b32_e32 v162, v252\n" \
"v_mov_b32_e32 v163, v252\n" \
"v_mov_b32_e32 v164, v252\n" \
"v_mov_b32_e32 v165, v252\n" \
"v_mov_b32_e32 v166, v252\n" \
"v_mov_b32_e32 v167, v252\n" \
"v_mov_b32_e32 v168, v252\n" \
"v_mov_b32_e32 v169, v252\n" \
"v_mov_b32_e32 v170, v252\n" \
"v_mov_b32_e32 v171, v252\n" \
"v_mov_b32_e32 v172, v252\n" \
"v_mov_b32_e32 v173, v252\n" \
"v_mov_b32_e32 v174, v252\n" \
"v_mov_b32_e32 v175, v252\n" \
"s_nop 0\n" \
"s_branch .LATLmb0\n" \
".LATLmk1:\n" \
"s_cmp_eq_i32 s74, -2\n" \
"s_cbranch_scc0 .LATLma1\n" \
"v_cmp_le_i32_e64 s[88:89], 0, v225\n" \
"v_cmp_le_i32_e64 s[90:91], 1, v225\n" \
"v_cmp_le_i32_e64 s[92:93], 2, v225\n" \
"v_cmp_le_i32_e64 s[94:95], 3, v225\n" \
"v_mov_b32_e32 v164, v252\n" \
"v_mov_b32_e32 v165, v252\n" \
"v_mov_b32_e32 v166, v252\n" \
"v_mov_b32_e32 v167, v252\n" \
"v_cndmask_b32_e64 v160, v252, v160, s[88:89]\n" \
"v_cndmask_b32_e64 v161, v252, v161, s[90:91]\n" \
"v_cndmask_b32_e64 v162, v252, v162, s[92:93]\n" \
"v_cndmask_b32_e64 v163, v252, v163, s[94:95]\n" \
"v_cndmask_b32_e64 v172, v252, v172, s[88:89]\n" \
"v_cndmask_b32_e64 v173, v252, v173, s[90:91]\n" \
"v_cndmask_b32_e64 v174, v252, v174, s[92:93]\n" \
"v_cndmask_b32_e64 v175, v252, v175, s[94:95]\n" \
"s_nop 0\n" \
"s_branch .LATLmb1\n" \
".LATLma1:\n" \
"v_mov_b32_e32 v160, v252\n" \
"v_mov_b32_e32 v161, v252\n" \
"v_mov_b32_e32 v162, v252\n" \
"v_mov_b32_e32 v163, v252\n" \
"v_mov_b32_e32 v164, v252\n" \
"v_mov_b32_e32 v165, v252\n" \
"v_mov_b32_e32 v166, v252\n" \
"v_mov_b32_e32 v167, v252\n" \
"v_mov_b32_e32 v168, v252\n" \
"v_mov_b32_e32 v169, v252\n" \
"v_mov_b32_e32 v170, v252\n" \
"v_mov_b32_e32 v171, v252\n" \
"v_mov_b32_e32 v172, v252\n" \
"v_mov_b32_e32 v173, v252\n" \
"v_mov_b32_e32 v174, v252\n" \
"v_mov_b32_e32 v175, v252\n" \
"s_nop 0\n" \
"s_branch .LATLmb1\n" \
".LATLt0:\n" \
"s_add_u32 s60, s64, 57344\n" \
"buffer_load_dwordx4 v[226:229], v241, %[wsr], s60 offen offset:0 sc1\n" \
"buffer_load_dwordx4 v[230:233], v241, %[wsr], s60 offen offset:1024 sc1\n" \
"buffer_load_dwordx4 v[236:239], v241, %[wsr], s60 offen offset:2048 sc1\n" \
"buffer_load_dwordx4 a[244:247], v241, %[wsr], s60 offen offset:3072 sc1\n" \
"buffer_load_dwordx4 v[208:211], v249, %[wsr], s60 offen offset:0 sc1\n" \
"buffer_load_dwordx4 v[212:215], v249, %[wsr], s60 offen offset:1024 sc1\n" \
"buffer_load_dwordx4 v[216:219], v249, %[wsr], s60 offen offset:2048 sc1\n" \
"buffer_load_dwordx4 v[220:223], v249, %[wsr], s60 offen offset:3072 sc1\n" \
"s_branch .LATLtr\n" \
".LATLmk2:\n" \
"s_cmp_eq_i32 s74, -3\n" \
"s_cbranch_scc0 .LATLma2\n" \
"v_cmp_le_i32_e64 s[88:89], 0, v225\n" \
"v_cmp_le_i32_e64 s[90:91], 1, v225\n" \
"v_cmp_le_i32_e64 s[92:93], 2, v225\n" \
"v_cmp_le_i32_e64 s[94:95], 3, v225\n" \
"v_mov_b32_e32 v164, v252\n" \
"v_mov_b32_e32 v165, v252\n" \
"v_mov_b32_e32 v166, v252\n" \
"v_mov_b32_e32 v167, v252\n" \
"v_cndmask_b32_e64 v160, v252, v160, s[88:89]\n" \
"v_cndmask_b32_e64 v161, v252, v161, s[90:91]\n" \
"v_cndmask_b32_e64 v162, v252, v162, s[92:93]\n" \
"v_cndmask_b32_e64 v163, v252, v163, s[94:95]\n" \
"v_cndmask_b32_e64 v172, v252, v172, s[88:89]\n" \
"v_cndmask_b32_e64 v173, v252, v173, s[90:91]\n" \
"v_cndmask_b32_e64 v174, v252, v174, s[92:93]\n" \
"v_cndmask_b32_e64 v175, v252, v175, s[94:95]\n" \
"s_nop 0\n" \
"s_branch .LATLmb2\n" \
".LATLma2:\n" \
"v_mov_b32_e32 v160, v252\n" \
"v_mov_b32_e32 v161, v252\n" \
"v_mov_b32_e32 v162, v252\n" \
"v_mov_b32_e32 v163, v252\n" \
"v_mov_b32_e32 v164, v252\n" \
"v_mov_b32_e32 v165, v252\n" \
"v_mov_b32_e32 v166, v252\n" \
"v_mov_b32_e32 v167, v252\n" \
"v_mov_b32_e32 v168, v252\n" \
"v_mov_b32_e32 v169, v252\n" \
"v_mov_b32_e32 v170, v252\n" \
"v_mov_b32_e32 v171, v252\n" \
"v_mov_b32_e32 v172, v252\n" \
"v_mov_b32_e32 v173, v252\n" \
"v_mov_b32_e32 v174, v252\n" \
"v_mov_b32_e32 v175, v252\n" \
"s_nop 0\n" \
"s_branch .LATLmb2\n" \
".LATLmk3:\n" \
"v_cmp_le_i32_e64 s[88:89], 0, v225\n" \
"v_cmp_le_i32_e64 s[90:91], 1, v225\n" \
"v_cmp_le_i32_e64 s[92:93], 2, v225\n" \
"v_cmp_le_i32_e64 s[94:95], 3, v225\n" \
"v_mov_b32_e32 v164, v252\n" \
"v_mov_b32_e32 v165, v252\n" \
"v_mov_b32_e32 v166, v252\n" \
"v_mov_b32_e32 v167, v252\n" \
"v_cndmask_b32_e64 v160, v252, v160, s[88:89]\n" \
"v_cndmask_b32_e64 v161, v252, v161, s[90:91]\n" \
"v_cndmask_b32_e64 v162, v252, v162, s[92:93]\n" \
"v_cndmask_b32_e64 v163, v252, v163, s[94:95]\n" \
"v_cndmask_b32_e64 v172, v252, v172, s[88:89]\n" \
"v_cndmask_b32_e64 v173, v252, v173, s[90:91]\n" \
"v_cndmask_b32_e64 v174, v252, v174, s[92:93]\n" \
"v_cndmask_b32_e64 v175, v252, v175, s[94:95]\n" \
"s_nop 0\n" \
"s_branch .LATLmb3\n" \
".LATLmk4:\n" \
"s_cmp_eq_i32 s74, -1\n" \
"s_cbranch_scc0 .LATLma4\n" \
"v_cmp_le_i32_e64 s[88:89], 0, v225\n" \
"v_cmp_le_i32_e64 s[90:91], 1, v225\n" \
"v_cmp_le_i32_e64 s[92:93], 2, v225\n" \
"v_cmp_le_i32_e64 s[94:95], 3, v225\n" \
"v_mov_b32_e32 v164, v252\n" \
"v_mov_b32_e32 v165, v252\n" \
"v_mov_b32_e32 v166, v252\n" \
"v_mov_b32_e32 v167, v252\n" \
"v_cndmask_b32_e64 v160, v252, v160, s[88:89]\n" \
"v_cndmask_b32_e64 v161, v252, v161, s[90:91]\n" \
"v_cndmask_b32_e64 v162, v252, v162, s[92:93]\n" \
"v_cndmask_b32_e64 v163, v252, v163, s[94:95]\n" \
"v_cndmask_b32_e64 v172, v252, v172, s[88:89]\n" \
"v_cndmask_b32_e64 v173, v252, v173, s[90:91]\n" \
"v_cndmask_b32_e64 v174, v252, v174, s[92:93]\n" \
"v_cndmask_b32_e64 v175, v252, v175, s[94:95]\n" \
"s_nop 0\n" \
"s_branch .LATLmb4\n" \
".LATLma4:\n" \
"v_mov_b32_e32 v160, v252\n" \
"v_mov_b32_e32 v161, v252\n" \
"v_mov_b32_e32 v162, v252\n" \
"v_mov_b32_e32 v163, v252\n" \
"v_mov_b32_e32 v164, v252\n" \
"v_mov_b32_e32 v165, v252\n" \
"v_mov_b32_e32 v166, v252\n" \
"v_mov_b32_e32 v167, v252\n" \
"v_mov_b32_e32 v168, v252\n" \
"v_mov_b32_e32 v169, v252\n" \
"v_mov_b32_e32 v170, v252\n" \
"v_mov_b32_e32 v171, v252\n" \
"v_mov_b32_e32 v172, v252\n" \
"v_mov_b32_e32 v173, v252\n" \
"v_mov_b32_e32 v174, v252\n" \
"v_mov_b32_e32 v175, v252\n" \
"s_nop 0\n" \
"s_branch .LATLmb4\n" \
".LATLmk5:\n" \
"s_cmp_eq_i32 s74, -2\n" \
"s_cbranch_scc0 .LATLma5\n" \
"v_cmp_le_i32_e64 s[88:89], 0, v225\n" \
"v_cmp_le_i32_e64 s[90:91], 1, v225\n" \
"v_cmp_le_i32_e64 s[92:93], 2, v225\n" \
"v_cmp_le_i32_e64 s[94:95], 3, v225\n" \
"v_mov_b32_e32 v164, v252\n" \
"v_mov_b32_e32 v165, v252\n" \
"v_mov_b32_e32 v166, v252\n" \
"v_mov_b32_e32 v167, v252\n" \
"v_cndmask_b32_e64 v160, v252, v160, s[88:89]\n" \
"v_cndmask_b32_e64 v161, v252, v161, s[90:91]\n" \
"v_cndmask_b32_e64 v162, v252, v162, s[92:93]\n" \
"v_cndmask_b32_e64 v163, v252, v163, s[94:95]\n" \
"v_cndmask_b32_e64 v172, v252, v172, s[88:89]\n" \
"v_cndmask_b32_e64 v173, v252, v173, s[90:91]\n" \
"v_cndmask_b32_e64 v174, v252, v174, s[92:93]\n" \
"v_cndmask_b32_e64 v175, v252, v175, s[94:95]\n" \
"s_nop 0\n" \
"s_branch .LATLmb5\n" \
".LATLma5:\n" \
"v_mov_b32_e32 v160, v252\n" \
"v_mov_b32_e32 v161, v252\n" \
"v_mov_b32_e32 v162, v252\n" \
"v_mov_b32_e32 v163, v252\n" \
"v_mov_b32_e32 v164, v252\n" \
"v_mov_b32_e32 v165, v252\n" \
"v_mov_b32_e32 v166, v252\n" \
"v_mov_b32_e32 v167, v252\n" \
"v_mov_b32_e32 v168, v252\n" \
"v_mov_b32_e32 v169, v252\n" \
"v_mov_b32_e32 v170, v252\n" \
"v_mov_b32_e32 v171, v252\n" \
"v_mov_b32_e32 v172, v252\n" \
"v_mov_b32_e32 v173, v252\n" \
"v_mov_b32_e32 v174, v252\n" \
"v_mov_b32_e32 v175, v252\n" \
"s_nop 0\n" \
"s_branch .LATLmb5\n" \
".LATLmk6:\n" \
"s_cmp_eq_i32 s74, -3\n" \
"s_cbranch_scc0 .LATLma6\n" \
"v_cmp_le_i32_e64 s[88:89], 0, v225\n" \
"v_cmp_le_i32_e64 s[90:91], 1, v225\n" \
"v_cmp_le_i32_e64 s[92:93], 2, v225\n" \
"v_cmp_le_i32_e64 s[94:95], 3, v225\n" \
"v_mov_b32_e32 v164, v252\n" \
"v_mov_b32_e32 v165, v252\n" \
"v_mov_b32_e32 v166, v252\n" \
"v_mov_b32_e32 v167, v252\n" \
"v_cndmask_b32_e64 v160, v252, v160, s[88:89]\n" \
"v_cndmask_b32_e64 v161, v252, v161, s[90:91]\n" \
"v_cndmask_b32_e64 v162, v252, v162, s[92:93]\n" \
"v_cndmask_b32_e64 v163, v252, v163, s[94:95]\n" \
"v_cndmask_b32_e64 v172, v252, v172, s[88:89]\n" \
"v_cndmask_b32_e64 v173, v252, v173, s[90:91]\n" \
"v_cndmask_b32_e64 v174, v252, v174, s[92:93]\n" \
"v_cndmask_b32_e64 v175, v252, v175, s[94:95]\n" \
"s_nop 0\n" \
"s_branch .LATLmb6\n" \
".LATLma6:\n" \
"v_mov_b32_e32 v160, v252\n" \
"v_mov_b32_e32 v161, v252\n" \
"v_mov_b32_e32 v162, v252\n" \
"v_mov_b32_e32 v163, v252\n" \
"v_mov_b32_e32 v164, v252\n" \
"v_mov_b32_e32 v165, v252\n" \
"v_mov_b32_e32 v166, v252\n" \
"v_mov_b32_e32 v167, v252\n" \
"v_mov_b32_e32 v168, v252\n" \
"v_mov_b32_e32 v169, v252\n" \
"v_mov_b32_e32 v170, v252\n" \
"v_mov_b32_e32 v171, v252\n" \
"v_mov_b32_e32 v172, v252\n" \
"v_mov_b32_e32 v173, v252\n" \
"v_mov_b32_e32 v174, v252\n" \
"v_mov_b32_e32 v175, v252\n" \
"s_nop 0\n" \
"s_branch .LATLmb6\n" \
".LATLmk7:\n" \
"v_cmp_le_i32_e64 s[88:89], 0, v225\n" \
"v_cmp_le_i32_e64 s[90:91], 1, v225\n" \
"v_cmp_le_i32_e64 s[92:93], 2, v225\n" \
"v_cmp_le_i32_e64 s[94:95], 3, v225\n" \
"v_mov_b32_e32 v164, v252\n" \
"v_mov_b32_e32 v165, v252\n" \
"v_mov_b32_e32 v166, v252\n" \
"v_mov_b32_e32 v167, v252\n" \
"v_cndmask_b32_e64 v160, v252, v160, s[88:89]\n" \
"v_cndmask_b32_e64 v161, v252, v161, s[90:91]\n" \
"v_cndmask_b32_e64 v162, v252, v162, s[92:93]\n" \
"v_cndmask_b32_e64 v163, v252, v163, s[94:95]\n" \
"v_cndmask_b32_e64 v172, v252, v172, s[88:89]\n" \
"v_cndmask_b32_e64 v173, v252, v173, s[90:91]\n" \
"v_cndmask_b32_e64 v174, v252, v174, s[92:93]\n" \
"v_cndmask_b32_e64 v175, v252, v175, s[94:95]\n" \
"s_nop 0\n" \
"s_branch .LATLmb7\n" \
".LATLmk8:\n" \
"s_cmp_eq_i32 s74, -1\n" \
"s_cbranch_scc0 .LATLma8\n" \
"v_cmp_le_i32_e64 s[88:89], 0, v225\n" \
"v_cmp_le_i32_e64 s[90:91], 1, v225\n" \
"v_cmp_le_i32_e64 s[92:93], 2, v225\n" \
"v_cmp_le_i32_e64 s[94:95], 3, v225\n" \
"v_mov_b32_e32 v164, v252\n" \
"v_mov_b32_e32 v165, v252\n" \
"v_mov_b32_e32 v166, v252\n" \
"v_mov_b32_e32 v167, v252\n" \
"v_cndmask_b32_e64 v160, v252, v160, s[88:89]\n" \
"v_cndmask_b32_e64 v161, v252, v161, s[90:91]\n" \
"v_cndmask_b32_e64 v162, v252, v162, s[92:93]\n" \
"v_cndmask_b32_e64 v163, v252, v163, s[94:95]\n" \
"v_cndmask_b32_e64 v172, v252, v172, s[88:89]\n" \
"v_cndmask_b32_e64 v173, v252, v173, s[90:91]\n" \
"v_cndmask_b32_e64 v174, v252, v174, s[92:93]\n" \
"v_cndmask_b32_e64 v175, v252, v175, s[94:95]\n" \
"s_nop 0\n" \
"s_branch .LATLmb8\n" \
".LATLma8:\n" \
"v_mov_b32_e32 v160, v252\n" \
"v_mov_b32_e32 v161, v252\n" \
"v_mov_b32_e32 v162, v252\n" \
"v_mov_b32_e32 v163, v252\n" \
"v_mov_b32_e32 v164, v252\n" \
"v_mov_b32_e32 v165, v252\n" \
"v_mov_b32_e32 v166, v252\n" \
"v_mov_b32_e32 v167, v252\n" \
"v_mov_b32_e32 v168, v252\n" \
"v_mov_b32_e32 v169, v252\n" \
"v_mov_b32_e32 v170, v252\n" \
"v_mov_b32_e32 v171, v252\n" \
"v_mov_b32_e32 v172, v252\n" \
"v_mov_b32_e32 v173, v252\n" \
"v_mov_b32_e32 v174, v252\n" \
"v_mov_b32_e32 v175, v252\n" \
"s_nop 0\n" \
"s_branch .LATLmb8\n" \
".LATLmk9:\n" \
"v_cmp_le_i32_e64 s[88:89], 0, v225\n" \
"v_cmp_le_i32_e64 s[90:91], 1, v225\n" \
"v_cmp_le_i32_e64 s[92:93], 2, v225\n" \
"v_cmp_le_i32_e64 s[94:95], 3, v225\n" \
"v_mov_b32_e32 v180, v252\n" \
"v_mov_b32_e32 v181, v252\n" \
"v_mov_b32_e32 v182, v252\n" \
"v_mov_b32_e32 v183, v252\n" \
"v_cndmask_b32_e64 v176, v252, v176, s[88:89]\n" \
"v_cndmask_b32_e64 v177, v252, v177, s[90:91]\n" \
"v_cndmask_b32_e64 v178, v252, v178, s[92:93]\n" \
"v_cndmask_b32_e64 v179, v252, v179, s[94:95]\n" \
"v_cndmask_b32_e64 v188, v252, v188, s[88:89]\n" \
"v_cndmask_b32_e64 v189, v252, v189, s[90:91]\n" \
"v_cndmask_b32_e64 v190, v252, v190, s[92:93]\n" \
"v_cndmask_b32_e64 v191, v252, v191, s[94:95]\n" \
"s_nop 0\n" \
"s_branch .LATLmb9\n" \
".LATLsa:\n" \
"s_mov_b32 s96, 0x2000\n" \
".LATLsa_l:\n" \
"s_sleep 1\n" \
"buffer_load_dword v247, v242, %[wsr], s80 offen offset:768 sc1\n" \
"s_waitcnt vmcnt(0)\n" \
"v_cmp_eq_u32_e32 vcc, v246, v247\n" \
"s_sub_u32 s96, s96, 1\n" \
"s_cbranch_scc1 .LATLsab\n" \
"s_nop 2\n" \
"s_and_b32 s88, vcc_lo, 0xffff\n" \
"s_cbranch_scc1 .LATLsa_l\n" \
"s_branch .LATLsab\n" \
".LATLmk10:\n" \
"v_cmp_le_i32_e64 s[88:89], 0, v225\n" \
"v_cmp_le_i32_e64 s[90:91], 1, v225\n" \
"v_cmp_le_i32_e64 s[92:93], 2, v225\n" \
"v_cmp_le_i32_e64 s[94:95], 3, v225\n" \
"v_mov_b32_e32 v180, v252\n" \
"v_mov_b32_e32 v181, v252\n" \
"v_mov_b32_e32 v182, v252\n" \
"v_mov_b32_e32 v183, v252\n" \
"v_cndmask_b32_e64 v176, v252, v176, s[88:89]\n" \
"v_cndmask_b32_e64 v177, v252, v177, s[90:91]\n" \
"v_cndmask_b32_e64 v178, v252, v178, s[92:93]\n" \
"v_cndmask_b32_e64 v179, v252, v179, s[94:95]\n" \
"v_cndmask_b32_e64 v188, v252, v188, s[88:89]\n" \
"v_cndmask_b32_e64 v189, v252, v189, s[90:91]\n" \
"v_cndmask_b32_e64 v190, v252, v190, s[92:93]\n" \
"v_cndmask_b32_e64 v191, v252, v191, s[94:95]\n" \
"s_nop 0\n" \
"s_branch .LATLmb10\n" \
".LATLmk11:\n" \
"v_cmp_le_i32_e64 s[88:89], 0, v225\n" \
"v_cmp_le_i32_e64 s[90:91], 1, v225\n" \
"v_cmp_le_i32_e64 s[92:93], 2, v225\n" \
"v_cmp_le_i32_e64 s[94:95], 3, v225\n" \
"v_mov_b32_e32 v180, v252\n" \
"v_mov_b32_e32 v181, v252\n" \
"v_mov_b32_e32 v182, v252\n" \
"v_mov_b32_e32 v183, v252\n" \
"v_cndmask_b32_e64 v176, v252, v176, s[88:89]\n" \
"v_cndmask_b32_e64 v177, v252, v177, s[90:91]\n" \
"v_cndmask_b32_e64 v178, v252, v178, s[92:93]\n" \
"v_cndmask_b32_e64 v179, v252, v179, s[94:95]\n" \
"v_cndmask_b32_e64 v188, v252, v188, s[88:89]\n" \
"v_cndmask_b32_e64 v189, v252, v189, s[90:91]\n" \
"v_cndmask_b32_e64 v190, v252, v190, s[92:93]\n" \
"v_cndmask_b32_e64 v191, v252, v191, s[94:95]\n" \
"s_nop 0\n" \
"s_branch .LATLmb11\n" \
".LATLz0:\n" \
"buffer_load_dword v247, v242, %[wsr], s80 offen offset:768 sc1\n" \
"s_waitcnt vmcnt(1) lgkmcnt(0)\n" \
"s_barrier\n" \
"buffer_atomic_add v252, v253, %[wsr], s79 offen offset:512 sc1\n" \
"v_cmp_le_i32_e64 s[88:89], 0, v225\n" \
"v_cmp_le_i32_e64 s[90:91], 1, v225\n" \
"v_cmp_le_i32_e64 s[92:93], 2, v225\n" \
"v_cmp_le_i32_e64 s[94:95], 3, v225\n" \
"v_cndmask_b32_e64 v176, v252, v176, s[88:89]\n" \
"v_cndmask_b32_e64 v177, v252, v177, s[90:91]\n" \
"v_cndmask_b32_e64 v178, v252, v178, s[92:93]\n" \
"v_cndmask_b32_e64 v179, v252, v179, s[94:95]\n" \
"v_cndmask_b32_e64 v188, v252, v188, s[88:89]\n" \
"v_cndmask_b32_e64 v189, v252, v189, s[90:91]\n" \
"v_cndmask_b32_e64 v190, v252, v190, s[92:93]\n" \
"v_cndmask_b32_e64 v191, v252, v191, s[94:95]\n" \
"s_nop 0\n" \
"s_waitcnt lgkmcnt(8)\n" \
"v_mfma_f32_16x16x32_bf16 v[128:131], a[64:67], a[32:35], v[176:179]\n" \
"v_exp_f32_e32 v144, v144\n" \
"v_mfma_f32_16x16x32_bf16 v[136:139], a[64:67], a[48:51], v[184:187]\n" \
"v_exp_f32_e32 v145, v145\n" \
"v_mfma_f32_16x16x32_bf16 v[140:143], a[68:71], a[48:51], v[188:191]\n" \
"v_exp_f32_e32 v146, v146\n" \
"v_mfma_f32_16x16x32_bf16 v[128:131], a[72:75], a[36:39], v[128:131]\n" \
"v_cvt_pk_bf16_f32 v200, v144, v145\n" \
"v_mfma_f32_16x16x32_bf16 v[136:139], a[72:75], a[52:55], v[136:139]\n" \
"v_exp_f32_e32 v147, v147\n" \
"v_mfma_f32_16x16x32_bf16 v[140:143], a[76:79], a[52:55], v[140:143]\n" \
"v_exp_f32_e32 v148, v148\n" \
"v_mfma_f32_16x16x32_bf16 v[128:131], a[80:83], a[40:43], v[128:131]\n" \
"v_cvt_pk_bf16_f32 v201, v146, v147\n" \
"v_mfma_f32_16x16x32_bf16 v[136:139], a[80:83], a[56:59], v[136:139]\n" \
"v_exp_f32_e32 v149, v149\n" \
"v_mfma_f32_16x16x32_bf16 v[140:143], a[84:87], a[56:59], v[140:143]\n" \
"v_exp_f32_e32 v150, v150\n" \
"v_mfma_f32_16x16x32_bf16 v[128:131], a[88:91], a[44:47], v[128:131]\n" \
"v_cvt_pk_bf16_f32 v202, v148, v149\n" \
"v_mfma_f32_16x16x32_bf16 v[136:139], a[88:91], a[60:63], v[136:139]\n" \
"v_exp_f32_e32 v151, v151\n" \
"v_mfma_f32_16x16x32_bf16 v[140:143], a[92:95], a[60:63], v[140:143]\n" \
"s_nop 0\n" \
"v_exp_f32_e32 v152, v152\n" \
"v_mfma_f32_16x16x32_bf16 v[64:67], a[96:99], v[192:195], v[64:67]\n" \
"v_cvt_pk_bf16_f32 v203, v150, v151\n" \
"v_mfma_f32_16x16x32_bf16 v[96:99], a[96:99], v[196:199], v[96:99]\n" \
"ds_read_b128 a[96:99], v235 offset:57344\n" \
"v_mfma_f32_16x16x32_bf16 v[100:103], a[100:103], v[196:199], v[100:103]\n" \
"v_exp_f32_e32 v153, v153\n" \
"v_mfma_f32_16x16x32_bf16 v[68:71], a[100:103], v[192:195], v[68:71]\n" \
"ds_read_b128 a[100:103], v235 offset:58368\n" \
"v_mfma_f32_16x16x32_bf16 v[72:75], a[104:107], v[192:195], v[72:75]\n" \
"v_exp_f32_e32 v154, v154\n" \
"v_mfma_f32_16x16x32_bf16 v[104:107], a[104:107], v[196:199], v[104:107]\n" \
"ds_read_b128 a[104:107], v235 offset:59392\n" \
"v_cvt_pk_bf16_f32 v204, v152, v153\n" \
"v_mfma_f32_16x16x32_bf16 v[108:111], a[108:111], v[196:199], v[108:111]\n" \
"v_exp_f32_e32 v155, v155\n" \
"v_mfma_f32_16x16x32_bf16 v[76:79], a[108:111], v[192:195], v[76:79]\n" \
"ds_read_b128 a[108:111], v235 offset:60416\n" \
"v_mfma_f32_16x16x32_bf16 v[80:83], a[112:115], v[192:195], v[80:83]\n" \
"v_exp_f32_e32 v156, v156\n" \
"v_mfma_f32_16x16x32_bf16 v[112:115], a[112:115], v[196:199], v[112:115]\n" \
"ds_read_b128 a[112:115], v235 offset:61440\n" \
"v_cvt_pk_bf16_f32 v205, v154, v155\n" \
"v_mfma_f32_16x16x32_bf16 v[116:119], a[116:119], v[196:199], v[116:119]\n" \
"v_exp_f32_e32 v157, v157\n" \
"v_mfma_f32_16x16x32_bf16 v[84:87], a[116:119], v[192:195], v[84:87]\n" \
"ds_read_b128 a[116:119], v235 offset:62464\n" \
"v_mfma_f32_16x16x32_bf16 v[88:91], a[120:123], v[192:195], v[88:91]\n" \
"v_exp_f32_e32 v158, v158\n" \
"v_mfma_f32_16x16x32_bf16 v[120:123], a[120:123], v[196:199], v[120:123]\n" \
"ds_read_b128 a[120:123], v235 offset:63488\n" \
"v_cvt_pk_bf16_f32 v206, v156, v157\n" \
"v_mfma_f32_16x16x32_bf16 v[124:127], a[124:127], v[196:199], v[124:127]\n" \
"v_exp_f32_e32 v159, v159\n" \
"v_mfma_f32_16x16x32_bf16 v[92:95], a[124:127], v[192:195], v[92:95]\n" \
"ds_read_b128 a[124:127], v235 offset:64512\n" \
"v_cvt_pk_bf16_f32 v207, v158, v159\n" \
"v_smfmac_f32_16x16x64_bf16 a[248:251], a[236:239], v[192:199], v248\n" \
".LATLdw0:\n" \
"s_waitcnt vmcnt(1)\n" \
".LATLdr0:\n" \
"v_cmp_eq_u32_e32 vcc, v246, v247\n" \
"s_xor_b32 s70, s77, 4096\n" \
"s_xor_b32 s71, s77, 262144\n" \
"s_xor_b32 s72, s77, 266240\n" \
"s_and_b32 s88, vcc_lo, 0xffff\n" \
"s_cbranch_scc1 .LATLdn0\n" \
"s_waitcnt lgkmcnt(4)\n" \
"v_mfma_f32_16x16x32_bf16 v[64:67], a[96:99], v[200:203], v[64:67]\n" \
"v_mov_b32_e32 v178, 0\n" \
"v_mov_b32_e32 v179, 0\n" \
"v_exp_f32_e32 v128, v128\n" \
"s_add_u32 m0, %[w4k], 0x20000\n" \
"s_add_u32 s60, s70, 0x0\n" \
"v_mfma_f32_16x16x32_bf16 v[96:99], a[96:99], v[204:207], v[96:99]\n" \
"ds_read_b128 a[96:99], v234 offset:8192\n" \
"v_exp_f32_e32 v129, v129\n" \
"buffer_load_dwordx4 v241, %[wsr], s60 offen offset:0 sc1 lds\n" \
"v_mfma_f32_16x16x32_bf16 v[100:103], a[100:103], v[204:207], v[100:103]\n" \
"v_exp_f32_e32 v130, v130\n" \
"buffer_load_dwordx4 v241, %[wsr], s60 offen offset:1024 sc1 lds\n" \
"v_mfma_f32_16x16x32_bf16 v[68:71], a[100:103], v[200:203], v[68:71]\n" \
"ds_read_b128 a[100:103], v234 offset:9216\n" \
"v_cvt_pk_bf16_f32 v176, v128, v129\n" \
"buffer_load_dwordx4 v241, %[wsr], s60 offen offset:2048 sc1 lds\n" \
"v_mfma_f32_16x16x32_bf16 v[72:75], a[104:107], v[200:203], v[72:75]\n" \
"v_exp_f32_e32 v131, v131\n" \
"buffer_load_dwordx4 v241, %[wsr], s60 offen offset:3072 sc1 lds\n" \
"s_add_u32 m0, %[w4k], 0x21000\n" \
"s_add_u32 s60, s70, 0x2000\n" \
"v_mfma_f32_16x16x32_bf16 v[104:107], a[104:107], v[204:207], v[104:107]\n" \
"ds_read_b128 a[104:107], v234 offset:10240\n" \
"v_cvt_pk_bf16_f32 v177, v130, v131\n" \
"buffer_load_dwordx4 v241, %[wsr], s60 offen offset:0 sc1 lds\n" \
"v_mfma_f32_16x16x32_bf16 v[108:111], a[108:111], v[204:207], v[108:111]\n" \
"v_exp_f32_e32 v136, v136\n" \
"buffer_load_dwordx4 v241, %[wsr], s60 offen offset:1024 sc1 lds\n" \
"v_mfma_f32_16x16x32_bf16 v[76:79], a[108:111], v[200:203], v[76:79]\n" \
"s_waitcnt lgkmcnt(3)\n" \
"ds_read_b128 a[108:111], v234 offset:11264\n" \
"v_exp_f32_e32 v137, v137\n" \
"buffer_load_dwordx4 v241, %[wsr], s60 offen offset:2048 sc1 lds\n" \
"v_mfma_f32_16x16x32_bf16 v[80:83], a[112:115], v[200:203], v[80:83]\n" \
"v_exp_f32_e32 v138, v138\n" \
"v_cvt_pk_bf16_f32 v180, v136, v137\n" \
"buffer_load_dwordx4 v241, %[wsr], s60 offen offset:3072 sc1 lds\n" \
"s_add_u32 m0, %[w4k], 0x22000\n" \
"s_add_u32 s60, s70, 0x4000\n" \
"v_mfma_f32_16x16x32_bf16 v[112:115], a[112:115], v[204:207], v[112:115]\n" \
"ds_read_b128 a[112:115], v234 offset:12288\n" \
"v_exp_f32_e32 v139, v139\n" \
"buffer_load_dwordx4 v241, %[wsr], s60 offen offset:0 sc1 lds\n" \
"v_mfma_f32_16x16x32_bf16 v[116:119], a[116:119], v[204:207], v[116:119]\n" \
"v_exp_f32_e32 v140, v140\n" \
"v_cvt_pk_bf16_f32 v181, v138, v139\n" \
"buffer_load_dwordx4 v241, %[wsr], s60 offen offset:1024 sc1 lds\n" \
"v_mfma_f32_16x16x32_bf16 v[84:87], a[116:119], v[200:203], v[84:87]\n" \
"ds_read_b128 a[116:119], v234 offset:13312\n" \
"v_exp_f32_e32 v141, v141\n" \
"buffer_load_dwordx4 v241, %[wsr], s60 offen offset:2048 sc1 lds\n" \
"v_mfma_f32_16x16x32_bf16 v[88:91], a[120:123], v[200:203], v[88:91]\n" \
"v_exp_f32_e32 v142, v142\n" \
"buffer_load_dwordx4 v241, %[wsr], s60 offen offset:3072 sc1 lds\n" \
"s_add_u32 m0, %[w4k], 0x23000\n" \
"s_add_u32 s60, s70, 0x6000\n" \
"v_mfma_f32_16x16x32_bf16 v[120:123], a[120:123], v[204:207], v[120:123]\n" \
"ds_read_b128 a[120:123], v234 offset:14336\n" \
"v_cvt_pk_bf16_f32 v182, v140, v141\n" \
"buffer_load_dwordx4 v241, %[wsr], s60 offen offset:0 sc1 lds\n" \
"v_mfma_f32_16x16x32_bf16 v[124:127], a[124:127], v[204:207], v[124:127]\n" \
"v_exp_f32_e32 v143, v143\n" \
"buffer_load_dwordx4 v241, %[wsr], s60 offen offset:1024 sc1 lds\n" \
"v_mfma_f32_16x16x32_bf16 v[92:95], a[124:127], v[200:203], v[92:95]\n" \
"ds_read_b128 a[124:127], v234 offset:15360\n" \
"v_cvt_pk_bf16_f32 v183, v142, v143\n" \
"buffer_load_dwordx4 v241, %[wsr], s60 offen offset:2048 sc1 lds\n" \
"v_smfmac_f32_16x16x64_bf16 a[248:251], a[236:239], v[200:207], v248\n" \
"buffer_load_dwordx4 v241, %[wsr], s60 offen offset:3072 sc1 lds\n" \
"s_add_u32 m0, %[w4k], 0x24000\n" \
"s_add_u32 s60, s70, 0x8000\n" \
"buffer_load_dwordx4 v241, %[wsr], s60 offen offset:0 sc1 lds\n" \
"buffer_load_dwordx4 v241, %[wsr], s60 offen offset:1024 sc1 lds\n" \
"buffer_load_dwordx4 v241, %[wsr], s60 offen offset:2048 sc1 lds\n" \
"buffer_load_dwordx4 v241, %[wsr], s60 offen offset:3072 sc1 lds\n" \
"s_add_u32 m0, %[w4k], 0x25000\n" \
"s_add_u32 s60, s70, 0xa000\n" \
"buffer_load_dwordx4 v241, %[wsr], s60 offen offset:0 sc1 lds\n" \
"buffer_load_dwordx4 v241, %[wsr], s60 offen offset:1024 sc1 lds\n" \
"buffer_load_dwordx4 v241, %[wsr], s60 offen offset:2048 sc1 lds\n" \
"buffer_load_dwordx4 v241, %[wsr], s60 offen offset:3072 sc1 lds\n" \
"s_add_u32 m0, %[w4k], 0x26000\n" \
"s_add_u32 s60, s70, 0xc000\n" \
"buffer_load_dwordx4 v241, %[wsr], s60 offen offset:0 sc1 lds\n" \
"buffer_load_dwordx4 v241, %[wsr], s60 offen offset:1024 sc1 lds\n" \
"buffer_load_dwordx4 v241, %[wsr], s60 offen offset:2048 sc1 lds\n" \
"buffer_load_dwordx4 v241, %[wsr], s60 offen offset:3072 sc1 lds\n" \
"s_add_u32 m0, %[w4k], 0x27000\n" \
"s_add_u32 s60, s70, 0xe000\n" \
"buffer_load_dwordx4 v241, %[wsr], s60 offen offset:0 sc1 lds\n" \
"buffer_load_dwordx4 v241, %[wsr], s60 offen offset:1024 sc1 lds\n" \
"buffer_load_dwordx4 v241, %[wsr], s60 offen offset:2048 sc1 lds\n" \
"buffer_load_dwordx4 v241, %[wsr], s60 offen offset:3072 sc1 lds\n" \
"s_add_u32 m0, s82, 0x0\n" \
"s_add_u32 s60, s71, 0x0\n" \
"buffer_load_dwordx4 v241, %[wsr], s60 offen offset:0 sc1 lds\n" \
"buffer_load_dwordx4 v241, %[wsr], s60 offen offset:1024 sc1 lds\n" \
"buffer_load_dwordx4 v241, %[wsr], s60 offen offset:2048 sc1 lds\n" \
"buffer_load_dwordx4 v241, %[wsr], s60 offen offset:3072 sc1 lds\n" \
"s_add_u32 m0, s82, 0x1000\n" \
"s_add_u32 s60, s71, 0x2000\n" \
"buffer_load_dwordx4 v241, %[wsr], s60 offen offset:0 sc1 lds\n" \
"buffer_load_dwordx4 v241, %[wsr], s60 offen offset:1024 sc1 lds\n" \
"buffer_load_dwordx4 v241, %[wsr], s60 offen offset:2048 sc1 lds\n" \
"buffer_load_dwordx4 v241, %[wsr], s60 offen offset:3072 sc1 lds\n" \
"s_mov_b32 s73, 0\n" \
"s_branch .LATLfin\n" \
".LATLdw1:\n" \
"s_waitcnt vmcnt(0)\n" \
".LATLdr1:\n" \
"v_cmp_eq_u32_e32 vcc, v246, v247\n" \
"s_xor_b32 s70, s77, 4096\n" \
"s_xor_b32 s71, s77, 262144\n" \
"s_xor_b32 s72, s77, 266240\n" \
"s_and_b32 s88, vcc_lo, 0xffff\n" \
"s_cbranch_scc1 .LATLdn1\n" \
"s_waitcnt lgkmcnt(4)\n" \
"v_mfma_f32_16x16x32_bf16 v[64:67], a[96:99], v[192:195], v[64:67]\n" \
"v_mov_b32_e32 v178, 0\n" \
"v_mov_b32_e32 v179, 0\n" \
"v_exp_f32_e32 v144, v144\n" \
"s_add_u32 m0, s82, 0x1000\n" \
"s_add_u32 s60, s71, 0x2000\n" \
"v_mfma_f32_16x16x32_bf16 v[96:99], a[96:99], v[196:199], v[96:99]\n" \
"ds_read_b128 a[96:99], v234 offset:24576\n" \
"v_exp_f32_e32 v145, v145\n" \
"buffer_load_dwordx4 v241, %[wsr], s60 offen offset:0 sc1 lds\n" \
"v_mfma_f32_16x16x32_bf16 v[100:103], a[100:103], v[196:199], v[100:103]\n" \
"v_exp_f32_e32 v146, v146\n" \
"buffer_load_dwordx4 v241, %[wsr], s60 offen offset:1024 sc1 lds\n" \
"v_mfma_f32_16x16x32_bf16 v[68:71], a[100:103], v[192:195], v[68:71]\n" \
"ds_read_b128 a[100:103], v234 offset:25600\n" \
"v_cvt_pk_bf16_f32 v176, v144, v145\n" \
"buffer_load_dwordx4 v241, %[wsr], s60 offen offset:2048 sc1 lds\n" \
"v_mfma_f32_16x16x32_bf16 v[72:75], a[104:107], v[192:195], v[72:75]\n" \
"v_exp_f32_e32 v147, v147\n" \
"buffer_load_dwordx4 v241, %[wsr], s60 offen offset:3072 sc1 lds\n" \
"s_add_u32 m0, s82, 0x2000\n" \
"s_add_u32 s60, s71, 0x4000\n" \
"v_mfma_f32_16x16x32_bf16 v[104:107], a[104:107], v[196:199], v[104:107]\n" \
"ds_read_b128 a[104:107], v234 offset:26624\n" \
"v_cvt_pk_bf16_f32 v177, v146, v147\n" \
"buffer_load_dwordx4 v241, %[wsr], s60 offen offset:0 sc1 lds\n" \
"v_mfma_f32_16x16x32_bf16 v[108:111], a[108:111], v[196:199], v[108:111]\n" \
"v_exp_f32_e32 v152, v152\n" \
"buffer_load_dwordx4 v241, %[wsr], s60 offen offset:1024 sc1 lds\n" \
"v_mfma_f32_16x16x32_bf16 v[76:79], a[108:111], v[192:195], v[76:79]\n" \
"s_waitcnt lgkmcnt(3)\n" \
"ds_read_b128 a[108:111], v234 offset:27648\n" \
"v_exp_f32_e32 v153, v153\n" \
"buffer_load_dwordx4 v241, %[wsr], s60 offen offset:2048 sc1 lds\n" \
"v_mfma_f32_16x16x32_bf16 v[80:83], a[112:115], v[192:195], v[80:83]\n" \
"v_exp_f32_e32 v154, v154\n" \
"v_cvt_pk_bf16_f32 v180, v152, v153\n" \
"buffer_load_dwordx4 v241, %[wsr], s60 offen offset:3072 sc1 lds\n" \
"s_add_u32 m0, s82, 0x3000\n" \
"s_add_u32 s60, s71, 0x6000\n" \
"v_mfma_f32_16x16x32_bf16 v[112:115], a[112:115], v[196:199], v[112:115]\n" \
"ds_read_b128 a[112:115], v234 offset:28672\n" \
"v_exp_f32_e32 v155, v155\n" \
"buffer_load_dwordx4 v241, %[wsr], s60 offen offset:0 sc1 lds\n" \
"v_mfma_f32_16x16x32_bf16 v[116:119], a[116:119], v[196:199], v[116:119]\n" \
"v_exp_f32_e32 v156, v156\n" \
"v_cvt_pk_bf16_f32 v181, v154, v155\n" \
"buffer_load_dwordx4 v241, %[wsr], s60 offen offset:1024 sc1 lds\n" \
"v_mfma_f32_16x16x32_bf16 v[84:87], a[116:119], v[192:195], v[84:87]\n" \
"ds_read_b128 a[116:119], v234 offset:29696\n" \
"v_exp_f32_e32 v157, v157\n" \
"buffer_load_dwordx4 v241, %[wsr], s60 offen offset:2048 sc1 lds\n" \
"v_mfma_f32_16x16x32_bf16 v[88:91], a[120:123], v[192:195], v[88:91]\n" \
"v_exp_f32_e32 v158, v158\n" \
"buffer_load_dwordx4 v241, %[wsr], s60 offen offset:3072 sc1 lds\n" \
"s_add_u32 m0, s82, 0x4000\n" \
"s_add_u32 s60, s71, 0x8000\n" \
"v_mfma_f32_16x16x32_bf16 v[120:123], a[120:123], v[196:199], v[120:123]\n" \
"ds_read_b128 a[120:123], v234 offset:30720\n" \
"v_cvt_pk_bf16_f32 v182, v156, v157\n" \
"buffer_load_dwordx4 v241, %[wsr], s60 offen offset:0 sc1 lds\n" \
"v_mfma_f32_16x16x32_bf16 v[124:127], a[124:127], v[196:199], v[124:127]\n" \
"v_exp_f32_e32 v159, v159\n" \
"buffer_load_dwordx4 v241, %[wsr], s60 offen offset:1024 sc1 lds\n" \
"v_mfma_f32_16x16x32_bf16 v[92:95], a[124:127], v[192:195], v[92:95]\n" \
"ds_read_b128 a[124:127], v234 offset:31744\n" \
"v_cvt_pk_bf16_f32 v183, v158, v159\n" \
"buffer_load_dwordx4 v241, %[wsr], s60 offen offset:2048 sc1 lds\n" \
"v_smfmac_f32_16x16x64_bf16 a[248:251], a[236:239], v[192:199], v248\n" \
"buffer_load_dwordx4 v241, %[wsr], s60 offen offset:3072 sc1 lds\n" \
"s_add_u32 m0, s82, 0x5000\n" \
"s_add_u32 s60, s71, 0xa000\n" \
"buffer_load_dwordx4 v241, %[wsr], s60 offen offset:0 sc1 lds\n" \
"buffer_load_dwordx4 v241, %[wsr], s60 offen offset:1024 sc1 lds\n" \
"buffer_load_dwordx4 v241, %[wsr], s60 offen offset:2048 sc1 lds\n" \
"buffer_load_dwordx4 v241, %[wsr], s60 offen offset:3072 sc1 lds\n" \
"s_add_u32 m0, s82, 0x6000\n" \
"s_add_u32 s60, s71, 0xc000\n" \
"buffer_load_dwordx4 v241, %[wsr], s60 offen offset:0 sc1 lds\n" \
"buffer_load_dwordx4 v241, %[wsr], s60 offen offset:1024 sc1 lds\n" \
"buffer_load_dwordx4 v241, %[wsr], s60 offen offset:2048 sc1 lds\n" \
"buffer_load_dwordx4 v241, %[wsr], s60 offen offset:3072 sc1 lds\n" \
"s_mov_b32 s73, 0\n" \
"s_branch .LATLfin\n" \
".LATLdw2:\n" \
"s_waitcnt vmcnt(0)\n" \
".LATLdr2:\n" \
"s_waitcnt lgkmcnt(4)\n" \
"v_mfma_f32_16x16x32_bf16 v[64:67], a[96:99], v[200:203], v[64:67]\n" \
"v_mov_b32_e32 v178, 0\n" \
"v_mov_b32_e32 v179, 0\n" \
"v_exp_f32_e32 v128, v128\n" \
"v_mfma_f32_16x16x32_bf16 v[96:99], a[96:99], v[204:207], v[96:99]\n" \
"ds_read_b128 a[96:99], v234 offset:40960\n" \
"v_exp_f32_e32 v129, v129\n" \
"v_mfma_f32_16x16x32_bf16 v[100:103], a[100:103], v[204:207], v[100:103]\n" \
"s_xor_b32 s70, s77, 4096\n" \
"v_exp_f32_e32 v130, v130\n" \
"v_mfma_f32_16x16x32_bf16 v[68:71], a[100:103], v[200:203], v[68:71]\n" \
"s_xor_b32 s71, s77, 262144\n" \
"ds_read_b128 a[100:103], v234 offset:41984\n" \
"v_cvt_pk_bf16_f32 v176, v128, v129\n" \
"v_mfma_f32_16x16x32_bf16 v[72:75], a[104:107], v[200:203], v[72:75]\n" \
"s_xor_b32 s72, s77, 266240\n" \
"v_exp_f32_e32 v131, v131\n" \
"v_mfma_f32_16x16x32_bf16 v[104:107], a[104:107], v[204:207], v[104:107]\n" \
"ds_read_b128 a[104:107], v234 offset:43008\n" \
"v_cvt_pk_bf16_f32 v177, v130, v131\n" \
"v_mfma_f32_16x16x32_bf16 v[108:111], a[108:111], v[204:207], v[108:111]\n" \
"v_exp_f32_e32 v136, v136\n" \
"v_mfma_f32_16x16x32_bf16 v[76:79], a[108:111], v[200:203], v[76:79]\n" \
"s_waitcnt lgkmcnt(3)\n" \
"ds_read_b128 a[108:111], v234 offset:44032\n" \
"v_exp_f32_e32 v137, v137\n" \
"v_mfma_f32_16x16x32_bf16 v[80:83], a[112:115], v[200:203], v[80:83]\n" \
"v_exp_f32_e32 v138, v138\n" \
"v_cvt_pk_bf16_f32 v180, v136, v137\n" \
"v_mfma_f32_16x16x32_bf16 v[112:115], a[112:115], v[204:207], v[112:115]\n" \
"ds_read_b128 a[112:115], v234 offset:45056\n" \
"v_exp_f32_e32 v139, v139\n" \
"v_mfma_f32_16x16x32_bf16 v[116:119], a[116:119], v[204:207], v[116:119]\n" \
"v_exp_f32_e32 v140, v140\n" \
"v_cvt_pk_bf16_f32 v181, v138, v139\n" \
"v_mfma_f32_16x16x32_bf16 v[84:87], a[116:119], v[200:203], v[84:87]\n" \
"ds_read_b128 a[116:119], v234 offset:46080\n" \
"v_exp_f32_e32 v141, v141\n" \
"v_mfma_f32_16x16x32_bf16 v[88:91], a[120:123], v[200:203], v[88:91]\n" \
"v_exp_f32_e32 v142, v142\n" \
"v_mfma_f32_16x16x32_bf16 v[120:123], a[120:123], v[204:207], v[120:123]\n" \
"ds_read_b128 a[120:123], v234 offset:47104\n" \
"v_cvt_pk_bf16_f32 v182, v140, v141\n" \
"v_cmp_eq_u32_e32 vcc, v246, v247\n" \
"v_mfma_f32_16x16x32_bf16 v[124:127], a[124:127], v[204:207], v[124:127]\n" \
"v_exp_f32_e32 v143, v143\n" \
"v_mfma_f32_16x16x32_bf16 v[92:95], a[124:127], v[200:203], v[92:95]\n" \
"ds_read_b128 a[124:127], v234 offset:48128\n" \
"v_cvt_pk_bf16_f32 v183, v142, v143\n" \
"v_smfmac_f32_16x16x64_bf16 a[248:251], a[236:239], v[200:207], v248\n" \
"s_and_b32 s73, vcc_lo, 0xffff\n" \
"s_branch .LATLfin\n" \
".LATLdn0:\n" \
"buffer_load_dword v247, v242, %[wsr], s80 offen offset:768 sc1\n" \
"s_waitcnt lgkmcnt(4)\n" \
"v_mfma_f32_16x16x32_bf16 v[64:67], a[96:99], v[200:203], v[64:67]\n" \
"v_mov_b32_e32 v178, 0\n" \
"v_mov_b32_e32 v179, 0\n" \
"v_exp_f32_e32 v128, v128\n" \
"v_mfma_f32_16x16x32_bf16 v[96:99], a[96:99], v[204:207], v[96:99]\n" \
"ds_read_b128 a[96:99], v234 offset:8192\n" \
"v_exp_f32_e32 v129, v129\n" \
"v_mfma_f32_16x16x32_bf16 v[100:103], a[100:103], v[204:207], v[100:103]\n" \
"v_exp_f32_e32 v130, v130\n" \
"v_mfma_f32_16x16x32_bf16 v[68:71], a[100:103], v[200:203], v[68:71]\n" \
"ds_read_b128 a[100:103], v234 offset:9216\n" \
"v_cvt_pk_bf16_f32 v176, v128, v129\n" \
"v_mfma_f32_16x16x32_bf16 v[72:75], a[104:107], v[200:203], v[72:75]\n" \
"v_exp_f32_e32 v131, v131\n" \
"v_mfma_f32_16x16x32_bf16 v[104:107], a[104:107], v[204:207], v[104:107]\n" \
"ds_read_b128 a[104:107], v234 offset:10240\n" \
"v_cvt_pk_bf16_f32 v177, v130, v131\n" \
"v_mfma_f32_16x16x32_bf16 v[108:111], a[108:111], v[204:207], v[108:111]\n" \
"v_exp_f32_e32 v136, v136\n" \
"v_mfma_f32_16x16x32_bf16 v[76:79], a[108:111], v[200:203], v[76:79]\n" \
"s_waitcnt lgkmcnt(3)\n" \
"ds_read_b128 a[108:111], v234 offset:11264\n" \
"v_exp_f32_e32 v137, v137\n" \
"v_mfma_f32_16x16x32_bf16 v[80:83], a[112:115], v[200:203], v[80:83]\n" \
"v_exp_f32_e32 v138, v138\n" \
"v_cvt_pk_bf16_f32 v180, v136, v137\n" \
"v_mfma_f32_16x16x32_bf16 v[112:115], a[112:115], v[204:207], v[112:115]\n" \
"ds_read_b128 a[112:115], v234 offset:12288\n" \
"v_exp_f32_e32 v139, v139\n" \
"v_mfma_f32_16x16x32_bf16 v[116:119], a[116:119], v[204:207], v[116:119]\n" \
"v_exp_f32_e32 v140, v140\n" \
"v_cvt_pk_bf16_f32 v181, v138, v139\n" \
"v_mfma_f32_16x16x32_bf16 v[84:87], a[116:119], v[200:203], v[84:87]\n" \
"ds_read_b128 a[116:119], v234 offset:13312\n" \
"v_exp_f32_e32 v141, v141\n" \
"v_mfma_f32_16x16x32_bf16 v[88:91], a[120:123], v[200:203], v[88:91]\n" \
"v_exp_f32_e32 v142, v142\n" \
"v_mfma_f32_16x16x32_bf16 v[120:123], a[120:123], v[204:207], v[120:123]\n" \
"ds_read_b128 a[120:123], v234 offset:14336\n" \
"v_cvt_pk_bf16_f32 v182, v140, v141\n" \
"v_mfma_f32_16x16x32_bf16 v[124:127], a[124:127], v[204:207], v[124:127]\n" \
"v_exp_f32_e32 v143, v143\n" \
"s_waitcnt vmcnt(0)\n" \
"v_cmp_eq_u32_e32 vcc, v246, v247\n" \
"v_mfma_f32_16x16x32_bf16 v[92:95], a[124:127], v[200:203], v[92:95]\n" \
"ds_read_b128 a[124:127], v234 offset:15360\n" \
"v_cvt_pk_bf16_f32 v183, v142, v143\n" \
"v_smfmac_f32_16x16x64_bf16 a[248:251], a[236:239], v[200:207], v248\n" \
"s_and_b32 s73, vcc_lo, 0xffff\n" \
"s_cbranch_scc1 .LATLbu0\n" \
"s_add_u32 m0, %[w4k], 0x20000\n" \
"s_add_u32 s60, s70, 0x0\n" \
"buffer_load_dwordx4 v241, %[wsr], s60 offen offset:0 sc1 lds\n" \
"buffer_load_dwordx4 v241, %[wsr], s60 offen offset:1024 sc1 lds\n" \
"buffer_load_dwordx4 v241, %[wsr], s60 offen offset:2048 sc1 lds\n" \
"buffer_load_dwordx4 v241, %[wsr], s60 offen offset:3072 sc1 lds\n" \
"s_add_u32 m0, %[w4k], 0x21000\n" \
"s_add_u32 s60, s70, 0x2000\n" \
"buffer_load_dwordx4 v241, %[wsr], s60 offen offset:0 sc1 lds\n" \
"buffer_load_dwordx4 v241, %[wsr], s60 offen offset:1024 sc1 lds\n" \
"buffer_load_dwordx4 v241, %[wsr], s60 offen offset:2048 sc1 lds\n" \
"buffer_load_dwordx4 v241, %[wsr], s60 offen offset:3072 sc1 lds\n" \
"s_add_u32 m0, %[w4k], 0x22000\n" \
"s_add_u32 s60, s70, 0x4000\n" \
"buffer_load_dwordx4 v241, %[wsr], s60 offen offset:0 sc1 lds\n" \
"buffer_load_dwordx4 v241, %[wsr], s60 offen offset:1024 sc1 lds\n" \
"buffer_load_dwordx4 v241, %[wsr], s60 offen offset:2048 sc1 lds\n" \
"buffer_load_dwordx4 v241, %[wsr], s60 offen offset:3072 sc1 lds\n" \
"s_add_u32 m0, %[w4k], 0x23000\n" \
"s_add_u32 s60, s70, 0x6000\n" \
"buffer_load_dwordx4 v241, %[wsr], s60 offen offset:0 sc1 lds\n" \
"buffer_load_dwordx4 v241, %[wsr], s60 offen offset:1024 sc1 lds\n" \
"buffer_load_dwordx4 v241, %[wsr], s60 offen offset:2048 sc1 lds\n" \
"buffer_load_dwordx4 v241, %[wsr], s60 offen offset:3072 sc1 lds\n" \
"s_add_u32 m0, %[w4k], 0x24000\n" \
"s_add_u32 s60, s70, 0x8000\n" \
"buffer_load_dwordx4 v241, %[wsr], s60 offen offset:0 sc1 lds\n" \
"buffer_load_dwordx4 v241, %[wsr], s60 offen offset:1024 sc1 lds\n" \
"buffer_load_dwordx4 v241, %[wsr], s60 offen offset:2048 sc1 lds\n" \
"buffer_load_dwordx4 v241, %[wsr], s60 offen offset:3072 sc1 lds\n" \
"s_add_u32 m0, %[w4k], 0x25000\n" \
"s_add_u32 s60, s70, 0xa000\n" \
"buffer_load_dwordx4 v241, %[wsr], s60 offen offset:0 sc1 lds\n" \
"buffer_load_dwordx4 v241, %[wsr], s60 offen offset:1024 sc1 lds\n" \
"buffer_load_dwordx4 v241, %[wsr], s60 offen offset:2048 sc1 lds\n" \
"buffer_load_dwordx4 v241, %[wsr], s60 offen offset:3072 sc1 lds\n" \
"s_add_u32 m0, %[w4k], 0x26000\n" \
"s_add_u32 s60, s70, 0xc000\n" \
"buffer_load_dwordx4 v241, %[wsr], s60 offen offset:0 sc1 lds\n" \
"buffer_load_dwordx4 v241, %[wsr], s60 offen offset:1024 sc1 lds\n" \
"buffer_load_dwordx4 v241, %[wsr], s60 offen offset:2048 sc1 lds\n" \
"buffer_load_dwordx4 v241, %[wsr], s60 offen offset:3072 sc1 lds\n" \
"s_add_u32 m0, %[w4k], 0x27000\n" \
"s_add_u32 s60, s70, 0xe000\n" \
"buffer_load_dwordx4 v241, %[wsr], s60 offen offset:0 sc1 lds\n" \
"buffer_load_dwordx4 v241, %[wsr], s60 offen offset:1024 sc1 lds\n" \
"buffer_load_dwordx4 v241, %[wsr], s60 offen offset:2048 sc1 lds\n" \
"buffer_load_dwordx4 v241, %[wsr], s60 offen offset:3072 sc1 lds\n" \
"s_add_u32 m0, s82, 0x0\n" \
"s_add_u32 s60, s71, 0x0\n" \
"buffer_load_dwordx4 v241, %[wsr], s60 offen offset:0 sc1 lds\n" \
"buffer_load_dwordx4 v241, %[wsr], s60 offen offset:1024 sc1 lds\n" \
"buffer_load_dwordx4 v241, %[wsr], s60 offen offset:2048 sc1 lds\n" \
"buffer_load_dwordx4 v241, %[wsr], s60 offen offset:3072 sc1 lds\n" \
"s_add_u32 m0, s82, 0x1000\n" \
"s_add_u32 s60, s71, 0x2000\n" \
"buffer_load_dwordx4 v241, %[wsr], s60 offen offset:0 sc1 lds\n" \
"buffer_load_dwordx4 v241, %[wsr], s60 offen offset:1024 sc1 lds\n" \
"buffer_load_dwordx4 v241, %[wsr], s60 offen offset:2048 sc1 lds\n" \
"buffer_load_dwordx4 v241, %[wsr], s60 offen offset:3072 sc1 lds\n" \
"s_branch .LATLfin\n" \
".LATLbu0:\n" \
"buffer_load_dword v247, v242, %[wsr], s80 offen offset:768 sc1\n" \
"s_branch .LATLfin\n" \
".LATLdn1:\n" \
"buffer_load_dword v247, v242, %[wsr], s80 offen offset:768 sc1\n" \
"s_waitcnt lgkmcnt(4)\n" \
"v_mfma_f32_16x16x32_bf16 v[64:67], a[96:99], v[192:195], v[64:67]\n" \
"v_mov_b32_e32 v178, 0\n" \
"v_mov_b32_e32 v179, 0\n" \
"v_exp_f32_e32 v144, v144\n" \
"v_mfma_f32_16x16x32_bf16 v[96:99], a[96:99], v[196:199], v[96:99]\n" \
"ds_read_b128 a[96:99], v234 offset:24576\n" \
"v_exp_f32_e32 v145, v145\n" \
"v_mfma_f32_16x16x32_bf16 v[100:103], a[100:103], v[196:199], v[100:103]\n" \
"v_exp_f32_e32 v146, v146\n" \
"v_mfma_f32_16x16x32_bf16 v[68:71], a[100:103], v[192:195], v[68:71]\n" \
"ds_read_b128 a[100:103], v234 offset:25600\n" \
"v_cvt_pk_bf16_f32 v176, v144, v145\n" \
"v_mfma_f32_16x16x32_bf16 v[72:75], a[104:107], v[192:195], v[72:75]\n" \
"v_exp_f32_e32 v147, v147\n" \
"v_mfma_f32_16x16x32_bf16 v[104:107], a[104:107], v[196:199], v[104:107]\n" \
"ds_read_b128 a[104:107], v234 offset:26624\n" \
"v_cvt_pk_bf16_f32 v177, v146, v147\n" \
"v_mfma_f32_16x16x32_bf16 v[108:111], a[108:111], v[196:199], v[108:111]\n" \
"v_exp_f32_e32 v152, v152\n" \
"v_mfma_f32_16x16x32_bf16 v[76:79], a[108:111], v[192:195], v[76:79]\n" \
"s_waitcnt lgkmcnt(3)\n" \
"ds_read_b128 a[108:111], v234 offset:27648\n" \
"v_exp_f32_e32 v153, v153\n" \
"v_mfma_f32_16x16x32_bf16 v[80:83], a[112:115], v[192:195], v[80:83]\n" \
"v_exp_f32_e32 v154, v154\n" \
"v_cvt_pk_bf16_f32 v180, v152, v153\n" \
"v_mfma_f32_16x16x32_bf16 v[112:115], a[112:115], v[196:199], v[112:115]\n" \
"ds_read_b128 a[112:115], v234 offset:28672\n" \
"v_exp_f32_e32 v155, v155\n" \
"v_mfma_f32_16x16x32_bf16 v[116:119], a[116:119], v[196:199], v[116:119]\n" \
"v_exp_f32_e32 v156, v156\n" \
"v_cvt_pk_bf16_f32 v181, v154, v155\n" \
"v_mfma_f32_16x16x32_bf16 v[84:87], a[116:119], v[192:195], v[84:87]\n" \
"ds_read_b128 a[116:119], v234 offset:29696\n" \
"v_exp_f32_e32 v157, v157\n" \
"v_mfma_f32_16x16x32_bf16 v[88:91], a[120:123], v[192:195], v[88:91]\n" \
"v_exp_f32_e32 v158, v158\n" \
"v_mfma_f32_16x16x32_bf16 v[120:123], a[120:123], v[196:199], v[120:123]\n" \
"ds_read_b128 a[120:123], v234 offset:30720\n" \
"v_cvt_pk_bf16_f32 v182, v156, v157\n" \
"v_mfma_f32_16x16x32_bf16 v[124:127], a[124:127], v[196:199], v[124:127]\n" \
"v_exp_f32_e32 v159, v159\n" \
"s_waitcnt vmcnt(0)\n" \
"v_cmp_eq_u32_e32 vcc, v246, v247\n" \
"v_mfma_f32_16x16x32_bf16 v[92:95], a[124:127], v[192:195], v[92:95]\n" \
"ds_read_b128 a[124:127], v234 offset:31744\n" \
"v_cvt_pk_bf16_f32 v183, v158, v159\n" \
"v_smfmac_f32_16x16x64_bf16 a[248:251], a[236:239], v[192:199], v248\n" \
"s_and_b32 s73, vcc_lo, 0xffff\n" \
"s_cbranch_scc1 .LATLbu1\n" \
"s_add_u32 m0, s82, 0x1000\n" \
"s_add_u32 s60, s71, 0x2000\n" \
"buffer_load_dwordx4 v241, %[wsr], s60 offen offset:0 sc1 lds\n" \
"buffer_load_dwordx4 v241, %[wsr], s60 offen offset:1024 sc1 lds\n" \
"buffer_load_dwordx4 v241, %[wsr], s60 offen offset:2048 sc1 lds\n" \
"buffer_load_dwordx4 v241, %[wsr], s60 offen offset:3072 sc1 lds\n" \
"s_add_u32 m0, s82, 0x2000\n" \
"s_add_u32 s60, s71, 0x4000\n" \
"buffer_load_dwordx4 v241, %[wsr], s60 offen offset:0 sc1 lds\n" \
"buffer_load_dwordx4 v241, %[wsr], s60 offen offset:1024 sc1 lds\n" \
"buffer_load_dwordx4 v241, %[wsr], s60 offen offset:2048 sc1 lds\n" \
"buffer_load_dwordx4 v241, %[wsr], s60 offen offset:3072 sc1 lds\n" \
"s_add_u32 m0, s82, 0x3000\n" \
"s_add_u32 s60, s71, 0x6000\n" \
"buffer_load_dwordx4 v241, %[wsr], s60 offen offset:0 sc1 lds\n" \
"buffer_load_dwordx4 v241, %[wsr], s60 offen offset:1024 sc1 lds\n" \
"buffer_load_dwordx4 v241, %[wsr], s60 offen offset:2048 sc1 lds\n" \
"buffer_load_dwordx4 v241, %[wsr], s60 offen offset:3072 sc1 lds\n" \
"s_add_u32 m0, s82, 0x4000\n" \
"s_add_u32 s60, s71, 0x8000\n" \
"buffer_load_dwordx4 v241, %[wsr], s60 offen offset:0 sc1 lds\n" \
"buffer_load_dwordx4 v241, %[wsr], s60 offen offset:1024 sc1 lds\n" \
"buffer_load_dwordx4 v241, %[wsr], s60 offen offset:2048 sc1 lds\n" \
"buffer_load_dwordx4 v241, %[wsr], s60 offen offset:3072 sc1 lds\n" \
"s_add_u32 m0, s82, 0x5000\n" \
"s_add_u32 s60, s71, 0xa000\n" \
"buffer_load_dwordx4 v241, %[wsr], s60 offen offset:0 sc1 lds\n" \
"buffer_load_dwordx4 v241, %[wsr], s60 offen offset:1024 sc1 lds\n" \
"buffer_load_dwordx4 v241, %[wsr], s60 offen offset:2048 sc1 lds\n" \
"buffer_load_dwordx4 v241, %[wsr], s60 offen offset:3072 sc1 lds\n" \
"s_add_u32 m0, s82, 0x6000\n" \
"s_add_u32 s60, s71, 0xc000\n" \
"buffer_load_dwordx4 v241, %[wsr], s60 offen offset:0 sc1 lds\n" \
"buffer_load_dwordx4 v241, %[wsr], s60 offen offset:1024 sc1 lds\n" \
"buffer_load_dwordx4 v241, %[wsr], s60 offen offset:2048 sc1 lds\n" \
"buffer_load_dwordx4 v241, %[wsr], s60 offen offset:3072 sc1 lds\n" \
"s_branch .LATLfin\n" \
".LATLbu1:\n" \
"buffer_load_dword v247, v242, %[wsr], s80 offen offset:768 sc1\n" \
"s_branch .LATLfin\n" \
".LATLmk12:\n" \
"v_cmp_le_i32_e64 s[88:89], 0, v225\n" \
"v_cmp_le_i32_e64 s[90:91], 1, v225\n" \
"v_cmp_le_i32_e64 s[92:93], 2, v225\n" \
"v_cmp_le_i32_e64 s[94:95], 3, v225\n" \
"v_mov_b32_e32 v180, v252\n" \
"v_mov_b32_e32 v181, v252\n" \
"v_mov_b32_e32 v182, v252\n" \
"v_mov_b32_e32 v183, v252\n" \
"v_cndmask_b32_e64 v176, v252, v176, s[88:89]\n" \
"v_cndmask_b32_e64 v177, v252, v177, s[90:91]\n" \
"v_cndmask_b32_e64 v178, v252, v178, s[92:93]\n" \
"v_cndmask_b32_e64 v179, v252, v179, s[94:95]\n" \
"v_cndmask_b32_e64 v188, v252, v188, s[88:89]\n" \
"v_cndmask_b32_e64 v189, v252, v189, s[90:91]\n" \
"v_cndmask_b32_e64 v190, v252, v190, s[92:93]\n" \
"v_cndmask_b32_e64 v191, v252, v191, s[94:95]\n" \
"s_nop 0\n" \
"s_branch .LATLmb12\n" \
".LATLmk13:\n" \
"v_cmp_le_i32_e64 s[88:89], 0, v225\n" \
"v_cmp_le_i32_e64 s[90:91], 1, v225\n" \
"v_cmp_le_i32_e64 s[92:93], 2, v225\n" \
"v_cmp_le_i32_e64 s[94:95], 3, v225\n" \
"v_mov_b32_e32 v180, v252\n" \
"v_mov_b32_e32 v181, v252\n" \
"v_mov_b32_e32 v182, v252\n" \
"v_mov_b32_e32 v183, v252\n" \
"v_cndmask_b32_e64 v176, v252, v176, s[88:89]\n" \
"v_cndmask_b32_e64 v177, v252, v177, s[90:91]\n" \
"v_cndmask_b32_e64 v178, v252, v178, s[92:93]\n" \
"v_cndmask_b32_e64 v179, v252, v179, s[94:95]\n" \
"v_cndmask_b32_e64 v188, v252, v188, s[88:89]\n" \
"v_cndmask_b32_e64 v189, v252, v189, s[90:91]\n" \
"v_cndmask_b32_e64 v190, v252, v190, s[92:93]\n" \
"v_cndmask_b32_e64 v191, v252, v191, s[94:95]\n" \
"s_nop 0\n" \
"s_branch .LATLmb13\n" \
".LATLn1:\n" \
"buffer_load_dword v247, v242, %[wsr], s80 offen offset:768 sc1\n" \
"s_add_u32 s60, s64, 24576\n" \
"buffer_load_dwordx4 v[0:3], v241, %[wsr], s60 offen offset:3072 sc1\n" \
"buffer_load_dwordx4 v[4:7], v249, %[wsr], s60 offen offset:0 sc1\n" \
"buffer_load_dwordx4 v[8:11], v249, %[wsr], s60 offen offset:1024 sc1\n" \
"buffer_load_dwordx4 v[12:15], v249, %[wsr], s60 offen offset:2048 sc1\n" \
"buffer_load_dwordx4 v[16:19], v249, %[wsr], s60 offen offset:3072 sc1\n" \
"s_add_u32 s60, s64, 32768\n" \
"buffer_load_dwordx4 v[20:23], v241, %[wsr], s60 offen offset:0 sc1\n" \
"buffer_load_dwordx4 v[24:27], v241, %[wsr], s60 offen offset:1024 sc1\n" \
"buffer_load_dwordx4 v[28:31], v241, %[wsr], s60 offen offset:2048 sc1\n" \
"s_mul_i32 s61, %[da], 0x2020\n" \
"s_and_b32 s61, s61, 0x3c020\n" \
"s_mul_i32 s62, %[h], 0x84\n" \
"s_and_b32 s62, s62, 0xc1c\n" \
"s_or_b32 s61, s61, s62\n" \
"v_and_b32_e32 v251, 60, v240\n" \
"v_lshlrev_b32_e32 v251, 25, v251\n" \
"v_lshl_or_b32 v251, v240, 12, v251\n" \
"v_lshl_add_u32 v250, %[w4k], 1, v241\n" \
"v_add_u32_e32 v250, 131072, v250\n" \
"s_waitcnt vmcnt(17) lgkmcnt(0)\n" \
"ds_read_b128 a[20:23], v250 offset:0\n" \
"ds_read_b128 a[24:27], v250 offset:1024\n" \
"ds_read_b128 a[28:31], v250 offset:2048\n" \
"ds_read_b128 v[160:163], v250 offset:3072\n" \
"ds_read_b128 v[164:167], v250 offset:4096\n" \
"ds_read_b128 v[168:171], v250 offset:5120\n" \
"ds_read_b128 v[172:175], v250 offset:6144\n" \
"ds_read_b128 a[240:243], v250 offset:7168\n" \
"s_waitcnt vmcnt(8)\n" \
"buffer_atomic_add v252, v251, %[wsr], s61 offen offset:768 sc1\n" \
"buffer_atomic_add v252, v253, %[wsr], s79 offen offset:512 sc1\n" \
"s_waitcnt lgkmcnt(0)\n" \
"s_barrier\n" \
"s_cmp_eq_u32 %[w4k], 12288\n" \
"s_cbranch_scc1 .LATLo1\n" \
"s_add_u32 s60, s64, 40960\n" \
"buffer_load_dwordx4 a[0:3], v241, %[wsr], s60 offen offset:3072 sc1\n" \
"buffer_load_dwordx4 a[4:7], v249, %[wsr], s60 offen offset:0 sc1\n" \
"buffer_load_dwordx4 a[8:11], v249, %[wsr], s60 offen offset:1024 sc1\n" \
"buffer_load_dwordx4 a[12:15], v249, %[wsr], s60 offen offset:2048 sc1\n" \
"buffer_load_dwordx4 a[16:19], v249, %[wsr], s60 offen offset:3072 sc1\n" \
"s_cmp_eq_u32 %[w4k], 8192\n" \
"s_cbranch_scc1 .LATLo1\n" \
"s_add_u32 s60, s64, 32768\n" \
"buffer_load_dwordx4 v[48:51], v249, %[wsr], s60 offen offset:3072 sc1\n" \
"s_add_u32 s60, s64, 40960\n" \
"buffer_load_dwordx4 v[52:55], v241, %[wsr], s60 offen offset:0 sc1\n" \
"buffer_load_dwordx4 v[56:59], v241, %[wsr], s60 offen offset:1024 sc1\n" \
"buffer_load_dwordx4 v[60:63], v241, %[wsr], s60 offen offset:2048 sc1\n" \
"s_cmp_eq_u32 %[w4k], 4096\n" \
"s_cbranch_scc1 .LATLo1\n" \
"s_add_u32 s60, s64, 32768\n" \
"buffer_load_dwordx4 v[32:35], v241, %[wsr], s60 offen offset:3072 sc1\n" \
"buffer_load_dwordx4 v[36:39], v249, %[wsr], s60 offen offset:0 sc1\n" \
"buffer_load_dwordx4 v[40:43], v249, %[wsr], s60 offen offset:1024 sc1\n" \
"buffer_load_dwordx4 v[44:47], v249, %[wsr], s60 offen offset:2048 sc1\n" \
"s_branch .LATLdr0\n" \
".LATLo1:\n" \
"buffer_load_dword v247, v242, %[wsr], s80 offen offset:768 sc1\n" \
"s_cmp_eq_i32 %[w4k], 4096\n" \
"s_cbranch_scc1 .LATLmk12\n" \
".LATLmb12:\n" \
"s_waitcnt lgkmcnt(8)\n" \
"v_mfma_f32_16x16x32_bf16 v[144:147], a[64:67], a[32:35], v[176:179]\n" \
"s_add_u32 s60, s64, 32768\n" \
"v_exp_f32_e32 v128, v128\n" \
"v_mfma_f32_16x16x32_bf16 v[152:155], a[64:67], a[48:51], v[184:187]\n" \
"ds_read_b128 a[64:67], v234 offset:32768\n" \
"buffer_load_dwordx4 v[32:35], v241, %[wsr], s60 offen offset:3072 sc1\n" \
"v_mfma_f32_16x16x32_bf16 v[156:159], a[68:71], a[48:51], v[188:191]\n" \
"v_exp_f32_e32 v129, v129\n" \
"v_mfma_f32_16x16x32_bf16 v[148:151], a[68:71], a[32:35], v[180:183]\n" \
"ds_read_b128 a[68:71], v234 offset:33792\n" \
"buffer_load_dwordx4 v[36:39], v249, %[wsr], s60 offen offset:0 sc1\n" \
"v_cvt_pk_bf16_f32 v192, v128, v129\n" \
"v_mfma_f32_16x16x32_bf16 v[144:147], a[72:75], a[36:39], v[144:147]\n" \
"v_exp_f32_e32 v130, v130\n" \
"v_mfma_f32_16x16x32_bf16 v[152:155], a[72:75], a[52:55], v[152:155]\n" \
"ds_read_b128 a[72:75], v234 offset:34816\n" \
"buffer_load_dwordx4 v[40:43], v249, %[wsr], s60 offen offset:1024 sc1\n" \
"v_mfma_f32_16x16x32_bf16 v[156:159], a[76:79], a[52:55], v[156:159]\n" \
"v_exp_f32_e32 v131, v131\n" \
"v_mfma_f32_16x16x32_bf16 v[148:151], a[76:79], a[36:39], v[148:151]\n" \
"ds_read_b128 a[76:79], v234 offset:35840\n" \
"buffer_load_dwordx4 v[44:47], v249, %[wsr], s60 offen offset:2048 sc1\n" \
"v_cvt_pk_bf16_f32 v193, v130, v131\n" \
"v_mfma_f32_16x16x32_bf16 v[144:147], a[80:83], a[40:43], v[144:147]\n" \
"v_exp_f32_e32 v132, v132\n" \
"v_mfma_f32_16x16x32_bf16 v[152:155], a[80:83], a[56:59], v[152:155]\n" \
"ds_read_b128 a[80:83], v234 offset:36864\n" \
"v_cmp_le_i32_e64 s[88:89], 0, v225\n" \
"v_mfma_f32_16x16x32_bf16 v[156:159], a[84:87], a[56:59], v[156:159]\n" \
"v_exp_f32_e32 v133, v133\n" \
"v_mfma_f32_16x16x32_bf16 v[148:151], a[84:87], a[40:43], v[148:151]\n" \
"ds_read_b128 a[84:87], v234 offset:37888\n" \
"v_cvt_pk_bf16_f32 v194, v132, v133\n" \
"v_mfma_f32_16x16x32_bf16 v[144:147], a[88:91], a[44:47], v[144:147]\n" \
"v_exp_f32_e32 v134, v134\n" \
"v_mfma_f32_16x16x32_bf16 v[152:155], a[88:91], a[60:63], v[152:155]\n" \
"ds_read_b128 a[88:91], v234 offset:38912\n" \
"v_cmp_le_i32_e64 s[90:91], 1, v225\n" \
"v_mfma_f32_16x16x32_bf16 v[156:159], a[92:95], a[60:63], v[156:159]\n" \
"v_exp_f32_e32 v135, v135\n" \
"v_mfma_f32_16x16x32_bf16 v[148:151], a[92:95], a[44:47], v[148:151]\n" \
"ds_read_b128 a[92:95], v234 offset:39936\n" \
"s_waitcnt lgkmcnt(8)\n" \
"v_mfma_f32_16x16x32_bf16 v[64:67], a[96:99], v[200:203], v[64:67]\n" \
"v_exp_f32_e32 v136, v136\n" \
"v_mfma_f32_16x16x32_bf16 v[96:99], a[96:99], v[204:207], v[96:99]\n" \
"ds_read_b128 a[96:99], v234 offset:8192\n" \
"v_cvt_pk_bf16_f32 v195, v134, v135\n" \
"v_mfma_f32_16x16x32_bf16 v[100:103], a[100:103], v[204:207], v[100:103]\n" \
"v_exp_f32_e32 v137, v137\n" \
"v_mfma_f32_16x16x32_bf16 v[68:71], a[100:103], v[200:203], v[68:71]\n" \
"ds_read_b128 a[100:103], v234 offset:9216\n" \
"v_cvt_pk_bf16_f32 v196, v136, v137\n" \
"v_mfma_f32_16x16x32_bf16 v[72:75], a[104:107], v[200:203], v[72:75]\n" \
"v_exp_f32_e32 v138, v138\n" \
"v_mfma_f32_16x16x32_bf16 v[104:107], a[104:107], v[204:207], v[104:107]\n" \
"ds_read_b128 a[104:107], v234 offset:10240\n" \
"v_cmp_le_i32_e64 s[92:93], 2, v225\n" \
"v_mfma_f32_16x16x32_bf16 v[108:111], a[108:111], v[204:207], v[108:111]\n" \
"v_exp_f32_e32 v139, v139\n" \
"v_mfma_f32_16x16x32_bf16 v[76:79], a[108:111], v[200:203], v[76:79]\n" \
"ds_read_b128 a[108:111], v234 offset:11264\n" \
"v_cvt_pk_bf16_f32 v197, v138, v139\n" \
"v_mfma_f32_16x16x32_bf16 v[80:83], a[112:115], v[200:203], v[80:83]\n" \
"v_exp_f32_e32 v140, v140\n" \
"v_mfma_f32_16x16x32_bf16 v[112:115], a[112:115], v[204:207], v[112:115]\n" \
"ds_read_b128 a[112:115], v234 offset:12288\n" \
"v_cmp_le_i32_e64 s[94:95], 3, v225\n" \
"v_mfma_f32_16x16x32_bf16 v[116:119], a[116:119], v[204:207], v[116:119]\n" \
"v_exp_f32_e32 v141, v141\n" \
"v_mfma_f32_16x16x32_bf16 v[84:87], a[116:119], v[200:203], v[84:87]\n" \
"ds_read_b128 a[116:119], v234 offset:13312\n" \
"v_cvt_pk_bf16_f32 v198, v140, v141\n" \
"v_mfma_f32_16x16x32_bf16 v[88:91], a[120:123], v[200:203], v[88:91]\n" \
"v_exp_f32_e32 v142, v142\n" \
"v_mfma_f32_16x16x32_bf16 v[120:123], a[120:123], v[204:207], v[120:123]\n" \
"ds_read_b128 a[120:123], v234 offset:14336\n" \
"v_mfma_f32_16x16x32_bf16 v[124:127], a[124:127], v[204:207], v[124:127]\n" \
"v_exp_f32_e32 v143, v143\n" \
"v_mfma_f32_16x16x32_bf16 v[92:95], a[124:127], v[200:203], v[92:95]\n" \
"ds_read_b128 a[124:127], v234 offset:15360\n" \
"v_cvt_pk_bf16_f32 v199, v142, v143\n" \
"v_smfmac_f32_16x16x64_bf16 a[248:251], a[236:239], v[200:207], v248\n" \
"s_waitcnt vmcnt(4)\n" \
"s_cmp_eq_u32 %[w4k], 4096\n" \
"s_cbranch_scc1 .LATLdr1\n" \
"s_cmp_eq_i32 %[w4k], 8192\n" \
"s_cbranch_scc1 .LATLmk13\n" \
".LATLmb13:\n" \
"s_waitcnt lgkmcnt(8)\n" \
"v_mfma_f32_16x16x32_bf16 v[128:131], a[64:67], a[32:35], v[176:179]\n" \
"s_add_u32 s60, s64, 32768\n" \
"v_exp_f32_e32 v144, v144\n" \
"v_mfma_f32_16x16x32_bf16 v[136:139], a[64:67], a[48:51], v[184:187]\n" \
"ds_read_b128 a[64:67], v234 offset:49152\n" \
"buffer_load_dwordx4 v[48:51], v249, %[wsr], s60 offen offset:3072 sc1\n" \
"v_mfma_f32_16x16x32_bf16 v[140:143], a[68:71], a[48:51], v[188:191]\n" \
"s_add_u32 s60, s64, 40960\n" \
"v_exp_f32_e32 v145, v145\n" \
"v_mfma_f32_16x16x32_bf16 v[132:135], a[68:71], a[32:35], v[180:183]\n" \
"ds_read_b128 a[68:71], v234 offset:50176\n" \
"buffer_load_dwordx4 v[52:55], v241, %[wsr], s60 offen offset:0 sc1\n" \
"v_cvt_pk_bf16_f32 v200, v144, v145\n" \
"v_mfma_f32_16x16x32_bf16 v[128:131], a[72:75], a[36:39], v[128:131]\n" \
"v_exp_f32_e32 v146, v146\n" \
"v_mfma_f32_16x16x32_bf16 v[136:139], a[72:75], a[52:55], v[136:139]\n" \
"ds_read_b128 a[72:75], v234 offset:51200\n" \
"buffer_load_dwordx4 v[56:59], v241, %[wsr], s60 offen offset:1024 sc1\n" \
"v_mfma_f32_16x16x32_bf16 v[140:143], a[76:79], a[52:55], v[140:143]\n" \
"v_exp_f32_e32 v147, v147\n" \
"v_cndmask_b32_e64 v189, v252, v189, s[90:91]\n" \
"v_mfma_f32_16x16x32_bf16 v[132:135], a[76:79], a[36:39], v[132:135]\n" \
"ds_read_b128 a[76:79], v234 offset:52224\n" \
"buffer_load_dwordx4 v[60:63], v241, %[wsr], s60 offen offset:2048 sc1\n" \
"v_cvt_pk_bf16_f32 v201, v146, v147\n" \
"v_mfma_f32_16x16x32_bf16 v[128:131], a[80:83], a[40:43], v[128:131]\n" \
"v_exp_f32_e32 v148, v148\n" \
"v_cndmask_b32_e64 v190, v252, v190, s[92:93]\n" \
"v_mfma_f32_16x16x32_bf16 v[136:139], a[80:83], a[56:59], v[136:139]\n" \
"ds_read_b128 a[80:83], v234 offset:53248\n" \
"v_cndmask_b32_e64 v176, v252, v176, s[88:89]\n" \
"v_cndmask_b32_e64 v191, v252, v191, s[94:95]\n" \
"v_mfma_f32_16x16x32_bf16 v[140:143], a[84:87], a[56:59], v[140:143]\n" \
"v_exp_f32_e32 v149, v149\n" \
"v_mfma_f32_16x16x32_bf16 v[132:135], a[84:87], a[40:43], v[132:135]\n" \
"ds_read_b128 a[84:87], v234 offset:54272\n" \
"v_cvt_pk_bf16_f32 v202, v148, v149\n" \
"v_mfma_f32_16x16x32_bf16 v[128:131], a[88:91], a[44:47], v[128:131]\n" \
"v_exp_f32_e32 v150, v150\n" \
"v_mfma_f32_16x16x32_bf16 v[136:139], a[88:91], a[60:63], v[136:139]\n" \
"ds_read_b128 a[88:91], v234 offset:55296\n" \
"v_cndmask_b32_e64 v177, v252, v177, s[90:91]\n" \
"v_mfma_f32_16x16x32_bf16 v[140:143], a[92:95], a[60:63], v[140:143]\n" \
"v_exp_f32_e32 v151, v151\n" \
"v_mfma_f32_16x16x32_bf16 v[132:135], a[92:95], a[44:47], v[132:135]\n" \
"ds_read_b128 a[92:95], v234 offset:56320\n" \
"s_waitcnt lgkmcnt(8)\n" \
"v_mfma_f32_16x16x32_bf16 v[64:67], a[96:99], v[192:195], v[64:67]\n" \
"v_exp_f32_e32 v152, v152\n" \
"v_mfma_f32_16x16x32_bf16 v[96:99], a[96:99], v[196:199], v[96:99]\n" \
"ds_read_b128 a[96:99], v234 offset:24576\n" \
"v_cvt_pk_bf16_f32 v203, v150, v151\n" \
"v_mfma_f32_16x16x32_bf16 v[100:103], a[100:103], v[196:199], v[100:103]\n" \
"v_exp_f32_e32 v153, v153\n" \
"v_mfma_f32_16x16x32_bf16 v[68:71], a[100:103], v[192:195], v[68:71]\n" \
"ds_read_b128 a[100:103], v234 offset:25600\n" \
"v_cvt_pk_bf16_f32 v204, v152, v153\n" \
"v_mfma_f32_16x16x32_bf16 v[72:75], a[104:107], v[192:195], v[72:75]\n" \
"v_exp_f32_e32 v154, v154\n" \
"v_mfma_f32_16x16x32_bf16 v[104:107], a[104:107], v[196:199], v[104:107]\n" \
"ds_read_b128 a[104:107], v234 offset:26624\n" \
"v_cndmask_b32_e64 v178, v252, v178, s[92:93]\n" \
"v_mfma_f32_16x16x32_bf16 v[108:111], a[108:111], v[196:199], v[108:111]\n" \
"v_exp_f32_e32 v155, v155\n" \
"v_mfma_f32_16x16x32_bf16 v[76:79], a[108:111], v[192:195], v[76:79]\n" \
"ds_read_b128 a[108:111], v234 offset:27648\n" \
"v_cvt_pk_bf16_f32 v205, v154, v155\n" \
"v_mfma_f32_16x16x32_bf16 v[80:83], a[112:115], v[192:195], v[80:83]\n" \
"v_exp_f32_e32 v156, v156\n" \
"v_mfma_f32_16x16x32_bf16 v[112:115], a[112:115], v[196:199], v[112:115]\n" \
"ds_read_b128 a[112:115], v234 offset:28672\n" \
"v_cndmask_b32_e64 v179, v252, v179, s[94:95]\n" \
"v_mfma_f32_16x16x32_bf16 v[116:119], a[116:119], v[196:199], v[116:119]\n" \
"v_exp_f32_e32 v157, v157\n" \
"v_mfma_f32_16x16x32_bf16 v[84:87], a[116:119], v[192:195], v[84:87]\n" \
"ds_read_b128 a[116:119], v234 offset:29696\n" \
"v_cvt_pk_bf16_f32 v206, v156, v157\n" \
"v_mfma_f32_16x16x32_bf16 v[88:91], a[120:123], v[192:195], v[88:91]\n" \
"v_exp_f32_e32 v158, v158\n" \
"v_mfma_f32_16x16x32_bf16 v[120:123], a[120:123], v[196:199], v[120:123]\n" \
"ds_read_b128 a[120:123], v234 offset:30720\n" \
"v_cndmask_b32_e64 v188, v252, v188, s[88:89]\n" \
"v_mfma_f32_16x16x32_bf16 v[124:127], a[124:127], v[196:199], v[124:127]\n" \
"v_exp_f32_e32 v159, v159\n" \
"v_mfma_f32_16x16x32_bf16 v[92:95], a[124:127], v[192:195], v[92:95]\n" \
"ds_read_b128 a[124:127], v234 offset:31744\n" \
"v_cvt_pk_bf16_f32 v207, v158, v159\n" \
"v_smfmac_f32_16x16x64_bf16 a[248:251], a[236:239], v[192:199], v248\n" \
"s_cmp_eq_u32 %[w4k], 8192\n" \
"s_cbranch_scc1 .LATLdr2\n" \
"s_waitcnt lgkmcnt(8)\n" \
"v_mfma_f32_16x16x32_bf16 v[144:147], a[64:67], a[32:35], v[176:179]\n" \
"s_add_u32 s60, s64, 40960\n" \
"v_exp_f32_e32 v128, v128\n" \
"v_mfma_f32_16x16x32_bf16 v[152:155], a[64:67], a[48:51], v[184:187]\n" \
"buffer_load_dwordx4 a[0:3], v241, %[wsr], s60 offen offset:3072 sc1\n" \
"v_mfma_f32_16x16x32_bf16 v[156:159], a[68:71], a[48:51], v[188:191]\n" \
"v_exp_f32_e32 v129, v129\n" \
"v_mfma_f32_16x16x32_bf16 v[144:147], a[72:75], a[36:39], v[144:147]\n" \
"v_exp_f32_e32 v130, v130\n" \
"v_cvt_pk_bf16_f32 v192, v128, v129\n" \
"v_mfma_f32_16x16x32_bf16 v[152:155], a[72:75], a[52:55], v[152:155]\n" \
"buffer_load_dwordx4 a[4:7], v249, %[wsr], s60 offen offset:0 sc1\n" \
"v_mfma_f32_16x16x32_bf16 v[156:159], a[76:79], a[52:55], v[156:159]\n" \
"v_exp_f32_e32 v131, v131\n" \
"v_mfma_f32_16x16x32_bf16 v[144:147], a[80:83], a[40:43], v[144:147]\n" \
"v_exp_f32_e32 v132, v132\n" \
"v_cvt_pk_bf16_f32 v193, v130, v131\n" \
"v_mfma_f32_16x16x32_bf16 v[152:155], a[80:83], a[56:59], v[152:155]\n" \
"buffer_load_dwordx4 a[8:11], v249, %[wsr], s60 offen offset:1024 sc1\n" \
"v_mfma_f32_16x16x32_bf16 v[156:159], a[84:87], a[56:59], v[156:159]\n" \
"v_exp_f32_e32 v133, v133\n" \
"v_mfma_f32_16x16x32_bf16 v[144:147], a[88:91], a[44:47], v[144:147]\n" \
"v_exp_f32_e32 v134, v134\n" \
"v_cvt_pk_bf16_f32 v194, v132, v133\n" \
"v_mfma_f32_16x16x32_bf16 v[152:155], a[88:91], a[60:63], v[152:155]\n" \
"buffer_load_dwordx4 a[12:15], v249, %[wsr], s60 offen offset:2048 sc1\n" \
"v_mfma_f32_16x16x32_bf16 v[156:159], a[92:95], a[60:63], v[156:159]\n" \
"s_waitcnt lgkmcnt(0)\n" \
"v_exp_f32_e32 v135, v135\n" \
"v_mfma_f32_16x16x32_bf16 v[64:67], a[96:99], v[200:203], v[64:67]\n" \
"v_exp_f32_e32 v136, v136\n" \
"v_cvt_pk_bf16_f32 v195, v134, v135\n" \
"v_mfma_f32_16x16x32_bf16 v[96:99], a[96:99], v[204:207], v[96:99]\n" \
"ds_read_b128 a[96:99], v234 offset:40960\n" \
"buffer_load_dwordx4 a[16:19], v249, %[wsr], s60 offen offset:3072 sc1\n" \
"v_mfma_f32_16x16x32_bf16 v[100:103], a[100:103], v[204:207], v[100:103]\n" \
"v_exp_f32_e32 v137, v137\n" \
"v_mfma_f32_16x16x32_bf16 v[68:71], a[100:103], v[200:203], v[68:71]\n" \
"ds_read_b128 a[100:103], v234 offset:41984\n" \
"v_mfma_f32_16x16x32_bf16 v[72:75], a[104:107], v[200:203], v[72:75]\n" \
"v_exp_f32_e32 v138, v138\n" \
"v_mfma_f32_16x16x32_bf16 v[104:107], a[104:107], v[204:207], v[104:107]\n" \
"ds_read_b128 a[104:107], v234 offset:43008\n" \
"v_cvt_pk_bf16_f32 v196, v136, v137\n" \
"v_mfma_f32_16x16x32_bf16 v[108:111], a[108:111], v[204:207], v[108:111]\n" \
"v_exp_f32_e32 v139, v139\n" \
"v_mfma_f32_16x16x32_bf16 v[76:79], a[108:111], v[200:203], v[76:79]\n" \
"ds_read_b128 a[108:111], v234 offset:44032\n" \
"v_mfma_f32_16x16x32_bf16 v[80:83], a[112:115], v[200:203], v[80:83]\n" \
"v_exp_f32_e32 v140, v140\n" \
"v_mfma_f32_16x16x32_bf16 v[112:115], a[112:115], v[204:207], v[112:115]\n" \
"ds_read_b128 a[112:115], v234 offset:45056\n" \
"v_cvt_pk_bf16_f32 v197, v138, v139\n" \
"v_mfma_f32_16x16x32_bf16 v[116:119], a[116:119], v[204:207], v[116:119]\n" \
"v_exp_f32_e32 v141, v141\n" \
"v_mfma_f32_16x16x32_bf16 v[84:87], a[116:119], v[200:203], v[84:87]\n" \
"ds_read_b128 a[116:119], v234 offset:46080\n" \
"v_mfma_f32_16x16x32_bf16 v[88:91], a[120:123], v[200:203], v[88:91]\n" \
"v_exp_f32_e32 v142, v142\n" \
"v_mfma_f32_16x16x32_bf16 v[120:123], a[120:123], v[204:207], v[120:123]\n" \
"ds_read_b128 a[120:123], v234 offset:47104\n" \
"v_cvt_pk_bf16_f32 v198, v140, v141\n" \
"v_mfma_f32_16x16x32_bf16 v[124:127], a[124:127], v[204:207], v[124:127]\n" \
"v_exp_f32_e32 v143, v143\n" \
"v_mfma_f32_16x16x32_bf16 v[92:95], a[124:127], v[200:203], v[92:95]\n" \
"ds_read_b128 a[124:127], v234 offset:48128\n" \
"v_cvt_pk_bf16_f32 v199, v142, v143\n" \
"v_smfmac_f32_16x16x64_bf16 a[248:251], a[236:239], v[200:207], v248\n" \
"s_branch .LATLdr3\n" \
".LATLsb:\n" \
"s_mov_b32 s96, 0x2000\n" \
".LATLsb_l:\n" \
"s_sleep 1\n" \
"buffer_load_dword v247, v242, %[wsr], s80 offen offset:768 sc1\n" \
"s_waitcnt vmcnt(0)\n" \
"v_cmp_eq_u32_e32 vcc, v246, v247\n" \
"s_sub_u32 s96, s96, 1\n" \
"s_cbranch_scc1 .LATLsbb\n" \
"s_nop 2\n" \
"s_and_b32 s88, vcc_hi, s81\n" \
"s_cbranch_scc1 .LATLsb_l\n" \
"s_branch .LATLsbb\n" \
".LATLsy:\n" \
"s_mov_b32 s96, 0x2000\n" \
".LATLsy_l:\n" \
"s_sleep 1\n" \
"buffer_load_dword v247, v242, %[wsr], s80 offen offset:512 sc1\n" \
"s_waitcnt vmcnt(0)\n" \
"v_cmp_eq_u32_e32 vcc, v245, v247\n" \
"s_sub_u32 s96, s96, 1\n" \
"s_cbranch_scc1 .LATLsyb\n" \
"s_nop 2\n" \
"s_and_b32 s88, vcc_lo, -1\n" \
"s_cbranch_scc1 .LATLsy_l\n" \
"s_branch .LATLsyb\n" \
".LATLxa:\n" \
"s_mov_b32 s96, 0x2000\n" \
".LATLxa_l:\n" \
"s_xor_b32 s61, %[po], 1024\n" \
"buffer_load_dwordx4 v[128:131], v241, %[wsr], s61 offen offset:0 sc1\n" \
"s_xor_b32 s61, %[po], 2048\n" \
"buffer_load_dwordx4 v[136:139], v241, %[wsr], s61 offen offset:0 sc1\n" \
"s_xor_b32 s61, %[po], 3072\n" \
"buffer_load_dwordx4 v[144:147], v241, %[wsr], s61 offen offset:0 sc1\n" \
"s_waitcnt vmcnt(0)\n" \
".LATLxa_m:\n" \
"v_max3_u32 v250, v130, v131, v128\n" \
"v_max3_u32 v251, v136, v137, v129\n" \
"v_max3_u32 v250, v138, v139, v250\n" \
"v_max3_u32 v251, v144, v145, v251\n" \
"v_max3_u32 v250, v146, v147, v250\n" \
"v_max_u32_e32 v250, v250, v251\n" \
"v_cmp_eq_u32_e32 vcc, -1, v250\n" \
"s_sub_u32 s96, s96, 1\n" \
"s_cbranch_scc1 .LATLxab\n" \
"s_nop 2\n" \
"s_cbranch_vccnz .LATLxa_l\n" \
"s_branch .LATLxab\n" \
".LATLpp:\n" \
"s_waitcnt vmcnt(0)\n" \
"buffer_atomic_add v252, v251, %[wsr], s61 offen offset:896 sc1\n" \
"s_branch .LATLppb\n" \
".LATLlt:\n" \
"s_cmp_ge_u32 %[w4k], 8192\n" \
"s_cbranch_scc1 .LATLpp\n" \
"s_waitcnt vmcnt(8)\n" \
"v_cmp_eq_u32_e32 vcc, v246, v247\n" \
"s_nop 2\n" \
"s_and_b32 s88, vcc_lo, 0xffff\n" \
"s_cbranch_scc1 .LATLsa\n" \
".LATLsab:\n" \
"s_cmp_lg_u32 %[w4k], 0\n" \
"s_cbranch_scc1 .LATLlt0\n" \
"s_add_u32 m0, %[w4k], 0x20000\n" \
"s_add_u32 s60, s70, 0x0\n" \
"buffer_load_dwordx4 v241, %[wsr], s60 offen offset:0 sc1 lds\n" \
"buffer_load_dwordx4 v241, %[wsr], s60 offen offset:1024 sc1 lds\n" \
"buffer_load_dwordx4 v241, %[wsr], s60 offen offset:2048 sc1 lds\n" \
"buffer_load_dwordx4 v241, %[wsr], s60 offen offset:3072 sc1 lds\n" \
"s_add_u32 m0, %[w4k], 0x21000\n" \
"s_add_u32 s60, s70, 0x2000\n" \
"buffer_load_dwordx4 v241, %[wsr], s60 offen offset:0 sc1 lds\n" \
"buffer_load_dwordx4 v241, %[wsr], s60 offen offset:1024 sc1 lds\n" \
"buffer_load_dwordx4 v241, %[wsr], s60 offen offset:2048 sc1 lds\n" \
"buffer_load_dwordx4 v241, %[wsr], s60 offen offset:3072 sc1 lds\n" \
"s_add_u32 m0, %[w4k], 0x22000\n" \
"s_add_u32 s60, s70, 0x4000\n" \
"buffer_load_dwordx4 v241, %[wsr], s60 offen offset:0 sc1 lds\n" \
"buffer_load_dwordx4 v241, %[wsr], s60 offen offset:1024 sc1 lds\n" \
"buffer_load_dwordx4 v241, %[wsr], s60 offen offset:2048 sc1 lds\n" \
"buffer_load_dwordx4 v241, %[wsr], s60 offen offset:3072 sc1 lds\n" \
"s_add_u32 m0, %[w4k], 0x23000\n" \
"s_add_u32 s60, s70, 0x6000\n" \
"buffer_load_dwordx4 v241, %[wsr], s60 offen offset:0 sc1 lds\n" \
"buffer_load_dwordx4 v241, %[wsr], s60 offen offset:1024 sc1 lds\n" \
"buffer_load_dwordx4 v241, %[wsr], s60 offen offset:2048 sc1 lds\n" \
"buffer_load_dwordx4 v241, %[wsr], s60 offen offset:3072 sc1 lds\n" \
"s_add_u32 m0, %[w4k], 0x24000\n" \
"s_add_u32 s60, s70, 0x8000\n" \
"buffer_load_dwordx4 v241, %[wsr], s60 offen offset:0 sc1 lds\n" \
"buffer_load_dwordx4 v241, %[wsr], s60 offen offset:1024 sc1 lds\n" \
"buffer_load_dwordx4 v241, %[wsr], s60 offen offset:2048 sc1 lds\n" \
"buffer_load_dwordx4 v241, %[wsr], s60 offen offset:3072 sc1 lds\n" \
"s_add_u32 m0, %[w4k], 0x25000\n" \
"s_add_u32 s60, s70, 0xa000\n" \
"buffer_load_dwordx4 v241, %[wsr], s60 offen offset:0 sc1 lds\n" \
"buffer_load_dwordx4 v241, %[wsr], s60 offen offset:1024 sc1 lds\n" \
"buffer_load_dwordx4 v241, %[wsr], s60 offen offset:2048 sc1 lds\n" \
"buffer_load_dwordx4 v241, %[wsr], s60 offen offset:3072 sc1 lds\n" \
"s_add_u32 m0, %[w4k], 0x26000\n" \
"s_add_u32 s60, s70, 0xc000\n" \
"buffer_load_dwordx4 v241, %[wsr], s60 offen offset:0 sc1 lds\n" \
"buffer_load_dwordx4 v241, %[wsr], s60 offen offset:1024 sc1 lds\n" \
"buffer_load_dwordx4 v241, %[wsr], s60 offen offset:2048 sc1 lds\n" \
"buffer_load_dwordx4 v241, %[wsr], s60 offen offset:3072 sc1 lds\n" \
"s_add_u32 m0, %[w4k], 0x27000\n" \
"s_add_u32 s60, s70, 0xe000\n" \
"buffer_load_dwordx4 v241, %[wsr], s60 offen offset:0 sc1 lds\n" \
"buffer_load_dwordx4 v241, %[wsr], s60 offen offset:1024 sc1 lds\n" \
"buffer_load_dwordx4 v241, %[wsr], s60 offen offset:2048 sc1 lds\n" \
"buffer_load_dwordx4 v241, %[wsr], s60 offen offset:3072 sc1 lds\n" \
"s_add_u32 m0, s82, 0x0\n" \
"s_add_u32 s60, s71, 0x0\n" \
"buffer_load_dwordx4 v241, %[wsr], s60 offen offset:0 sc1 lds\n" \
"buffer_load_dwordx4 v241, %[wsr], s60 offen offset:1024 sc1 lds\n" \
"buffer_load_dwordx4 v241, %[wsr], s60 offen offset:2048 sc1 lds\n" \
"buffer_load_dwordx4 v241, %[wsr], s60 offen offset:3072 sc1 lds\n" \
"s_add_u32 m0, s82, 0x1000\n" \
"s_add_u32 s60, s71, 0x2000\n" \
"buffer_load_dwordx4 v241, %[wsr], s60 offen offset:0 sc1 lds\n" \
"buffer_load_dwordx4 v241, %[wsr], s60 offen offset:1024 sc1 lds\n" \
"buffer_load_dwordx4 v241, %[wsr], s60 offen offset:2048 sc1 lds\n" \
"buffer_load_dwordx4 v241, %[wsr], s60 offen offset:3072 sc1 lds\n" \
"s_waitcnt vmcnt(8)\n" \
"s_branch .LATLppb\n" \
".LATLlt0:\n" \
"s_add_u32 m0, s82, 0x1000\n" \
"s_add_u32 s60, s71, 0x2000\n" \
"buffer_load_dwordx4 v241, %[wsr], s60 offen offset:0 sc1 lds\n" \
"buffer_load_dwordx4 v241, %[wsr], s60 offen offset:1024 sc1 lds\n" \
"buffer_load_dwordx4 v241, %[wsr], s60 offen offset:2048 sc1 lds\n" \
"buffer_load_dwordx4 v241, %[wsr], s60 offen offset:3072 sc1 lds\n" \
"s_add_u32 m0, s82, 0x2000\n" \
"s_add_u32 s60, s71, 0x4000\n" \
"buffer_load_dwordx4 v241, %[wsr], s60 offen offset:0 sc1 lds\n" \
"buffer_load_dwordx4 v241, %[wsr], s60 offen offset:1024 sc1 lds\n" \
"buffer_load_dwordx4 v241, %[wsr], s60 offen offset:2048 sc1 lds\n" \
"buffer_load_dwordx4 v241, %[wsr], s60 offen offset:3072 sc1 lds\n" \
"s_add_u32 m0, s82, 0x3000\n" \
"s_add_u32 s60, s71, 0x6000\n" \
"buffer_load_dwordx4 v241, %[wsr], s60 offen offset:0 sc1 lds\n" \
"buffer_load_dwordx4 v241, %[wsr], s60 offen offset:1024 sc1 lds\n" \
"buffer_load_dwordx4 v241, %[wsr], s60 offen offset:2048 sc1 lds\n" \
"buffer_load_dwordx4 v241, %[wsr], s60 offen offset:3072 sc1 lds\n" \
"s_add_u32 m0, s82, 0x4000\n" \
"s_add_u32 s60, s71, 0x8000\n" \
"buffer_load_dwordx4 v241, %[wsr], s60 offen offset:0 sc1 lds\n" \
"buffer_load_dwordx4 v241, %[wsr], s60 offen offset:1024 sc1 lds\n" \
"buffer_load_dwordx4 v241, %[wsr], s60 offen offset:2048 sc1 lds\n" \
"buffer_load_dwordx4 v241, %[wsr], s60 offen offset:3072 sc1 lds\n" \
"s_add_u32 m0, s82, 0x5000\n" \
"s_add_u32 s60, s71, 0xa000\n" \
"buffer_load_dwordx4 v241, %[wsr], s60 offen offset:0 sc1 lds\n" \
"buffer_load_dwordx4 v241, %[wsr], s60 offen offset:1024 sc1 lds\n" \
"buffer_load_dwordx4 v241, %[wsr], s60 offen offset:2048 sc1 lds\n" \
"buffer_load_dwordx4 v241, %[wsr], s60 offen offset:3072 sc1 lds\n" \
"s_add_u32 m0, s82, 0x6000\n" \
"s_add_u32 s60, s71, 0xc000\n" \
"buffer_load_dwordx4 v241, %[wsr], s60 offen offset:0 sc1 lds\n" \
"buffer_load_dwordx4 v241, %[wsr], s60 offen offset:1024 sc1 lds\n" \
"buffer_load_dwordx4 v241, %[wsr], s60 offen offset:2048 sc1 lds\n" \
"buffer_load_dwordx4 v241, %[wsr], s60 offen offset:3072 sc1 lds\n" \
"s_waitcnt vmcnt(24)\n" \
"s_branch .LATLppb\n" \
".LATLslowA:\n" \
"s_waitcnt vmcnt(0)\n" \
"v_mov_b32_e32 v0, 0\n" \
"v_mov_b32_e32 v1, 0\n" \
"v_mov_b32_e32 v2, 0\n" \
"v_mov_b32_e32 v3, 0\n" \
"v_mov_b32_e32 v4, 0\n" \
"v_mov_b32_e32 v5, 0\n" \
"v_mov_b32_e32 v6, 0\n" \
"v_mov_b32_e32 v7, 0\n" \
"v_mov_b32_e32 v8, 0\n" \
"v_mov_b32_e32 v9, 0\n" \
"v_mov_b32_e32 v10, 0\n" \
"v_mov_b32_e32 v11, 0\n" \
"v_mov_b32_e32 v12, 0\n" \
"v_mov_b32_e32 v13, 0\n" \
"v_mov_b32_e32 v14, 0\n" \
"v_mov_b32_e32 v15, 0\n" \
"v_mov_b32_e32 v16, 0\n" \
"v_mov_b32_e32 v17, 0\n" \
"v_mov_b32_e32 v18, 0\n" \
"v_mov_b32_e32 v19, 0\n" \
"v_mov_b32_e32 v20, 0\n" \
"v_mov_b32_e32 v21, 0\n" \
"v_mov_b32_e32 v22, 0\n" \
"v_mov_b32_e32 v23, 0\n" \
"v_mov_b32_e32 v24, 0\n" \
"v_mov_b32_e32 v25, 0\n" \
"v_mov_b32_e32 v26, 0\n" \
"v_mov_b32_e32 v27, 0\n" \
"v_mov_b32_e32 v28, 0\n" \
"v_mov_b32_e32 v29, 0\n" \
"v_mov_b32_e32 v30, 0\n" \
"v_mov_b32_e32 v31, 0\n" \
"v_mov_b32_e32 v32, 0\n" \
"v_mov_b32_e32 v33, 0\n" \
"v_mov_b32_e32 v34, 0\n" \
"v_mov_b32_e32 v35, 0\n" \
"v_mov_b32_e32 v36, 0\n" \
"v_mov_b32_e32 v37, 0\n" \
"v_mov_b32_e32 v38, 0\n" \
"v_mov_b32_e32 v39, 0\n" \
"v_mov_b32_e32 v40, 0\n" \
"v_mov_b32_e32 v41, 0\n" \
"v_mov_b32_e32 v42, 0\n" \
"v_mov_b32_e32 v43, 0\n" \
"v_mov_b32_e32 v44, 0\n" \
"v_mov_b32_e32 v45, 0\n" \
"v_mov_b32_e32 v46, 0\n" \
"v_mov_b32_e32 v47, 0\n" \
"v_mov_b32_e32 v48, 0\n" \
"v_mov_b32_e32 v49, 0\n" \
"v_mov_b32_e32 v50, 0\n" \
"v_mov_b32_e32 v51, 0\n" \
"v_mov_b32_e32 v52, 0\n" \
"v_mov_b32_e32 v53, 0\n" \
"v_mov_b32_e32 v54, 0\n" \
"v_mov_b32_e32 v55, 0\n" \
"v_mov_b32_e32 v56, 0\n" \
"v_mov_b32_e32 v57, 0\n" \
"v_mov_b32_e32 v58, 0\n" \
"v_mov_b32_e32 v59, 0\n" \
"v_mov_b32_e32 v60, 0\n" \
"v_mov_b32_e32 v61, 0\n" \
"v_mov_b32_e32 v62, 0\n" \
"v_mov_b32_e32 v63, 0\n" \
"v_mov_b32_e32 v222, 0\n" \
"v_mov_b32_e32 v218, v252\n" \
"v_mov_b32_e32 v223, 0\n" \
"v_mov_b32_e32 v219, v252\n" \
"v_mov_b32_e32 v250, 0xceabae43\n" \
"s_mov_b32 s62, 0\n" \
"s_mov_b32 s63, %[da]\n" \
"s_mov_b32 s60, s78\n" \
".LATLslowA_loop:\n" \
"buffer_load_dwordx4 a[128:131], v241, %[wsr], s60 offen offset:0 sc1\n" \
"buffer_load_dwordx4 a[132:135], v241, %[wsr], s60 offen offset:1024 sc1\n" \
"buffer_load_dwordx4 a[136:139], v241, %[wsr], s60 offen offset:2048 sc1\n" \
"buffer_load_dwordx4 a[140:143], v241, %[wsr], s60 offen offset:3072 sc1\n" \
"s_add_u32 s60, s60, 0x1000\n" \
"buffer_load_dwordx4 a[144:147], v241, %[wsr], s60 offen offset:0 sc1\n" \
"buffer_load_dwordx4 a[148:151], v241, %[wsr], s60 offen offset:1024 sc1\n" \
"buffer_load_dwordx4 a[152:155], v241, %[wsr], s60 offen offset:2048 sc1\n" \
"buffer_load_dwordx4 a[156:159], v241, %[wsr], s60 offen offset:3072 sc1\n" \
"s_add_u32 s60, s60, 0x1000\n" \
"buffer_load_dwordx4 a[160:163], v241, %[wsr], s60 offen offset:0 sc1\n" \
"buffer_load_dwordx4 a[164:167], v241, %[wsr], s60 offen offset:1024 sc1\n" \
"buffer_load_dwordx4 a[168:171], v241, %[wsr], s60 offen offset:2048 sc1\n" \
"buffer_load_dwordx4 a[172:175], v241, %[wsr], s60 offen offset:3072 sc1\n" \
"s_add_u32 s60, s60, 0x1000\n" \
"buffer_load_dwordx4 a[176:179], v241, %[wsr], s60 offen offset:0 sc1\n" \
"buffer_load_dwordx4 a[180:183], v241, %[wsr], s60 offen offset:1024 sc1\n" \
"buffer_load_dwordx4 a[184:187], v241, %[wsr], s60 offen offset:2048 sc1\n" \
"buffer_load_dwordx4 a[188:191], v241, %[wsr], s60 offen offset:3072 sc1\n" \
"s_add_u32 s60, s60, 0x1000\n" \
"s_waitcnt vmcnt(0)\n" \
"v_mfma_f32_16x16x32_bf16 v[160:163], a[128:131], a[0:3], 0\n" \
"v_mfma_f32_16x16x32_bf16 v[168:171], a[128:131], a[16:19], 0\n" \
"v_mfma_f32_16x16x32_bf16 v[172:175], a[132:135], a[16:19], 0\n" \
"v_mfma_f32_16x16x32_bf16 v[164:167], a[132:135], a[0:3], 0\n" \
"v_mfma_f32_16x16x32_bf16 v[160:163], a[136:139], a[4:7], v[160:163]\n" \
"v_mfma_f32_16x16x32_bf16 v[168:171], a[136:139], a[20:23], v[168:171]\n" \
"v_mfma_f32_16x16x32_bf16 v[172:175], a[140:143], a[20:23], v[172:175]\n" \
"v_mfma_f32_16x16x32_bf16 v[164:167], a[140:143], a[4:7], v[164:167]\n" \
"v_mfma_f32_16x16x32_bf16 v[160:163], a[144:147], a[8:11], v[160:163]\n" \
"v_mfma_f32_16x16x32_bf16 v[168:171], a[144:147], a[24:27], v[168:171]\n" \
"v_mfma_f32_16x16x32_bf16 v[172:175], a[148:151], a[24:27], v[172:175]\n" \
"v_mfma_f32_16x16x32_bf16 v[164:167], a[148:151], a[8:11], v[164:167]\n" \
"v_mfma_f32_16x16x32_bf16 v[160:163], a[152:155], a[12:15], v[160:163]\n" \
"v_mfma_f32_16x16x32_bf16 v[168:171], a[152:155], a[28:31], v[168:171]\n" \
"v_mfma_f32_16x16x32_bf16 v[172:175], a[156:159], a[28:31], v[172:175]\n" \
"v_mfma_f32_16x16x32_bf16 v[164:167], a[156:159], a[12:15], v[164:167]\n" \
"s_nop 7\n" \
"s_cmp_lt_i32 s62, s63\n" \
"s_cbranch_scc1 .LATLslowA_go\n" \
"s_cmp_eq_i32 s62, s63\n" \
"s_cbranch_scc0 .LATLslowA_all\n" \
"v_cmp_le_i32_e64 s[88:89], 0, v225\n" \
"v_cmp_le_i32_e64 s[90:91], 1, v225\n" \
"v_cmp_le_i32_e64 s[92:93], 2, v225\n" \
"v_cmp_le_i32_e64 s[94:95], 3, v225\n" \
"v_mov_b32_e32 v164, v250\n" \
"v_mov_b32_e32 v165, v250\n" \
"v_mov_b32_e32 v166, v250\n" \
"v_mov_b32_e32 v167, v250\n" \
"v_cndmask_b32_e64 v160, v250, v160, s[88:89]\n" \
"v_cndmask_b32_e64 v161, v250, v161, s[90:91]\n" \
"v_cndmask_b32_e64 v162, v250, v162, s[92:93]\n" \
"v_cndmask_b32_e64 v163, v250, v163, s[94:95]\n" \
"v_cndmask_b32_e64 v172, v250, v172, s[88:89]\n" \
"v_cndmask_b32_e64 v173, v250, v173, s[90:91]\n" \
"v_cndmask_b32_e64 v174, v250, v174, s[92:93]\n" \
"v_cndmask_b32_e64 v175, v250, v175, s[94:95]\n" \
"s_branch .LATLslowA_go\n" \
".LATLslowA_all:\n" \
"v_mov_b32_e32 v160, v250\n" \
"v_mov_b32_e32 v161, v250\n" \
"v_mov_b32_e32 v162, v250\n" \
"v_mov_b32_e32 v163, v250\n" \
"v_mov_b32_e32 v164, v250\n" \
"v_mov_b32_e32 v165, v250\n" \
"v_mov_b32_e32 v166, v250\n" \
"v_mov_b32_e32 v167, v250\n" \
"v_mov_b32_e32 v168, v250\n" \
"v_mov_b32_e32 v169, v250\n" \
"v_mov_b32_e32 v170, v250\n" \
"v_mov_b32_e32 v171, v250\n" \
"v_mov_b32_e32 v172, v250\n" \
"v_mov_b32_e32 v173, v250\n" \
"v_mov_b32_e32 v174, v250\n" \
"v_mov_b32_e32 v175, v250\n" \
".LATLslowA_go:\n" \
"v_max3_f32 v216, v160, v161, v162\n" \
"v_max3_f32 v220, v163, v164, v165\n" \
"v_max3_f32 v217, v168, v169, v170\n" \
"v_max3_f32 v221, v171, v172, v173\n" \
"v_max3_f32 v216, v216, v166, v167\n" \
"v_max3_f32 v217, v217, v174, v175\n" \
"v_max_f32_e32 v216, v216, v220\n" \
"v_max_f32_e32 v217, v217, v221\n" \
"v_mov_b32_e32 v220, v216\n" \
"v_mov_b32_e32 v221, v217\n" \
"s_nop 0\n" \
"v_permlane32_swap_b32_e32 v216, v220\n" \
"v_permlane32_swap_b32_e32 v217, v221\n" \
"v_max_f32_e32 v216, v216, v220\n" \
"v_max_f32_e32 v217, v217, v221\n" \
"v_mov_b32_e32 v220, v216\n" \
"v_mov_b32_e32 v221, v217\n" \
"s_nop 0\n" \
"v_permlane16_swap_b32_e32 v216, v220\n" \
"v_permlane16_swap_b32_e32 v217, v221\n" \
"v_max_f32_e32 v216, v216, v220\n" \
"v_max_f32_e32 v217, v217, v221\n" \
"v_max_f32_e32 v216, v216, v218\n" \
"v_sub_f32_e32 v220, v218, v216\n" \
"v_exp_f32_e32 v220, v220\n" \
"v_mov_b32_e32 v218, v216\n" \
"v_max_f32_e32 v217, v217, v219\n" \
"v_sub_f32_e32 v221, v219, v217\n" \
"v_exp_f32_e32 v221, v221\n" \
"v_mov_b32_e32 v219, v217\n" \
"v_sub_f32_e32 v160, v160, v216\n" \
"v_sub_f32_e32 v161, v161, v216\n" \
"v_sub_f32_e32 v162, v162, v216\n" \
"v_sub_f32_e32 v163, v163, v216\n" \
"v_sub_f32_e32 v164, v164, v216\n" \
"v_sub_f32_e32 v165, v165, v216\n" \
"v_sub_f32_e32 v166, v166, v216\n" \
"v_sub_f32_e32 v167, v167, v216\n" \
"v_sub_f32_e32 v168, v168, v217\n" \
"v_sub_f32_e32 v169, v169, v217\n" \
"v_sub_f32_e32 v170, v170, v217\n" \
"v_sub_f32_e32 v171, v171, v217\n" \
"v_sub_f32_e32 v172, v172, v217\n" \
"v_sub_f32_e32 v173, v173, v217\n" \
"v_sub_f32_e32 v174, v174, v217\n" \
"v_sub_f32_e32 v175, v175, v217\n" \
"v_mul_f32_e32 v0, v0, v220\n" \
"v_mul_f32_e32 v1, v1, v220\n" \
"v_mul_f32_e32 v2, v2, v220\n" \
"v_mul_f32_e32 v3, v3, v220\n" \
"v_mul_f32_e32 v4, v4, v220\n" \
"v_mul_f32_e32 v5, v5, v220\n" \
"v_mul_f32_e32 v6, v6, v220\n" \
"v_mul_f32_e32 v7, v7, v220\n" \
"v_mul_f32_e32 v8, v8, v220\n" \
"v_mul_f32_e32 v9, v9, v220\n" \
"v_mul_f32_e32 v10, v10, v220\n" \
"v_mul_f32_e32 v11, v11, v220\n" \
"v_mul_f32_e32 v12, v12, v220\n" \
"v_mul_f32_e32 v13, v13, v220\n" \
"v_mul_f32_e32 v14, v14, v220\n" \
"v_mul_f32_e32 v15, v15, v220\n" \
"v_mul_f32_e32 v16, v16, v220\n" \
"v_mul_f32_e32 v17, v17, v220\n" \
"v_mul_f32_e32 v18, v18, v220\n" \
"v_mul_f32_e32 v19, v19, v220\n" \
"v_mul_f32_e32 v20, v20, v220\n" \
"v_mul_f32_e32 v21, v21, v220\n" \
"v_mul_f32_e32 v22, v22, v220\n" \
"v_mul_f32_e32 v23, v23, v220\n" \
"v_mul_f32_e32 v24, v24, v220\n" \
"v_mul_f32_e32 v25, v25, v220\n" \
"v_mul_f32_e32 v26, v26, v220\n" \
"v_mul_f32_e32 v27, v27, v220\n" \
"v_mul_f32_e32 v28, v28, v220\n" \
"v_mul_f32_e32 v29, v29, v220\n" \
"v_mul_f32_e32 v30, v30, v220\n" \
"v_mul_f32_e32 v31, v31, v220\n" \
"v_mul_f32_e32 v32, v32, v221\n" \
"v_mul_f32_e32 v33, v33, v221\n" \
"v_mul_f32_e32 v34, v34, v221\n" \
"v_mul_f32_e32 v35, v35, v221\n" \
"v_mul_f32_e32 v36, v36, v221\n" \
"v_mul_f32_e32 v37, v37, v221\n" \
"v_mul_f32_e32 v38, v38, v221\n" \
"v_mul_f32_e32 v39, v39, v221\n" \
"v_mul_f32_e32 v40, v40, v221\n" \
"v_mul_f32_e32 v41, v41, v221\n" \
"v_mul_f32_e32 v42, v42, v221\n" \
"v_mul_f32_e32 v43, v43, v221\n" \
"v_mul_f32_e32 v44, v44, v221\n" \
"v_mul_f32_e32 v45, v45, v221\n" \
"v_mul_f32_e32 v46, v46, v221\n" \
"v_mul_f32_e32 v47, v47, v221\n" \
"v_mul_f32_e32 v48, v48, v221\n" \
"v_mul_f32_e32 v49, v49, v221\n" \
"v_mul_f32_e32 v50, v50, v221\n" \
"v_mul_f32_e32 v51, v51, v221\n" \
"v_mul_f32_e32 v52, v52, v221\n" \
"v_mul_f32_e32 v53, v53, v221\n" \
"v_mul_f32_e32 v54, v54, v221\n" \
"v_mul_f32_e32 v55, v55, v221\n" \
"v_mul_f32_e32 v56, v56, v221\n" \
"v_mul_f32_e32 v57, v57, v221\n" \
"v_mul_f32_e32 v58, v58, v221\n" \
"v_mul_f32_e32 v59, v59, v221\n" \
"v_mul_f32_e32 v60, v60, v221\n" \
"v_mul_f32_e32 v61, v61, v221\n" \
"v_mul_f32_e32 v62, v62, v221\n" \
"v_mul_f32_e32 v63, v63, v221\n" \
"v_accvgpr_write_b32 a240, 0\n" \
"v_accvgpr_write_b32 a241, 0\n" \
"v_accvgpr_write_b32 a242, 0\n" \
"v_accvgpr_write_b32 a243, 0\n" \
"v_exp_f32_e32 v160, v160\n" \
"v_exp_f32_e32 v161, v161\n" \
"v_exp_f32_e32 v162, v162\n" \
"v_cvt_pk_bf16_f32 v208, v160, v161\n" \
"v_exp_f32_e32 v163, v163\n" \
"v_exp_f32_e32 v164, v164\n" \
"v_cvt_pk_bf16_f32 v209, v162, v163\n" \
"v_exp_f32_e32 v165, v165\n" \
"v_exp_f32_e32 v166, v166\n" \
"v_cvt_pk_bf16_f32 v210, v164, v165\n" \
"v_exp_f32_e32 v167, v167\n" \
"v_exp_f32_e32 v168, v168\n" \
"v_cvt_pk_bf16_f32 v211, v166, v167\n" \
"v_exp_f32_e32 v169, v169\n" \
"v_exp_f32_e32 v170, v170\n" \
"v_cvt_pk_bf16_f32 v212, v168, v169\n" \
"v_exp_f32_e32 v171, v171\n" \
"v_exp_f32_e32 v172, v172\n" \
"v_cvt_pk_bf16_f32 v213, v170, v171\n" \
"v_exp_f32_e32 v173, v173\n" \
"v_exp_f32_e32 v174, v174\n" \
"v_cvt_pk_bf16_f32 v214, v172, v173\n" \
"v_exp_f32_e32 v175, v175\n" \
"s_nop 0\n" \
"v_cvt_pk_bf16_f32 v215, v174, v175\n" \
"s_nop 1\n" \
"v_smfmac_f32_16x16x64_bf16 a[240:243], a[236:239], v[208:215], v248\n" \
"v_mfma_f32_16x16x32_bf16 v[0:3], a[160:163], v[208:211], v[0:3]\n" \
"v_mfma_f32_16x16x32_bf16 v[32:35], a[160:163], v[212:215], v[32:35]\n" \
"v_mfma_f32_16x16x32_bf16 v[36:39], a[164:167], v[212:215], v[36:39]\n" \
"v_mfma_f32_16x16x32_bf16 v[4:7], a[164:167], v[208:211], v[4:7]\n" \
"v_mfma_f32_16x16x32_bf16 v[8:11], a[168:171], v[208:211], v[8:11]\n" \
"v_mfma_f32_16x16x32_bf16 v[40:43], a[168:171], v[212:215], v[40:43]\n" \
"v_mfma_f32_16x16x32_bf16 v[44:47], a[172:175], v[212:215], v[44:47]\n" \
"v_mfma_f32_16x16x32_bf16 v[12:15], a[172:175], v[208:211], v[12:15]\n" \
"v_mfma_f32_16x16x32_bf16 v[16:19], a[176:179], v[208:211], v[16:19]\n" \
"v_mfma_f32_16x16x32_bf16 v[48:51], a[176:179], v[212:215], v[48:51]\n" \
"v_mfma_f32_16x16x32_bf16 v[52:55], a[180:183], v[212:215], v[52:55]\n" \
"v_mfma_f32_16x16x32_bf16 v[20:23], a[180:183], v[208:211], v[20:23]\n" \
"v_mfma_f32_16x16x32_bf16 v[24:27], a[184:187], v[208:211], v[24:27]\n" \
"v_mfma_f32_16x16x32_bf16 v[56:59], a[184:187], v[212:215], v[56:59]\n" \
"v_mfma_f32_16x16x32_bf16 v[60:63], a[188:191], v[212:215], v[60:63]\n" \
"v_mfma_f32_16x16x32_bf16 v[28:31], a[188:191], v[208:211], v[28:31]\n" \
"v_accvgpr_read_b32 v216, a241\n" \
"v_fma_f32 v222, v222, v220, v216\n" \
"v_mov_b32_e32 v220, 1.0\n" \
"v_accvgpr_read_b32 v217, a243\n" \
"v_fma_f32 v223, v223, v221, v217\n" \
"v_mov_b32_e32 v221, 1.0\n" \
"v_accvgpr_read_b32 v216, a240\n" \
"v_accvgpr_read_b32 v217, a242\n" \
"v_fma_f32 v222, v222, v220, v216\n" \
"v_fma_f32 v223, v223, v221, v217\n" \
"s_nop 7\n" \
"s_add_i32 s62, s62, 1\n" \
"s_cmp_lt_i32 s62, 64\n" \
"s_cbranch_scc1 .LATLslowA_loop\n" \
"v_accvgpr_write_b32 a240, v222\n" \
"v_accvgpr_write_b32 a242, v223\n" \
"v_accvgpr_write_b32 a241, 0\n" \
"v_accvgpr_write_b32 a243, 0\n" \
"s_add_u32 s60, s64, 0\n" \
"buffer_load_dwordx4 a[128:131], v241, %[wsr], s60 offen offset:0 sc1\n" \
"s_add_u32 s60, s64, 0\n" \
"buffer_load_dwordx4 a[132:135], v241, %[wsr], s60 offen offset:1024 sc1\n" \
"s_add_u32 s60, s64, 0\n" \
"buffer_load_dwordx4 a[136:139], v241, %[wsr], s60 offen offset:2048 sc1\n" \
"s_add_u32 s60, s64, 0\n" \
"buffer_load_dwordx4 a[140:143], v241, %[wsr], s60 offen offset:3072 sc1\n" \
"s_add_u32 s60, s64, 0\n" \
"buffer_load_dwordx4 a[144:147], v249, %[wsr], s60 offen offset:0 sc1\n" \
"s_add_u32 s60, s64, 0\n" \
"buffer_load_dwordx4 a[148:151], v249, %[wsr], s60 offen offset:1024 sc1\n" \
"s_add_u32 s60, s64, 0\n" \
"buffer_load_dwordx4 a[152:155], v249, %[wsr], s60 offen offset:2048 sc1\n" \
"s_add_u32 s60, s64, 0\n" \
"buffer_load_dwordx4 a[156:159], v249, %[wsr], s60 offen offset:3072 sc1\n" \
"s_add_u32 s60, s64, 8192\n" \
"buffer_load_dwordx4 a[160:163], v241, %[wsr], s60 offen offset:0 sc1\n" \
"s_add_u32 s60, s64, 8192\n" \
"buffer_load_dwordx4 a[164:167], v241, %[wsr], s60 offen offset:1024 sc1\n" \
"s_add_u32 s60, s64, 8192\n" \
"buffer_load_dwordx4 a[168:171], v241, %[wsr], s60 offen offset:2048 sc1\n" \
"s_add_u32 s60, s64, 8192\n" \
"buffer_load_dwordx4 a[172:175], v241, %[wsr], s60 offen offset:3072 sc1\n" \
"s_add_u32 s60, s64, 8192\n" \
"buffer_load_dwordx4 a[176:179], v249, %[wsr], s60 offen offset:0 sc1\n" \
"s_add_u32 s60, s64, 8192\n" \
"buffer_load_dwordx4 a[180:183], v249, %[wsr], s60 offen offset:1024 sc1\n" \
"s_add_u32 s60, s64, 8192\n" \
"buffer_load_dwordx4 a[184:187], v249, %[wsr], s60 offen offset:2048 sc1\n" \
"s_add_u32 s60, s64, 8192\n" \
"buffer_load_dwordx4 a[188:191], v249, %[wsr], s60 offen offset:3072 sc1\n" \
"s_add_u32 s60, s64, 57344\n" \
"buffer_load_dwordx4 v[208:211], v249, %[wsr], s60 offen offset:0 sc1\n" \
"s_add_u32 s60, s64, 57344\n" \
"buffer_load_dwordx4 v[212:215], v249, %[wsr], s60 offen offset:1024 sc1\n" \
"s_add_u32 s60, s64, 57344\n" \
"buffer_load_dwordx4 v[216:219], v249, %[wsr], s60 offen offset:2048 sc1\n" \
"s_add_u32 s60, s64, 57344\n" \
"buffer_load_dwordx4 v[220:223], v249, %[wsr], s60 offen offset:3072 sc1\n" \
"v_accvgpr_read_b32 v160, a240\n" \
"v_accvgpr_read_b32 v161, a241\n" \
"v_accvgpr_read_b32 v162, a242\n" \
"v_accvgpr_read_b32 v163, a243\n" \
"v_add_f32_e32 v164, v160, v161\n" \
"v_add_f32_e32 v165, v162, v163\n" \
"v_rcp_f32_e32 v160, v164\n" \
"v_rcp_f32_e32 v162, v165\n" \
"s_nop 0\n" \
"v_mov_b32_e32 v161, v160\n" \
"v_mov_b32_e32 v163, v162\n" \
"v_pk_mul_f32 v[32:33], v[32:33], v[162:163]\n" \
"v_pk_mul_f32 v[34:35], v[34:35], v[162:163]\n" \
"v_pk_mul_f32 v[36:37], v[36:37], v[162:163]\n" \
"v_pk_mul_f32 v[38:39], v[38:39], v[162:163]\n" \
"v_cvt_pk_bf16_f32 v32, v32, v33\n" \
"v_cvt_pk_bf16_f32 v33, v34, v35\n" \
"v_cvt_pk_bf16_f32 v34, v36, v37\n" \
"v_cvt_pk_bf16_f32 v35, v38, v39\n" \
"buffer_store_dwordx4 v[32:35], v249, %[wsr], s65 offen offset:0 sc1\n" \
"v_pk_mul_f32 v[40:41], v[40:41], v[162:163]\n" \
"v_pk_mul_f32 v[42:43], v[42:43], v[162:163]\n" \
"v_pk_mul_f32 v[44:45], v[44:45], v[162:163]\n" \
"v_pk_mul_f32 v[46:47], v[46:47], v[162:163]\n" \
"v_cvt_pk_bf16_f32 v36, v40, v41\n" \
"v_cvt_pk_bf16_f32 v37, v42, v43\n" \
"v_cvt_pk_bf16_f32 v38, v44, v45\n" \
"v_cvt_pk_bf16_f32 v39, v46, v47\n" \
"buffer_store_dwordx4 v[36:39], v249, %[wsr], s65 offen offset:1024 sc1\n" \
"v_pk_mul_f32 v[48:49], v[48:49], v[162:163]\n" \
"v_pk_mul_f32 v[50:51], v[50:51], v[162:163]\n" \
"v_pk_mul_f32 v[52:53], v[52:53], v[162:163]\n" \
"v_pk_mul_f32 v[54:55], v[54:55], v[162:163]\n" \
"v_cvt_pk_bf16_f32 v40, v48, v49\n" \
"v_cvt_pk_bf16_f32 v41, v50, v51\n" \
"v_cvt_pk_bf16_f32 v42, v52, v53\n" \
"v_cvt_pk_bf16_f32 v43, v54, v55\n" \
"buffer_store_dwordx4 v[40:43], v249, %[wsr], s65 offen offset:2048 sc1\n" \
"v_pk_mul_f32 v[56:57], v[56:57], v[162:163]\n" \
"v_pk_mul_f32 v[58:59], v[58:59], v[162:163]\n" \
"v_pk_mul_f32 v[60:61], v[60:61], v[162:163]\n" \
"v_pk_mul_f32 v[62:63], v[62:63], v[162:163]\n" \
"v_cvt_pk_bf16_f32 v44, v56, v57\n" \
"v_cvt_pk_bf16_f32 v45, v58, v59\n" \
"v_cvt_pk_bf16_f32 v46, v60, v61\n" \
"v_cvt_pk_bf16_f32 v47, v62, v63\n" \
"buffer_store_dwordx4 v[44:47], v249, %[wsr], s65 offen offset:3072 sc1\n" \
"v_pk_mul_f32 v[0:1], v[0:1], v[160:161]\n" \
"v_pk_mul_f32 v[2:3], v[2:3], v[160:161]\n" \
"v_pk_mul_f32 v[4:5], v[4:5], v[160:161]\n" \
"v_pk_mul_f32 v[6:7], v[6:7], v[160:161]\n" \
"v_cvt_pk_bf16_f32 v48, v0, v1\n" \
"v_cvt_pk_bf16_f32 v49, v2, v3\n" \
"v_cvt_pk_bf16_f32 v50, v4, v5\n" \
"v_cvt_pk_bf16_f32 v51, v6, v7\n" \
"buffer_store_dwordx4 v[48:51], v241, %[wsr], s65 offen offset:0 sc1\n" \
"v_pk_mul_f32 v[8:9], v[8:9], v[160:161]\n" \
"v_pk_mul_f32 v[10:11], v[10:11], v[160:161]\n" \
"v_pk_mul_f32 v[12:13], v[12:13], v[160:161]\n" \
"v_pk_mul_f32 v[14:15], v[14:15], v[160:161]\n" \
"v_cvt_pk_bf16_f32 v52, v8, v9\n" \
"v_cvt_pk_bf16_f32 v53, v10, v11\n" \
"v_cvt_pk_bf16_f32 v54, v12, v13\n" \
"v_cvt_pk_bf16_f32 v55, v14, v15\n" \
"buffer_store_dwordx4 v[52:55], v241, %[wsr], s65 offen offset:1024 sc1\n" \
"v_pk_mul_f32 v[16:17], v[16:17], v[160:161]\n" \
"v_pk_mul_f32 v[18:19], v[18:19], v[160:161]\n" \
"v_pk_mul_f32 v[20:21], v[20:21], v[160:161]\n" \
"v_pk_mul_f32 v[22:23], v[22:23], v[160:161]\n" \
"v_cvt_pk_bf16_f32 v56, v16, v17\n" \
"v_cvt_pk_bf16_f32 v57, v18, v19\n" \
"v_cvt_pk_bf16_f32 v58, v20, v21\n" \
"v_cvt_pk_bf16_f32 v59, v22, v23\n" \
"buffer_store_dwordx4 v[56:59], v241, %[wsr], s65 offen offset:2048 sc1\n" \
"v_pk_mul_f32 v[24:25], v[24:25], v[160:161]\n" \
"v_pk_mul_f32 v[26:27], v[26:27], v[160:161]\n" \
"v_pk_mul_f32 v[28:29], v[28:29], v[160:161]\n" \
"v_pk_mul_f32 v[30:31], v[30:31], v[160:161]\n" \
"v_cvt_pk_bf16_f32 v60, v24, v25\n" \
"v_cvt_pk_bf16_f32 v61, v26, v27\n" \
"v_cvt_pk_bf16_f32 v62, v28, v29\n" \
"v_cvt_pk_bf16_f32 v63, v30, v31\n" \
"buffer_store_dwordx4 v[60:63], v241, %[wsr], s65 offen offset:3072 sc1\n" \
"s_waitcnt vmcnt(0)\n" \
"s_branch .LATLbackA\n" \
".LATLslowB:\n" \
"s_waitcnt vmcnt(0)\n" \
"v_mov_b32_e32 v64, 0\n" \
"v_mov_b32_e32 v65, 0\n" \
"v_mov_b32_e32 v66, 0\n" \
"v_mov_b32_e32 v67, 0\n" \
"v_mov_b32_e32 v68, 0\n" \
"v_mov_b32_e32 v69, 0\n" \
"v_mov_b32_e32 v70, 0\n" \
"v_mov_b32_e32 v71, 0\n" \
"v_mov_b32_e32 v72, 0\n" \
"v_mov_b32_e32 v73, 0\n" \
"v_mov_b32_e32 v74, 0\n" \
"v_mov_b32_e32 v75, 0\n" \
"v_mov_b32_e32 v76, 0\n" \
"v_mov_b32_e32 v77, 0\n" \
"v_mov_b32_e32 v78, 0\n" \
"v_mov_b32_e32 v79, 0\n" \
"v_mov_b32_e32 v80, 0\n" \
"v_mov_b32_e32 v81, 0\n" \
"v_mov_b32_e32 v82, 0\n" \
"v_mov_b32_e32 v83, 0\n" \
"v_mov_b32_e32 v84, 0\n" \
"v_mov_b32_e32 v85, 0\n" \
"v_mov_b32_e32 v86, 0\n" \
"v_mov_b32_e32 v87, 0\n" \
"v_mov_b32_e32 v88, 0\n" \
"v_mov_b32_e32 v89, 0\n" \
"v_mov_b32_e32 v90, 0\n" \
"v_mov_b32_e32 v91, 0\n" \
"v_mov_b32_e32 v92, 0\n" \
"v_mov_b32_e32 v93, 0\n" \
"v_mov_b32_e32 v94, 0\n" \
"v_mov_b32_e32 v95, 0\n" \
"v_mov_b32_e32 v96, 0\n" \
"v_mov_b32_e32 v97, 0\n" \
"v_mov_b32_e32 v98, 0\n" \
"v_mov_b32_e32 v99, 0\n" \
"v_mov_b32_e32 v100, 0\n" \
"v_mov_b32_e32 v101, 0\n" \
"v_mov_b32_e32 v102, 0\n" \
"v_mov_b32_e32 v103, 0\n" \
"v_mov_b32_e32 v104, 0\n" \
"v_mov_b32_e32 v105, 0\n" \
"v_mov_b32_e32 v106, 0\n" \
"v_mov_b32_e32 v107, 0\n" \
"v_mov_b32_e32 v108, 0\n" \
"v_mov_b32_e32 v109, 0\n" \
"v_mov_b32_e32 v110, 0\n" \
"v_mov_b32_e32 v111, 0\n" \
"v_mov_b32_e32 v112, 0\n" \
"v_mov_b32_e32 v113, 0\n" \
"v_mov_b32_e32 v114, 0\n" \
"v_mov_b32_e32 v115, 0\n" \
"v_mov_b32_e32 v116, 0\n" \
"v_mov_b32_e32 v117, 0\n" \
"v_mov_b32_e32 v118, 0\n" \
"v_mov_b32_e32 v119, 0\n" \
"v_mov_b32_e32 v120, 0\n" \
"v_mov_b32_e32 v121, 0\n" \
"v_mov_b32_e32 v122, 0\n" \
"v_mov_b32_e32 v123, 0\n" \
"v_mov_b32_e32 v124, 0\n" \
"v_mov_b32_e32 v125, 0\n" \
"v_mov_b32_e32 v126, 0\n" \
"v_mov_b32_e32 v127, 0\n" \
"v_mov_b32_e32 v142, 0\n" \
"v_mov_b32_e32 v138, v252\n" \
"v_mov_b32_e32 v143, 0\n" \
"v_mov_b32_e32 v139, v252\n" \
"v_mov_b32_e32 v250, 0xceabae43\n" \
"s_mov_b32 s62, 0\n" \
"s_mov_b32 s63, %[db]\n" \
"s_mov_b32 s60, s78\n" \
".LATLslowB_loop:\n" \
"buffer_load_dwordx4 a[128:131], v241, %[wsr], s60 offen offset:0 sc1\n" \
"buffer_load_dwordx4 a[132:135], v241, %[wsr], s60 offen offset:1024 sc1\n" \
"buffer_load_dwordx4 a[136:139], v241, %[wsr], s60 offen offset:2048 sc1\n" \
"buffer_load_dwordx4 a[140:143], v241, %[wsr], s60 offen offset:3072 sc1\n" \
"s_add_u32 s60, s60, 0x1000\n" \
"buffer_load_dwordx4 a[144:147], v241, %[wsr], s60 offen offset:0 sc1\n" \
"buffer_load_dwordx4 a[148:151], v241, %[wsr], s60 offen offset:1024 sc1\n" \
"buffer_load_dwordx4 a[152:155], v241, %[wsr], s60 offen offset:2048 sc1\n" \
"buffer_load_dwordx4 a[156:159], v241, %[wsr], s60 offen offset:3072 sc1\n" \
"s_add_u32 s60, s60, 0x1000\n" \
"buffer_load_dwordx4 a[160:163], v241, %[wsr], s60 offen offset:0 sc1\n" \
"buffer_load_dwordx4 a[164:167], v241, %[wsr], s60 offen offset:1024 sc1\n" \
"buffer_load_dwordx4 a[168:171], v241, %[wsr], s60 offen offset:2048 sc1\n" \
"buffer_load_dwordx4 a[172:175], v241, %[wsr], s60 offen offset:3072 sc1\n" \
"s_add_u32 s60, s60, 0x1000\n" \
"buffer_load_dwordx4 a[176:179], v241, %[wsr], s60 offen offset:0 sc1\n" \
"buffer_load_dwordx4 a[180:183], v241, %[wsr], s60 offen offset:1024 sc1\n" \
"buffer_load_dwordx4 a[184:187], v241, %[wsr], s60 offen offset:2048 sc1\n" \
"buffer_load_dwordx4 a[188:191], v241, %[wsr], s60 offen offset:3072 sc1\n" \
"s_add_u32 s60, s60, 0x1000\n" \
"s_waitcnt vmcnt(0)\n" \
"v_mfma_f32_16x16x32_bf16 v[176:179], a[128:131], a[32:35], 0\n" \
"v_mfma_f32_16x16x32_bf16 v[184:187], a[128:131], a[48:51], 0\n" \
"v_mfma_f32_16x16x32_bf16 v[188:191], a[132:135], a[48:51], 0\n" \
"v_mfma_f32_16x16x32_bf16 v[180:183], a[132:135], a[32:35], 0\n" \
"v_mfma_f32_16x16x32_bf16 v[176:179], a[136:139], a[36:39], v[176:179]\n" \
"v_mfma_f32_16x16x32_bf16 v[184:187], a[136:139], a[52:55], v[184:187]\n" \
"v_mfma_f32_16x16x32_bf16 v[188:191], a[140:143], a[52:55], v[188:191]\n" \
"v_mfma_f32_16x16x32_bf16 v[180:183], a[140:143], a[36:39], v[180:183]\n" \
"v_mfma_f32_16x16x32_bf16 v[176:179], a[144:147], a[40:43], v[176:179]\n" \
"v_mfma_f32_16x16x32_bf16 v[184:187], a[144:147], a[56:59], v[184:187]\n" \
"v_mfma_f32_16x16x32_bf16 v[188:191], a[148:151], a[56:59], v[188:191]\n" \
"v_mfma_f32_16x16x32_bf16 v[180:183], a[148:151], a[40:43], v[180:183]\n" \
"v_mfma_f32_16x16x32_bf16 v[176:179], a[152:155], a[44:47], v[176:179]\n" \
"v_mfma_f32_16x16x32_bf16 v[184:187], a[152:155], a[60:63], v[184:187]\n" \
"v_mfma_f32_16x16x32_bf16 v[188:191], a[156:159], a[60:63], v[188:191]\n" \
"v_mfma_f32_16x16x32_bf16 v[180:183], a[156:159], a[44:47], v[180:183]\n" \
"s_nop 7\n" \
"s_cmp_lt_i32 s62, s63\n" \
"s_cbranch_scc1 .LATLslowB_go\n" \
"s_cmp_eq_i32 s62, s63\n" \
"s_cbranch_scc0 .LATLslowB_all\n" \
"v_cmp_le_i32_e64 s[88:89], 0, v225\n" \
"v_cmp_le_i32_e64 s[90:91], 1, v225\n" \
"v_cmp_le_i32_e64 s[92:93], 2, v225\n" \
"v_cmp_le_i32_e64 s[94:95], 3, v225\n" \
"v_mov_b32_e32 v180, v250\n" \
"v_mov_b32_e32 v181, v250\n" \
"v_mov_b32_e32 v182, v250\n" \
"v_mov_b32_e32 v183, v250\n" \
"v_cndmask_b32_e64 v176, v250, v176, s[88:89]\n" \
"v_cndmask_b32_e64 v177, v250, v177, s[90:91]\n" \
"v_cndmask_b32_e64 v178, v250, v178, s[92:93]\n" \
"v_cndmask_b32_e64 v179, v250, v179, s[94:95]\n" \
"v_cndmask_b32_e64 v188, v250, v188, s[88:89]\n" \
"v_cndmask_b32_e64 v189, v250, v189, s[90:91]\n" \
"v_cndmask_b32_e64 v190, v250, v190, s[92:93]\n" \
"v_cndmask_b32_e64 v191, v250, v191, s[94:95]\n" \
"s_branch .LATLslowB_go\n" \
".LATLslowB_all:\n" \
"v_mov_b32_e32 v176, v250\n" \
"v_mov_b32_e32 v177, v250\n" \
"v_mov_b32_e32 v178, v250\n" \
"v_mov_b32_e32 v179, v250\n" \
"v_mov_b32_e32 v180, v250\n" \
"v_mov_b32_e32 v181, v250\n" \
"v_mov_b32_e32 v182, v250\n" \
"v_mov_b32_e32 v183, v250\n" \
"v_mov_b32_e32 v184, v250\n" \
"v_mov_b32_e32 v185, v250\n" \
"v_mov_b32_e32 v186, v250\n" \
"v_mov_b32_e32 v187, v250\n" \
"v_mov_b32_e32 v188, v250\n" \
"v_mov_b32_e32 v189, v250\n" \
"v_mov_b32_e32 v190, v250\n" \
"v_mov_b32_e32 v191, v250\n" \
".LATLslowB_go:\n" \
"v_max3_f32 v136, v176, v177, v178\n" \
"v_max3_f32 v140, v179, v180, v181\n" \
"v_max3_f32 v137, v184, v185, v186\n" \
"v_max3_f32 v141, v187, v188, v189\n" \
"v_max3_f32 v136, v136, v182, v183\n" \
"v_max3_f32 v137, v137, v190, v191\n" \
"v_max_f32_e32 v136, v136, v140\n" \
"v_max_f32_e32 v137, v137, v141\n" \
"v_mov_b32_e32 v140, v136\n" \
"v_mov_b32_e32 v141, v137\n" \
"s_nop 0\n" \
"v_permlane32_swap_b32_e32 v136, v140\n" \
"v_permlane32_swap_b32_e32 v137, v141\n" \
"v_max_f32_e32 v136, v136, v140\n" \
"v_max_f32_e32 v137, v137, v141\n" \
"v_mov_b32_e32 v140, v136\n" \
"v_mov_b32_e32 v141, v137\n" \
"s_nop 0\n" \
"v_permlane16_swap_b32_e32 v136, v140\n" \
"v_permlane16_swap_b32_e32 v137, v141\n" \
"v_max_f32_e32 v136, v136, v140\n" \
"v_max_f32_e32 v137, v137, v141\n" \
"v_max_f32_e32 v136, v136, v138\n" \
"v_sub_f32_e32 v140, v138, v136\n" \
"v_exp_f32_e32 v140, v140\n" \
"v_mov_b32_e32 v138, v136\n" \
"v_max_f32_e32 v137, v137, v139\n" \
"v_sub_f32_e32 v141, v139, v137\n" \
"v_exp_f32_e32 v141, v141\n" \
"v_mov_b32_e32 v139, v137\n" \
"v_sub_f32_e32 v176, v176, v136\n" \
"v_sub_f32_e32 v177, v177, v136\n" \
"v_sub_f32_e32 v178, v178, v136\n" \
"v_sub_f32_e32 v179, v179, v136\n" \
"v_sub_f32_e32 v180, v180, v136\n" \
"v_sub_f32_e32 v181, v181, v136\n" \
"v_sub_f32_e32 v182, v182, v136\n" \
"v_sub_f32_e32 v183, v183, v136\n" \
"v_sub_f32_e32 v184, v184, v137\n" \
"v_sub_f32_e32 v185, v185, v137\n" \
"v_sub_f32_e32 v186, v186, v137\n" \
"v_sub_f32_e32 v187, v187, v137\n" \
"v_sub_f32_e32 v188, v188, v137\n" \
"v_sub_f32_e32 v189, v189, v137\n" \
"v_sub_f32_e32 v190, v190, v137\n" \
"v_sub_f32_e32 v191, v191, v137\n" \
"v_mul_f32_e32 v64, v64, v140\n" \
"v_mul_f32_e32 v65, v65, v140\n" \
"v_mul_f32_e32 v66, v66, v140\n" \
"v_mul_f32_e32 v67, v67, v140\n" \
"v_mul_f32_e32 v68, v68, v140\n" \
"v_mul_f32_e32 v69, v69, v140\n" \
"v_mul_f32_e32 v70, v70, v140\n" \
"v_mul_f32_e32 v71, v71, v140\n" \
"v_mul_f32_e32 v72, v72, v140\n" \
"v_mul_f32_e32 v73, v73, v140\n" \
"v_mul_f32_e32 v74, v74, v140\n" \
"v_mul_f32_e32 v75, v75, v140\n" \
"v_mul_f32_e32 v76, v76, v140\n" \
"v_mul_f32_e32 v77, v77, v140\n" \
"v_mul_f32_e32 v78, v78, v140\n" \
"v_mul_f32_e32 v79, v79, v140\n" \
"v_mul_f32_e32 v80, v80, v140\n" \
"v_mul_f32_e32 v81, v81, v140\n" \
"v_mul_f32_e32 v82, v82, v140\n" \
"v_mul_f32_e32 v83, v83, v140\n" \
"v_mul_f32_e32 v84, v84, v140\n" \
"v_mul_f32_e32 v85, v85, v140\n" \
"v_mul_f32_e32 v86, v86, v140\n" \
"v_mul_f32_e32 v87, v87, v140\n" \
"v_mul_f32_e32 v88, v88, v140\n" \
"v_mul_f32_e32 v89, v89, v140\n" \
"v_mul_f32_e32 v90, v90, v140\n" \
"v_mul_f32_e32 v91, v91, v140\n" \
"v_mul_f32_e32 v92, v92, v140\n" \
"v_mul_f32_e32 v93, v93, v140\n" \
"v_mul_f32_e32 v94, v94, v140\n" \
"v_mul_f32_e32 v95, v95, v140\n" \
"v_mul_f32_e32 v96, v96, v141\n" \
"v_mul_f32_e32 v97, v97, v141\n" \
"v_mul_f32_e32 v98, v98, v141\n" \
"v_mul_f32_e32 v99, v99, v141\n" \
"v_mul_f32_e32 v100, v100, v141\n" \
"v_mul_f32_e32 v101, v101, v141\n" \
"v_mul_f32_e32 v102, v102, v141\n" \
"v_mul_f32_e32 v103, v103, v141\n" \
"v_mul_f32_e32 v104, v104, v141\n" \
"v_mul_f32_e32 v105, v105, v141\n" \
"v_mul_f32_e32 v106, v106, v141\n" \
"v_mul_f32_e32 v107, v107, v141\n" \
"v_mul_f32_e32 v108, v108, v141\n" \
"v_mul_f32_e32 v109, v109, v141\n" \
"v_mul_f32_e32 v110, v110, v141\n" \
"v_mul_f32_e32 v111, v111, v141\n" \
"v_mul_f32_e32 v112, v112, v141\n" \
"v_mul_f32_e32 v113, v113, v141\n" \
"v_mul_f32_e32 v114, v114, v141\n" \
"v_mul_f32_e32 v115, v115, v141\n" \
"v_mul_f32_e32 v116, v116, v141\n" \
"v_mul_f32_e32 v117, v117, v141\n" \
"v_mul_f32_e32 v118, v118, v141\n" \
"v_mul_f32_e32 v119, v119, v141\n" \
"v_mul_f32_e32 v120, v120, v141\n" \
"v_mul_f32_e32 v121, v121, v141\n" \
"v_mul_f32_e32 v122, v122, v141\n" \
"v_mul_f32_e32 v123, v123, v141\n" \
"v_mul_f32_e32 v124, v124, v141\n" \
"v_mul_f32_e32 v125, v125, v141\n" \
"v_mul_f32_e32 v126, v126, v141\n" \
"v_mul_f32_e32 v127, v127, v141\n" \
"v_accvgpr_write_b32 a248, 0\n" \
"v_accvgpr_write_b32 a249, 0\n" \
"v_accvgpr_write_b32 a250, 0\n" \
"v_accvgpr_write_b32 a251, 0\n" \
"v_exp_f32_e32 v176, v176\n" \
"v_exp_f32_e32 v177, v177\n" \
"v_exp_f32_e32 v178, v178\n" \
"v_cvt_pk_bf16_f32 v128, v176, v177\n" \
"v_exp_f32_e32 v179, v179\n" \
"v_exp_f32_e32 v180, v180\n" \
"v_cvt_pk_bf16_f32 v129, v178, v179\n" \
"v_exp_f32_e32 v181, v181\n" \
"v_exp_f32_e32 v182, v182\n" \
"v_cvt_pk_bf16_f32 v130, v180, v181\n" \
"v_exp_f32_e32 v183, v183\n" \
"v_exp_f32_e32 v184, v184\n" \
"v_cvt_pk_bf16_f32 v131, v182, v183\n" \
"v_exp_f32_e32 v185, v185\n" \
"v_exp_f32_e32 v186, v186\n" \
"v_cvt_pk_bf16_f32 v132, v184, v185\n" \
"v_exp_f32_e32 v187, v187\n" \
"v_exp_f32_e32 v188, v188\n" \
"v_cvt_pk_bf16_f32 v133, v186, v187\n" \
"v_exp_f32_e32 v189, v189\n" \
"v_exp_f32_e32 v190, v190\n" \
"v_cvt_pk_bf16_f32 v134, v188, v189\n" \
"v_exp_f32_e32 v191, v191\n" \
"s_nop 0\n" \
"v_cvt_pk_bf16_f32 v135, v190, v191\n" \
"s_nop 1\n" \
"v_smfmac_f32_16x16x64_bf16 a[248:251], a[236:239], v[128:135], v248\n" \
"v_mfma_f32_16x16x32_bf16 v[64:67], a[160:163], v[128:131], v[64:67]\n" \
"v_mfma_f32_16x16x32_bf16 v[96:99], a[160:163], v[132:135], v[96:99]\n" \
"v_mfma_f32_16x16x32_bf16 v[100:103], a[164:167], v[132:135], v[100:103]\n" \
"v_mfma_f32_16x16x32_bf16 v[68:71], a[164:167], v[128:131], v[68:71]\n" \
"v_mfma_f32_16x16x32_bf16 v[72:75], a[168:171], v[128:131], v[72:75]\n" \
"v_mfma_f32_16x16x32_bf16 v[104:107], a[168:171], v[132:135], v[104:107]\n" \
"v_mfma_f32_16x16x32_bf16 v[108:111], a[172:175], v[132:135], v[108:111]\n" \
"v_mfma_f32_16x16x32_bf16 v[76:79], a[172:175], v[128:131], v[76:79]\n" \
"v_mfma_f32_16x16x32_bf16 v[80:83], a[176:179], v[128:131], v[80:83]\n" \
"v_mfma_f32_16x16x32_bf16 v[112:115], a[176:179], v[132:135], v[112:115]\n" \
"v_mfma_f32_16x16x32_bf16 v[116:119], a[180:183], v[132:135], v[116:119]\n" \
"v_mfma_f32_16x16x32_bf16 v[84:87], a[180:183], v[128:131], v[84:87]\n" \
"v_mfma_f32_16x16x32_bf16 v[88:91], a[184:187], v[128:131], v[88:91]\n" \
"v_mfma_f32_16x16x32_bf16 v[120:123], a[184:187], v[132:135], v[120:123]\n" \
"v_mfma_f32_16x16x32_bf16 v[124:127], a[188:191], v[132:135], v[124:127]\n" \
"v_mfma_f32_16x16x32_bf16 v[92:95], a[188:191], v[128:131], v[92:95]\n" \
"v_accvgpr_read_b32 v136, a249\n" \
"v_fma_f32 v142, v142, v140, v136\n" \
"v_mov_b32_e32 v140, 1.0\n" \
"v_accvgpr_read_b32 v137, a251\n" \
"v_fma_f32 v143, v143, v141, v137\n" \
"v_mov_b32_e32 v141, 1.0\n" \
"v_accvgpr_read_b32 v136, a248\n" \
"v_accvgpr_read_b32 v137, a250\n" \
"v_fma_f32 v142, v142, v140, v136\n" \
"v_fma_f32 v143, v143, v141, v137\n" \
"s_nop 7\n" \
"s_add_i32 s62, s62, 1\n" \
"s_cmp_lt_i32 s62, 64\n" \
"s_cbranch_scc1 .LATLslowB_loop\n" \
"v_accvgpr_write_b32 a248, v142\n" \
"v_accvgpr_write_b32 a250, v143\n" \
"v_accvgpr_write_b32 a249, 0\n" \
"v_accvgpr_write_b32 a251, 0\n" \
"s_add_u32 s60, s64, 0\n" \
"buffer_load_dwordx4 a[128:131], v241, %[wsr], s60 offen offset:0 sc1\n" \
"s_add_u32 s60, s64, 0\n" \
"buffer_load_dwordx4 a[132:135], v241, %[wsr], s60 offen offset:1024 sc1\n" \
"s_add_u32 s60, s64, 0\n" \
"buffer_load_dwordx4 a[136:139], v241, %[wsr], s60 offen offset:2048 sc1\n" \
"s_add_u32 s60, s64, 0\n" \
"buffer_load_dwordx4 a[140:143], v241, %[wsr], s60 offen offset:3072 sc1\n" \
"s_add_u32 s60, s64, 0\n" \
"buffer_load_dwordx4 a[144:147], v249, %[wsr], s60 offen offset:0 sc1\n" \
"s_add_u32 s60, s64, 0\n" \
"buffer_load_dwordx4 a[148:151], v249, %[wsr], s60 offen offset:1024 sc1\n" \
"s_add_u32 s60, s64, 0\n" \
"buffer_load_dwordx4 a[152:155], v249, %[wsr], s60 offen offset:2048 sc1\n" \
"s_add_u32 s60, s64, 0\n" \
"buffer_load_dwordx4 a[156:159], v249, %[wsr], s60 offen offset:3072 sc1\n" \
"s_add_u32 s60, s64, 8192\n" \
"buffer_load_dwordx4 a[160:163], v241, %[wsr], s60 offen offset:0 sc1\n" \
"s_add_u32 s60, s64, 8192\n" \
"buffer_load_dwordx4 a[164:167], v241, %[wsr], s60 offen offset:1024 sc1\n" \
"s_add_u32 s60, s64, 8192\n" \
"buffer_load_dwordx4 a[168:171], v241, %[wsr], s60 offen offset:2048 sc1\n" \
"s_add_u32 s60, s64, 8192\n" \
"buffer_load_dwordx4 a[172:175], v241, %[wsr], s60 offen offset:3072 sc1\n" \
"s_add_u32 s60, s64, 8192\n" \
"buffer_load_dwordx4 a[176:179], v249, %[wsr], s60 offen offset:0 sc1\n" \
"s_add_u32 s60, s64, 8192\n" \
"buffer_load_dwordx4 a[180:183], v249, %[wsr], s60 offen offset:1024 sc1\n" \
"s_add_u32 s60, s64, 8192\n" \
"buffer_load_dwordx4 a[184:187], v249, %[wsr], s60 offen offset:2048 sc1\n" \
"s_add_u32 s60, s64, 8192\n" \
"buffer_load_dwordx4 a[188:191], v249, %[wsr], s60 offen offset:3072 sc1\n" \
"v_accvgpr_read_b32 v128, a248\n" \
"v_accvgpr_read_b32 v129, a249\n" \
"v_accvgpr_read_b32 v130, a250\n" \
"v_accvgpr_read_b32 v131, a251\n" \
"v_add_f32_e32 v132, v128, v129\n" \
"v_add_f32_e32 v133, v130, v131\n" \
"v_rcp_f32_e32 v128, v132\n" \
"v_rcp_f32_e32 v130, v133\n" \
"s_nop 0\n" \
"v_mov_b32_e32 v129, v128\n" \
"v_mov_b32_e32 v131, v130\n" \
"v_pk_mul_f32 v[96:97], v[96:97], v[130:131]\n" \
"v_pk_mul_f32 v[98:99], v[98:99], v[130:131]\n" \
"v_pk_mul_f32 v[100:101], v[100:101], v[130:131]\n" \
"v_pk_mul_f32 v[102:103], v[102:103], v[130:131]\n" \
"v_cvt_pk_bf16_f32 v96, v96, v97\n" \
"v_cvt_pk_bf16_f32 v97, v98, v99\n" \
"v_cvt_pk_bf16_f32 v98, v100, v101\n" \
"v_cvt_pk_bf16_f32 v99, v102, v103\n" \
"buffer_store_dwordx4 v[96:99], v249, %[wsr], s66 offen offset:0 sc1\n" \
"v_pk_mul_f32 v[104:105], v[104:105], v[130:131]\n" \
"v_pk_mul_f32 v[106:107], v[106:107], v[130:131]\n" \
"v_pk_mul_f32 v[108:109], v[108:109], v[130:131]\n" \
"v_pk_mul_f32 v[110:111], v[110:111], v[130:131]\n" \
"v_cvt_pk_bf16_f32 v100, v104, v105\n" \
"v_cvt_pk_bf16_f32 v101, v106, v107\n" \
"v_cvt_pk_bf16_f32 v102, v108, v109\n" \
"v_cvt_pk_bf16_f32 v103, v110, v111\n" \
"buffer_store_dwordx4 v[100:103], v249, %[wsr], s66 offen offset:1024 sc1\n" \
"v_pk_mul_f32 v[112:113], v[112:113], v[130:131]\n" \
"v_pk_mul_f32 v[114:115], v[114:115], v[130:131]\n" \
"v_pk_mul_f32 v[116:117], v[116:117], v[130:131]\n" \
"v_pk_mul_f32 v[118:119], v[118:119], v[130:131]\n" \
"v_cvt_pk_bf16_f32 v104, v112, v113\n" \
"v_cvt_pk_bf16_f32 v105, v114, v115\n" \
"v_cvt_pk_bf16_f32 v106, v116, v117\n" \
"v_cvt_pk_bf16_f32 v107, v118, v119\n" \
"buffer_store_dwordx4 v[104:107], v249, %[wsr], s66 offen offset:2048 sc1\n" \
"v_pk_mul_f32 v[120:121], v[120:121], v[130:131]\n" \
"v_pk_mul_f32 v[122:123], v[122:123], v[130:131]\n" \
"v_pk_mul_f32 v[124:125], v[124:125], v[130:131]\n" \
"v_pk_mul_f32 v[126:127], v[126:127], v[130:131]\n" \
"v_cvt_pk_bf16_f32 v108, v120, v121\n" \
"v_cvt_pk_bf16_f32 v109, v122, v123\n" \
"v_cvt_pk_bf16_f32 v110, v124, v125\n" \
"v_cvt_pk_bf16_f32 v111, v126, v127\n" \
"buffer_store_dwordx4 v[108:111], v249, %[wsr], s66 offen offset:3072 sc1\n" \
"v_pk_mul_f32 v[64:65], v[64:65], v[128:129]\n" \
"v_pk_mul_f32 v[66:67], v[66:67], v[128:129]\n" \
"v_pk_mul_f32 v[68:69], v[68:69], v[128:129]\n" \
"v_pk_mul_f32 v[70:71], v[70:71], v[128:129]\n" \
"v_cvt_pk_bf16_f32 v112, v64, v65\n" \
"v_cvt_pk_bf16_f32 v113, v66, v67\n" \
"v_cvt_pk_bf16_f32 v114, v68, v69\n" \
"v_cvt_pk_bf16_f32 v115, v70, v71\n" \
"buffer_store_dwordx4 v[112:115], v241, %[wsr], s66 offen offset:0 sc1\n" \
"v_pk_mul_f32 v[72:73], v[72:73], v[128:129]\n" \
"v_pk_mul_f32 v[74:75], v[74:75], v[128:129]\n" \
"v_pk_mul_f32 v[76:77], v[76:77], v[128:129]\n" \
"v_pk_mul_f32 v[78:79], v[78:79], v[128:129]\n" \
"v_cvt_pk_bf16_f32 v116, v72, v73\n" \
"v_cvt_pk_bf16_f32 v117, v74, v75\n" \
"v_cvt_pk_bf16_f32 v118, v76, v77\n" \
"v_cvt_pk_bf16_f32 v119, v78, v79\n" \
"buffer_store_dwordx4 v[116:119], v241, %[wsr], s66 offen offset:1024 sc1\n" \
"v_pk_mul_f32 v[80:81], v[80:81], v[128:129]\n" \
"v_pk_mul_f32 v[82:83], v[82:83], v[128:129]\n" \
"v_pk_mul_f32 v[84:85], v[84:85], v[128:129]\n" \
"v_pk_mul_f32 v[86:87], v[86:87], v[128:129]\n" \
"v_cvt_pk_bf16_f32 v120, v80, v81\n" \
"v_cvt_pk_bf16_f32 v121, v82, v83\n" \
"v_cvt_pk_bf16_f32 v122, v84, v85\n" \
"v_cvt_pk_bf16_f32 v123, v86, v87\n" \
"buffer_store_dwordx4 v[120:123], v241, %[wsr], s66 offen offset:2048 sc1\n" \
"v_pk_mul_f32 v[88:89], v[88:89], v[128:129]\n" \
"v_pk_mul_f32 v[90:91], v[90:91], v[128:129]\n" \
"v_pk_mul_f32 v[92:93], v[92:93], v[128:129]\n" \
"v_pk_mul_f32 v[94:95], v[94:95], v[128:129]\n" \
"v_cvt_pk_bf16_f32 v124, v88, v89\n" \
"v_cvt_pk_bf16_f32 v125, v90, v91\n" \
"v_cvt_pk_bf16_f32 v126, v92, v93\n" \
"v_cvt_pk_bf16_f32 v127, v94, v95\n" \
"buffer_store_dwordx4 v[124:127], v241, %[wsr], s66 offen offset:3072 sc1\n" \
"s_waitcnt vmcnt(0)\n" \
"s_branch .LATLbackB\n" \
".LATLend:\n"
#define MAIN_CLOB "v24", "v25", "v26", "v27", "v28", "v29", "v30", "v31", "v32", "v33", "v34", "v35", "v36", "v37", "v38", "v39", "v40", "v41", "v42", "v43", "v44", "v45", "v46", "v47", "v48", "v49", "v50", "v51", "v52", "v53", "v54", "v55", "v56", "v57", "v58", "v59", "v62", "v63", "v64", "v65", "v66", "v67", "v68", "v69", "v70", "v71", "v72", "v73", "v74", "v75", "v76", "v77", "v78", "v79", "v80", "v81", "v82", "v83", "v84", "v85", "v86", "v87", "v88", "v89", "v90", "v91", "v92", "v93", "v94", "v95", "v96", "v97", "v98", "v99", "v100", "v101", "v102", "v103", "v104", "v105", "v106", "v107", "v108", "v109", "v110", "v111", "v112", "v113", "v114", "v115", "v116", "v117", "v118", "v119", "v120", "v121", "v122", "v123", "v124", "v125", "v126", "v127", "v128", "v129", "v130", "v131", "v132", "v133", "v134", "v135", "v136", "v137", "v138", "v139", "v140", "v141", "v142", "v143", "v144", "v145", "v146", "v147", "v148", "v149", "v150", "v151", "v152", "v153", "v154", "v155", "v156", "v157", "v158", "v159", "v160", "v161", "v162", "v163", "v164", "v165", "v166", "v167", "v168", "v169", "v170", "v171", "v172", "v173", "v174", "v175", "v176", "v177", "v178", "v179", "v180", "v181", "v182", "v183", "v184", "v185", "v186", "v187", "v188", "v189", "v190", "v191", "v192", "v193", "v194", "v195", "v196", "v197", "v198", "v199", "v200", "v201", "v202", "v203", "v204", "v205", "v206", "v207", "v208", "v209", "v210", "v211", "v212", "v213", "v214", "v215", "v216", "v217", "v218", "v219", "v220", "v221", "v222", "v223", "a0", "a1", "a2", "a3", "a4", "a5", "a6", "a7", "a8", "a9", "a10", "a11", "a12", "a13", "a14", "a15", "a16", "a17", "a18", "a19", "a20", "a21", "a22", "a23", "a24", "a25", "a26", "a27", "a28", "a29", "a30", "a31", "a32", "a33", "a34", "a35", "a36", "a37", "a38", "a39", "a40", "a41", "a42", "a43", "a44", "a45", "a46", "a47", "a48", "a49", "a50", "a51", "a52", "a53", "a54", "a55", "a56", "a57", "a58", "a59", "a60", "a61", "a62", "a63", "a64", "a65", "a66", "a67", "a68", "a69", "a70", "a71", "a72", "a73", "a74", "a75", "a76", "a77", "a78", "a79", "a80", "a81", "a82", "a83", "a84", "a85", "a86", "a87", "a88", "a89", "a90", "a91", "a92", "a93", "a94", "a95", "a96", "a97", "a98", "a99", "a100", "a101", "a102", "a103", "a104", "a105", "a106", "a107", "a108", "a109", "a110", "a111", "a112", "a113", "a114", "a115", "a116", "a117", "a118", "a119", "a120", "a121", "a122", "a123", "a124", "a125", "a126", "a127", "a128", "a129", "a130", "a131", "a132", "a133", "a134", "a135", "a136", "a137", "a138", "a139", "a140", "a141", "a142", "a143", "a144", "a145", "a146", "a147", "a148", "a149", "a150", "a151", "a152", "a153", "a154", "a155", "a156", "a157", "a158", "a159", "a160", "a161", "a162", "a163", "a164", "a165", "a166", "a167", "a168", "a169", "a170", "a171", "a172", "a173", "a174", "a175", "a176", "a177", "a178", "a179", "a180", "a181", "a182", "a183", "a184", "a185", "a186", "a187", "a188", "a189", "a190", "a191", "a192", "a193", "a194", "a195", "a196", "a197", "a198", "a199", "a200", "a201", "a202", "a203", "a204", "a205", "a206", "a207", "a208", "a209", "a210", "a211", "a212", "a213", "a214", "a215", "a216", "a217", "a218", "a219", "a220", "a221", "a222", "a223", "a224", "a225", "a226", "a227", "a228", "a229", "a230", "a231", "a232", "a233", "a234", "a235", "a236", "a237", "a238", "a239", "a240", "a241", "a242", "a243", "a244", "a245", "a246", "a247", "a248", "a249", "a250", "a251", "a252", "a253", "a254", "a255", "s60", "s61", "s62", "s63", "s88", "s89", "s90", "s91", "s92", "s93", "s94", "s95", "s96", "vcc", "scc", "memory"
static __device__ const u16 ssm_tab[4096] __attribute__((aligned(128))) = {TAB_DATA};
__global__ __attribute__((aligned(KAL))) __launch_bounds__(256, 1) void k_main(const u16* __restrict__ u, const u16* __restrict__ lar, const u16* __restrict__ aim, const u16* __restrict__ cre, const u16* __restrict__ cim,
                                                 const u16* __restrict__ ldt, const u16* __restrict__ dsk, const u16* __restrict__ x, const u16* __restrict__ ckv, const u16* __restrict__ wq,
                                                 const u16* __restrict__ wk, const u16* __restrict__ wv, const u16* __restrict__ wo, char* __restrict__ ws, u16* __restrict__ out) {
  __shared__ __attribute__((aligned(16))) char lds[163840];
  LAS char* L = (LAS char*)lds;
  const int lane = threadIdx.x & 63, w = __builtin_amdgcn_readfirstlane(threadIdx.x >> 6);
  const int bid = blockIdx.x, xcd = bid & 7, r = bid >> 3, h = xcd * 4 + (r & 3), xa = r >> 2, xb = 15 - xa, pair = h * 8 + xa, ta = xa * 4 + w;
  asm volatile("" :: "s"(L));
  unsigned long long pc; unsigned off, len, dm = 0;
  asm volatile("s_getpc_b64 %0\n.LPREpc:\n s_mov_b32 %1, .LATLstart-.LPREpc\n s_mov_b32 %2, .LATLend-.LPREpc" : "=s"(pc), "=s"(off), "=s"(len));
  { const unsigned o = min((w == 0 ? 0u : off + (unsigned)(r * 24 + (w - 1) * 8) * 128u) + (lane >> (w == 0 ? 0 : 3)) * 128u, len - 4); TOUCH(pc, o); }
  const int c = w >> 1, hf = w & 1, d = pair * 2 + c, b = lane & 15, g = lane >> 4, tb2 = xb * 4 + w;
  unsigned n = (unsigned)(d * NSSM + 2 * lane) * 2u, os = pair * 1024 + lane * 4;
  unsigned ou = (unsigned)((pair & 31) * 64 + w * 16 + (lane >> 3)) * 1024u + xcd * 128 + (lane & 7) * 16, ou2 = ou + 8192, oa = (unsigned)(bid * 4 + w) * 2048u + (lane >> 2) * 128;
  unsigned ow = (unsigned)(ta * 16 + (lane >> 2)) * 8192u + (lane & 2) * 64 + h * 256;
  unsigned o4 = pair * 4, p_dt, p_dd;
  asm volatile("" : "+s"(o4), "+v"(n), "+v"(os), "+v"(ou), "+v"(ou2), "+v"(oa), "+v"(ow));
  u32x4 in, ia; u32x8 iu; u32x32 rv; u32x16 rs; u32x8 rt;
#define G_p "global_load_dword v0, %[n], %[lar] nt\n global_load_dword v1, %[n], %[aim] nt\n global_load_dword v2, %[n], %[cre] nt\n global_load_dword v3, %[n], %[cim] nt\n"
#define G_n "global_load_dword v244, %[os], %[ws] offset:256 sc1\n"
#define G_u "global_load_dwordx4 v[4:7], %[ou], %[u] nt\n global_load_dwordx4 v[8:11], %[ou2], %[u] nt\n"
#define G_x "global_load_dword %[dm], %[oa], %[x]\n"
#define G_q "global_load_dword %[dm], %[ow], %[wq] nt\n global_load_dword %[dm], %[ow], %[wq]\n"
  asm volatile("s_load_dword s97, %[ldt], %[o4]\n s_load_dword s98, %[dsk], %[o4]\n" HS_SEQ : "=&{s97}"(p_dt), "=&{s98}"(p_dd), "=&{v[0:3]}"(in), "=&{v[4:11]}"(iu), "=&{v[224:255]}"(rv), [dm] "+v"(dm)
               : [n] "v"(n), [os] "v"(os), [ou] "v"(ou), [ou2] "v"(ou2), [oa] "v"(oa), [ow] "v"(ow), [lar] "s"(lar), [aim] "s"(aim), [cre] "s"(cre), [cim] "s"(cim), [ws] "s"(ws), [u] "s"(u), [x] "s"(x), [wq] "s"(wq),
                 [ldt] "s"(ldt), [dsk] "s"(dsk), [o4] "s"(o4) : "memory");
  asm volatile("s_load_dword s96, %0, %1" :: "s"(ssm_tab), "s"((r * 4 + w) * 64) : "s96", "memory");
  asm volatile(SETUP0_ASM : "+{v[224:255]}"(rv), "={s[64:79]}"(rs), "={s[80:87]}"(rt) : [w4k] "s"(w * 4096) : "s60", "scc");
  const int wb = h * 256 - 4096;
  rsrc_t qsrd = RSRC((const char*)wq + wb, (4 << 20) - wb), xas = RSRC((const char*)x + ta * 32768, 32768), xbs = RSRC((const char*)x + tb2 * 32768, 32768);
  rsrc_t ksrd = RSRC((const char*)wk + wb, (4 << 20) - wb), vsrd = RSRC((const char*)wv + wb, (4 << 20) - wb);
  u32x8 sel;
  ia[0] = S_T + w * 8192 + (lane >> 3) * 144 + (lane & 7) * 16; ia[1] = S_T + w * 8192 + (lane & 3) * 576 + (lane >> 2) * 4;
  ia[2] = UTO + ((pair & ~31) + (lane >> 2)) * 8192 + (pair & 31) * 256 + w * 64 + (lane & 3) * 16;
  { const int q = g == 0 || g == 3 ? b >> 1 : 8; const unsigned one = 0x3c00u << ((b & 1) * 16);
#pragma unroll
    for (int i = 0; i < 8; ++i) sel[i] = q == i ? one : 0u; }
  asm volatile("" : "+{v[16:23]}"(sel));
  const int wj = (xa & 4) | (h & 3);
  rsrc_t wsr = RSRC(ws, 96 << 20), kvs = RSRC(ws + KVO + (size_t)h * (1u << 20), (xb * 4 + 4) * 16384), wos = RSRC((const char*)wo + ((((xa & 3) * 8 + xcd) * 128 + 32 * w) * 1024 + wj * 128), 32768 - wj * 128);
  rsrc_t cas = RSRC((const char*)ckv + ta * 65536, 65536), cbs = RSRC((const char*)ckv + ta * 65536 + 32768, 32768);
  asm volatile(SETUP1_ASM : FIX : [w4k] "s"(w * 4096), [da] "s"(ta), [h] "s"(h), [pair] "s"(pair) : "s60", "s61", "s62", "s63", "scc");
  unsigned ps = lane < 8 ? (unsigned)((pair & ~31) + 8 * w + lane) * 1024u + 256 + (pair & 31) * 4 : 0x80000000u;
  PIN(ps);
  unsigned long long tabp = (unsigned long long)ssm_tab + hf * 4096;
  asm volatile("" : "+s"(kvs), "+s"(vsrd), "+s"(ksrd), "+s"(cas), "+s"(cbs), "+s"(out), "+s"(wos), "+s"(tabp), "+s"(wsr));
  asm volatile(MAIN_ASM
               : FIX, "+{v60}"(ps), "+{v61}"(dm), "+{v[0:3]}"(in), "+{v[4:11]}"(iu), "+{v[12:15]}"(ia), "+{v[16:23]}"(sel)
               : [wsr] "s"(wsr), [kvs] "s"(kvs), [xa] "s"(xas), [xb] "s"(xbs), [ca] "s"(cas), [cb] "s"(cbs), [wk] "s"(ksrd), [wv] "s"(vsrd), [wq] "s"(qsrd), [out] "s"(out), [dd] "{s98}"(p_dd), [dt] "{s97}"(p_dt),
                 [wo] "s"(wos), [tab] "s"(tabp), [w4k] "s"(w * 4096), [da] "s"(ta), [db] "s"(tb2), [h] "s"(h), [pair] "s"(pair), [wj] "s"(wj * 3968), [po] "s"((80u << 20) + (((pair >> 2) * 262144 + (pair & 3) * 34816 + w * 8192) >> PSH))
               : MAIN_CLOB);
}

extern "C" void kernel_launch(void* const* d_in, const int* in_sizes, int n_in, void* d_out, int out_size, void* d_ws, size_t ws_size, hipStream_t stream) {
  const u16 *u = (const u16*)d_in[0], *lar = (const u16*)d_in[1], *aim = (const u16*)d_in[2], *cre = (const u16*)d_in[3], *cim = (const u16*)d_in[4], *ldt = (const u16*)d_in[5], *dsk = (const u16*)d_in[6];
  const u16 *x = (const u16*)d_in[7], *ckv = (const u16*)d_in[8], *wq = (const u16*)d_in[9], *wk = (const u16*)d_in[10], *wv = (const u16*)d_in[11], *wo = (const u16*)d_in[12];
  k_main<<<256, 256, 0, stream>>>(u, lar, aim, cre, cim, ldt, dsk, x, ckv, wq, wk, wv, wo, (char*)d_ws, (u16*)d_out);
}
```
